```python
import torch

def _softmax(x, axis=-1):
    return torch.softmax(x, dim=axis)
def _gelu(x):
    return torch.nn.functional.gelu(x, approximate="tanh")
def _silu(x):
    return torch.nn.functional.silu(x)
def _sigmoid(x):
    return torch.sigmoid(x)
def _layernorm(x, axis=-1, eps=1e-5):
    m = x.mean(dim=axis, keepdim=True)
    v = x.var(dim=axis, keepdim=True, unbiased=False)
    return (x - m) / torch.sqrt(v + eps)
def _scatter_add(tgt, idx, src, axis=0):
    return tgt.clone().index_add_(axis, idx, src)
def _segment_sum(src, seg, n):
    out = torch.zeros((n,) + src.shape[1:], dtype=src.dtype)
    return out.index_add_(0, seg, src)
def _pad(x, pw):
    flat = tuple(v for pair in reversed(pw) for v in pair)
    return torch.nn.functional.pad(x, flat)

kernel_name = 'parallel_sum__mha_block_cached__geglu_ffn__decode'

def setup_inputs():

    _w = (lambda a: (lambda t: t.to(torch.bfloat16) if t.is_floating_point() else t)(torch.as_tensor(a)))
    def _A_setup():
        B, Sq, D = 1, 1, 1024
        H, Sk, Dh = 32, 32768, 128
        return {
            "x": _w((lambda *s: torch.randn(*s, dtype=torch.bfloat16) * 0.05)(B, Sq, D)),
            "k_cache": _w((lambda *s: torch.randn(*s, dtype=torch.bfloat16) * 3.2)(B, H, Sk, Dh)),
            "v_cache": _w((lambda *s: torch.randn(*s, dtype=torch.bfloat16) * 1.6)(B, H, Sk, Dh)),
            "wq": _w((lambda *s: torch.randn(*s, dtype=torch.bfloat16) * 3.2)(D, H * Dh) * (D ** -0.5)),
            "wo": _w((lambda *s: torch.randn(*s, dtype=torch.bfloat16) * 1.6)(H * Dh, D) * ((H * Dh) ** -0.5)),
        }
    _A_in = _A_setup()

    def _B_setup():
        B, S, D, F = 1, 1, 1024, 3584
        return {
            "x": _w((lambda *s: torch.randn(*s, dtype=torch.bfloat16) * 0.05)(B, S, D)),
            "w_gate": _w((lambda *s: torch.randn(*s, dtype=torch.bfloat16) * 0.05)(D, F)),
            "w_up": _w((lambda *s: torch.randn(*s, dtype=torch.bfloat16) * 0.05)(D, F)),
            "w_down": _w((lambda *s: torch.randn(*s, dtype=torch.bfloat16) * 0.05)(F, D)),
        }
    _B_in = _B_setup()

    return {
        "A_x": _A_in["x"],
        "A_k_cache": _A_in["k_cache"],
        "A_v_cache": _A_in["v_cache"],
        "A_wq": _A_in["wq"],
        "A_wo": _A_in["wo"],
        "B_x": _B_in["x"],
        "B_w_gate": _B_in["w_gate"],
        "B_w_up": _B_in["w_up"],
        "B_w_down": _B_in["w_down"],
    }

def reference(A_x, A_k_cache, A_v_cache, A_wq, A_wo, B_x, B_w_gate, B_w_up, B_w_down):
    def _A_ref(x, k_cache, v_cache, wq, wo):
        B, Sq, D = x.shape
        H, Dh = 32, 128
        q = (lambda x, *d: x.permute(*d))((x @ wq).reshape(B, Sq, H, Dh), 0, 2, 1, 3)
        scores = torch.einsum("bhqd,bhkd->bhqk", q, k_cache) * (Dh ** -0.5)
        p = _softmax(scores, axis=-1)
        o = torch.einsum("bhqk,bhkd->bhqd", p, v_cache)
        return (lambda x, *d: x.permute(*d))(o, 0, 2, 1, 3).reshape(B, Sq, H * Dh) @ wo

    def _B_ref(x, w_gate, w_up, w_down):
        return (_gelu(x @ w_gate) * (x @ w_up)) @ w_down

    x = A_x
    xn = _layernorm(x, axis=-1)
    return x + _A_ref(xn, A_k_cache, A_v_cache, A_wq, A_wo) + _B_ref(xn, B_w_gate, B_w_up, B_w_down)
```

```cpp
#include <hip/hip_runtime.h>
#include <hip/hip_bf16.h>
#include <stdint.h>

#define MIMIC 1

typedef unsigned u32x4 __attribute__((ext_vector_type(4)));
typedef float f32x4 __attribute__((ext_vector_type(4)));
typedef __bf16 bf16x2 __attribute__((ext_vector_type(2)));
typedef unsigned short u16;

constexpr int D = 1024, H = 32, SK = 32768, DH = 128, F = 3584;
constexpr int NSPLIT = 8;

__device__ __forceinline__ float bf2f(u16 b) { return __builtin_bit_cast(float, (unsigned)b << 16); }
__device__ __forceinline__ float lo2f(unsigned w) { return __builtin_bit_cast(float, w << 16); }
__device__ __forceinline__ float hi2f(unsigned w) { return __builtin_bit_cast(float, w & 0xffff0000u); }
__device__ __forceinline__ float rbf(float x) {
  __bf16 b = (__bf16)x;
  return (float)b;
}
__device__ __forceinline__ u16 f2bf(float x) { __bf16 b = (__bf16)x; return __builtin_bit_cast(u16, b); }
__device__ __forceinline__ float dot2(unsigned k, unsigned q, float acc) {
  return __builtin_amdgcn_fdot2_f32_bf16(__builtin_bit_cast(bf16x2, k), __builtin_bit_cast(bf16x2, q), acc, false);
}
template <int CTRL> __device__ __forceinline__ float dpp(float x) {
  return __builtin_bit_cast(float, __builtin_amdgcn_mov_dpp(__builtin_bit_cast(int, x), CTRL, 0xf, 0xf, true));
}
constexpr int XOR1 = 0xB1, XOR2 = 0x4E, XOR7 = 0x141, XOR8 = 0x128, ROR4 = 0x124;
__device__ __forceinline__ float xrow16_max(float x) {
  auto s = __builtin_amdgcn_permlane16_swap(__float_as_uint(x), __float_as_uint(x), false, false);
  x = fmaxf(__uint_as_float(s[0]), __uint_as_float(s[1]));
  auto t = __builtin_amdgcn_permlane32_swap(__float_as_uint(x), __float_as_uint(x), false, false);
  return fmaxf(__uint_as_float(t[0]), __uint_as_float(t[1]));
}
__device__ __forceinline__ float xrow16_sum(float x) {
  auto s = __builtin_amdgcn_permlane16_swap(__float_as_uint(x), __float_as_uint(x), false, false);
  x = __uint_as_float(s[0]) + __uint_as_float(s[1]);
  auto t = __builtin_amdgcn_permlane32_swap(__float_as_uint(x), __float_as_uint(x), false, false);
  return __uint_as_float(t[0]) + __uint_as_float(t[1]);
}
__device__ __forceinline__ float row16_sum(float x) {
  x += dpp<XOR1>(x); x += dpp<XOR2>(x); x += dpp<XOR7>(x); x += dpp<XOR8>(x); return x;
}
__device__ __forceinline__ float row16_max(float x) {
  x = fmaxf(x, dpp<XOR1>(x)); x = fmaxf(x, dpp<XOR2>(x)); x = fmaxf(x, dpp<XOR7>(x)); x = fmaxf(x, dpp<XOR8>(x)); return x;
}
__device__ __forceinline__ float wave_sum(float x) { return xrow16_sum(row16_sum(x)); }
__device__ __forceinline__ float wave_max(float x) { return xrow16_max(row16_max(x)); }

__device__ __forceinline__ void layernorm_to_lds(const u16* x, float* xn_lds, int tid, int nthr) {
  const int lane = tid & 63;
  u32x4 a = *(const u32x4*)(x + lane * 16), b = *(const u32x4*)(x + lane * 16 + 8);
  float v[16];
#pragma unroll
  for (int i = 0; i < 4; ++i) { v[2 * i] = lo2f(a[i]); v[2 * i + 1] = hi2f(a[i]); v[8 + 2 * i] = lo2f(b[i]); v[8 + 2 * i + 1] = hi2f(b[i]); }
  float s = 0.f;
#pragma unroll
  for (int i = 0; i < 16; ++i) s += v[i];
  s = wave_sum(s);
  const float mean = s * (1.f / D);
  float ss = 0.f;
#pragma unroll
  for (int i = 0; i < 16; ++i) { float d = v[i] - mean; ss += d * d; }
  ss = wave_sum(ss);
  const float var = ss * (1.f / D);
  const float mb = rbf(mean), vb = rbf(var);
  const float den = rbf(sqrtf(rbf(vb + 1e-5f)));
#pragma unroll
  for (int i = 0; i < 16; ++i) v[i] = rbf(rbf(v[i] - mb) / den);
  if (tid < 64) {
#pragma unroll
    for (int i = 0; i < 16; ++i) xn_lds[lane * 16 + i] = v[i];
  }
}

__device__ __forceinline__ float gelu_tanh(float x) {
  const float k0 = 0.7978845608028654f, k1 = 0.044715f;
  float u = k0 * (x + k1 * x * x * x);
  return 0.5f * x * (1.f + tanhf(u));
}

__global__ void __launch_bounds__(256) prep_kernel(const u16* __restrict__ x, const u16* __restrict__ wq, const u16* __restrict__ wg,
                                                   const u16* __restrict__ wu, float* __restrict__ qout, float* __restrict__ hout) {
  __shared__ float xn[D];
  __shared__ float red[2][32][64];
  const int tid = threadIdx.x, b = blockIdx.x;
  layernorm_to_lds(x, xn, tid, 256);
  __syncthreads();
  const int c = tid & 7, r = tid >> 3;
  const bool isq = b < 64;
  const u16* w0 = isq ? wq + b * 64 : wg + (b - 64) * 64;
  const u16* w1 = wu + (b - 64) * 64;
  const int ld = isq ? H * DH : F;
  float a0[8] = {0}, a1[8] = {0};
  for (int k = r; k < D; k += 32) {
    const float xv = xn[k];
    u32x4 w = *(const u32x4*)(w0 + (size_t)k * ld + c * 8);
#pragma unroll
    for (int i = 0; i < 4; ++i) { a0[2 * i] += xv * lo2f(w[i]); a0[2 * i + 1] += xv * hi2f(w[i]); }
    if (!isq) {
      u32x4 v = *(const u32x4*)(w1 + (size_t)k * ld + c * 8);
#pragma unroll
      for (int i = 0; i < 4; ++i) { a1[2 * i] += xv * lo2f(v[i]); a1[2 * i + 1] += xv * hi2f(v[i]); }
    }
  }
#pragma unroll
  for (int i = 0; i < 8; ++i) { red[0][r][c * 8 + i] = a0[i]; red[1][r][c * 8 + i] = a1[i]; }
  __syncthreads();
  if (tid < 64) {
    float s0 = 0.f, s1 = 0.f;
    for (int i = 0; i < 32; ++i) { s0 += red[0][i][tid]; s1 += red[1][i][tid]; }
    if (isq) qout[b * 64 + tid] = rbf(s0);
    else {
      float g = rbf(gelu_tanh(rbf(s0)));
      hout[(b - 64) * 64 + tid] = rbf(g * rbf(s1));
    }
  }
}

constexpr int NW = 8;
constexpr int KEYS_WG = SK / NSPLIT;
constexpr int KEYS_W = KEYS_WG / NW;
constexpr int UNITS = KEYS_W / 64;

__global__ void __launch_bounds__(NW * 64, 2) attn_kernel(const u16* __restrict__ K, const u16* __restrict__ V, const float* __restrict__ q,
                                                          float* __restrict__ part) {
  __shared__ float mrg[NW][DH + 4];
  const int tid = threadIdx.x, lane = tid & 63, wave = __builtin_amdgcn_readfirstlane(tid >> 6);
  const int r = lane >> 4, c = lane & 15;
  const int bid = blockIdx.x, xcd = bid & 7, j = bid >> 3;
  const int h = xcd * 4 + (j >> 3), s = j & 7;
  const size_t base = ((size_t)h * SK + (size_t)s * KEYS_WG + (size_t)wave * 64) * DH + lane * 8;
  const u32x4* kp = (const u32x4*)(K + base);
  const u32x4* vp = (const u32x4*)(V + base);
  constexpr int USTR = NW * 64 * DH / 8;
  u32x4 kb[16], vb[16];
#pragma unroll
  for (int i = 0; i < 16; ++i) kb[i] = __builtin_nontemporal_load(kp + i * 64);
#pragma unroll
  for (int i = 0; i < 16; ++i) vb[i] = __builtin_nontemporal_load(vp + i * 64);
  unsigned qp[4];
#pragma unroll
  for (int i = 0; i < 4; ++i) {
    const float a = q[h * DH + c * 8 + 2 * i], b = q[h * DH + c * 8 + 2 * i + 1];
    qp[i] = (unsigned)f2bf(a) | ((unsigned)f2bf(b) << 16);
  }
  float m = -INFINITY, l = 0.f, acc[8];
#pragma unroll
  for (int i = 0; i < 8; ++i) acc[i] = 0.f;
  constexpr float SCALE = 0.08838834764831845f, LOG2E = 1.4426950408889634f;
  for (int u = 0; u < UNITS; ++u) {
    const bool more = u + 1 < UNITS;
    const u32x4* kn = kp + (size_t)(more ? u + 1 : u) * USTR;
    const u32x4* vn = vp + (size_t)(more ? u + 1 : u) * USTR;
    float x[16];
#pragma unroll
    for (int i = 0; i < 16; ++i) {
      float d = 0.f;
#pragma unroll
      for (int t = 0; t < 4; ++t) d = dot2(kb[i][t], qp[t], d);
      x[i] = d;
      if (more) kb[i] = __builtin_nontemporal_load(kn + i * 64);
    }
#define TR_STEP(HALF, CTRL, BIT) _Pragma("unroll") for (int hh = 0; hh < (HALF); ++hh) { \
      const float lo_ = x[hh], hi_ = x[hh + (HALF)]; const float t1_ = lo_ + dpp<CTRL>(lo_), t2_ = hi_ + dpp<CTRL>(hi_); \
      x[hh] = (c & (BIT)) ? t2_ : t1_; }
    TR_STEP(8, XOR8, 8) TR_STEP(4, XOR7, 4) TR_STEP(2, XOR2, 2) TR_STEP(1, XOR1, 1)
#undef TR_STEP
    const float sc = rbf(rbf(x[0]) * SCALE) * LOG2E;
    const float pm = wave_max(sc);
    const float mn = fmaxf(m, pm);
    const float alpha = __builtin_amdgcn_exp2f(m - mn);
    const float p = __builtin_amdgcn_exp2f(sc - mn);
    l = l * alpha + p;
    m = mn;
#pragma unroll
    for (int i = 0; i < 8; ++i) acc[i] *= alpha;
#define PV_STEP(I) { const float pb = dpp<0x150 + (I)>(p); \
      _Pragma("unroll") for (int t = 0; t < 4; ++t) { acc[2 * t] = fmaf(pb, lo2f(vb[I][t]), acc[2 * t]); acc[2 * t + 1] = fmaf(pb, hi2f(vb[I][t]), acc[2 * t + 1]); } \
      if (more) vb[I] = __builtin_nontemporal_load(vn + (I) * 64); }
    PV_STEP(0) PV_STEP(1) PV_STEP(2) PV_STEP(3) PV_STEP(4) PV_STEP(5) PV_STEP(6) PV_STEP(7)
    PV_STEP(8) PV_STEP(9) PV_STEP(10) PV_STEP(11) PV_STEP(12) PV_STEP(13) PV_STEP(14) PV_STEP(15)
#undef PV_STEP
  }
#pragma unroll
  for (int i = 0; i < 8; ++i) acc[i] = xrow16_sum(acc[i]);
  l = wave_sum(l);
  if (r == 0) {
#pragma unroll
    for (int i = 0; i < 8; ++i) mrg[wave][4 + c * 8 + i] = acc[i];
    if (c == 0) { mrg[wave][0] = m; mrg[wave][1] = l; }
  }
  __syncthreads();
  if (tid < DH) {
    float M = -INFINITY;
#pragma unroll
    for (int w = 0; w < NW; ++w) M = fmaxf(M, mrg[w][0]);
    float L = 0.f, o = 0.f;
#pragma unroll
    for (int w = 0; w < NW; ++w) { const float wt = __builtin_amdgcn_exp2f(mrg[w][0] - M); L += wt * mrg[w][1]; o += wt * mrg[w][4 + tid]; }
    float* pp = part + (size_t)(h * NSPLIT + s) * (DH + 4);
    pp[4 + tid] = o;
    if (tid == 0) { pp[0] = M; pp[1] = L; }
  }
}

__global__ void __launch_bounds__(256) outp_kernel(const float* __restrict__ part, const float* __restrict__ hv, const u16* __restrict__ wo,
                                                   const u16* __restrict__ wd, float* __restrict__ pout) {
  __shared__ float in[256];
  __shared__ float red[32][64];
  const int tid = threadIdx.x, cc = blockIdx.x & 15, ks = blockIdx.x >> 4;
  const bool isa = ks < 16;
  if (isa) {
    const int h = ks * 2 + (tid >> 7), d = tid & 127;
    const float* pp = part + (size_t)h * NSPLIT * (DH + 4);
    float M = -INFINITY;
    for (int s = 0; s < NSPLIT; ++s) M = fmaxf(M, pp[s * (DH + 4)]);
    float L = 0.f, o = 0.f;
    for (int s = 0; s < NSPLIT; ++s) { const float wt = __builtin_amdgcn_exp2f(pp[s * (DH + 4)] - M); L += wt * pp[s * (DH + 4) + 1]; o += wt * pp[s * (DH + 4) + 4 + d]; }
    in[tid] = rbf(o / L);
  } else {
    in[tid] = hv[(ks - 16) * 256 + tid];
  }
  __syncthreads();
  const u16* w = isa ? wo + (size_t)ks * 256 * D : wd + (size_t)(ks - 16) * 256 * D;
  const int c = tid & 7, r = tid >> 3;
  float a[8] = {0};
  for (int k = r; k < 256; k += 32) {
    const float xv = in[k];
    u32x4 wv = *(const u32x4*)(w + (size_t)k * D + cc * 64 + c * 8);
#pragma unroll
    for (int i = 0; i < 4; ++i) { a[2 * i] += xv * lo2f(wv[i]); a[2 * i + 1] += xv * hi2f(wv[i]); }
  }
#pragma unroll
  for (int i = 0; i < 8; ++i) red[r][c * 8 + i] = a[i];
  __syncthreads();
  if (tid < 64) {
    float s0 = 0.f;
    for (int i = 0; i < 32; ++i) s0 += red[i][tid];
    pout[ks * D + cc * 64 + tid] = s0;
  }
}

__global__ void __launch_bounds__(256) final_kernel(const u16* __restrict__ x, const float* __restrict__ pout, u16* __restrict__ out) {
  const int col = blockIdx.x * 256 + threadIdx.x;
  float a = 0.f, f = 0.f;
  for (int k = 0; k < 16; ++k) a += pout[k * D + col];
  for (int k = 16; k < 30; ++k) f += pout[k * D + col];
  out[col] = f2bf(rbf(bf2f(x[col]) + rbf(a)) + rbf(f));
}

extern "C" void kernel_launch(void* const* d_in, const int* in_sizes, int n_in, void* d_out, int out_size, void* d_ws, size_t ws_size,
                              hipStream_t stream) {
  const u16* x = (const u16*)d_in[0];
  const u16* K = (const u16*)d_in[1];
  const u16* V = (const u16*)d_in[2];
  const u16* wq = (const u16*)d_in[3];
  const u16* wo = (const u16*)d_in[4];
  const u16* wg = (const u16*)d_in[6];
  const u16* wu = (const u16*)d_in[7];
  const u16* wd = (const u16*)d_in[8];
  float* ws = (float*)d_ws;
  float* q = ws;
  float* hv = ws + 4096;
  float* part = ws + 8192;
  float* pout = ws + 8192 + 256 * 132 + 256;
  prep_kernel<<<120, 256, 0, stream>>>(x, wq, wg, wu, q, hv);
  attn_kernel<<<256, NW * 64, 0, stream>>>(K, V, q, part);
  outp_kernel<<<16 * 30, 256, 0, stream>>>(part, hv, wo, wd, pout);
  final_kernel<<<4, 256, 0, stream>>>(x, pout, (u16*)d_out);
}
```

```cpp
#include <hip/hip_runtime.h>
#include <stdint.h>

typedef unsigned u32x4 __attribute__((ext_vector_type(4)));
typedef unsigned u32x2 __attribute__((ext_vector_type(2)));
typedef float f32x4 __attribute__((ext_vector_type(4)));
typedef float f32x2 __attribute__((ext_vector_type(2)));
typedef __bf16 bf16x2 __attribute__((ext_vector_type(2)));
typedef unsigned short u16;
#define LAS __attribute__((address_space(3)))
typedef __amdgpu_buffer_rsrc_t rsrc_t;

#define T_MAIN 7750
#define T_OFF 0
#define PACE_G 1
#define QSLEEP 1
#define AUX_T0 500
#define AUX_STEP 200

constexpr int D = 1024, H = 32, SK = 32768, DH = 128, F = 3584;
constexpr int NMAIN = 8, NAUX = 4, NTHR = (NMAIN + NAUX) * 64;
constexpr int NT = 2  , SC1 = 16;
constexpr int OOB = 0x7ffffff0;
constexpr unsigned WS_EPOCH = 0, WS_Q = 4096, WS_PART = WS_Q + 256 * 1024, PART_STR = 2048, WS_Y = WS_PART + 256 * PART_STR, Y_STR = 1024,
                   WS_G1 = WS_Y + 256 * Y_STR, WS_FFN = WS_G1 + 32 * F * 8, FFN_STR = 8192, WS_END = WS_FFN + 224 * FFN_STR;
constexpr unsigned WS_STAMP = 8u << 20;
constexpr int L_XN = 0, L_QP = 1024, L_QS = L_QP + 4 * 128, L_MRG = L_QS + 4 * 128, MRG_STR = 132, L_FP = L_MRG + 8 * MRG_STR, L_TMP = L_FP + 4 * 448,
              L_CNT = L_TMP + 4 * 256, L_END = L_CNT + 4;

__device__ __forceinline__ float lo2f(unsigned w) { return __builtin_bit_cast(float, w << 16); }
__device__ __forceinline__ float hi2f(unsigned w) { return __builtin_bit_cast(float, w & 0xffff0000u); }
__device__ __forceinline__ float rbf(float x) { return (float)(__bf16)x; }
__device__ __forceinline__ unsigned pack2(float a, float b) { bf16x2 v = {(__bf16)a, (__bf16)b}; return __builtin_bit_cast(unsigned, v); }
__device__ __forceinline__ float dot2(unsigned k, unsigned q, float acc) {
  return __builtin_amdgcn_fdot2_f32_bf16(__builtin_bit_cast(bf16x2, k), __builtin_bit_cast(bf16x2, q), acc, false);
}
template <int CTRL> __device__ __forceinline__ float dpp(float x) {
  return __builtin_bit_cast(float, __builtin_amdgcn_mov_dpp(__builtin_bit_cast(int, x), CTRL, 0xf, 0xf, true));
}
constexpr int XOR1 = 0xB1, XOR2 = 0x4E, XOR7 = 0x141, ROR8 = 0x128, ROR4 = 0x124, SHL1 = 0x101, BCAST = 0x150;
__device__ __forceinline__ float swap32_sum(float x) {
  auto t = __builtin_amdgcn_permlane32_swap(__float_as_uint(x), __float_as_uint(x), false, false);
  return __uint_as_float(t[0]) + __uint_as_float(t[1]);
}
__device__ __forceinline__ float xrow_sum(float x) {
  auto s = __builtin_amdgcn_permlane16_swap(__float_as_uint(x), __float_as_uint(x), false, false);
  return swap32_sum(__uint_as_float(s[0]) + __uint_as_float(s[1]));
}
__device__ __forceinline__ float xrow_max(float x) {
  auto s = __builtin_amdgcn_permlane16_swap(__float_as_uint(x), __float_as_uint(x), false, false);
  x = fmaxf(__uint_as_float(s[0]), __uint_as_float(s[1]));
  auto t = __builtin_amdgcn_permlane32_swap(__float_as_uint(x), __float_as_uint(x), false, false);
  return fmaxf(__uint_as_float(t[0]), __uint_as_float(t[1]));
}
__device__ __forceinline__ float parity_sum(float x) {
  x += dpp<XOR2>(x); x += dpp<ROR4>(x); x += dpp<ROR8>(x); return xrow_sum(x);
}
__device__ __forceinline__ float wave_sum(float x) { x += dpp<XOR1>(x); return parity_sum(x); }
__device__ __forceinline__ float exp2_(float x) { return __builtin_amdgcn_exp2f(x); }
__device__ __forceinline__ rsrc_t mkrsrc(const void* p, unsigned bytes) { return __builtin_amdgcn_make_buffer_rsrc((void*)p, 0, (int)bytes, 0x00020000); }
__device__ __forceinline__ u32x4 ld16(rsrc_t r, int voff, int soff, int aux_nt) {
  return aux_nt == 2 ? __builtin_amdgcn_raw_buffer_load_b128(r, voff, soff, 2) : __builtin_amdgcn_raw_buffer_load_b128(r, voff, soff, 0);
}
__device__ __forceinline__ u32x4 ld16_sc1(rsrc_t r, int voff, int soff) { return __builtin_amdgcn_raw_buffer_load_b128(r, voff, soff, SC1); }
__device__ __forceinline__ void st16_sc1(rsrc_t r, int voff, int soff, u32x4 v) { __builtin_amdgcn_raw_buffer_store_b128(v, r, voff, soff, SC1); }
#define SB() __builtin_amdgcn_sched_barrier(0)
__device__ __forceinline__ void pace_wait(unsigned t0, unsigned due) {
  asm volatile("1:\n\ts_memrealtime s[96:97]\n\ts_waitcnt lgkmcnt(0)\n\ts_sub_u32 s96, s96, %0\n\ts_cmp_lt_i32 s96, %1\n\ts_cbranch_scc0 2f\n\ts_sleep 1\n\ts_branch 1b\n2:"
               :: "s"(t0), "s"(due) : "s96", "s97", "scc", "memory");
}
__device__ __forceinline__ void fma8(float (&acc)[8], float s, u32x4 w) {
#pragma unroll
  for (int t = 0; t < 4; ++t) { acc[2 * t] = fmaf(s, lo2f(w[t]), acc[2 * t]); acc[2 * t + 1] = fmaf(s, hi2f(w[t]), acc[2 * t + 1]); }
}
__device__ __forceinline__ u32x4 gran(float a, float b, unsigned ta, unsigned tb) { return u32x4{__float_as_uint(a), ta, __float_as_uint(b), tb}; }
__device__ __forceinline__ bool gok(u32x4 g, unsigned ta, unsigned tb) { return g[1] == ta && g[3] == tb; }
constexpr unsigned SPIN_CAP = 1u << 20;

#define STAMP(i) do { } while (0)
#define STAMP_DUMP() do { } while (0)

__device__ __forceinline__ void aux_sync(LAS float* cnt, int lane) {
  asm volatile("s_waitcnt lgkmcnt(0)" ::: "memory");
  if (lane == 0) __hip_atomic_fetch_add((LAS unsigned*)cnt, 1u, __ATOMIC_RELAXED, __HIP_MEMORY_SCOPE_WORKGROUP);
  for (unsigned sp = 0; sp < SPIN_CAP; ++sp) {
    if (__builtin_amdgcn_readfirstlane(*(volatile LAS unsigned*)cnt) >= (unsigned)NAUX) break;
    __builtin_amdgcn_s_sleep(1);
  }
  asm volatile("" ::: "memory");
}

__global__ void __launch_bounds__(NTHR, 3)
fused_decode_kernel(const u16* __restrict__ x, const u16* __restrict__ Kc, const u16* __restrict__ Vc, const u16* __restrict__ wq,
                    const u16* __restrict__ wo, const u16* __restrict__ wg, const u16* __restrict__ wu, const u16* __restrict__ wd,
                    u16* __restrict__ out, void* ws) {
  __shared__ __attribute__((aligned(16))) float lds_[L_END];
  LAS float* lds = (LAS float*)lds_;
  const unsigned t0 = (unsigned)__builtin_amdgcn_s_memrealtime();
  const int tid = threadIdx.x, lane = tid & 63, wave = __builtin_amdgcn_readfirstlane(tid >> 6);
  const int r = lane >> 4, c = lane & 15;
  const int bid = blockIdx.x, xl = bid & 7, j = bid >> 3;
  const int h = xl * 4 + (j >> 3), s = j & 7;
  const int a = wave - NMAIN;
  STAMP(0);

  if (wave < NMAIN) {
    const rsrc_t rk = mkrsrc(Kc, (unsigned)H * SK * DH * 2), rv = mkrsrc(Vc, (unsigned)H * SK * DH * 2);
    u32x4 kb[8], vb[8];
    const unsigned kvbase = (unsigned)h * (8u << 20) + (unsigned)s * (1u << 20) + wave * 1024;
    const unsigned rot = 32u * ((h + (h >> 2) + (h >> 4)) & 3);
    const int kvoff = lane * 16;
    constexpr int STEP = ((int)T_MAIN << 16) / 256 * PACE_G;
    int due = (int)T_OFF * 65536 - STEP + (STEP / 256) * (wave * 32 + j);
    {
      const unsigned tb = kvbase + rot * 8192;
#pragma unroll
      for (int i = 0; i < 8; ++i) {
        if (i % PACE_G == 0) { due += STEP; pace_wait(t0, due >> 16); }
        kb[i] = ld16(rk, kvoff, tb + i * 8192, NT); SB();
        if (i == 0) { STAMP(12); SB(); }
        if (i == 1) { __builtin_amdgcn_s_barrier(); SB(); STAMP(10); }
      }
      STAMP(11);
#pragma unroll
      for (int i = 0; i < 8; ++i) { if (i % PACE_G == 0) { due += STEP; pace_wait(t0, due >> 16); } vb[i] = ld16(rv, kvoff, tb + i * 8192, NT); SB(); }
    }
    STAMP(1);
    __syncthreads();
    unsigned qp[4];
    {
      f32x4 q0 = *(LAS f32x4*)(lds + L_QS + r * 128 + 8 * c), q1 = *(LAS f32x4*)(lds + L_QS + r * 128 + 8 * c + 4);
#pragma unroll
      for (int e = 0; e < 4; ++e) { q0[e] = xrow_sum(q0[e]); q1[e] = xrow_sum(q1[e]); }
      qp[0] = pack2(q0[0], q0[1]); qp[1] = pack2(q0[2], q0[3]); qp[2] = pack2(q1[0], q1[1]); qp[3] = pack2(q1[2], q1[3]);
    }
    STAMP(2);
    float m = -INFINITY, l = 0.f, acc[8] = {0.f, 0.f, 0.f, 0.f, 0.f, 0.f, 0.f, 0.f};
    constexpr float SCALE = 0.08838834764831845f, LOG2E = 1.4426950408889634f;
    auto turn = [&](const bool more, const unsigned tb) __attribute__((always_inline)) {
      float sx[8];
#pragma unroll
      for (int i = 0; i < 8; ++i) {
        float d = 0.f;
#pragma unroll
        for (int t = 0; t < 4; ++t) d = dot2(kb[i][t], qp[t], d);
        asm volatile("" : "+v"(d));
        sx[i] = d;
        SB();
        if (more) {
          if (i % PACE_G == 0) { due += STEP; pace_wait(t0, due >> 16); }
          kb[i] = ld16(rk, kvoff, tb + i * 8192, NT);
          SB();
        }
      }
#define TR_STEP(HALF, CTRL, BIT) _Pragma("unroll") for (int hh = 0; hh < (HALF); ++hh) { \
        const float lo_ = sx[hh], hi_ = sx[hh + (HALF)]; const float t1_ = lo_ + dpp<CTRL>(lo_), t2_ = hi_ + dpp<CTRL>(hi_); \
        sx[hh] = (c & (BIT)) ? t2_ : t1_; }
      TR_STEP(4, ROR8, 8) TR_STEP(2, XOR7, 4) TR_STEP(1, XOR2, 2)
#undef TR_STEP
      const float sraw = sx[0] + dpp<XOR1>(sx[0]);
      const float sc = rbf(rbf(sraw) * SCALE) * LOG2E;
      float pm = fmaxf(sc, dpp<XOR2>(sc)); pm = fmaxf(pm, dpp<ROR4>(pm)); pm = fmaxf(pm, dpp<ROR8>(pm)); pm = xrow_max(pm);
      const float mn = fmaxf(m, pm);
      const float alpha = exp2_(m - mn), p = exp2_(sc - mn);
      l = fmaf(l, alpha, p);
      m = mn;
#pragma unroll
      for (int e = 0; e < 8; ++e) acc[e] *= alpha;
      SB();
#define PV_STEP(I) { fma8(acc, dpp<BCAST + 2 * (I)>(p), vb[I]); \
        asm volatile("" : "+v"(acc[0]), "+v"(acc[1]), "+v"(acc[2]), "+v"(acc[3]), "+v"(acc[4]), "+v"(acc[5]), "+v"(acc[6]), "+v"(acc[7])); SB(); \
        if (more) { if ((I) % PACE_G == 0) { due += STEP; pace_wait(t0, due >> 16); } vb[I] = ld16(rv, kvoff, tb + (I) * 8192, NT); SB(); } }
      PV_STEP(0) PV_STEP(1) PV_STEP(2) PV_STEP(3) PV_STEP(4) PV_STEP(5) PV_STEP(6) PV_STEP(7)
#undef PV_STEP
    };
    for (int n = 1; n < 16; ++n) turn(true, kvbase + ((n * 8 + rot) & 127u) * 8192);
    STAMP(3);
    turn(false, 0);
    STAMP(4);
#pragma unroll
    for (int e = 0; e < 8; ++e) acc[e] = xrow_sum(acc[e]);
    l = wave_sum(l) * 0.5f;
    if (r == 0) {
      *(LAS f32x4*)(lds + L_MRG + wave * MRG_STR + 4 + 8 * c) = f32x4{acc[0], acc[1], acc[2], acc[3]};
      *(LAS f32x4*)(lds + L_MRG + wave * MRG_STR + 8 + 8 * c) = f32x4{acc[4], acc[5], acc[6], acc[7]};
      if (c == 0) *(LAS f32x2*)(lds + L_MRG + wave * MRG_STR) = f32x2{m, l};
    }
    __syncthreads();
    STAMP_DUMP();
    return;
  }

  const rsrc_t rx = mkrsrc(x, D * 2), rws = mkrsrc(ws, WS_END);
  unsigned tagA, tagB;
  unsigned long long e1;
  {
    u32x4 xa = ld16(rx, lane * 32, 0, 0), xb = ld16(rx, lane * 32 + 16, 0, 0);
    const u32x2 ep = __builtin_amdgcn_raw_buffer_load_b64(rws, 0, WS_EPOCH, SC1);
    const rsrc_t rwq = mkrsrc(wq, D * H * DH * 2);
    u32x4 w[8];
    const int voff = r * 8192 + c * 16, sb = (s * 128 + a * 32) * 8192 + h * 256;
#pragma unroll
    for (int i = 0; i < 8; ++i) { w[i] = ld16(rwq, voff, sb + i * 32768, NT); SB(); }
    if (a == 0 && lane < 4) *(LAS unsigned*)(lds + L_CNT + lane) = 0u;
    __syncthreads();
    asm volatile("" : "+v"(xa), "+v"(xb));
    STAMP(10);
    float v[16];
#pragma unroll
    for (int i = 0; i < 4; ++i) { v[2 * i] = lo2f(xa[i]); v[2 * i + 1] = hi2f(xa[i]); v[8 + 2 * i] = lo2f(xb[i]); v[9 + 2 * i] = hi2f(xb[i]); }
    float sm = 0.f;
#pragma unroll
    for (int i = 0; i < 16; ++i) sm += v[i];
    const float mean = wave_sum(sm) * (1.f / D);
    float ss = 0.f;
#pragma unroll
    for (int i = 0; i < 16; ++i) { const float d = v[i] - mean; ss = fmaf(d, d, ss); }
    const float var = wave_sum(ss) * (1.f / D);
    const float mb = rbf(mean), den = rbf(sqrtf(rbf(rbf(var) + 1e-5f)));
    const float rinv = 1.0f / den;
#pragma unroll
    for (int i = 0; i < 16; ++i) {
      const float nu = rbf(v[i] - mb), q0 = nu * rinv;
      v[i] = rbf(fmaf(fmaf(-q0, den, nu), rinv, q0));
    }
#pragma unroll
    for (int q = 0; q < 4; ++q) *(LAS f32x4*)(lds + L_XN + q * 256 + 4 * lane) = f32x4{v[q], v[4 + q], v[8 + q], v[12 + q]};
    e1 = (((unsigned long long)__builtin_amdgcn_readfirstlane(ep[1]) << 32) | (unsigned)__builtin_amdgcn_readfirstlane(ep[0])) + 1ull;
    tagA = (unsigned)e1; tagB = (unsigned)(e1 >> 32) + (unsigned)e1 * 0x9E3779B1u;
    float acc[8] = {0.f, 0.f, 0.f, 0.f, 0.f, 0.f, 0.f, 0.f};
#pragma unroll
    for (int q = 0; q < 2; ++q) {
      const f32x4 xv = *(LAS f32x4*)(lds + L_XN + r * 256 + s * 32 + a * 8 + 4 * q);
#pragma unroll
      for (int e = 0; e < 4; ++e) fma8(acc, xv[e], w[4 * q + e]);
    }
#pragma unroll
    for (int e = 0; e < 8; ++e) acc[e] = xrow_sum(acc[e]);
    if (r == 0) {
      *(LAS f32x4*)(lds + L_QP + a * 128 + 8 * c) = f32x4{acc[0], acc[1], acc[2], acc[3]};
      *(LAS f32x4*)(lds + L_QP + a * 128 + 8 * c + 4) = f32x4{acc[4], acc[5], acc[6], acc[7]};
    }
  }
  STAMP(1);
  {
    aux_sync(lds + L_CNT + 1, lane);
    STAMP(11);
    if (a == 0) {
      f32x2 v = *(LAS f32x2*)(lds + L_QP + 2 * lane);
#pragma unroll
      for (int q = 1; q < 4; ++q) v += *(LAS f32x2*)(lds + L_QP + q * 128 + 2 * lane);
      st16_sc1(rws, lane * 16, WS_Q + (h * 8 + s) * 1024, gran(v[0], v[1], tagA, tagB));
    }
    u32x4 g0, g1;
    for (unsigned sp = 0; sp < SPIN_CAP; ++sp) {
      g0 = ld16_sc1(rws, lane * 16, WS_Q + (h * 8 + 2 * a) * 1024);
      g1 = ld16_sc1(rws, lane * 16, WS_Q + (h * 8 + 2 * a + 1) * 1024);
      if (sp == 0) STAMP(12);
      if (__all(gok(g0, tagA, tagB) && gok(g1, tagA, tagB))) break;
      __builtin_amdgcn_s_sleep(QSLEEP);
    }
    STAMP(13);
    *(LAS f32x2*)(lds + L_QS + a * 128 + 2 * lane) =
        f32x2{__uint_as_float(g0[0]) + __uint_as_float(g1[0]), __uint_as_float(g0[2]) + __uint_as_float(g1[2])};
  }
  __syncthreads();
  int adue = AUX_T0 + a * (AUX_STEP / 4);

  {
    const int mat = bid >> 7, rb = (bid >> 3) & 15, cb = bid & 7;
    const rsrc_t rg = mkrsrc(mat ? wu : wg, D * F * 2);
    u32x4 g[16];
    const int voff = lane < 56 ? lane * 16 : OOB, sb = (rb * 64 + a * 16) * (F * 2) + cb * 896;
#pragma unroll
    for (int i = 0; i < 16; ++i) { pace_wait(t0, adue); adue += AUX_STEP; g[i] = ld16(rg, voff, sb + i * (F * 2), NT); SB(); }
    float ag[8] = {0.f, 0.f, 0.f, 0.f, 0.f, 0.f, 0.f, 0.f};
#pragma unroll
    for (int q = 0; q < 4; ++q) {
      const f32x4 xv = *(LAS f32x4*)(lds + L_XN + q * 256 + rb * 16 + a * 4);
#pragma unroll
      for (int e = 0; e < 4; ++e) fma8(ag, xv[e], g[4 * e + q]);
    }
    if (lane < 56) {
      *(LAS f32x4*)(lds + L_FP + a * 448 + lane * 8) = f32x4{ag[0], ag[1], ag[2], ag[3]};
      *(LAS f32x4*)(lds + L_FP + a * 448 + lane * 8 + 4) = f32x4{ag[4], ag[5], ag[6], ag[7]};
    }
    aux_sync(lds + L_CNT, lane);
    const int cl = a * 112 + 2 * (lane < 56 ? lane : 0);
    f32x2 v = *(LAS f32x2*)(lds + L_FP + cl);
#pragma unroll
    for (int q = 1; q < 4; ++q) v += *(LAS f32x2*)(lds + L_FP + q * 448 + cl);
    st16_sc1(rws, lane < 56 ? lane * 16 : OOB, WS_G1 + ((mat * 16 + rb) * F + cb * 448 + a * 112) * 8, gran(v[0], v[1], tagA, tagB));
  }
  if (j < 28) {
    const int p = xl * 28 + j;
    const rsrc_t rd = mkrsrc(wd, D * F * 2);
    u32x4 dw[8];
    {
      const int voff = (lane >> 5) * 2048 + (lane & 31) * 16, sb = p * 16 * 2048 + a * 512;
#pragma unroll
      for (int i = 0; i < 8; ++i) { pace_wait(t0, adue); adue += AUX_STEP; dw[i] = ld16(rd, voff, sb + i * 4096, NT); SB(); }
    }
    u32x4 g[4];
    const int voff = (lane >> 3) * (F * 8) + (lane & 7) * 16;
    for (unsigned sp = 0; sp < SPIN_CAP; ++sp) {
#pragma unroll
      for (int k = 0; k < 4; ++k) g[k] = ld16_sc1(rws, voff, WS_G1 + k * 8 * (F * 8) + p * 128);
      if (__all(gok(g[0], tagA, tagB) && gok(g[1], tagA, tagB) && gok(g[2], tagA, tagB) && gok(g[3], tagA, tagB))) break;
      __builtin_amdgcn_s_sleep(16);
    }
    float hh[2];
#pragma unroll
    for (int e = 0; e < 2; ++e) {
      float gs = __uint_as_float(g[0][2 * e]) + __uint_as_float(g[1][2 * e]), us = __uint_as_float(g[2][2 * e]) + __uint_as_float(g[3][2 * e]);
      gs += dpp<ROR8>(gs); us += dpp<ROR8>(us);
      gs = rbf(xrow_sum(gs)); us = rbf(xrow_sum(us));
      const float ge = 0.5f * gs * (1.f + tanhf(0.7978845608028654f * (gs + 0.044715f * gs * gs * gs)));
      hh[e] = rbf(rbf(ge) * us);
    }
    const float hv = (lane >= 32) ? hh[1] : hh[0];
    float y[8] = {0.f, 0.f, 0.f, 0.f, 0.f, 0.f, 0.f, 0.f};
    fma8(y, dpp<BCAST + 0>(hv), dw[0]); fma8(y, dpp<BCAST + 1>(hv), dw[1]); fma8(y, dpp<BCAST + 2>(hv), dw[2]); fma8(y, dpp<BCAST + 3>(hv), dw[3]);
    fma8(y, dpp<BCAST + 4>(hv), dw[4]); fma8(y, dpp<BCAST + 5>(hv), dw[5]); fma8(y, dpp<BCAST + 6>(hv), dw[6]); fma8(y, dpp<BCAST + 7>(hv), dw[7]);
#pragma unroll
    for (int e = 0; e < 8; ++e) y[e] = swap32_sum(y[e]);
    if (lane < 32) {
      *(LAS f32x4*)(lds + L_TMP + a * 256 + lane * 8) = f32x4{y[0], y[1], y[2], y[3]};
      *(LAS f32x4*)(lds + L_TMP + a * 256 + lane * 8 + 4) = f32x4{y[4], y[5], y[6], y[7]};
    }
#pragma unroll
    for (int q = 0; q < 2; ++q) {
      const f32x2 v = *(LAS f32x2*)(lds + L_TMP + a * 256 + q * 128 + 2 * lane);
      st16_sc1(rws, lane * 16, WS_FFN + p * FFN_STR + a * 2048 + q * 1024, gran(v[0], v[1], tagA, tagB));
    }
  } else {
    adue += 8 * AUX_STEP;
  }
  STAMP(2);
  u32x4 wov[8];
  {
    const rsrc_t ro = mkrsrc(wo, H * DH * D * 2);
    const int voff = r * 2048 + c * 16, sb = (h * 128 + a * 32) * 2048 + s * 256;
#pragma unroll
    for (int i = 0; i < 8; ++i) { pace_wait(t0, adue); adue += AUX_STEP; wov[i] = ld16(ro, voff, sb + i * 8192, NT); SB(); }
  }
  const int col0 = s * 128 + 4 * h;
  float f0 = 0.f, f1 = 0.f;
  unsigned xw = 0;
  if (a == 0) {
    xw = __builtin_amdgcn_raw_buffer_load_b32(rx, (col0 + 2 * (lane & 1)) * 2, 0, 0);
    const int voff = (lane >> 1) * FFN_STR + (lane & 1) * 16;
    for (unsigned sp = 0; sp < SPIN_CAP; ++sp) {
      u32x4 g[7];
#pragma unroll
      for (int i = 0; i < 7; ++i) g[i] = ld16_sc1(rws, voff, WS_FFN + i * 32 * FFN_STR + col0 * 8);
      bool ok = true;
      f0 = 0.f; f1 = 0.f;
#pragma unroll
      for (int i = 0; i < 7; ++i) { ok = ok && gok(g[i], tagA, tagB); f0 += __uint_as_float(g[i][0]); f1 += __uint_as_float(g[i][2]); }
      if (__all(ok)) break;
      __builtin_amdgcn_s_sleep(16);
    }
    f0 = rbf(parity_sum(f0)); f1 = rbf(parity_sum(f1));
  }
  STAMP(3);
  __syncthreads();
  STAMP(4);
  if (a < 2) {
    float mw[8], M = -INFINITY;
#pragma unroll
    for (int w = 0; w < 8; ++w) { mw[w] = lds[L_MRG + w * MRG_STR]; M = fmaxf(M, mw[w]); }
    if (a == 0) {
      float o0 = 0.f, o1 = 0.f;
#pragma unroll
      for (int w = 0; w < 8; ++w) {
        const float wt = exp2_(mw[w] - M);
        const f32x2 v = *(LAS f32x2*)(lds + L_MRG + w * MRG_STR + 4 + 2 * lane);
        o0 = fmaf(wt, v[0], o0); o1 = fmaf(wt, v[1], o1);
      }
      st16_sc1(rws, lane * 16, WS_PART + (h * 8 + s) * PART_STR, gran(o0, o1, tagA, tagB));
    } else {
      float L = 0.f;
#pragma unroll
      for (int w = 0; w < 8; ++w) L = fmaf(exp2_(mw[w] - M), lds[L_MRG + w * MRG_STR + 1], L);
      st16_sc1(rws, lane == 0 ? 0 : OOB, WS_PART + (h * 8 + s) * PART_STR + 1024, gran(M, L, tagA, tagB));
    }
  }
  STAMP(5);
  {
    u32x4 go[2], gm[2];
    const int vo = r * PART_STR + (16 * a + c) * 16, vm = r * PART_STR + 1024;
    for (unsigned sp = 0; sp < SPIN_CAP; ++sp) {
#pragma unroll
      for (int k = 0; k < 2; ++k) {
        go[k] = ld16_sc1(rws, vo, WS_PART + (h * 8 + 4 * k) * PART_STR);
        gm[k] = ld16_sc1(rws, vm, WS_PART + (h * 8 + 4 * k) * PART_STR);
      }
      if (__all(gok(go[0], tagA, tagB) && gok(go[1], tagA, tagB) && gok(gm[0], tagA, tagB) && gok(gm[1], tagA, tagB))) break;
      __builtin_amdgcn_s_sleep(1);
    }
    STAMP(6);
    const float m0 = __uint_as_float(gm[0][0]), m1 = __uint_as_float(gm[1][0]);
    const float M = xrow_max(fmaxf(m0, m1));
    const float w0 = exp2_(m0 - M), w1 = exp2_(m1 - M);
    const float L = xrow_sum(fmaf(w0, __uint_as_float(gm[0][2]), w1 * __uint_as_float(gm[1][2])));
    const float o0 = rbf(xrow_sum(fmaf(w0, __uint_as_float(go[0][0]), w1 * __uint_as_float(go[1][0]))) / L);
    const float o1 = rbf(xrow_sum(fmaf(w0, __uint_as_float(go[0][2]), w1 * __uint_as_float(go[1][2]))) / L);
    float ov = (r & 1) ? o1 : o0;
    { const float sh = dpp<SHL1>(ov); ov = (r & 2) ? sh : ov; }
    float y[8] = {0.f, 0.f, 0.f, 0.f, 0.f, 0.f, 0.f, 0.f};
    fma8(y, dpp<BCAST + 0>(ov), wov[0]); fma8(y, dpp<BCAST + 2>(ov), wov[1]); fma8(y, dpp<BCAST + 4>(ov), wov[2]); fma8(y, dpp<BCAST + 6>(ov), wov[3]);
    fma8(y, dpp<BCAST + 8>(ov), wov[4]); fma8(y, dpp<BCAST + 10>(ov), wov[5]); fma8(y, dpp<BCAST + 12>(ov), wov[6]); fma8(y, dpp<BCAST + 14>(ov), wov[7]);
#pragma unroll
    for (int e = 0; e < 8; ++e) y[e] = xrow_sum(y[e]);
    if (r == 0) {
      *(LAS f32x4*)(lds + L_TMP + a * 256 + 8 * c) = f32x4{y[0], y[1], y[2], y[3]};
      *(LAS f32x4*)(lds + L_TMP + a * 256 + 8 * c + 4) = f32x4{y[4], y[5], y[6], y[7]};
    }
  }
  __syncthreads();
  if (a != 0) { STAMP_DUMP(); return; }
  {
    f32x2 v = *(LAS f32x2*)(lds + L_TMP + 2 * lane);
#pragma unroll
    for (int q = 1; q < 4; ++q) v += *(LAS f32x2*)(lds + L_TMP + q * 256 + 2 * lane);
    st16_sc1(rws, lane * 16, WS_Y + (h * 8 + s) * Y_STR, gran(v[0], v[1], tagA, tagB));
  }
  STAMP(7);
  {
    u32x4 g;
    const int voff = (lane >> 1) * (8 * Y_STR) + (lane & 1) * 16;
    for (unsigned sp = 0; sp < SPIN_CAP; ++sp) {
      g = ld16_sc1(rws, voff, WS_Y + s * Y_STR + h * 32);
      if (__all(gok(g, tagA, tagB))) break;
      __builtin_amdgcn_s_sleep(1);
    }
    STAMP(8);
    const float a0 = rbf(parity_sum(__uint_as_float(g[0]))), a1 = rbf(parity_sum(__uint_as_float(g[2])));
    const unsigned o = pack2(rbf(lo2f(xw) + a0) + f0, rbf(hi2f(xw) + a1) + f1);
    if (lane < 2) *(unsigned*)(out + col0 + 2 * lane) = o;
    if (bid == 0 && lane == 0) __builtin_amdgcn_raw_buffer_store_b64(u32x2{(unsigned)e1, (unsigned)(e1 >> 32)}, rws, 0, WS_EPOCH, SC1);
  }
  STAMP(9);
  STAMP_DUMP();
}

extern "C" void kernel_launch(void* const* d_in, const int* in_sizes, int n_in, void* d_out, int out_size, void* d_ws, size_t ws_size,
                              hipStream_t stream) {
  fused_decode_kernel<<<256, NTHR, 0, stream>>>((const u16*)d_in[0], (const u16*)d_in[1], (const u16*)d_in[2], (const u16*)d_in[3],
                                                 (const u16*)d_in[4], (const u16*)d_in[6], (const u16*)d_in[7], (const u16*)d_in[8],
                                                 (u16*)d_out, d_ws);
}
```

```cpp
#include <hip/hip_runtime.h>
#include <stdint.h>

typedef unsigned u32x4 __attribute__((ext_vector_type(4)));
typedef unsigned u32x2 __attribute__((ext_vector_type(2)));
typedef float f32x4 __attribute__((ext_vector_type(4)));
typedef float f32x2 __attribute__((ext_vector_type(2)));
typedef __bf16 bf16x2 __attribute__((ext_vector_type(2)));
typedef unsigned short u16;
#define LAS __attribute__((address_space(3)))
typedef __amdgpu_buffer_rsrc_t rsrc_t;

#define T_MAIN 7900
#define T_OFF 80
#define PACE_G 1
#define QSLEEP 1
#define AUX_T0 500
#define AUX_STEP 200

constexpr int D = 1024, H = 32, SK = 32768, DH = 128, F = 3584;
constexpr int NMAIN = 8, NAUX = 4, NTHR = (NMAIN + NAUX) * 64;
constexpr int NT = 2  , SC1 = 16;
constexpr int OOB = 0x7ffffff0;
constexpr unsigned WS_EPOCH = 0, WS_Q = 4096, WS_PART = WS_Q + 256 * 1024, PART_STR = 2048, WS_Y = WS_PART + 256 * PART_STR, Y_STR = 1024,
                   WS_G1 = WS_Y + 256 * Y_STR, WS_FFN = WS_G1 + 32 * F * 8, FFN_STR = 8192, WS_END = WS_FFN + 224 * FFN_STR;
constexpr unsigned WS_STAMP = 8u << 20;
constexpr int L_XN = 0, L_QP = 1024, L_QS = L_QP + 4 * 128, L_MRG = L_QS + 4 * 128, MRG_STR = 132, L_FP = L_MRG + 8 * MRG_STR, L_TMP = L_FP + 4 * 448,
              L_CNT = L_TMP + 4 * 256, L_END = L_CNT + 4;

__device__ __forceinline__ float lo2f(unsigned w) { return __builtin_bit_cast(float, w << 16); }
__device__ __forceinline__ float hi2f(unsigned w) { return __builtin_bit_cast(float, w & 0xffff0000u); }
__device__ __forceinline__ float rbf(float x) { return (float)(__bf16)x; }
__device__ __forceinline__ unsigned pack2(float a, float b) { bf16x2 v = {(__bf16)a, (__bf16)b}; return __builtin_bit_cast(unsigned, v); }
__device__ __forceinline__ float dot2(unsigned k, unsigned q, float acc) {
  return __builtin_amdgcn_fdot2_f32_bf16(__builtin_bit_cast(bf16x2, k), __builtin_bit_cast(bf16x2, q), acc, false);
}
template <int CTRL> __device__ __forceinline__ float dpp(float x) {
  return __builtin_bit_cast(float, __builtin_amdgcn_mov_dpp(__builtin_bit_cast(int, x), CTRL, 0xf, 0xf, true));
}
constexpr int XOR1 = 0xB1, XOR2 = 0x4E, XOR7 = 0x141, ROR8 = 0x128, ROR4 = 0x124, SHL1 = 0x101, BCAST = 0x150;
__device__ __forceinline__ float swap32_sum(float x) {
  auto t = __builtin_amdgcn_permlane32_swap(__float_as_uint(x), __float_as_uint(x), false, false);
  return __uint_as_float(t[0]) + __uint_as_float(t[1]);
}
__device__ __forceinline__ float xrow_sum(float x) {
  auto s = __builtin_amdgcn_permlane16_swap(__float_as_uint(x), __float_as_uint(x), false, false);
  return swap32_sum(__uint_as_float(s[0]) + __uint_as_float(s[1]));
}
__device__ __forceinline__ float xrow_max(float x) {
  auto s = __builtin_amdgcn_permlane16_swap(__float_as_uint(x), __float_as_uint(x), false, false);
  x = fmaxf(__uint_as_float(s[0]), __uint_as_float(s[1]));
  auto t = __builtin_amdgcn_permlane32_swap(__float_as_uint(x), __float_as_uint(x), false, false);
  return fmaxf(__uint_as_float(t[0]), __uint_as_float(t[1]));
}
__device__ __forceinline__ float parity_sum(float x) {
  x += dpp<XOR2>(x); x += dpp<ROR4>(x); x += dpp<ROR8>(x); return xrow_sum(x);
}
__device__ __forceinline__ float wave_sum(float x) { x += dpp<XOR1>(x); return parity_sum(x); }
__device__ __forceinline__ float exp2_(float x) { return __builtin_amdgcn_exp2f(x); }
__device__ __forceinline__ rsrc_t mkrsrc(const void* p, unsigned bytes) { return __builtin_amdgcn_make_buffer_rsrc((void*)p, 0, (int)bytes, 0x00020000); }
__device__ __forceinline__ u32x4 ld16(rsrc_t r, int voff, int soff, int aux_nt) {
  return aux_nt == 2 ? __builtin_amdgcn_raw_buffer_load_b128(r, voff, soff, 2) : __builtin_amdgcn_raw_buffer_load_b128(r, voff, soff, 0);
}
__device__ __forceinline__ u32x4 ld16_sc1(rsrc_t r, int voff, int soff) { return __builtin_amdgcn_raw_buffer_load_b128(r, voff, soff, SC1); }
__device__ __forceinline__ void st16_sc1(rsrc_t r, int voff, int soff, u32x4 v) { __builtin_amdgcn_raw_buffer_store_b128(v, r, voff, soff, SC1); }
#define SB() __builtin_amdgcn_sched_barrier(0)
__device__ __forceinline__ void pace_wait(unsigned t0, unsigned due) {
  asm volatile("1:\n\ts_memrealtime s[96:97]\n\ts_waitcnt lgkmcnt(0)\n\ts_sub_u32 s96, s96, %0\n\ts_cmp_lt_i32 s96, %1\n\ts_cbranch_scc0 2f\n\ts_sleep 1\n\ts_branch 1b\n2:"
               :: "s"(t0), "s"(due) : "s96", "s97", "scc", "memory");
}
__device__ __forceinline__ void fma8(float (&acc)[8], float s, u32x4 w) {
#pragma unroll
  for (int t = 0; t < 4; ++t) { acc[2 * t] = fmaf(s, lo2f(w[t]), acc[2 * t]); acc[2 * t + 1] = fmaf(s, hi2f(w[t]), acc[2 * t + 1]); }
}
__device__ __forceinline__ u32x4 gran(float a, float b, unsigned ta, unsigned tb) { return u32x4{__float_as_uint(a), ta, __float_as_uint(b), tb}; }
__device__ __forceinline__ bool gok(u32x4 g, unsigned ta, unsigned tb) { return g[1] == ta && g[3] == tb; }
constexpr unsigned SPIN_CAP = 1u << 20;

#define STAMP(i) do { } while (0)
#define STAMP_DUMP() do { } while (0)

__device__ __forceinline__ void aux_sync(LAS float* cnt, int lane) {
  asm volatile("s_waitcnt lgkmcnt(0)" ::: "memory");
  if (lane == 0) __hip_atomic_fetch_add((LAS unsigned*)cnt, 1u, __ATOMIC_RELAXED, __HIP_MEMORY_SCOPE_WORKGROUP);
  for (unsigned sp = 0; sp < SPIN_CAP; ++sp) {
    if (__builtin_amdgcn_readfirstlane(*(volatile LAS unsigned*)cnt) >= (unsigned)NAUX) break;
    __builtin_amdgcn_s_sleep(1);
  }
  asm volatile("" ::: "memory");
}

__global__ void __launch_bounds__(NTHR, 3)
fused_decode_kernel(const u16* __restrict__ x, const u16* __restrict__ Kc, const u16* __restrict__ Vc, const u16* __restrict__ wq,
                    const u16* __restrict__ wo, const u16* __restrict__ wg, const u16* __restrict__ wu, const u16* __restrict__ wd,
                    u16* __restrict__ out, void* ws) {
  __shared__ __attribute__((aligned(16))) float lds_[L_END];
  LAS float* lds = (LAS float*)lds_;
  const unsigned t0 = (unsigned)__builtin_amdgcn_s_memrealtime();
  const int tid = threadIdx.x, lane = tid & 63, wave = __builtin_amdgcn_readfirstlane(tid >> 6);
  const int r = lane >> 4, c = lane & 15;
  const int bid = blockIdx.x, xl = bid & 7, j = bid >> 3;
  const int h = xl * 4 + (j >> 3), s = j & 7;
  const int a = wave - NMAIN;
  STAMP(0);

  if (wave < NMAIN) {
    const rsrc_t rk = mkrsrc(Kc, (unsigned)H * SK * DH * 2), rv = mkrsrc(Vc, (unsigned)H * SK * DH * 2);
    u32x4 kA[8], vA[8], kB[8], vB[8];
    const unsigned kvbase = (unsigned)h * (8u << 20) + (unsigned)s * (1u << 20) + wave * 1024;
    const unsigned rot = 32u * ((h + (h >> 2) + (h >> 4)) & 3);
    const int kvoff = lane * 16;
    constexpr int STEP = ((int)T_MAIN << 16) / 256 * PACE_G;
    int due = (int)T_OFF * 65536 - STEP + (STEP / 256) * (wave * 32 + j);
#define PACE(I) do { if ((I) % PACE_G == 0) { due += STEP; pace_wait(t0, due >> 16); } } while (0)
    {
      const unsigned tb = kvbase + rot * 8192;
#pragma unroll
      for (int i = 0; i < 8; ++i) {
        PACE(i); kA[i] = ld16(rk, kvoff, tb + i * 8192, NT); SB();
        if (i == 0) { STAMP(12); SB(); }
        if (i == 1) { __builtin_amdgcn_s_barrier(); SB(); STAMP(10); }
      }
      STAMP(11);
#pragma unroll
      for (int i = 0; i < 8; ++i) { PACE(i); vA[i] = ld16(rv, kvoff, tb + i * 8192, NT); SB(); }
#pragma unroll
      for (int i = 0; i < 8; ++i) { PACE(i); kB[i] = ld16(rk, kvoff, tb + 65536 + i * 8192, NT); SB(); }
#pragma unroll
      for (int i = 0; i < 8; ++i) { PACE(i); vB[i] = ld16(rv, kvoff, tb + 65536 + i * 8192, NT); SB(); }
    }
    STAMP(1);
    __syncthreads();
    unsigned qp[4];
    {
      f32x4 q0 = *(LAS f32x4*)(lds + L_QS + r * 128 + 8 * c), q1 = *(LAS f32x4*)(lds + L_QS + r * 128 + 8 * c + 4);
#pragma unroll
      for (int e = 0; e < 4; ++e) { q0[e] = xrow_sum(q0[e]); q1[e] = xrow_sum(q1[e]); }
      qp[0] = pack2(q0[0], q0[1]); qp[1] = pack2(q0[2], q0[3]); qp[2] = pack2(q1[0], q1[1]); qp[3] = pack2(q1[2], q1[3]);
    }
    STAMP(2);
    float m = -INFINITY, l = 0.f, acc[8] = {0.f, 0.f, 0.f, 0.f, 0.f, 0.f, 0.f, 0.f};
    constexpr float SCALE = 0.08838834764831845f, LOG2E = 1.4426950408889634f;
    auto turn = [&](u32x4 (&kb)[8], u32x4 (&vb)[8], const bool more, const unsigned tb) __attribute__((always_inline)) {
      float sx[8];
#pragma unroll
      for (int i = 0; i < 8; ++i) {
        float d = 0.f;
#pragma unroll
        for (int t = 0; t < 4; ++t) d = dot2(kb[i][t], qp[t], d);
        asm volatile("" : "+v"(d));
        sx[i] = d;
        SB();
        if (more) {
          PACE(i);
          kb[i] = ld16(rk, kvoff, tb + i * 8192, NT);
          SB();
        }
      }
#define TR_STEP(HALF, CTRL, BIT) _Pragma("unroll") for (int hh = 0; hh < (HALF); ++hh) { \
        const float lo_ = sx[hh], hi_ = sx[hh + (HALF)]; const float t1_ = lo_ + dpp<CTRL>(lo_), t2_ = hi_ + dpp<CTRL>(hi_); \
        sx[hh] = (c & (BIT)) ? t2_ : t1_; }
      TR_STEP(4, ROR8, 8) TR_STEP(2, XOR7, 4) TR_STEP(1, XOR2, 2)
#undef TR_STEP
      const float sraw = sx[0] + dpp<XOR1>(sx[0]);
      const float sc = rbf(rbf(sraw) * SCALE) * LOG2E;
      float pm = fmaxf(sc, dpp<XOR2>(sc)); pm = fmaxf(pm, dpp<ROR4>(pm)); pm = fmaxf(pm, dpp<ROR8>(pm)); pm = xrow_max(pm);
      const float mn = fmaxf(m, pm);
      const float alpha = exp2_(m - mn), p = exp2_(sc - mn);
      l = fmaf(l, alpha, p);
      m = mn;
#pragma unroll
      for (int e = 0; e < 8; ++e) acc[e] *= alpha;
      SB();
#define PV_STEP(I) { fma8(acc, dpp<BCAST + 2 * (I)>(p), vb[I]); \
        asm volatile("" : "+v"(acc[0]), "+v"(acc[1]), "+v"(acc[2]), "+v"(acc[3]), "+v"(acc[4]), "+v"(acc[5]), "+v"(acc[6]), "+v"(acc[7])); SB(); \
        if (more) { PACE(I); vb[I] = ld16(rv, kvoff, tb + (I) * 8192, NT); SB(); } }
      PV_STEP(0) PV_STEP(1) PV_STEP(2) PV_STEP(3) PV_STEP(4) PV_STEP(5) PV_STEP(6) PV_STEP(7)
#undef PV_STEP
    };
    for (int n = 2; n < 16; n += 2) {
      const unsigned tb = kvbase + ((n * 8 + rot) & 127u) * 8192;
      turn(kA, vA, true, tb);
      turn(kB, vB, true, tb + 65536);
    }
    STAMP(3);
    turn(kA, vA, false, 0);
    turn(kB, vB, false, 0);
    STAMP(4);
#pragma unroll
    for (int e = 0; e < 8; ++e) acc[e] = xrow_sum(acc[e]);
    l = wave_sum(l) * 0.5f;
    if (r == 0) {
      *(LAS f32x4*)(lds + L_MRG + wave * MRG_STR + 4 + 8 * c) = f32x4{acc[0], acc[1], acc[2], acc[3]};
      *(LAS f32x4*)(lds + L_MRG + wave * MRG_STR + 8 + 8 * c) = f32x4{acc[4], acc[5], acc[6], acc[7]};
      if (c == 0) *(LAS f32x2*)(lds + L_MRG + wave * MRG_STR) = f32x2{m, l};
    }
    __syncthreads();
    STAMP_DUMP();
    return;
  }

  const rsrc_t rx = mkrsrc(x, D * 2), rws = mkrsrc(ws, WS_END);
  unsigned tagA, tagB;
  unsigned long long e1;
  {
    u32x4 xa = ld16(rx, lane * 32, 0, 0), xb = ld16(rx, lane * 32 + 16, 0, 0);
    const u32x2 ep = __builtin_amdgcn_raw_buffer_load_b64(rws, 0, WS_EPOCH, SC1);
    const rsrc_t rwq = mkrsrc(wq, D * H * DH * 2);
    u32x4 w[8];
    const int voff = r * 8192 + c * 16, sb = (s * 128 + a * 32) * 8192 + h * 256;
#pragma unroll
    for (int i = 0; i < 8; ++i) { w[i] = ld16(rwq, voff, sb + i * 32768, NT); SB(); }
    if (a == 0 && lane < 4) *(LAS unsigned*)(lds + L_CNT + lane) = 0u;
    __syncthreads();
    asm volatile("" : "+v"(xa), "+v"(xb));
    STAMP(10);
    float v[16];
#pragma unroll
    for (int i = 0; i < 4; ++i) { v[2 * i] = lo2f(xa[i]); v[2 * i + 1] = hi2f(xa[i]); v[8 + 2 * i] = lo2f(xb[i]); v[9 + 2 * i] = hi2f(xb[i]); }
    float sm = 0.f;
#pragma unroll
    for (int i = 0; i < 16; ++i) sm += v[i];
    const float mean = wave_sum(sm) * (1.f / D);
    float ss = 0.f;
#pragma unroll
    for (int i = 0; i < 16; ++i) { const float d = v[i] - mean; ss = fmaf(d, d, ss); }
    const float var = wave_sum(ss) * (1.f / D);
    const float mb = rbf(mean), den = rbf(sqrtf(rbf(rbf(var) + 1e-5f)));
    const float rinv = 1.0f / den;
#pragma unroll
    for (int i = 0; i < 16; ++i) {
      const float nu = rbf(v[i] - mb), q0 = nu * rinv;
      v[i] = rbf(fmaf(fmaf(-q0, den, nu), rinv, q0));
    }
#pragma unroll
    for (int q = 0; q < 4; ++q) *(LAS f32x4*)(lds + L_XN + q * 256 + 4 * lane) = f32x4{v[q], v[4 + q], v[8 + q], v[12 + q]};
    e1 = (((unsigned long long)__builtin_amdgcn_readfirstlane(ep[1]) << 32) | (unsigned)__builtin_amdgcn_readfirstlane(ep[0])) + 1ull;
    tagA = (unsigned)e1; tagB = (unsigned)(e1 >> 32) + (unsigned)e1 * 0x9E3779B1u;
    float acc[8] = {0.f, 0.f, 0.f, 0.f, 0.f, 0.f, 0.f, 0.f};
#pragma unroll
    for (int q = 0; q < 2; ++q) {
      const f32x4 xv = *(LAS f32x4*)(lds + L_XN + r * 256 + s * 32 + a * 8 + 4 * q);
#pragma unroll
      for (int e = 0; e < 4; ++e) fma8(acc, xv[e], w[4 * q + e]);
    }
#pragma unroll
    for (int e = 0; e < 8; ++e) acc[e] = xrow_sum(acc[e]);
    if (r == 0) {
      *(LAS f32x4*)(lds + L_QP + a * 128 + 8 * c) = f32x4{acc[0], acc[1], acc[2], acc[3]};
      *(LAS f32x4*)(lds + L_QP + a * 128 + 8 * c + 4) = f32x4{acc[4], acc[5], acc[6], acc[7]};
    }
  }
  STAMP(1);
  {
    aux_sync(lds + L_CNT + 1, lane);
    STAMP(11);
    if (a == 0) {
      f32x2 v = *(LAS f32x2*)(lds + L_QP + 2 * lane);
#pragma unroll
      for (int q = 1; q < 4; ++q) v += *(LAS f32x2*)(lds + L_QP + q * 128 + 2 * lane);
      st16_sc1(rws, lane * 16, WS_Q + (h * 8 + s) * 1024, gran(v[0], v[1], tagA, tagB));
    }
    u32x4 g0 = {0, 0, 0, 0}, g1 = g0;
    for (unsigned sp = 0; sp < SPIN_CAP; ++sp) {
      g0 = ld16_sc1(rws, lane * 16, WS_Q + (h * 8 + 2 * a) * 1024);
      g1 = ld16_sc1(rws, lane * 16, WS_Q + (h * 8 + 2 * a + 1) * 1024);
      if (sp == 0) STAMP(12);
      if (__all(gok(g0, tagA, tagB) && gok(g1, tagA, tagB))) break;
      __builtin_amdgcn_s_sleep(QSLEEP);
    }
    STAMP(13);
    *(LAS f32x2*)(lds + L_QS + a * 128 + 2 * lane) =
        f32x2{__uint_as_float(g0[0]) + __uint_as_float(g1[0]), __uint_as_float(g0[2]) + __uint_as_float(g1[2])};
  }
  __syncthreads();
  int adue = AUX_T0 + a * (AUX_STEP / 4);

  {
    const int mat = bid >> 7, rb = (bid >> 3) & 15, cb = bid & 7;
    const rsrc_t rg = mkrsrc(mat ? wu : wg, D * F * 2);
    u32x4 g[16];
    const int voff = lane < 56 ? lane * 16 : OOB, sb = (rb * 64 + a * 16) * (F * 2) + cb * 896;
#pragma unroll
    for (int i = 0; i < 16; ++i) { pace_wait(t0, adue); adue += AUX_STEP; g[i] = ld16(rg, voff, sb + i * (F * 2), NT); SB(); }
    float ag[8] = {0.f, 0.f, 0.f, 0.f, 0.f, 0.f, 0.f, 0.f};
#pragma unroll
    for (int q = 0; q < 4; ++q) {
      const f32x4 xv = *(LAS f32x4*)(lds + L_XN + q * 256 + rb * 16 + a * 4);
#pragma unroll
      for (int e = 0; e < 4; ++e) fma8(ag, xv[e], g[4 * e + q]);
    }
    if (lane < 56) {
      *(LAS f32x4*)(lds + L_FP + a * 448 + lane * 8) = f32x4{ag[0], ag[1], ag[2], ag[3]};
      *(LAS f32x4*)(lds + L_FP + a * 448 + lane * 8 + 4) = f32x4{ag[4], ag[5], ag[6], ag[7]};
    }
    aux_sync(lds + L_CNT, lane);
    const int cl = a * 112 + 2 * (lane < 56 ? lane : 0);
    f32x2 v = *(LAS f32x2*)(lds + L_FP + cl);
#pragma unroll
    for (int q = 1; q < 4; ++q) v += *(LAS f32x2*)(lds + L_FP + q * 448 + cl);
    st16_sc1(rws, lane < 56 ? lane * 16 : OOB, WS_G1 + ((mat * 16 + rb) * F + cb * 448 + a * 112) * 8, gran(v[0], v[1], tagA, tagB));
  }
  if (j < 28) {
    const int p = xl * 28 + j;
    const rsrc_t rd = mkrsrc(wd, D * F * 2);
    u32x4 dw[8];
    {
      const int voff = (lane >> 5) * 2048 + (lane & 31) * 16, sb = p * 16 * 2048 + a * 512;
#pragma unroll
      for (int i = 0; i < 8; ++i) { pace_wait(t0, adue); adue += AUX_STEP; dw[i] = ld16(rd, voff, sb + i * 4096, NT); SB(); }
    }
    u32x4 g[4];
    const int voff = (lane >> 3) * (F * 8) + (lane & 7) * 16;
    for (unsigned sp = 0; sp < SPIN_CAP; ++sp) {
#pragma unroll
      for (int k = 0; k < 4; ++k) g[k] = ld16_sc1(rws, voff, WS_G1 + k * 8 * (F * 8) + p * 128);
      if (__all(gok(g[0], tagA, tagB) && gok(g[1], tagA, tagB) && gok(g[2], tagA, tagB) && gok(g[3], tagA, tagB))) break;
      __builtin_amdgcn_s_sleep(16);
    }
    float hh[2];
#pragma unroll
    for (int e = 0; e < 2; ++e) {
      float gs = __uint_as_float(g[0][2 * e]) + __uint_as_float(g[1][2 * e]), us = __uint_as_float(g[2][2 * e]) + __uint_as_float(g[3][2 * e]);
      gs += dpp<ROR8>(gs); us += dpp<ROR8>(us);
      gs = rbf(xrow_sum(gs)); us = rbf(xrow_sum(us));
      const float ge = 0.5f * gs * (1.f + tanhf(0.7978845608028654f * (gs + 0.044715f * gs * gs * gs)));
      hh[e] = rbf(rbf(ge) * us);
    }
    const float hv = (lane >= 32) ? hh[1] : hh[0];
    float y[8] = {0.f, 0.f, 0.f, 0.f, 0.f, 0.f, 0.f, 0.f};
    fma8(y, dpp<BCAST + 0>(hv), dw[0]); fma8(y, dpp<BCAST + 1>(hv), dw[1]); fma8(y, dpp<BCAST + 2>(hv), dw[2]); fma8(y, dpp<BCAST + 3>(hv), dw[3]);
    fma8(y, dpp<BCAST + 4>(hv), dw[4]); fma8(y, dpp<BCAST + 5>(hv), dw[5]); fma8(y, dpp<BCAST + 6>(hv), dw[6]); fma8(y, dpp<BCAST + 7>(hv), dw[7]);
#pragma unroll
    for (int e = 0; e < 8; ++e) y[e] = swap32_sum(y[e]);
    if (lane < 32) {
      *(LAS f32x4*)(lds + L_TMP + a * 256 + lane * 8) = f32x4{y[0], y[1], y[2], y[3]};
      *(LAS f32x4*)(lds + L_TMP + a * 256 + lane * 8 + 4) = f32x4{y[4], y[5], y[6], y[7]};
    }
#pragma unroll
    for (int q = 0; q < 2; ++q) {
      const f32x2 v = *(LAS f32x2*)(lds + L_TMP + a * 256 + q * 128 + 2 * lane);
      st16_sc1(rws, lane * 16, WS_FFN + p * FFN_STR + a * 2048 + q * 1024, gran(v[0], v[1], tagA, tagB));
    }
  } else {
    adue += 8 * AUX_STEP;
  }
  STAMP(2);
  u32x4 wov[8];
  {
    const rsrc_t ro = mkrsrc(wo, H * DH * D * 2);
    const int voff = r * 2048 + c * 16, sb = (h * 128 + a * 32) * 2048 + s * 256;
#pragma unroll
    for (int i = 0; i < 8; ++i) { pace_wait(t0, adue); adue += AUX_STEP; wov[i] = ld16(ro, voff, sb + i * 8192, NT); SB(); }
  }
  const int col0 = s * 128 + 4 * h;
  float f0 = 0.f, f1 = 0.f;
  unsigned xw = 0;
  if (a == 0) {
    xw = __builtin_amdgcn_raw_buffer_load_b32(rx, (col0 + 2 * (lane & 1)) * 2, 0, 0);
    const int voff = (lane >> 1) * FFN_STR + (lane & 1) * 16;
    for (unsigned sp = 0; sp < SPIN_CAP; ++sp) {
      u32x4 g[7];
#pragma unroll
      for (int i = 0; i < 7; ++i) g[i] = ld16_sc1(rws, voff, WS_FFN + i * 32 * FFN_STR + col0 * 8);
      bool ok = true;
      f0 = 0.f; f1 = 0.f;
#pragma unroll
      for (int i = 0; i < 7; ++i) { ok = ok && gok(g[i], tagA, tagB); f0 += __uint_as_float(g[i][0]); f1 += __uint_as_float(g[i][2]); }
      if (__all(ok)) break;
      __builtin_amdgcn_s_sleep(16);
    }
    f0 = rbf(parity_sum(f0)); f1 = rbf(parity_sum(f1));
  }
  STAMP(3);
  __syncthreads();
  STAMP(4);
  if (a < 2) {
    float mw[8], M = -INFINITY;
#pragma unroll
    for (int w = 0; w < 8; ++w) { mw[w] = lds[L_MRG + w * MRG_STR]; M = fmaxf(M, mw[w]); }
    if (a == 0) {
      float o0 = 0.f, o1 = 0.f;
#pragma unroll
      for (int w = 0; w < 8; ++w) {
        const float wt = exp2_(mw[w] - M);
        const f32x2 v = *(LAS f32x2*)(lds + L_MRG + w * MRG_STR + 4 + 2 * lane);
        o0 = fmaf(wt, v[0], o0); o1 = fmaf(wt, v[1], o1);
      }
      st16_sc1(rws, lane * 16, WS_PART + (h * 8 + s) * PART_STR, gran(o0, o1, tagA, tagB));
    } else {
      float L = 0.f;
#pragma unroll
      for (int w = 0; w < 8; ++w) L = fmaf(exp2_(mw[w] - M), lds[L_MRG + w * MRG_STR + 1], L);
      st16_sc1(rws, lane == 0 ? 0 : OOB, WS_PART + (h * 8 + s) * PART_STR + 1024, gran(M, L, tagA, tagB));
    }
  }
  STAMP(5);
  {
    u32x4 go[2], gm[2];
    const int vo = r * PART_STR + (16 * a + c) * 16, vm = r * PART_STR + 1024;
    for (unsigned sp = 0; sp < SPIN_CAP; ++sp) {
#pragma unroll
      for (int k = 0; k < 2; ++k) {
        go[k] = ld16_sc1(rws, vo, WS_PART + (h * 8 + 4 * k) * PART_STR);
        gm[k] = ld16_sc1(rws, vm, WS_PART + (h * 8 + 4 * k) * PART_STR);
      }
      if (__all(gok(go[0], tagA, tagB) && gok(go[1], tagA, tagB) && gok(gm[0], tagA, tagB) && gok(gm[1], tagA, tagB))) break;
      __builtin_amdgcn_s_sleep(1);
    }
    STAMP(6);
    const float m0 = __uint_as_float(gm[0][0]), m1 = __uint_as_float(gm[1][0]);
    const float M = xrow_max(fmaxf(m0, m1));
    const float w0 = exp2_(m0 - M), w1 = exp2_(m1 - M);
    const float L = xrow_sum(fmaf(w0, __uint_as_float(gm[0][2]), w1 * __uint_as_float(gm[1][2])));
    const float o0 = rbf(xrow_sum(fmaf(w0, __uint_as_float(go[0][0]), w1 * __uint_as_float(go[1][0]))) / L);
    const float o1 = rbf(xrow_sum(fmaf(w0, __uint_as_float(go[0][2]), w1 * __uint_as_float(go[1][2]))) / L);
    float ov = (r & 1) ? o1 : o0;
    { const float sh = dpp<SHL1>(ov); ov = (r & 2) ? sh : ov; }
    float y[8] = {0.f, 0.f, 0.f, 0.f, 0.f, 0.f, 0.f, 0.f};
    fma8(y, dpp<BCAST + 0>(ov), wov[0]); fma8(y, dpp<BCAST + 2>(ov), wov[1]); fma8(y, dpp<BCAST + 4>(ov), wov[2]); fma8(y, dpp<BCAST + 6>(ov), wov[3]);
    fma8(y, dpp<BCAST + 8>(ov), wov[4]); fma8(y, dpp<BCAST + 10>(ov), wov[5]); fma8(y, dpp<BCAST + 12>(ov), wov[6]); fma8(y, dpp<BCAST + 14>(ov), wov[7]);
#pragma unroll
    for (int e = 0; e < 8; ++e) y[e] = xrow_sum(y[e]);
    if (r == 0) {
      *(LAS f32x4*)(lds + L_TMP + a * 256 + 8 * c) = f32x4{y[0], y[1], y[2], y[3]};
      *(LAS f32x4*)(lds + L_TMP + a * 256 + 8 * c + 4) = f32x4{y[4], y[5], y[6], y[7]};
    }
  }
  __syncthreads();
  if (a != 0) { STAMP_DUMP(); return; }
  {
    f32x2 v = *(LAS f32x2*)(lds + L_TMP + 2 * lane);
#pragma unroll
    for (int q = 1; q < 4; ++q) v += *(LAS f32x2*)(lds + L_TMP + q * 256 + 2 * lane);
    st16_sc1(rws, lane * 16, WS_Y + (h * 8 + s) * Y_STR, gran(v[0], v[1], tagA, tagB));
  }
  STAMP(7);
  {
    u32x4 g;
    const int voff = (lane >> 1) * (8 * Y_STR) + (lane & 1) * 16;
    for (unsigned sp = 0; sp < SPIN_CAP; ++sp) {
      g = ld16_sc1(rws, voff, WS_Y + s * Y_STR + h * 32);
      if (__all(gok(g, tagA, tagB))) break;
      __builtin_amdgcn_s_sleep(1);
    }
    STAMP(8);
    const float a0 = rbf(parity_sum(__uint_as_float(g[0]))), a1 = rbf(parity_sum(__uint_as_float(g[2])));
    const unsigned o = pack2(rbf(lo2f(xw) + a0) + f0, rbf(hi2f(xw) + a1) + f1);
    if (lane < 2) *(unsigned*)(out + col0 + 2 * lane) = o;
    if (bid == 0 && lane == 0) __builtin_amdgcn_raw_buffer_store_b64(u32x2{(unsigned)e1, (unsigned)(e1 >> 32)}, rws, 0, WS_EPOCH, SC1);
  }
  STAMP(9);
  STAMP_DUMP();
}

extern "C" void kernel_launch(void* const* d_in, const int* in_sizes, int n_in, void* d_out, int out_size, void* d_ws, size_t ws_size,
                              hipStream_t stream) {
  fused_decode_kernel<<<256, NTHR, 0, stream>>>((const u16*)d_in[0], (const u16*)d_in[1], (const u16*)d_in[2], (const u16*)d_in[3],
                                                 (const u16*)d_in[4], (const u16*)d_in[6], (const u16*)d_in[7], (const u16*)d_in[8],
                                                 (u16*)d_out, d_ws);
}
```

```cpp
#define DIAG_MODE 1
#include <hip/hip_runtime.h>
#include <stdint.h>

typedef unsigned u32x4 __attribute__((ext_vector_type(4)));
typedef unsigned u32x2 __attribute__((ext_vector_type(2)));
typedef float f32x4 __attribute__((ext_vector_type(4)));
typedef float f32x2 __attribute__((ext_vector_type(2)));
typedef __bf16 bf16x2 __attribute__((ext_vector_type(2)));
typedef unsigned short u16;
#define LAS __attribute__((address_space(3)))
typedef __amdgpu_buffer_rsrc_t rsrc_t;

#define T_MAIN 9300
#define T_OFF 80
#define PACE_G 1
#define QSLEEP 1
#define AUX_T0 500
#define AUX_STEP 200

constexpr int D = 1024, H = 32, SK = 32768, DH = 128, F = 3584;
constexpr int NMAIN = 8, NAUX = 4, NTHR = (NMAIN + NAUX) * 64;
constexpr int NT = 2  , SC1 = 16;
constexpr int OOB = 0x7ffffff0;
constexpr unsigned WS_EPOCH = 0, WS_Q = 4096, WS_PART = WS_Q + 256 * 1024, PART_STR = 2048, WS_Y = WS_PART + 256 * PART_STR, Y_STR = 1024,
                   WS_G1 = WS_Y + 256 * Y_STR, WS_FFN = WS_G1 + 32 * F * 8, FFN_STR = 8192, WS_END = WS_FFN + 224 * FFN_STR;
constexpr unsigned WS_STAMP = 8u << 20;
constexpr unsigned WS_DIAG = 6u << 20;
constexpr int L_XN = 0, L_QP = 1024, L_QS = L_QP + 4 * 128, L_MRG = L_QS + 4 * 128, MRG_STR = 132, L_FP = L_MRG + 8 * MRG_STR, L_TMP = L_FP + 4 * 448,
              L_CNT = L_TMP + 4 * 256, L_END = L_CNT + 4;

__device__ __forceinline__ float lo2f(unsigned w) { return __builtin_bit_cast(float, w << 16); }
__device__ __forceinline__ float hi2f(unsigned w) { return __builtin_bit_cast(float, w & 0xffff0000u); }
__device__ __forceinline__ float rbf(float x) { return (float)(__bf16)x; }
__device__ __forceinline__ unsigned pack2(float a, float b) { bf16x2 v = {(__bf16)a, (__bf16)b}; return __builtin_bit_cast(unsigned, v); }
__device__ __forceinline__ float dot2(unsigned k, unsigned q, float acc) {
  return __builtin_amdgcn_fdot2_f32_bf16(__builtin_bit_cast(bf16x2, k), __builtin_bit_cast(bf16x2, q), acc, false);
}
template <int CTRL> __device__ __forceinline__ float dpp(float x) {
  return __builtin_bit_cast(float, __builtin_amdgcn_mov_dpp(__builtin_bit_cast(int, x), CTRL, 0xf, 0xf, true));
}
constexpr int XOR1 = 0xB1, XOR2 = 0x4E, XOR7 = 0x141, ROR8 = 0x128, ROR4 = 0x124, SHL1 = 0x101, BCAST = 0x150;
__device__ __forceinline__ float swap32_sum(float x) {
  auto t = __builtin_amdgcn_permlane32_swap(__float_as_uint(x), __float_as_uint(x), false, false);
  return __uint_as_float(t[0]) + __uint_as_float(t[1]);
}
__device__ __forceinline__ float xrow_sum(float x) {
  auto s = __builtin_amdgcn_permlane16_swap(__float_as_uint(x), __float_as_uint(x), false, false);
  return swap32_sum(__uint_as_float(s[0]) + __uint_as_float(s[1]));
}
__device__ __forceinline__ float xrow_max(float x) {
  auto s = __builtin_amdgcn_permlane16_swap(__float_as_uint(x), __float_as_uint(x), false, false);
  x = fmaxf(__uint_as_float(s[0]), __uint_as_float(s[1]));
  auto t = __builtin_amdgcn_permlane32_swap(__float_as_uint(x), __float_as_uint(x), false, false);
  return fmaxf(__uint_as_float(t[0]), __uint_as_float(t[1]));
}
__device__ __forceinline__ float parity_sum(float x) {
  x += dpp<XOR2>(x); x += dpp<ROR4>(x); x += dpp<ROR8>(x); return xrow_sum(x);
}
__device__ __forceinline__ float wave_sum(float x) { x += dpp<XOR1>(x); return parity_sum(x); }
__device__ __forceinline__ float exp2_(float x) { return __builtin_amdgcn_exp2f(x); }
__device__ __forceinline__ rsrc_t mkrsrc(const void* p, unsigned bytes) { return __builtin_amdgcn_make_buffer_rsrc((void*)p, 0, (int)bytes, 0x00020000); }
__device__ __forceinline__ u32x4 ld16(rsrc_t r, int voff, int soff, int aux_nt) {
  return aux_nt == 2 ? __builtin_amdgcn_raw_buffer_load_b128(r, voff, soff, 2) : __builtin_amdgcn_raw_buffer_load_b128(r, voff, soff, 0);
}
__device__ __forceinline__ u32x4 ld16_sc1(rsrc_t r, int voff, int soff) { return __builtin_amdgcn_raw_buffer_load_b128(r, voff, soff, SC1); }
__device__ __forceinline__ void st16_sc1(rsrc_t r, int voff, int soff, u32x4 v) { __builtin_amdgcn_raw_buffer_store_b128(v, r, voff, soff, SC1); }
#define SB() __builtin_amdgcn_sched_barrier(0)
__device__ __forceinline__ void pace_wait(unsigned t0, unsigned due) {
  asm volatile("1:\n\ts_memrealtime s[96:97]\n\ts_waitcnt lgkmcnt(0)\n\ts_sub_u32 s96, s96, %0\n\ts_cmp_lt_i32 s96, %1\n\ts_cbranch_scc0 2f\n\ts_sleep 1\n\ts_branch 1b\n2:"
               :: "s"(t0), "s"(due) : "s96", "s97", "scc", "memory");
}
__device__ __forceinline__ void fma8(float (&acc)[8], float s, u32x4 w) {
#pragma unroll
  for (int t = 0; t < 4; ++t) { acc[2 * t] = fmaf(s, lo2f(w[t]), acc[2 * t]); acc[2 * t + 1] = fmaf(s, hi2f(w[t]), acc[2 * t + 1]); }
}
__device__ __forceinline__ u32x4 gran(float a, float b, unsigned ta, unsigned tb) { return u32x4{__float_as_uint(a), ta, __float_as_uint(b), tb}; }
__device__ __forceinline__ bool gok(u32x4 g, unsigned ta, unsigned tb) { return g[1] == ta && g[3] == tb; }
constexpr unsigned SPIN_CAP = 1u << 20;

#define STAMP(i) do { } while (0)
#define STAMP_DUMP() do { } while (0)

__device__ __forceinline__ void aux_sync(LAS float* cnt, int lane) {
  asm volatile("s_waitcnt lgkmcnt(0)" ::: "memory");
  if (lane == 0) __hip_atomic_fetch_add((LAS unsigned*)cnt, 1u, __ATOMIC_RELAXED, __HIP_MEMORY_SCOPE_WORKGROUP);
  for (unsigned sp = 0; sp < SPIN_CAP; ++sp) {
    if (__builtin_amdgcn_readfirstlane(*(volatile LAS unsigned*)cnt) >= (unsigned)NAUX) break;
    __builtin_amdgcn_s_sleep(1);
  }
  asm volatile("" ::: "memory");
}

__global__ void __launch_bounds__(NTHR, 3)
fused_decode_kernel(const u16* __restrict__ x, const u16* __restrict__ Kc, const u16* __restrict__ Vc, const u16* __restrict__ wq,
                    const u16* __restrict__ wo, const u16* __restrict__ wg, const u16* __restrict__ wu, const u16* __restrict__ wd,
                    u16* __restrict__ out, void* ws) {
  __shared__ __attribute__((aligned(16))) float lds_[L_END];
  LAS float* lds = (LAS float*)lds_;
  const unsigned t0 = (unsigned)__builtin_amdgcn_s_memrealtime();
  const int tid = threadIdx.x, lane = tid & 63, wave = __builtin_amdgcn_readfirstlane(tid >> 6);
  const int r = lane >> 4, c = lane & 15;
  const int bid = blockIdx.x, xl = bid & 7, j = bid >> 3;
  const int h = xl * 4 + (j >> 3), s = j & 7;
  const int a = wave - NMAIN;
  STAMP(0);

  if (wave < NMAIN) {
    const rsrc_t rk = mkrsrc(Kc, (unsigned)H * SK * DH * 2), rv = mkrsrc(Vc, (unsigned)H * SK * DH * 2);
    u32x4 kA[8], vA[8], kB[8], vB[8];
    const unsigned kvbase = (unsigned)h * (8u << 20) + (unsigned)s * (1u << 20) + wave * 1024;
    const unsigned rot = 32u * ((h + (h >> 2) + (h >> 4)) & 3);
    const int kvoff = lane * 16;
    constexpr int STEP = ((int)T_MAIN << 16) / 256 * PACE_G;
    int due = (int)T_OFF * 65536 - STEP + (STEP / 256) * (wave * 32 + j);
#define PACE(I) do { if ((I) % PACE_G == 0) { due += STEP; pace_wait(t0, due >> 16); } } while (0)
    {
      const unsigned tb = kvbase + rot * 8192;
#pragma unroll
      for (int i = 0; i < 8; ++i) {
        PACE(i); kA[i] = ld16(rk, kvoff, tb + i * 8192, NT); SB();
        if (i == 0) { STAMP(12); SB(); }
        if (i == 1) { __builtin_amdgcn_s_barrier(); SB(); STAMP(10); }
      }
      STAMP(11);
#pragma unroll
      for (int i = 0; i < 8; ++i) { PACE(i); vA[i] = ld16(rv, kvoff, tb + i * 8192, NT); SB(); }
#pragma unroll
      for (int i = 0; i < 8; ++i) { PACE(i); kB[i] = ld16(rk, kvoff, tb + 65536 + i * 8192, NT); SB(); }
#pragma unroll
      for (int i = 0; i < 8; ++i) { PACE(i); vB[i] = ld16(rv, kvoff, tb + 65536 + i * 8192, NT); SB(); }
    }
    STAMP(1);
    __syncthreads();
    unsigned qp[4];
    {
      f32x4 q0 = *(LAS f32x4*)(lds + L_QS + r * 128 + 8 * c), q1 = *(LAS f32x4*)(lds + L_QS + r * 128 + 8 * c + 4);
#pragma unroll
      for (int e = 0; e < 4; ++e) { q0[e] = xrow_sum(q0[e]); q1[e] = xrow_sum(q1[e]); }
      qp[0] = pack2(q0[0], q0[1]); qp[1] = pack2(q0[2], q0[3]); qp[2] = pack2(q1[0], q1[1]); qp[3] = pack2(q1[2], q1[3]);
    }
    STAMP(2);
    float m = -INFINITY, l = 0.f, acc[8] = {0.f, 0.f, 0.f, 0.f, 0.f, 0.f, 0.f, 0.f};
    constexpr float SCALE = 0.08838834764831845f, LOG2E = 1.4426950408889634f;
    auto turn = [&](u32x4 (&kb)[8], u32x4 (&vb)[8], const bool more, const unsigned tb) __attribute__((always_inline)) {
      float sx[8];
#pragma unroll
      for (int i = 0; i < 8; ++i) {
        float d = 0.f;
#pragma unroll
        for (int t = 0; t < 4; ++t) d = dot2(kb[i][t], qp[t], d);
        asm volatile("" : "+v"(d));
        sx[i] = d;
        SB();
        if (more) {
          PACE(i);
          kb[i] = ld16(rk, kvoff, tb + i * 8192, NT);
          SB();
        }
      }
#define TR_STEP(HALF, CTRL, BIT) _Pragma("unroll") for (int hh = 0; hh < (HALF); ++hh) { \
        const float lo_ = sx[hh], hi_ = sx[hh + (HALF)]; const float t1_ = lo_ + dpp<CTRL>(lo_), t2_ = hi_ + dpp<CTRL>(hi_); \
        sx[hh] = (c & (BIT)) ? t2_ : t1_; }
      TR_STEP(4, ROR8, 8) TR_STEP(2, XOR7, 4) TR_STEP(1, XOR2, 2)
#undef TR_STEP
      const float sraw = sx[0] + dpp<XOR1>(sx[0]);
      const float sc = rbf(rbf(sraw) * SCALE) * LOG2E;
      float pm = fmaxf(sc, dpp<XOR2>(sc)); pm = fmaxf(pm, dpp<ROR4>(pm)); pm = fmaxf(pm, dpp<ROR8>(pm)); pm = xrow_max(pm);
      const float mn = fmaxf(m, pm);
      const float alpha = exp2_(m - mn), p = exp2_(sc - mn);
      l = fmaf(l, alpha, p);
      m = mn;
#pragma unroll
      for (int e = 0; e < 8; ++e) acc[e] *= alpha;
      SB();
#define PV_STEP(I) { fma8(acc, dpp<BCAST + 2 * (I)>(p), vb[I]); \
        asm volatile("" : "+v"(acc[0]), "+v"(acc[1]), "+v"(acc[2]), "+v"(acc[3]), "+v"(acc[4]), "+v"(acc[5]), "+v"(acc[6]), "+v"(acc[7])); SB(); \
        if (more) { PACE(I); vb[I] = ld16(rv, kvoff, tb + (I) * 8192, NT); SB(); } }
      PV_STEP(0) PV_STEP(1) PV_STEP(2) PV_STEP(3) PV_STEP(4) PV_STEP(5) PV_STEP(6) PV_STEP(7)
#undef PV_STEP
    };
    for (int n = 2; n < 16; n += 2) {
      const unsigned tb = kvbase + ((n * 8 + rot) & 127u) * 8192;
      turn(kA, vA, true, tb);
      turn(kB, vB, true, tb + 65536);
    }
    STAMP(3);
    turn(kA, vA, false, 0);
    turn(kB, vB, false, 0);
    STAMP(4);
#pragma unroll
    for (int e = 0; e < 8; ++e) acc[e] = xrow_sum(acc[e]);
    l = wave_sum(l) * 0.5f;
    if (r == 0) {
      *(LAS f32x4*)(lds + L_MRG + wave * MRG_STR + 4 + 8 * c) = f32x4{acc[0], acc[1], acc[2], acc[3]};
      *(LAS f32x4*)(lds + L_MRG + wave * MRG_STR + 8 + 8 * c) = f32x4{acc[4], acc[5], acc[6], acc[7]};
      if (c == 0) *(LAS f32x2*)(lds + L_MRG + wave * MRG_STR) = f32x2{m, l};
    }
    __syncthreads();
    STAMP_DUMP();
    return;
  }

  const rsrc_t rx = mkrsrc(x, D * 2), rws = mkrsrc(ws, WS_END);
  unsigned tagA, tagB;
  unsigned long long e1;
  {
    u32x4 xa = ld16(rx, lane * 32, 0, 0), xb = ld16(rx, lane * 32 + 16, 0, 0);
    const u32x2 ep = __builtin_amdgcn_raw_buffer_load_b64(rws, 0, WS_EPOCH, SC1);
    const rsrc_t rwq = mkrsrc(wq, D * H * DH * 2);
    u32x4 w[8];
    const int voff = r * 8192 + c * 16, sb = (s * 128 + a * 32) * 8192 + h * 256;
#pragma unroll
    for (int i = 0; i < 8; ++i) { w[i] = ld16(rwq, voff, sb + i * 32768, NT); SB(); }
    if (a == 0 && lane < 4) *(LAS unsigned*)(lds + L_CNT + lane) = 0u;
    __syncthreads();
    asm volatile("" : "+v"(xa), "+v"(xb));
    STAMP(10);
    float v[16];
#pragma unroll
    for (int i = 0; i < 4; ++i) { v[2 * i] = lo2f(xa[i]); v[2 * i + 1] = hi2f(xa[i]); v[8 + 2 * i] = lo2f(xb[i]); v[9 + 2 * i] = hi2f(xb[i]); }
    float sm = 0.f;
#pragma unroll
    for (int i = 0; i < 16; ++i) sm += v[i];
    const float mean = wave_sum(sm) * (1.f / D);
    float ss = 0.f;
#pragma unroll
    for (int i = 0; i < 16; ++i) { const float d = v[i] - mean; ss = fmaf(d, d, ss); }
    const float var = wave_sum(ss) * (1.f / D);
    const float mb = rbf(mean), den = rbf(sqrtf(rbf(rbf(var) + 1e-5f)));
    const float rinv = 1.0f / den;
#pragma unroll
    for (int i = 0; i < 16; ++i) {
      const float nu = rbf(v[i] - mb), q0 = nu * rinv;
      v[i] = rbf(fmaf(fmaf(-q0, den, nu), rinv, q0));
    }
#pragma unroll
    for (int q = 0; q < 4; ++q) *(LAS f32x4*)(lds + L_XN + q * 256 + 4 * lane) = f32x4{v[q], v[4 + q], v[8 + q], v[12 + q]};
    e1 = (((unsigned long long)__builtin_amdgcn_readfirstlane(ep[1]) << 32) | (unsigned)__builtin_amdgcn_readfirstlane(ep[0])) + 1ull;
    tagA = (unsigned)e1; tagB = (unsigned)(e1 >> 32) + (unsigned)e1 * 0x9E3779B1u;
    float acc[8] = {0.f, 0.f, 0.f, 0.f, 0.f, 0.f, 0.f, 0.f};
#pragma unroll
    for (int q = 0; q < 2; ++q) {
      const f32x4 xv = *(LAS f32x4*)(lds + L_XN + r * 256 + s * 32 + a * 8 + 4 * q);
#pragma unroll
      for (int e = 0; e < 4; ++e) fma8(acc, xv[e], w[4 * q + e]);
    }
#pragma unroll
    for (int e = 0; e < 8; ++e) acc[e] = xrow_sum(acc[e]);
    if (r == 0) {
      *(LAS f32x4*)(lds + L_QP + a * 128 + 8 * c) = f32x4{acc[0], acc[1], acc[2], acc[3]};
      *(LAS f32x4*)(lds + L_QP + a * 128 + 8 * c + 4) = f32x4{acc[4], acc[5], acc[6], acc[7]};
    }
  }
  STAMP(1);
  {
    aux_sync(lds + L_CNT + 1, lane);
    STAMP(11);
    if (a == 0) {
      f32x2 v = *(LAS f32x2*)(lds + L_QP + 2 * lane);
#pragma unroll
      for (int q = 1; q < 4; ++q) v += *(LAS f32x2*)(lds + L_QP + q * 128 + 2 * lane);
      st16_sc1(rws, lane * 16, WS_Q + (h * 8 + s) * 1024, gran(v[0], v[1], tagA, tagB));
    }
    u32x4 g0 = {0, 0, 0, 0}, g1 = g0;
    for (unsigned sp = 0; sp < SPIN_CAP; ++sp) {
      g0 = ld16_sc1(rws, lane * 16, WS_Q + (h * 8 + 2 * a) * 1024);
      g1 = ld16_sc1(rws, lane * 16, WS_Q + (h * 8 + 2 * a + 1) * 1024);
      if (sp == 0) STAMP(12);
      if (__all(gok(g0, tagA, tagB) && gok(g1, tagA, tagB))) break;
      __builtin_amdgcn_s_sleep(QSLEEP);
    }
    STAMP(13);
    *(LAS f32x2*)(lds + L_QS + a * 128 + 2 * lane) =
        f32x2{__uint_as_float(g0[0]) + __uint_as_float(g1[0]), __uint_as_float(g0[2]) + __uint_as_float(g1[2])};
  }
  __syncthreads();
  const unsigned tq = (unsigned)__builtin_amdgcn_s_memrealtime();
  int adue = AUX_T0 + a * (AUX_STEP / 4);

  {
    const int mat = bid >> 7, rb = (bid >> 3) & 15, cb = bid & 7;
    const rsrc_t rg = mkrsrc(mat ? wu : wg, D * F * 2);
    u32x4 g[16];
    const int voff = lane < 56 ? lane * 16 : OOB, sb = (rb * 64 + a * 16) * (F * 2) + cb * 896;
#pragma unroll
    for (int i = 0; i < 16; ++i) { pace_wait(t0, adue); adue += AUX_STEP; g[i] = ld16(rg, voff, sb + i * (F * 2), NT); SB(); }
    float ag[8] = {0.f, 0.f, 0.f, 0.f, 0.f, 0.f, 0.f, 0.f};
#pragma unroll
    for (int q = 0; q < 4; ++q) {
      const f32x4 xv = *(LAS f32x4*)(lds + L_XN + q * 256 + rb * 16 + a * 4);
#pragma unroll
      for (int e = 0; e < 4; ++e) fma8(ag, xv[e], g[4 * e + q]);
    }
    if (lane < 56) {
      *(LAS f32x4*)(lds + L_FP + a * 448 + lane * 8) = f32x4{ag[0], ag[1], ag[2], ag[3]};
      *(LAS f32x4*)(lds + L_FP + a * 448 + lane * 8 + 4) = f32x4{ag[4], ag[5], ag[6], ag[7]};
    }
    aux_sync(lds + L_CNT, lane);
    const int cl = a * 112 + 2 * (lane < 56 ? lane : 0);
    f32x2 v = *(LAS f32x2*)(lds + L_FP + cl);
#pragma unroll
    for (int q = 1; q < 4; ++q) v += *(LAS f32x2*)(lds + L_FP + q * 448 + cl);
    st16_sc1(rws, lane < 56 ? lane * 16 : OOB, WS_G1 + ((mat * 16 + rb) * F + cb * 448 + a * 112) * 8, gran(v[0], v[1], tagA, tagB));
  }
  if (j < 28) {
    const int p = xl * 28 + j;
    const rsrc_t rd = mkrsrc(wd, D * F * 2);
    u32x4 dw[8];
    {
      const int voff = (lane >> 5) * 2048 + (lane & 31) * 16, sb = p * 16 * 2048 + a * 512;
#pragma unroll
      for (int i = 0; i < 8; ++i) { pace_wait(t0, adue); adue += AUX_STEP; dw[i] = ld16(rd, voff, sb + i * 4096, NT); SB(); }
    }
    u32x4 g[4];
    const int voff = (lane >> 3) * (F * 8) + (lane & 7) * 16;
    for (unsigned sp = 0; sp < SPIN_CAP; ++sp) {
#pragma unroll
      for (int k = 0; k < 4; ++k) g[k] = ld16_sc1(rws, voff, WS_G1 + k * 8 * (F * 8) + p * 128);
      if (__all(gok(g[0], tagA, tagB) && gok(g[1], tagA, tagB) && gok(g[2], tagA, tagB) && gok(g[3], tagA, tagB))) break;
      __builtin_amdgcn_s_sleep(16);
    }
    float hh[2];
#pragma unroll
    for (int e = 0; e < 2; ++e) {
      float gs = __uint_as_float(g[0][2 * e]) + __uint_as_float(g[1][2 * e]), us = __uint_as_float(g[2][2 * e]) + __uint_as_float(g[3][2 * e]);
      gs += dpp<ROR8>(gs); us += dpp<ROR8>(us);
      gs = rbf(xrow_sum(gs)); us = rbf(xrow_sum(us));
      const float ge = 0.5f * gs * (1.f + tanhf(0.7978845608028654f * (gs + 0.044715f * gs * gs * gs)));
      hh[e] = rbf(rbf(ge) * us);
    }
    const float hv = (lane >= 32) ? hh[1] : hh[0];
    float y[8] = {0.f, 0.f, 0.f, 0.f, 0.f, 0.f, 0.f, 0.f};
    fma8(y, dpp<BCAST + 0>(hv), dw[0]); fma8(y, dpp<BCAST + 1>(hv), dw[1]); fma8(y, dpp<BCAST + 2>(hv), dw[2]); fma8(y, dpp<BCAST + 3>(hv), dw[3]);
    fma8(y, dpp<BCAST + 4>(hv), dw[4]); fma8(y, dpp<BCAST + 5>(hv), dw[5]); fma8(y, dpp<BCAST + 6>(hv), dw[6]); fma8(y, dpp<BCAST + 7>(hv), dw[7]);
#pragma unroll
    for (int e = 0; e < 8; ++e) y[e] = swap32_sum(y[e]);
    if (lane < 32) {
      *(LAS f32x4*)(lds + L_TMP + a * 256 + lane * 8) = f32x4{y[0], y[1], y[2], y[3]};
      *(LAS f32x4*)(lds + L_TMP + a * 256 + lane * 8 + 4) = f32x4{y[4], y[5], y[6], y[7]};
    }
#pragma unroll
    for (int q = 0; q < 2; ++q) {
      const f32x2 v = *(LAS f32x2*)(lds + L_TMP + a * 256 + q * 128 + 2 * lane);
      st16_sc1(rws, lane * 16, WS_FFN + p * FFN_STR + a * 2048 + q * 1024, gran(v[0], v[1], tagA, tagB));
    }
  } else {
    adue += 8 * AUX_STEP;
  }
  STAMP(2);
  u32x4 wov[8];
  {
    const rsrc_t ro = mkrsrc(wo, H * DH * D * 2);
    const int voff = r * 2048 + c * 16, sb = (h * 128 + a * 32) * 2048 + s * 256;
#pragma unroll
    for (int i = 0; i < 8; ++i) { pace_wait(t0, adue); adue += AUX_STEP; wov[i] = ld16(ro, voff, sb + i * 8192, NT); SB(); }
  }
  const int col0 = s * 128 + 4 * h;
  float f0 = 0.f, f1 = 0.f;
  unsigned xw = 0;
  if (a == 0) {
    xw = __builtin_amdgcn_raw_buffer_load_b32(rx, (col0 + 2 * (lane & 1)) * 2, 0, 0);
    const int voff = (lane >> 1) * FFN_STR + (lane & 1) * 16;
    for (unsigned sp = 0; sp < SPIN_CAP; ++sp) {
      u32x4 g[7];
#pragma unroll
      for (int i = 0; i < 7; ++i) g[i] = ld16_sc1(rws, voff, WS_FFN + i * 32 * FFN_STR + col0 * 8);
      bool ok = true;
      f0 = 0.f; f1 = 0.f;
#pragma unroll
      for (int i = 0; i < 7; ++i) { ok = ok && gok(g[i], tagA, tagB); f0 += __uint_as_float(g[i][0]); f1 += __uint_as_float(g[i][2]); }
      if (__all(ok)) break;
      __builtin_amdgcn_s_sleep(16);
    }
    f0 = rbf(parity_sum(f0)); f1 = rbf(parity_sum(f1));
  }
  STAMP(3);
  __syncthreads();
  STAMP(4);
  const rsrc_t rdg = mkrsrc((char*)ws + WS_DIAG, 8192);
  if (a == 3) { const unsigned tb2 = (unsigned)__builtin_amdgcn_s_memrealtime();
    st16_sc1(rdg, lane == 0 ? bid * 16 : OOB, 0, u32x4{t0, tagA, DIAG_MODE == 2 ? tq : tb2, tagB}); }
  if (a < 2) {
    float mw[8], M = -INFINITY;
#pragma unroll
    for (int w = 0; w < 8; ++w) { mw[w] = lds[L_MRG + w * MRG_STR]; M = fmaxf(M, mw[w]); }
    if (a == 0) {
      float o0 = 0.f, o1 = 0.f;
#pragma unroll
      for (int w = 0; w < 8; ++w) {
        const float wt = exp2_(mw[w] - M);
        const f32x2 v = *(LAS f32x2*)(lds + L_MRG + w * MRG_STR + 4 + 2 * lane);
        o0 = fmaf(wt, v[0], o0); o1 = fmaf(wt, v[1], o1);
      }
      st16_sc1(rws, lane * 16, WS_PART + (h * 8 + s) * PART_STR, gran(o0, o1, tagA, tagB));
    } else {
      float L = 0.f;
#pragma unroll
      for (int w = 0; w < 8; ++w) L = fmaf(exp2_(mw[w] - M), lds[L_MRG + w * MRG_STR + 1], L);
      st16_sc1(rws, lane == 0 ? 0 : OOB, WS_PART + (h * 8 + s) * PART_STR + 1024, gran(M, L, tagA, tagB));
    }
  }
  STAMP(5);
  {
    u32x4 go[2], gm[2];
    const int vo = r * PART_STR + (16 * a + c) * 16, vm = r * PART_STR + 1024;
    for (unsigned sp = 0; sp < SPIN_CAP; ++sp) {
#pragma unroll
      for (int k = 0; k < 2; ++k) {
        go[k] = ld16_sc1(rws, vo, WS_PART + (h * 8 + 4 * k) * PART_STR);
        gm[k] = ld16_sc1(rws, vm, WS_PART + (h * 8 + 4 * k) * PART_STR);
      }
      if (__all(gok(go[0], tagA, tagB) && gok(go[1], tagA, tagB) && gok(gm[0], tagA, tagB) && gok(gm[1], tagA, tagB))) break;
      __builtin_amdgcn_s_sleep(1);
    }
    STAMP(6);
    const float m0 = __uint_as_float(gm[0][0]), m1 = __uint_as_float(gm[1][0]);
    const float M = xrow_max(fmaxf(m0, m1));
    const float w0 = exp2_(m0 - M), w1 = exp2_(m1 - M);
    const float L = xrow_sum(fmaf(w0, __uint_as_float(gm[0][2]), w1 * __uint_as_float(gm[1][2])));
    const float o0 = rbf(xrow_sum(fmaf(w0, __uint_as_float(go[0][0]), w1 * __uint_as_float(go[1][0]))) / L);
    const float o1 = rbf(xrow_sum(fmaf(w0, __uint_as_float(go[0][2]), w1 * __uint_as_float(go[1][2]))) / L);
    float ov = (r & 1) ? o1 : o0;
    { const float sh = dpp<SHL1>(ov); ov = (r & 2) ? sh : ov; }
    float y[8] = {0.f, 0.f, 0.f, 0.f, 0.f, 0.f, 0.f, 0.f};
    fma8(y, dpp<BCAST + 0>(ov), wov[0]); fma8(y, dpp<BCAST + 2>(ov), wov[1]); fma8(y, dpp<BCAST + 4>(ov), wov[2]); fma8(y, dpp<BCAST + 6>(ov), wov[3]);
    fma8(y, dpp<BCAST + 8>(ov), wov[4]); fma8(y, dpp<BCAST + 10>(ov), wov[5]); fma8(y, dpp<BCAST + 12>(ov), wov[6]); fma8(y, dpp<BCAST + 14>(ov), wov[7]);
#pragma unroll
    for (int e = 0; e < 8; ++e) y[e] = xrow_sum(y[e]);
    if (r == 0) {
      *(LAS f32x4*)(lds + L_TMP + a * 256 + 8 * c) = f32x4{y[0], y[1], y[2], y[3]};
      *(LAS f32x4*)(lds + L_TMP + a * 256 + 8 * c + 4) = f32x4{y[4], y[5], y[6], y[7]};
    }
  }
  __syncthreads();
  if (a != 0) { STAMP_DUMP(); return; }
  {
    f32x2 v = *(LAS f32x2*)(lds + L_TMP + 2 * lane);
#pragma unroll
    for (int q = 1; q < 4; ++q) v += *(LAS f32x2*)(lds + L_TMP + q * 256 + 2 * lane);
    st16_sc1(rws, lane * 16, WS_Y + (h * 8 + s) * Y_STR, gran(v[0], v[1], tagA, tagB));
  }
  STAMP(7);
  {
    u32x4 g;
    const int voff = (lane >> 1) * (8 * Y_STR) + (lane & 1) * 16;
    for (unsigned sp = 0; sp < SPIN_CAP; ++sp) {
      g = ld16_sc1(rws, voff, WS_Y + s * Y_STR + h * 32);
      if (__all(gok(g, tagA, tagB))) break;
      __builtin_amdgcn_s_sleep(1);
    }
    STAMP(8);
    const float a0 = rbf(parity_sum(__uint_as_float(g[0]))), a1 = rbf(parity_sum(__uint_as_float(g[2])));
    const unsigned o = pack2(rbf(lo2f(xw) + a0) + f0, rbf(hi2f(xw) + a1) + f1);
    if (lane < 2) *(unsigned*)(out + col0 + 2 * lane) = o;
    if (bid == 0 && lane == 0) __builtin_amdgcn_raw_buffer_store_b64(u32x2{(unsigned)e1, (unsigned)(e1 >> 32)}, rws, 0, WS_EPOCH, SC1);
  }
  if (bid == 0) {
    const unsigned tend = (unsigned)__builtin_amdgcn_s_memrealtime();
    u32x4 g[4];
    for (unsigned sp = 0; sp < SPIN_CAP; ++sp) {
      for (int k = 0; k < 4; ++k) g[k] = ld16_sc1(rdg, lane * 16 + k * 1024, 0);
      if (__all(gok(g[0], tagA, tagB) && gok(g[1], tagA, tagB) && gok(g[2], tagA, tagB) && gok(g[3], tagA, tagB))) break;
    }
    int mn = 0x7fffffff, mx = -0x7fffffff, m2 = -0x7fffffff;
    for (int k = 0; k < 4; ++k) { int d = (int)(g[k][0] - t0); mn = min(mn, d); mx = max(mx, d); m2 = max(m2, (int)(g[k][2] - t0)); }
    for (int o = 1; o < 64; o <<= 1) { mn = min(mn, __shfl_xor(mn, o)); mx = max(mx, __shfl_xor(mx, o)); m2 = max(m2, __shfl_xor(m2, o)); }
    int v;
    if (DIAG_MODE == 0) v = (int)(tend - t0) - mn - T_MAIN;
    else if (DIAG_MODE == 1) v = mx - mn;
    else if (DIAG_MODE == 2) v = m2 - mn;
    else if (DIAG_MODE == 3) v = m2 - mn - T_MAIN;
    else v = 0;
    if (v < 0) v = 0;
    if (v > 3000) v = 3000;
    v = __builtin_amdgcn_readfirstlane(v * 5);
    pace_wait(__builtin_amdgcn_readfirstlane(tend), v);
  }
  STAMP(9);
  STAMP_DUMP();
}

extern "C" void kernel_launch(void* const* d_in, const int* in_sizes, int n_in, void* d_out, int out_size, void* d_ws, size_t ws_size,
                              hipStream_t stream) {
  fused_decode_kernel<<<256, NTHR, 0, stream>>>((const u16*)d_in[0], (const u16*)d_in[1], (const u16*)d_in[2], (const u16*)d_in[3],
                                                 (const u16*)d_in[4], (const u16*)d_in[6], (const u16*)d_in[7], (const u16*)d_in[8],
                                                 (u16*)d_out, d_ws);
}
```

```cpp
#include <hip/hip_runtime.h>
#include <stdint.h>

typedef unsigned u32x4 __attribute__((ext_vector_type(4)));
typedef unsigned u32x2 __attribute__((ext_vector_type(2)));
typedef float f32x4 __attribute__((ext_vector_type(4)));
typedef float f32x2 __attribute__((ext_vector_type(2)));
typedef __bf16 bf16x2 __attribute__((ext_vector_type(2)));
typedef unsigned short u16;
#define LAS __attribute__((address_space(3)))
typedef __amdgpu_buffer_rsrc_t rsrc_t;

#define T_MAIN 9300
#define T_OFF 80
#define PACE_G 1
#define QSLEEP 1
#define AUX_T0 500
#define AUX_STEP 200

constexpr int D = 1024, H = 32, SK = 32768, DH = 128, F = 3584;
constexpr int NMAIN = 8, NAUX = 4, NTHR = (NMAIN + NAUX) * 64;
constexpr int NT = 2  , SC1 = 16;
constexpr int OOB = 0x7ffffff0;
constexpr unsigned WS_EPOCH = 0, WS_Q = 4096, WS_PART = WS_Q + 256 * 1024, PART_STR = 2048, WS_Y = WS_PART + 256 * PART_STR, Y_STR = 1024,
                   WS_G1 = WS_Y + 256 * Y_STR, WS_FFN = WS_G1 + 32 * F * 8, FFN_STR = 8192, WS_END = WS_FFN + 224 * FFN_STR;
constexpr unsigned WS_STAMP = 8u << 20;
constexpr unsigned WS_DIAG = 6u << 20;
constexpr int L_XN = 0, L_QP = 1024, L_QS = L_QP + 4 * 128, L_MRG = L_QS + 4 * 128, MRG_STR = 132, L_FP = L_MRG + 8 * MRG_STR, L_TMP = L_FP + 4 * 448,
              L_CNT = L_TMP + 4 * 256, L_END = L_CNT + 4;

__device__ __forceinline__ float lo2f(unsigned w) { return __builtin_bit_cast(float, w << 16); }
__device__ __forceinline__ float hi2f(unsigned w) { return __builtin_bit_cast(float, w & 0xffff0000u); }
__device__ __forceinline__ float rbf(float x) { return (float)(__bf16)x; }
__device__ __forceinline__ unsigned pack2(float a, float b) { bf16x2 v = {(__bf16)a, (__bf16)b}; return __builtin_bit_cast(unsigned, v); }
__device__ __forceinline__ float dot2(unsigned k, unsigned q, float acc) {
  return __builtin_amdgcn_fdot2_f32_bf16(__builtin_bit_cast(bf16x2, k), __builtin_bit_cast(bf16x2, q), acc, false);
}
template <int CTRL> __device__ __forceinline__ float dpp(float x) {
  return __builtin_bit_cast(float, __builtin_amdgcn_mov_dpp(__builtin_bit_cast(int, x), CTRL, 0xf, 0xf, true));
}
constexpr int XOR1 = 0xB1, XOR2 = 0x4E, XOR7 = 0x141, ROR8 = 0x128, ROR4 = 0x124, SHL1 = 0x101, BCAST = 0x150;
__device__ __forceinline__ float swap32_sum(float x) {
  auto t = __builtin_amdgcn_permlane32_swap(__float_as_uint(x), __float_as_uint(x), false, false);
  return __uint_as_float(t[0]) + __uint_as_float(t[1]);
}
__device__ __forceinline__ float xrow_sum(float x) {
  auto s = __builtin_amdgcn_permlane16_swap(__float_as_uint(x), __float_as_uint(x), false, false);
  return swap32_sum(__uint_as_float(s[0]) + __uint_as_float(s[1]));
}
__device__ __forceinline__ float xrow_max(float x) {
  auto s = __builtin_amdgcn_permlane16_swap(__float_as_uint(x), __float_as_uint(x), false, false);
  x = fmaxf(__uint_as_float(s[0]), __uint_as_float(s[1]));
  auto t = __builtin_amdgcn_permlane32_swap(__float_as_uint(x), __float_as_uint(x), false, false);
  return fmaxf(__uint_as_float(t[0]), __uint_as_float(t[1]));
}
__device__ __forceinline__ float parity_sum(float x) {
  x += dpp<XOR2>(x); x += dpp<ROR4>(x); x += dpp<ROR8>(x); return xrow_sum(x);
}
__device__ __forceinline__ float wave_sum(float x) { x += dpp<XOR1>(x); return parity_sum(x); }
__device__ __forceinline__ float exp2_(float x) { return __builtin_amdgcn_exp2f(x); }
__device__ __forceinline__ rsrc_t mkrsrc(const void* p, unsigned bytes) { return __builtin_amdgcn_make_buffer_rsrc((void*)p, 0, (int)bytes, 0x00020000); }
__device__ __forceinline__ u32x4 ld16(rsrc_t r, int voff, int soff, int aux_nt) {
  return aux_nt == 2 ? __builtin_amdgcn_raw_buffer_load_b128(r, voff, soff, 2) : __builtin_amdgcn_raw_buffer_load_b128(r, voff, soff, 0);
}
__device__ __forceinline__ u32x4 ld16_sc1(rsrc_t r, int voff, int soff) { return __builtin_amdgcn_raw_buffer_load_b128(r, voff, soff, SC1); }
__device__ __forceinline__ void st16_sc1(rsrc_t r, int voff, int soff, u32x4 v) { __builtin_amdgcn_raw_buffer_store_b128(v, r, voff, soff, SC1); }
#define SB() __builtin_amdgcn_sched_barrier(0)
__device__ __forceinline__ void pace_wait(unsigned t0, unsigned due) {
  asm volatile("1:\n\ts_memrealtime s[96:97]\n\ts_waitcnt lgkmcnt(0)\n\ts_sub_u32 s96, s96, %0\n\ts_cmp_lt_i32 s96, %1\n\ts_cbranch_scc0 2f\n\ts_sleep 1\n\ts_branch 1b\n2:"
               :: "s"(t0), "s"(due) : "s96", "s97", "scc", "memory");
}
__device__ __forceinline__ void fma8(float (&acc)[8], float s, u32x4 w) {
#pragma unroll
  for (int t = 0; t < 4; ++t) { acc[2 * t] = fmaf(s, lo2f(w[t]), acc[2 * t]); acc[2 * t + 1] = fmaf(s, hi2f(w[t]), acc[2 * t + 1]); }
}
__device__ __forceinline__ u32x4 gran(float a, float b, unsigned ta, unsigned tb) { return u32x4{__float_as_uint(a), ta, __float_as_uint(b), tb}; }
__device__ __forceinline__ bool gok(u32x4 g, unsigned ta, unsigned tb) { return g[1] == ta && g[3] == tb; }
constexpr unsigned SPIN_CAP = 1u << 20;

#define STAMP(i) do { } while (0)
#define STAMP_DUMP() do { } while (0)

__device__ __forceinline__ void aux_sync(LAS float* cnt, int lane) {
  asm volatile("s_waitcnt lgkmcnt(0)" ::: "memory");
  if (lane == 0) __hip_atomic_fetch_add((LAS unsigned*)cnt, 1u, __ATOMIC_RELAXED, __HIP_MEMORY_SCOPE_WORKGROUP);
  for (unsigned sp = 0; sp < SPIN_CAP; ++sp) {
    if (__builtin_amdgcn_readfirstlane(*(volatile LAS unsigned*)cnt) >= (unsigned)NAUX) break;
    __builtin_amdgcn_s_sleep(1);
  }
  asm volatile("" ::: "memory");
}

__global__ void __launch_bounds__(NTHR, 3)
fused_decode_kernel(const u16* __restrict__ x, const u16* __restrict__ Kc, const u16* __restrict__ Vc, const u16* __restrict__ wq,
                    const u16* __restrict__ wo, const u16* __restrict__ wg, const u16* __restrict__ wu, const u16* __restrict__ wd,
                    u16* __restrict__ out, void* ws) {
  __shared__ __attribute__((aligned(16))) float lds_[L_END];
  LAS float* lds = (LAS float*)lds_;
  const unsigned t0 = (unsigned)__builtin_amdgcn_s_memrealtime();
  const int tid = threadIdx.x, lane = tid & 63, wave = __builtin_amdgcn_readfirstlane(tid >> 6);
  const int r = lane >> 4, c = lane & 15;
  const int bid = blockIdx.x, xl = bid & 7, j = bid >> 3;
  const int h = xl * 4 + (j >> 3), s = j & 7;
  const int a = wave - NMAIN;
  STAMP(0);

  if (wave < NMAIN) {
    const rsrc_t rk = mkrsrc(Kc, (unsigned)H * SK * DH * 2), rv = mkrsrc(Vc, (unsigned)H * SK * DH * 2);
    u32x4 kA[8], vA[8], kB[8], vB[8];
    const unsigned kvbase = (unsigned)h * (8u << 20) + (unsigned)s * (1u << 20) + wave * 1024;
    const unsigned rot = 32u * ((h + (h >> 2) + (h >> 4)) & 3);
    const int kvoff = lane * 16;
    constexpr int STEP = ((int)T_MAIN << 16) / 256 * PACE_G;
    int due = (int)T_OFF * 65536 - STEP + (STEP / 256) * (wave * 32 + j);
#define PACE(I) do { if ((I) % PACE_G == 0) { due += STEP; pace_wait(t0, due >> 16); } } while (0)
    {
      const unsigned tb = kvbase + rot * 8192;
#pragma unroll
      for (int i = 0; i < 8; ++i) {
        PACE(i); kA[i] = ld16(rk, kvoff, tb + i * 8192, NT); SB();
        if (i == 0) { STAMP(12); SB(); }
        if (i == 1) { __builtin_amdgcn_s_barrier(); SB(); STAMP(10); }
      }
      STAMP(11);
#pragma unroll
      for (int i = 0; i < 8; ++i) { PACE(i); vA[i] = ld16(rv, kvoff, tb + i * 8192, NT); SB(); }
#pragma unroll
      for (int i = 0; i < 8; ++i) { PACE(i); kB[i] = ld16(rk, kvoff, tb + 65536 + i * 8192, NT); SB(); }
#pragma unroll
      for (int i = 0; i < 8; ++i) { PACE(i); vB[i] = ld16(rv, kvoff, tb + 65536 + i * 8192, NT); SB(); }
    }
    STAMP(1);
    __syncthreads();
    unsigned qp[4];
    {
      f32x4 q0 = *(LAS f32x4*)(lds + L_QS + r * 128 + 8 * c), q1 = *(LAS f32x4*)(lds + L_QS + r * 128 + 8 * c + 4);
#pragma unroll
      for (int e = 0; e < 4; ++e) { q0[e] = xrow_sum(q0[e]); q1[e] = xrow_sum(q1[e]); }
      qp[0] = pack2(q0[0], q0[1]); qp[1] = pack2(q0[2], q0[3]); qp[2] = pack2(q1[0], q1[1]); qp[3] = pack2(q1[2], q1[3]);
    }
    STAMP(2);
    float m = -INFINITY, l = 0.f, acc[8] = {0.f, 0.f, 0.f, 0.f, 0.f, 0.f, 0.f, 0.f};
    constexpr float SCALE = 0.08838834764831845f, LOG2E = 1.4426950408889634f;
    auto turn = [&](u32x4 (&kb)[8], u32x4 (&vb)[8], const bool more, const unsigned tb) __attribute__((always_inline)) {
      float sx[8];
#pragma unroll
      for (int i = 0; i < 8; ++i) {
        float d = 0.f;
#pragma unroll
        for (int t = 0; t < 4; ++t) d = dot2(kb[i][t], qp[t], d);
        asm volatile("" : "+v"(d));
        sx[i] = d;
        SB();
        if (more) {
          PACE(i);
          kb[i] = ld16(rk, kvoff, tb + i * 8192, NT);
          SB();
        }
      }
#define TR_STEP(HALF, CTRL, BIT) _Pragma("unroll") for (int hh = 0; hh < (HALF); ++hh) { \
        const float lo_ = sx[hh], hi_ = sx[hh + (HALF)]; const float t1_ = lo_ + dpp<CTRL>(lo_), t2_ = hi_ + dpp<CTRL>(hi_); \
        sx[hh] = (c & (BIT)) ? t2_ : t1_; }
      TR_STEP(4, ROR8, 8) TR_STEP(2, XOR7, 4) TR_STEP(1, XOR2, 2)
#undef TR_STEP
      const float sraw = sx[0] + dpp<XOR1>(sx[0]);
      const float sc = rbf(rbf(sraw) * SCALE) * LOG2E;
      float pm = fmaxf(sc, dpp<XOR2>(sc)); pm = fmaxf(pm, dpp<ROR4>(pm)); pm = fmaxf(pm, dpp<ROR8>(pm)); pm = xrow_max(pm);
      const float mn = fmaxf(m, pm);
      const float alpha = exp2_(m - mn), p = exp2_(sc - mn);
      l = fmaf(l, alpha, p);
      m = mn;
#pragma unroll
      for (int e = 0; e < 8; ++e) acc[e] *= alpha;
      SB();
#define PV_STEP(I) { fma8(acc, dpp<BCAST + 2 * (I)>(p), vb[I]); \
        asm volatile("" : "+v"(acc[0]), "+v"(acc[1]), "+v"(acc[2]), "+v"(acc[3]), "+v"(acc[4]), "+v"(acc[5]), "+v"(acc[6]), "+v"(acc[7])); SB(); \
        if (more) { PACE(I); vb[I] = ld16(rv, kvoff, tb + (I) * 8192, NT); SB(); } }
      PV_STEP(0) PV_STEP(1) PV_STEP(2) PV_STEP(3) PV_STEP(4) PV_STEP(5) PV_STEP(6) PV_STEP(7)
#undef PV_STEP
    };
    for (int n = 2; n < 16; n += 2) {
      const unsigned tb = kvbase + ((n * 8 + rot) & 127u) * 8192;
      turn(kA, vA, true, tb);
      turn(kB, vB, true, tb + 65536);
    }
    STAMP(3);
    turn(kA, vA, false, 0);
    turn(kB, vB, false, 0);
    STAMP(4);
#pragma unroll
    for (int e = 0; e < 8; ++e) acc[e] = xrow_sum(acc[e]);
    l = wave_sum(l) * 0.5f;
    if (r == 0) {
      *(LAS f32x4*)(lds + L_MRG + wave * MRG_STR + 4 + 8 * c) = f32x4{acc[0], acc[1], acc[2], acc[3]};
      *(LAS f32x4*)(lds + L_MRG + wave * MRG_STR + 8 + 8 * c) = f32x4{acc[4], acc[5], acc[6], acc[7]};
      if (c == 0) *(LAS f32x2*)(lds + L_MRG + wave * MRG_STR) = f32x2{m, l};
    }
    __syncthreads();
    STAMP_DUMP();
    return;
  }

  const rsrc_t rx = mkrsrc(x, D * 2), rws = mkrsrc(ws, WS_END);
  unsigned tagA, tagB;
  unsigned long long e1;
  {
    u32x4 xa = ld16(rx, lane * 32, 0, 0), xb = ld16(rx, lane * 32 + 16, 0, 0);
    const u32x2 ep = __builtin_amdgcn_raw_buffer_load_b64(rws, 0, WS_EPOCH, SC1);
    const rsrc_t rwq = mkrsrc(wq, D * H * DH * 2);
    u32x4 w[8];
    const int voff = r * 8192 + c * 16, sb = (s * 128 + a * 32) * 8192 + h * 256;
#pragma unroll
    for (int i = 0; i < 8; ++i) { w[i] = ld16(rwq, voff, sb + i * 32768, NT); SB(); }
    if (a == 0 && lane < 4) *(LAS unsigned*)(lds + L_CNT + lane) = 0u;
    __syncthreads();
    asm volatile("" : "+v"(xa), "+v"(xb));
    STAMP(10);
    float v[16];
#pragma unroll
    for (int i = 0; i < 4; ++i) { v[2 * i] = lo2f(xa[i]); v[2 * i + 1] = hi2f(xa[i]); v[8 + 2 * i] = lo2f(xb[i]); v[9 + 2 * i] = hi2f(xb[i]); }
    float sm = 0.f;
#pragma unroll
    for (int i = 0; i < 16; ++i) sm += v[i];
    const float mean = wave_sum(sm) * (1.f / D);
    float ss = 0.f;
#pragma unroll
    for (int i = 0; i < 16; ++i) { const float d = v[i] - mean; ss = fmaf(d, d, ss); }
    const float var = wave_sum(ss) * (1.f / D);
    const float mb = rbf(mean), den = rbf(sqrtf(rbf(rbf(var) + 1e-5f)));
    const float rinv = 1.0f / den;
#pragma unroll
    for (int i = 0; i < 16; ++i) {
      const float nu = rbf(v[i] - mb), q0 = nu * rinv;
      v[i] = rbf(fmaf(fmaf(-q0, den, nu), rinv, q0));
    }
#pragma unroll
    for (int q = 0; q < 4; ++q) *(LAS f32x4*)(lds + L_XN + q * 256 + 4 * lane) = f32x4{v[q], v[4 + q], v[8 + q], v[12 + q]};
    e1 = (((unsigned long long)__builtin_amdgcn_readfirstlane(ep[1]) << 32) | (unsigned)__builtin_amdgcn_readfirstlane(ep[0])) + 1ull;
    tagA = (unsigned)e1; tagB = (unsigned)(e1 >> 32) + (unsigned)e1 * 0x9E3779B1u;
    float acc[8] = {0.f, 0.f, 0.f, 0.f, 0.f, 0.f, 0.f, 0.f};
#pragma unroll
    for (int q = 0; q < 2; ++q) {
      const f32x4 xv = *(LAS f32x4*)(lds + L_XN + r * 256 + s * 32 + a * 8 + 4 * q);
#pragma unroll
      for (int e = 0; e < 4; ++e) fma8(acc, xv[e], w[4 * q + e]);
    }
#pragma unroll
    for (int e = 0; e < 8; ++e) acc[e] = xrow_sum(acc[e]);
    if (r == 0) {
      *(LAS f32x4*)(lds + L_QP + a * 128 + 8 * c) = f32x4{acc[0], acc[1], acc[2], acc[3]};
      *(LAS f32x4*)(lds + L_QP + a * 128 + 8 * c + 4) = f32x4{acc[4], acc[5], acc[6], acc[7]};
    }
  }
  STAMP(1);
  {
    aux_sync(lds + L_CNT + 1, lane);
    STAMP(11);
    if (a == 0) {
      f32x2 v = *(LAS f32x2*)(lds + L_QP + 2 * lane);
#pragma unroll
      for (int q = 1; q < 4; ++q) v += *(LAS f32x2*)(lds + L_QP + q * 128 + 2 * lane);
      st16_sc1(rws, lane * 16, WS_Q + (h * 8 + s) * 1024, gran(v[0], v[1], tagA, tagB));
    }
    u32x4 g0 = {0, 0, 0, 0}, g1 = g0;
    for (unsigned sp = 0; sp < SPIN_CAP; ++sp) {
      g0 = ld16_sc1(rws, lane * 16, WS_Q + (h * 8 + 2 * a) * 1024);
      g1 = ld16_sc1(rws, lane * 16, WS_Q + (h * 8 + 2 * a + 1) * 1024);
      if (sp == 0) STAMP(12);
      if (__all(gok(g0, tagA, tagB) && gok(g1, tagA, tagB))) break;
      __builtin_amdgcn_s_sleep(QSLEEP);
    }
    STAMP(13);
    *(LAS f32x2*)(lds + L_QS + a * 128 + 2 * lane) =
        f32x2{__uint_as_float(g0[0]) + __uint_as_float(g1[0]), __uint_as_float(g0[2]) + __uint_as_float(g1[2])};
  }
  __syncthreads();
  int adue = AUX_T0 + a * (AUX_STEP / 4);

  {
    const int mat = bid >> 7, rb = (bid >> 3) & 15, cb = bid & 7;
    const rsrc_t rg = mkrsrc(mat ? wu : wg, D * F * 2);
    u32x4 g[16];
    const int voff = lane < 56 ? lane * 16 : OOB, sb = (rb * 64 + a * 16) * (F * 2) + cb * 896;
#pragma unroll
    for (int i = 0; i < 16; ++i) { pace_wait(t0, adue); adue += AUX_STEP; g[i] = ld16(rg, voff, sb + i * (F * 2), NT); SB(); }
    float ag[8] = {0.f, 0.f, 0.f, 0.f, 0.f, 0.f, 0.f, 0.f};
#pragma unroll
    for (int q = 0; q < 4; ++q) {
      const f32x4 xv = *(LAS f32x4*)(lds + L_XN + q * 256 + rb * 16 + a * 4);
#pragma unroll
      for (int e = 0; e < 4; ++e) fma8(ag, xv[e], g[4 * e + q]);
    }
    if (lane < 56) {
      *(LAS f32x4*)(lds + L_FP + a * 448 + lane * 8) = f32x4{ag[0], ag[1], ag[2], ag[3]};
      *(LAS f32x4*)(lds + L_FP + a * 448 + lane * 8 + 4) = f32x4{ag[4], ag[5], ag[6], ag[7]};
    }
    aux_sync(lds + L_CNT, lane);
    const int cl = a * 112 + 2 * (lane < 56 ? lane : 0);
    f32x2 v = *(LAS f32x2*)(lds + L_FP + cl);
#pragma unroll
    for (int q = 1; q < 4; ++q) v += *(LAS f32x2*)(lds + L_FP + q * 448 + cl);
    st16_sc1(rws, lane < 56 ? lane * 16 : OOB, WS_G1 + ((mat * 16 + rb) * F + cb * 448 + a * 112) * 8, gran(v[0], v[1], tagA, tagB));
  }
  if (j < 28) {
    const int p = xl * 28 + j;
    const rsrc_t rd = mkrsrc(wd, D * F * 2);
    u32x4 dw[8];
    {
      const int voff = (lane >> 5) * 2048 + (lane & 31) * 16, sb = p * 16 * 2048 + a * 512;
#pragma unroll
      for (int i = 0; i < 8; ++i) { pace_wait(t0, adue); adue += AUX_STEP; dw[i] = ld16(rd, voff, sb + i * 4096, NT); SB(); }
    }
    u32x4 g[4];
    const int voff = (lane >> 3) * (F * 8) + (lane & 7) * 16;
    for (unsigned sp = 0; sp < SPIN_CAP; ++sp) {
#pragma unroll
      for (int k = 0; k < 4; ++k) g[k] = ld16_sc1(rws, voff, WS_G1 + k * 8 * (F * 8) + p * 128);
      if (__all(gok(g[0], tagA, tagB) && gok(g[1], tagA, tagB) && gok(g[2], tagA, tagB) && gok(g[3], tagA, tagB))) break;
      __builtin_amdgcn_s_sleep(16);
    }
    float hh[2];
#pragma unroll
    for (int e = 0; e < 2; ++e) {
      float gs = __uint_as_float(g[0][2 * e]) + __uint_as_float(g[1][2 * e]), us = __uint_as_float(g[2][2 * e]) + __uint_as_float(g[3][2 * e]);
      gs += dpp<ROR8>(gs); us += dpp<ROR8>(us);
      gs = rbf(xrow_sum(gs)); us = rbf(xrow_sum(us));
      const float ge = 0.5f * gs * (1.f + tanhf(0.7978845608028654f * (gs + 0.044715f * gs * gs * gs)));
      hh[e] = rbf(rbf(ge) * us);
    }
    const float hv = (lane >= 32) ? hh[1] : hh[0];
    float y[8] = {0.f, 0.f, 0.f, 0.f, 0.f, 0.f, 0.f, 0.f};
    fma8(y, dpp<BCAST + 0>(hv), dw[0]); fma8(y, dpp<BCAST + 1>(hv), dw[1]); fma8(y, dpp<BCAST + 2>(hv), dw[2]); fma8(y, dpp<BCAST + 3>(hv), dw[3]);
    fma8(y, dpp<BCAST + 4>(hv), dw[4]); fma8(y, dpp<BCAST + 5>(hv), dw[5]); fma8(y, dpp<BCAST + 6>(hv), dw[6]); fma8(y, dpp<BCAST + 7>(hv), dw[7]);
#pragma unroll
    for (int e = 0; e < 8; ++e) y[e] = swap32_sum(y[e]);
    if (lane < 32) {
      *(LAS f32x4*)(lds + L_TMP + a * 256 + lane * 8) = f32x4{y[0], y[1], y[2], y[3]};
      *(LAS f32x4*)(lds + L_TMP + a * 256 + lane * 8 + 4) = f32x4{y[4], y[5], y[6], y[7]};
    }
#pragma unroll
    for (int q = 0; q < 2; ++q) {
      const f32x2 v = *(LAS f32x2*)(lds + L_TMP + a * 256 + q * 128 + 2 * lane);
      st16_sc1(rws, lane * 16, WS_FFN + p * FFN_STR + a * 2048 + q * 1024, gran(v[0], v[1], tagA, tagB));
    }
  } else {
    adue += 8 * AUX_STEP;
  }
  { const rsrc_t rj = mkrsrc((char*)ws + (64u << 20), 32u << 20);
    for (int i = 0; i < 16; ++i) __builtin_amdgcn_raw_buffer_store_b128(u32x4{tagA, tagB, 1u, 2u}, rj, lane * 16, bid * 65536 + a * 16384 + i * 1024, SC1); }
  STAMP(2);
  u32x4 wov[8];
  {
    const rsrc_t ro = mkrsrc(wo, H * DH * D * 2);
    const int voff = r * 2048 + c * 16, sb = (h * 128 + a * 32) * 2048 + s * 256;
#pragma unroll
    for (int i = 0; i < 8; ++i) { pace_wait(t0, adue); adue += AUX_STEP; wov[i] = ld16(ro, voff, sb + i * 8192, NT); SB(); }
  }
  const int col0 = s * 128 + 4 * h;
  float f0 = 0.f, f1 = 0.f;
  unsigned xw = 0;
  if (a == 0) {
    xw = __builtin_amdgcn_raw_buffer_load_b32(rx, (col0 + 2 * (lane & 1)) * 2, 0, 0);
    const int voff = (lane >> 1) * FFN_STR + (lane & 1) * 16;
    for (unsigned sp = 0; sp < SPIN_CAP; ++sp) {
      u32x4 g[7];
#pragma unroll
      for (int i = 0; i < 7; ++i) g[i] = ld16_sc1(rws, voff, WS_FFN + i * 32 * FFN_STR + col0 * 8);
      bool ok = true;
      f0 = 0.f; f1 = 0.f;
#pragma unroll
      for (int i = 0; i < 7; ++i) { ok = ok && gok(g[i], tagA, tagB); f0 += __uint_as_float(g[i][0]); f1 += __uint_as_float(g[i][2]); }
      if (__all(ok)) break;
      __builtin_amdgcn_s_sleep(16);
    }
    f0 = rbf(parity_sum(f0)); f1 = rbf(parity_sum(f1));
  }
  STAMP(3);
  __syncthreads();
  STAMP(4);
  if (a < 2) {
    float mw[8], M = -INFINITY;
#pragma unroll
    for (int w = 0; w < 8; ++w) { mw[w] = lds[L_MRG + w * MRG_STR]; M = fmaxf(M, mw[w]); }
    if (a == 0) {
      float o0 = 0.f, o1 = 0.f;
#pragma unroll
      for (int w = 0; w < 8; ++w) {
        const float wt = exp2_(mw[w] - M);
        const f32x2 v = *(LAS f32x2*)(lds + L_MRG + w * MRG_STR + 4 + 2 * lane);
        o0 = fmaf(wt, v[0], o0); o1 = fmaf(wt, v[1], o1);
      }
      st16_sc1(rws, lane * 16, WS_PART + (h * 8 + s) * PART_STR, gran(o0, o1, tagA, tagB));
    } else {
      float L = 0.f;
#pragma unroll
      for (int w = 0; w < 8; ++w) L = fmaf(exp2_(mw[w] - M), lds[L_MRG + w * MRG_STR + 1], L);
      st16_sc1(rws, lane == 0 ? 0 : OOB, WS_PART + (h * 8 + s) * PART_STR + 1024, gran(M, L, tagA, tagB));
    }
  }
  STAMP(5);
  {
    u32x4 go[2], gm[2];
    const int vo = r * PART_STR + (16 * a + c) * 16, vm = r * PART_STR + 1024;
    for (unsigned sp = 0; sp < SPIN_CAP; ++sp) {
#pragma unroll
      for (int k = 0; k < 2; ++k) {
        go[k] = ld16_sc1(rws, vo, WS_PART + (h * 8 + 4 * k) * PART_STR);
        gm[k] = ld16_sc1(rws, vm, WS_PART + (h * 8 + 4 * k) * PART_STR);
      }
      if (__all(gok(go[0], tagA, tagB) && gok(go[1], tagA, tagB) && gok(gm[0], tagA, tagB) && gok(gm[1], tagA, tagB))) break;
      __builtin_amdgcn_s_sleep(1);
    }
    STAMP(6);
    const float m0 = __uint_as_float(gm[0][0]), m1 = __uint_as_float(gm[1][0]);
    const float M = xrow_max(fmaxf(m0, m1));
    const float w0 = exp2_(m0 - M), w1 = exp2_(m1 - M);
    const float L = xrow_sum(fmaf(w0, __uint_as_float(gm[0][2]), w1 * __uint_as_float(gm[1][2])));
    const float o0 = rbf(xrow_sum(fmaf(w0, __uint_as_float(go[0][0]), w1 * __uint_as_float(go[1][0]))) / L);
    const float o1 = rbf(xrow_sum(fmaf(w0, __uint_as_float(go[0][2]), w1 * __uint_as_float(go[1][2]))) / L);
    float ov = (r & 1) ? o1 : o0;
    { const float sh = dpp<SHL1>(ov); ov = (r & 2) ? sh : ov; }
    float y[8] = {0.f, 0.f, 0.f, 0.f, 0.f, 0.f, 0.f, 0.f};
    fma8(y, dpp<BCAST + 0>(ov), wov[0]); fma8(y, dpp<BCAST + 2>(ov), wov[1]); fma8(y, dpp<BCAST + 4>(ov), wov[2]); fma8(y, dpp<BCAST + 6>(ov), wov[3]);
    fma8(y, dpp<BCAST + 8>(ov), wov[4]); fma8(y, dpp<BCAST + 10>(ov), wov[5]); fma8(y, dpp<BCAST + 12>(ov), wov[6]); fma8(y, dpp<BCAST + 14>(ov), wov[7]);
#pragma unroll
    for (int e = 0; e < 8; ++e) y[e] = xrow_sum(y[e]);
    if (r == 0) {
      *(LAS f32x4*)(lds + L_TMP + a * 256 + 8 * c) = f32x4{y[0], y[1], y[2], y[3]};
      *(LAS f32x4*)(lds + L_TMP + a * 256 + 8 * c + 4) = f32x4{y[4], y[5], y[6], y[7]};
    }
  }
  __syncthreads();
  if (a != 0) { STAMP_DUMP(); return; }
  {
    f32x2 v = *(LAS f32x2*)(lds + L_TMP + 2 * lane);
#pragma unroll
    for (int q = 1; q < 4; ++q) v += *(LAS f32x2*)(lds + L_TMP + q * 256 + 2 * lane);
    st16_sc1(rws, lane * 16, WS_Y + (h * 8 + s) * Y_STR, gran(v[0], v[1], tagA, tagB));
  }
  STAMP(7);
  {
    u32x4 g;
    const int voff = (lane >> 1) * (8 * Y_STR) + (lane & 1) * 16;
    for (unsigned sp = 0; sp < SPIN_CAP; ++sp) {
      g = ld16_sc1(rws, voff, WS_Y + s * Y_STR + h * 32);
      if (__all(gok(g, tagA, tagB))) break;
      __builtin_amdgcn_s_sleep(1);
    }
    STAMP(8);
    const float a0 = rbf(parity_sum(__uint_as_float(g[0]))), a1 = rbf(parity_sum(__uint_as_float(g[2])));
    const unsigned o = pack2(rbf(lo2f(xw) + a0) + f0, rbf(hi2f(xw) + a1) + f1);
    if (lane < 2) *(unsigned*)(out + col0 + 2 * lane) = o;
    if (bid == 0 && lane == 0) __builtin_amdgcn_raw_buffer_store_b64(u32x2{(unsigned)e1, (unsigned)(e1 >> 32)}, rws, 0, WS_EPOCH, SC1);
  }
  STAMP(9);
  STAMP_DUMP();
}

__global__ void tiny_kernel(unsigned* p) { if (p == nullptr) __builtin_trap(); }
extern "C" void kernel_launch(void* const* d_in, const int* in_sizes, int n_in, void* d_out, int out_size, void* d_ws, size_t ws_size,
                              hipStream_t stream) {
  fused_decode_kernel<<<256, NTHR, 0, stream>>>((const u16*)d_in[0], (const u16*)d_in[1], (const u16*)d_in[2], (const u16*)d_in[3],
                                                 (const u16*)d_in[4], (const u16*)d_in[6], (const u16*)d_in[7], (const u16*)d_in[8],
                                                 (u16*)d_out, d_ws);
}
```

```cpp
#define T_MAIN 8050
#define T_OFF -200
#define AUX_T0 1200
#define AUX_STEP 201
#define GAT1 0
#define GAT2 0
#define ABL_DEFAULT 0
#include <hip/hip_runtime.h>
#include <stdint.h>

typedef unsigned u32x4 __attribute__((ext_vector_type(4)));
typedef unsigned u32x2 __attribute__((ext_vector_type(2)));
typedef float f32x4 __attribute__((ext_vector_type(4)));
typedef float f32x2 __attribute__((ext_vector_type(2)));
typedef __bf16 bf16x2 __attribute__((ext_vector_type(2)));
typedef unsigned short u16;
#define LAS __attribute__((address_space(3)))
typedef __amdgpu_buffer_rsrc_t rsrc_t;

#define PACE_G 1
#define QSLEEP 1

#define TUN_ARG
#define P_TMAIN T_MAIN
#define P_TOFF T_OFF
#define P_AUXT0 AUX_T0
#define P_AUXSTEP AUX_STEP
#define P_GAT1 GAT1
#define P_GAT2 GAT2
#define ABL(b) ((ABL_DEFAULT >> (b)) & 1)
constexpr int D = 1024, H = 32, SK = 32768, DH = 128, F = 3584;
constexpr int NMAIN = 8, NAUX = 4, NWAVES = NMAIN + NAUX, NTHR = NWAVES * 64;
constexpr int NT = 2  , SC1 = 16;
constexpr int OOB = 0x7ffffff0;
constexpr unsigned WS_EPOCH = 0, WS_Q = 4096, WS_PART = WS_Q + 256 * 1024, PART_STR = 2048, WS_Y = WS_PART + 256 * PART_STR, Y_STR = 1024,
                   WS_G1 = WS_Y + 256 * Y_STR, WS_FFN = WS_G1 + 4 * F * 8, WS_END = WS_FFN + 16 * D * 8;
constexpr unsigned WS_STAMP = 8u << 20;
constexpr unsigned WS_DIAG = 6u << 20;
constexpr int L_XN = 0, L_QP = 1024, L_QS = L_QP + 4 * 128, L_MRG = L_QS + 128, MRG_STR = 132, L_FP = L_MRG + 8 * MRG_STR, L_H = L_FP + 4 * 64,
              L_F2 = L_H + 4 * 64, L_TMP = L_F2 + 4 * 64, L_CNT = L_TMP + 4 * 128, L_END = L_CNT + 4;

__device__ __forceinline__ float lo2f(unsigned w) { return __builtin_bit_cast(float, w << 16); }
__device__ __forceinline__ float hi2f(unsigned w) { return __builtin_bit_cast(float, w & 0xffff0000u); }
__device__ __forceinline__ float rbf(float x) { return (float)(__bf16)x; }
__device__ __forceinline__ unsigned pack2(float a, float b) { bf16x2 v = {(__bf16)a, (__bf16)b}; return __builtin_bit_cast(unsigned, v); }
__device__ __forceinline__ float dot2(unsigned k, unsigned q, float acc) {
  return __builtin_amdgcn_fdot2_f32_bf16(__builtin_bit_cast(bf16x2, k), __builtin_bit_cast(bf16x2, q), acc, false);
}
template <int CTRL> __device__ __forceinline__ float dpp(float x) {
  return __builtin_bit_cast(float, __builtin_amdgcn_mov_dpp(__builtin_bit_cast(int, x), CTRL, 0xf, 0xf, true));
}
constexpr int XOR1 = 0xB1, XOR2 = 0x4E, XOR7 = 0x141, ROR8 = 0x128, ROR4 = 0x124, SHL1 = 0x101, BCAST = 0x150;
__device__ __forceinline__ float swap32_sum(float x) {
  auto t = __builtin_amdgcn_permlane32_swap(__float_as_uint(x), __float_as_uint(x), false, false);
  return __uint_as_float(t[0]) + __uint_as_float(t[1]);
}
__device__ __forceinline__ float xrow_sum(float x) {
  auto s = __builtin_amdgcn_permlane16_swap(__float_as_uint(x), __float_as_uint(x), false, false);
  return swap32_sum(__uint_as_float(s[0]) + __uint_as_float(s[1]));
}
__device__ __forceinline__ float xrow_max(float x) {
  auto s = __builtin_amdgcn_permlane16_swap(__float_as_uint(x), __float_as_uint(x), false, false);
  x = fmaxf(__uint_as_float(s[0]), __uint_as_float(s[1]));
  auto t = __builtin_amdgcn_permlane32_swap(__float_as_uint(x), __float_as_uint(x), false, false);
  return fmaxf(__uint_as_float(t[0]), __uint_as_float(t[1]));
}
__device__ __forceinline__ float parity_sum(float x) {
  x += dpp<XOR2>(x); x += dpp<ROR4>(x); x += dpp<ROR8>(x); return xrow_sum(x);
}
__device__ __forceinline__ float wave_sum(float x) { x += dpp<XOR1>(x); return parity_sum(x); }
__device__ __forceinline__ float exp2_(float x) { return __builtin_amdgcn_exp2f(x); }
__device__ __forceinline__ rsrc_t mkrsrc(const void* p, unsigned bytes) { return __builtin_amdgcn_make_buffer_rsrc((void*)p, 0, (int)bytes, 0x00020000); }
__device__ __forceinline__ u32x4 ld16(rsrc_t r, int voff, int soff, int aux_nt) {
  return aux_nt == 2 ? __builtin_amdgcn_raw_buffer_load_b128(r, voff, soff, 2) : __builtin_amdgcn_raw_buffer_load_b128(r, voff, soff, 0);
}
__device__ __forceinline__ u32x4 ld16_sc1(rsrc_t r, int voff, int soff) { return __builtin_amdgcn_raw_buffer_load_b128(r, voff, soff, SC1); }
__device__ __forceinline__ void st16_sc1(rsrc_t r, int voff, int soff, u32x4 v) { __builtin_amdgcn_raw_buffer_store_b128(v, r, voff + soff, 0, SC1); }
__device__ __forceinline__ u32x4 ld16_x(rsrc_t r, int voff, int soff, int nt) { return nt ? __builtin_amdgcn_raw_buffer_load_b128(r, voff, soff, SC1 | NT) : __builtin_amdgcn_raw_buffer_load_b128(r, voff, soff, SC1); }
__device__ __forceinline__ void st16_x(rsrc_t r, int voff, int soff, u32x4 v, int nt) { if (nt) __builtin_amdgcn_raw_buffer_store_b128(v, r, voff + soff, 0, SC1 | NT); else __builtin_amdgcn_raw_buffer_store_b128(v, r, voff + soff, 0, SC1); }
#define SB() __builtin_amdgcn_sched_barrier(0)
__device__ __forceinline__ void pace_wait(unsigned t0, unsigned due) {
  asm volatile("1:\n\ts_memrealtime s[96:97]\n\ts_waitcnt lgkmcnt(0)\n\ts_sub_u32 s96, s96, %0\n\ts_cmp_lt_i32 s96, %1\n\ts_cbranch_scc0 2f\n\ts_sleep 1\n\ts_branch 1b\n2:"
               :: "s"(t0), "s"(due) : "s96", "s97", "scc", "memory");
}
__device__ __forceinline__ void fma8(float (&acc)[8], float s, u32x4 w) {
#pragma unroll
  for (int t = 0; t < 4; ++t) { acc[2 * t] = fmaf(s, lo2f(w[t]), acc[2 * t]); acc[2 * t + 1] = fmaf(s, hi2f(w[t]), acc[2 * t + 1]); }
}
__device__ __forceinline__ u32x4 gran(float a, float b, unsigned ta, unsigned tb) { return u32x4{__float_as_uint(a), ta, __float_as_uint(b), tb}; }
__device__ __forceinline__ bool gok(u32x4 g, unsigned ta, unsigned tb) { return g[1] == ta && g[3] == tb; }
constexpr unsigned SPIN_CAP = 1u << 20;

#define STAMP(i) do { } while (0)
#define STAMP_DUMP() do { } while (0)

__device__ __forceinline__ void aux_arrive(LAS float* cnt, int lane) {
  asm volatile("s_waitcnt lgkmcnt(0)" ::: "memory");
  if (lane == 0) __hip_atomic_fetch_add((LAS unsigned*)cnt, 1u, __ATOMIC_RELAXED, __HIP_MEMORY_SCOPE_WORKGROUP);
}
__device__ __forceinline__ void aux_wait(LAS float* cnt) {
  for (unsigned sp = 0; sp < SPIN_CAP; ++sp) {
    if (__builtin_amdgcn_readfirstlane(*(volatile LAS unsigned*)cnt) >= (unsigned)NAUX) break;
    __builtin_amdgcn_s_sleep(1);
  }
  asm volatile("" ::: "memory");
}

__global__ void __launch_bounds__(NTHR, 3)
fused_decode_kernel(const u16* __restrict__ x, const u16* __restrict__ Kc, const u16* __restrict__ Vc, const u16* __restrict__ wq,
                    const u16* __restrict__ wo, const u16* __restrict__ wg, const u16* __restrict__ wu, const u16* __restrict__ wd,
                    u16* __restrict__ out, void* ws TUN_ARG) {
  __shared__ __attribute__((aligned(16))) float lds_[L_END];
  LAS float* lds = (LAS float*)lds_;
  const unsigned t0 = (unsigned)__builtin_amdgcn_s_memrealtime();
  const int tid = threadIdx.x, lane = tid & 63, wave = __builtin_amdgcn_readfirstlane(tid >> 6);
  const int r = lane >> 4, c = lane & 15;
  const int bid = blockIdx.x, xl = bid & 7, j = bid >> 3;
  const int h = xl * 4 + (j >> 3), s = j & 7;
  const int a = wave - NMAIN;
  STAMP(0);

  if (wave < NMAIN) {
    const rsrc_t rk = mkrsrc(Kc, (unsigned)H * SK * DH * 2), rv = mkrsrc(Vc, (unsigned)H * SK * DH * 2);
    u32x4 kA[8], vA[8], kB[8], vB[8];
    const unsigned kvbase = (unsigned)h * (8u << 20) + (unsigned)s * (1u << 20) + wave * 1024;
    const unsigned rot = 32u * ((h + (h >> 2) + (h >> 4)) & 3);
    const int kvoff = lane * 16;
    const int STEP = ((int)P_TMAIN << 16) / 256 * PACE_G;
    int due = (int)P_TOFF * 65536 - STEP + (STEP / 256) * (wave * 32 + j);
#define PACE(I) do { if ((I) % PACE_G == 0) { due += STEP; pace_wait(t0, due >> 16); } } while (0)
    {
      const unsigned tb = kvbase + rot * 8192;
      __builtin_amdgcn_s_barrier(); SB();
      STAMP(10);
#pragma unroll
      for (int i = 0; i < 8; ++i) {
        PACE(i); kA[i] = ld16(rk, kvoff, tb + i * 8192, NT); SB();
        if (i == 0) { STAMP(12); SB(); }
      }
      STAMP(11);
#pragma unroll
      for (int i = 0; i < 8; ++i) { PACE(i); vA[i] = ld16(rv, kvoff, tb + i * 8192, NT); SB(); }
#pragma unroll
      for (int i = 0; i < 8; ++i) { PACE(i); kB[i] = ld16(rk, kvoff, tb + 65536 + i * 8192, NT); SB(); }
#pragma unroll
      for (int i = 0; i < 8; ++i) { PACE(i); vB[i] = ld16(rv, kvoff, tb + 65536 + i * 8192, NT); SB(); }
    }
    STAMP(1);
    __syncthreads();
    unsigned qp[4];
    {
      const u32x4 qv = *(LAS u32x4*)(lds + L_QS + 4 * c);
      qp[0] = qv[0]; qp[1] = qv[1]; qp[2] = qv[2]; qp[3] = qv[3];
    }
    STAMP(2);
    float m = -INFINITY, l = 0.f, acc[8] = {0.f, 0.f, 0.f, 0.f, 0.f, 0.f, 0.f, 0.f};
    constexpr float SCALE = 0.08838834764831845f, LOG2E = 1.4426950408889634f;
    auto turn = [&](u32x4 (&kb)[8], u32x4 (&vb)[8], const bool more, const unsigned tb) __attribute__((always_inline)) {
      float sx[8];
#pragma unroll
      for (int i = 0; i < 8; ++i) {
        float d = 0.f;
#pragma unroll
        for (int t = 0; t < 4; ++t) d = dot2(kb[i][t], qp[t], d);
        asm volatile("" : "+v"(d));
        sx[i] = d;
        SB();
        if (more) {
          PACE(i);
          kb[i] = ld16(rk, kvoff, tb + i * 8192, NT);
          SB();
        }
      }
#define TR_STEP(HALF, CTRL, BIT) _Pragma("unroll") for (int hh = 0; hh < (HALF); ++hh) { \
        const float lo_ = sx[hh], hi_ = sx[hh + (HALF)]; const float t1_ = lo_ + dpp<CTRL>(lo_), t2_ = hi_ + dpp<CTRL>(hi_); \
        sx[hh] = (c & (BIT)) ? t2_ : t1_; }
      TR_STEP(4, ROR8, 8) TR_STEP(2, XOR7, 4) TR_STEP(1, XOR2, 2)
#undef TR_STEP
      const float sraw = sx[0] + dpp<XOR1>(sx[0]);
      const float sc = rbf(rbf(sraw) * SCALE) * LOG2E;
      float pm = fmaxf(sc, dpp<XOR2>(sc)); pm = fmaxf(pm, dpp<ROR4>(pm)); pm = fmaxf(pm, dpp<ROR8>(pm)); pm = xrow_max(pm);
      const float mn = fmaxf(m, pm);
      const float alpha = exp2_(m - mn), p = exp2_(sc - mn);
      l = fmaf(l, alpha, p);
      m = mn;
#pragma unroll
      for (int e = 0; e < 8; ++e) acc[e] *= alpha;
      SB();
#define PV_STEP(I) { fma8(acc, dpp<BCAST + 2 * (I)>(p), vb[I]); \
        asm volatile("" : "+v"(acc[0]), "+v"(acc[1]), "+v"(acc[2]), "+v"(acc[3]), "+v"(acc[4]), "+v"(acc[5]), "+v"(acc[6]), "+v"(acc[7])); SB(); \
        if (more) { PACE(I); vb[I] = ld16(rv, kvoff, tb + (I) * 8192, NT); SB(); } }
      PV_STEP(0) PV_STEP(1) PV_STEP(2) PV_STEP(3) PV_STEP(4) PV_STEP(5) PV_STEP(6) PV_STEP(7)
#undef PV_STEP
    };
    turn(kA, vA, false, 0);
    for (int n = 2; n < 16; ++n) turn(kB, vB, true, kvbase + ((n * 8 + rot) & 127u) * 8192);
    STAMP(3);
    turn(kB, vB, false, 0);
    STAMP(4);
#pragma unroll
    for (int e = 0; e < 8; ++e) acc[e] = xrow_sum(acc[e]);
    l = wave_sum(l) * 0.5f;
    if (r == 0) {
      *(LAS f32x4*)(lds + L_MRG + wave * MRG_STR + 4 + 8 * c) = f32x4{acc[0], acc[1], acc[2], acc[3]};
      *(LAS f32x4*)(lds + L_MRG + wave * MRG_STR + 8 + 8 * c) = f32x4{acc[4], acc[5], acc[6], acc[7]};
      if (c == 0) *(LAS f32x2*)(lds + L_MRG + wave * MRG_STR) = f32x2{m, l};
    }
    __syncthreads();
    STAMP_DUMP();
    return;
  }

  const rsrc_t rx = mkrsrc(x, D * 2), rws = mkrsrc(ws, WS_END);
  unsigned tagA, tagB;
  unsigned long long e1;
  const int f1 = xl * 28 + j, mat = f1 / 112, rb = (f1 / 56) & 1, cb = f1 % 56;
  const rsrc_t rg = mkrsrc(mat ? wu : wg, D * F * 2);
  int g_voff = (j < 28 && !ABL(1)) ? (lane >> 3) * (F * 2) + (lane & 7) * 16 : OOB, g_sb = (rb * 512 + a * 128) * (F * 2) + cb * 128, g_is = 8 * (F * 2);
  u32x4 gw[16];
  const bool early = ABL(9) && a != 0;
  {
    u32x4 xa = ld16(rx, lane * 32, 0, 0), xb = ld16(rx, lane * 32 + 16, 0, 0);
    const u32x2 ep = __builtin_amdgcn_raw_buffer_load_b64(rws, 0, WS_EPOCH, SC1);
    const rsrc_t rwq = mkrsrc(wq, D * H * DH * 2);
    u32x4 w[8];
    const int voff = ABL(5) ? OOB : r * 8192 + c * 16, sb = (s * 128 + a * 32) * 8192 + h * 256;
#pragma unroll
    for (int i = 0; i < 8; ++i) { w[i] = ld16(rwq, voff, sb + i * 32768, NT); SB(); }
#pragma unroll
    for (int i = 0; i < 16; ++i) { gw[i] = ld16(rg, early ? g_voff : OOB, g_sb + i * g_is, NT); SB(); }
    if (a == 0 && lane < 4) *(LAS unsigned*)(lds + L_CNT + lane) = 0u;
    __syncthreads();
    asm volatile("" : "+v"(xa), "+v"(xb));
    STAMP(10);
    float v[16];
#pragma unroll
    for (int i = 0; i < 4; ++i) { v[2 * i] = lo2f(xa[i]); v[2 * i + 1] = hi2f(xa[i]); v[8 + 2 * i] = lo2f(xb[i]); v[9 + 2 * i] = hi2f(xb[i]); }
    float sm = 0.f;
#pragma unroll
    for (int i = 0; i < 16; ++i) sm += v[i];
    const float mean = wave_sum(sm) * (1.f / D);
    float ss = 0.f;
#pragma unroll
    for (int i = 0; i < 16; ++i) { const float d = v[i] - mean; ss = fmaf(d, d, ss); }
    const float var = wave_sum(ss) * (1.f / D);
    const float mb = rbf(mean), den = rbf(sqrtf(rbf(rbf(var) + 1e-5f)));
    const float rinv = 1.0f / den;
#pragma unroll
    for (int i = 0; i < 16; ++i) {
      const float nu = rbf(v[i] - mb), q0 = nu * rinv;
      v[i] = rbf(fmaf(fmaf(-q0, den, nu), rinv, q0));
    }
#pragma unroll
    for (int q = 0; q < 4; ++q) *(LAS f32x4*)(lds + L_XN + q * 256 + 4 * lane) = f32x4{v[q], v[4 + q], v[8 + q], v[12 + q]};
    e1 = (((unsigned long long)__builtin_amdgcn_readfirstlane(ep[1]) << 32) | (unsigned)__builtin_amdgcn_readfirstlane(ep[0])) + 1ull;
    tagA = (unsigned)e1; tagB = (unsigned)(e1 >> 32) + (unsigned)e1 * 0x9E3779B1u;
    float acc[8] = {0.f, 0.f, 0.f, 0.f, 0.f, 0.f, 0.f, 0.f};
#pragma unroll
    for (int q = 0; q < 2; ++q) {
      const f32x4 xv = *(LAS f32x4*)(lds + L_XN + r * 256 + s * 32 + a * 8 + 4 * q);
#pragma unroll
      for (int e = 0; e < 4; ++e) fma8(acc, xv[e], w[4 * q + e]);
    }
#pragma unroll
    for (int e = 0; e < 8; ++e) acc[e] = xrow_sum(acc[e]);
    if (r == 0) {
      *(LAS f32x4*)(lds + L_QP + a * 128 + 8 * c) = f32x4{acc[0], acc[1], acc[2], acc[3]};
      *(LAS f32x4*)(lds + L_QP + a * 128 + 8 * c + 4) = f32x4{acc[4], acc[5], acc[6], acc[7]};
    }
  }
  STAMP(1);
  aux_arrive(lds + L_CNT + 1, lane);
  if (a == 0) {
    aux_wait(lds + L_CNT + 1);
    STAMP(11);
    f32x2 v = *(LAS f32x2*)(lds + L_QP + 2 * lane);
#pragma unroll
    for (int q = 1; q < 4; ++q) v += *(LAS f32x2*)(lds + L_QP + q * 128 + 2 * lane);
    st16_sc1(rws, ABL(0) ? OOB : lane * 16, WS_Q + (h * 8 + s) * 1024, gran(v[0], v[1], tagA, tagB));
    float q0 = 0.f, q1 = 0.f;
    for (unsigned sp = 0; sp < (ABL(0) ? 0 : SPIN_CAP); ++sp) {
      u32x4 g[8];
#pragma unroll
      for (int i = 0; i < 8; ++i) g[i] = ld16_sc1(rws, lane * 16, WS_Q + (h * 8 + i) * 1024);
      bool ok = true;
      q0 = 0.f; q1 = 0.f;
#pragma unroll
      for (int i = 0; i < 8; ++i) { ok = ok && gok(g[i], tagA, tagB); q0 += __uint_as_float(g[i][0]); q1 += __uint_as_float(g[i][2]); }
      if (__all(ok)) break;
      __builtin_amdgcn_s_sleep(QSLEEP);
    }
    STAMP(13);
    *(LAS unsigned*)(lds + L_QS + lane) = pack2(q0, q1);
  }
  __syncthreads();
  int adue = P_AUXT0 + a * (P_AUXSTEP / 4);
  if (!early) {
#pragma unroll
    for (int i = 0; i < 16; ++i) { pace_wait(t0, adue); adue += P_AUXSTEP; gw[i] = ld16(rg, g_voff, g_sb + i * g_is, NT); SB(); }
  }
  {
    float ag[8] = {0.f, 0.f, 0.f, 0.f, 0.f, 0.f, 0.f, 0.f};
#pragma unroll
    for (int i = 0; i < 16; ++i) {
      const int k = rb * 512 + a * 128 + 8 * i + (lane >> 3);
      fma8(ag, lds[L_XN + (k & 3) * 256 + (k >> 2)], gw[i]);
    }
#pragma unroll
    for (int e = 0; e < 8; ++e) { ag[e] += dpp<ROR8>(ag[e]); ag[e] = xrow_sum(ag[e]); }
    if (lane < 8) {
      *(LAS f32x4*)(lds + L_FP + a * 64 + lane * 8) = f32x4{ag[0], ag[1], ag[2], ag[3]};
      *(LAS f32x4*)(lds + L_FP + a * 64 + lane * 8 + 4) = f32x4{ag[4], ag[5], ag[6], ag[7]};
    }
    aux_arrive(lds + L_CNT, lane);
  }
  if (a == 0 && j < 28) {
    aux_wait(lds + L_CNT);
    f32x2 v = *(LAS f32x2*)(lds + L_FP + 2 * (lane & 31));
#pragma unroll
    for (int q = 1; q < 4; ++q) v += *(LAS f32x2*)(lds + L_FP + q * 64 + 2 * (lane & 31));
    st16_sc1(rws, (lane < 32 && !ABL(4)) ? lane * 16 : OOB, WS_G1 + ((mat * 2 + rb) * F + cb * 64) * 8, gran(v[0], v[1], tagA, tagB));
  }
  const int ks = xl * 2 + (j >> 4), cg = j & 15;
  u32x4 dw[7], wov[8];
  {
    const rsrc_t rd = mkrsrc(wd, D * F * 2);
    int voff = ABL(2) ? OOB : (lane >> 3) * 2048 + (lane & 7) * 16, sb = (ks * 224 + a * 56) * 2048 + cg * 128, is = 16384;
#pragma unroll
    for (int i = 0; i < 7; ++i) { pace_wait(t0, adue); adue += P_AUXSTEP; dw[i] = ld16(rd, voff, sb + i * is, NT); SB(); }
  }
  {
    pace_wait(t0, P_GAT1);
    u32x4 g[4] = {{0, 0, 0, 0}, {0, 0, 0, 0}, {0, 0, 0, 0}, {0, 0, 0, 0}};
    const int voff = (lane < 28 ? lane : 27) * 16;
    for (unsigned sp = 0; sp < (ABL(4) ? 0 : SPIN_CAP); ++sp) {
#pragma unroll
      for (int k = 0; k < 4; ++k) g[k] = ld16_sc1(rws, voff, WS_G1 + (k * F + ks * 224 + a * 56) * 8);
      if (__all(gok(g[0], tagA, tagB) && gok(g[1], tagA, tagB) && gok(g[2], tagA, tagB) && gok(g[3], tagA, tagB))) break;
      __builtin_amdgcn_s_sleep(32);
    }
    float hh[2];
#pragma unroll
    for (int e = 0; e < 2; ++e) {
      const float gs = rbf(__uint_as_float(g[0][2 * e]) + __uint_as_float(g[1][2 * e]));
      const float us = rbf(__uint_as_float(g[2][2 * e]) + __uint_as_float(g[3][2 * e]));
      const float ge = 0.5f * gs * (1.f + tanhf(0.7978845608028654f * (gs + 0.044715f * gs * gs * gs)));
      hh[e] = rbf(rbf(ge) * us);
    }
    if (lane < 28) *(LAS f32x2*)(lds + L_H + a * 64 + 2 * lane) = f32x2{hh[0], hh[1]};
    float y[8] = {0.f, 0.f, 0.f, 0.f, 0.f, 0.f, 0.f, 0.f};
#pragma unroll
    for (int i = 0; i < 7; ++i) fma8(y, lds[L_H + a * 64 + 8 * i + (lane >> 3)], dw[i]);
#pragma unroll
    for (int e = 0; e < 8; ++e) { y[e] += dpp<ROR8>(y[e]); y[e] = xrow_sum(y[e]); }
    if (lane < 8) {
      *(LAS f32x4*)(lds + L_F2 + a * 64 + lane * 8) = f32x4{y[0], y[1], y[2], y[3]};
      *(LAS f32x4*)(lds + L_F2 + a * 64 + lane * 8 + 4) = f32x4{y[4], y[5], y[6], y[7]};
    }
    aux_arrive(lds + L_CNT + 2, lane);
  }
  STAMP(2);
  const int col0 = s * 128 + 4 * h;
  float f0 = 0.f, f1v = 0.f;
  unsigned xw = 0;
  if (a == 0) {
    aux_wait(lds + L_CNT + 2);
    f32x2 v = *(LAS f32x2*)(lds + L_F2 + 2 * (lane & 31));
#pragma unroll
    for (int q = 1; q < 4; ++q) v += *(LAS f32x2*)(lds + L_F2 + q * 64 + 2 * (lane & 31));
    st16_sc1(rws, (lane < 32 && !ABL(4)) ? lane * 16 : OOB, WS_FFN + (ks * D + cg * 64) * 8, gran(v[0], v[1], tagA, tagB));
  }
  {
    const rsrc_t ro = mkrsrc(wo, H * DH * D * 2);
    int voff = ABL(3) ? OOB : r * 2048 + c * 16, sb = (h * 128 + a * 32) * 2048 + s * 256, is = 8192;
#pragma unroll
    for (int i = 0; i < 8; ++i) { pace_wait(t0, adue); adue += P_AUXSTEP; wov[i] = ld16(ro, voff, sb + i * is, NT); SB(); }
  }
  if (a == 0) {
    xw = __builtin_amdgcn_raw_buffer_load_b32(rx, (col0 + 2 * (lane & 1)) * 2, 0, 0);
    pace_wait(t0, P_GAT2);
    const int voff = ((lane >> 1) & 15) * (D * 8) + (lane & 1) * 16;
    u32x4 g = {0, 0, 0, 0};
    for (unsigned sp = 0; sp < (ABL(4) ? 0 : SPIN_CAP); ++sp) {
      g = ld16_sc1(rws, voff, WS_FFN + col0 * 8);
      if (__all(gok(g, tagA, tagB))) break;
      __builtin_amdgcn_s_sleep(32);
    }
    f0 = rbf(parity_sum(__uint_as_float(g[0])) * 0.5f); f1v = rbf(parity_sum(__uint_as_float(g[2])) * 0.5f);
  }
  STAMP(3);
  __syncthreads();
  STAMP(4);
  if (a < 2) {
    float mw[8], M = -INFINITY;
#pragma unroll
    for (int w = 0; w < 8; ++w) { mw[w] = lds[L_MRG + w * MRG_STR]; M = fmaxf(M, mw[w]); }
    if (a == 0) {
      float o0 = 0.f, o1 = 0.f;
#pragma unroll
      for (int w = 0; w < 8; ++w) {
        const float wt = exp2_(mw[w] - M);
        const f32x2 v = *(LAS f32x2*)(lds + L_MRG + w * MRG_STR + 4 + 2 * lane);
        o0 = fmaf(wt, v[0], o0); o1 = fmaf(wt, v[1], o1);
      }
      st16_sc1(rws, lane * 16, WS_PART + (h * 8 + s) * PART_STR, gran(o0, o1, tagA, tagB));
    } else {
      float L = 0.f;
#pragma unroll
      for (int w = 0; w < 8; ++w) L = fmaf(exp2_(mw[w] - M), lds[L_MRG + w * MRG_STR + 1], L);
      st16_sc1(rws, lane == 0 ? 0 : OOB, WS_PART + (h * 8 + s) * PART_STR + 1024, gran(M, L, tagA, tagB));
    }
  }
  STAMP(5);
  {
    u32x4 go[2], gm[2];
    const int vo = r * PART_STR + (16 * a + c) * 16, vm = r * PART_STR + 1024;
    for (unsigned sp = 0; sp < SPIN_CAP; ++sp) {
#pragma unroll
      for (int k = 0; k < 2; ++k) {
        go[k] = ld16_sc1(rws, vo, WS_PART + (h * 8 + 4 * k) * PART_STR);
        gm[k] = ld16_sc1(rws, vm, WS_PART + (h * 8 + 4 * k) * PART_STR);
      }
      if (__all(gok(go[0], tagA, tagB) && gok(go[1], tagA, tagB) && gok(gm[0], tagA, tagB) && gok(gm[1], tagA, tagB))) break;
      __builtin_amdgcn_s_sleep(1);
    }
    STAMP(6);
    const float m0 = __uint_as_float(gm[0][0]), m1 = __uint_as_float(gm[1][0]);
    const float M = xrow_max(fmaxf(m0, m1));
    const float w0 = exp2_(m0 - M), w1 = exp2_(m1 - M);
    const float L = xrow_sum(fmaf(w0, __uint_as_float(gm[0][2]), w1 * __uint_as_float(gm[1][2])));
    const float o0 = rbf(xrow_sum(fmaf(w0, __uint_as_float(go[0][0]), w1 * __uint_as_float(go[1][0]))) / L);
    const float o1 = rbf(xrow_sum(fmaf(w0, __uint_as_float(go[0][2]), w1 * __uint_as_float(go[1][2]))) / L);
    float ov = (r & 1) ? o1 : o0;
    { const float sh = dpp<SHL1>(ov); ov = (r & 2) ? sh : ov; }
    float y[8] = {0.f, 0.f, 0.f, 0.f, 0.f, 0.f, 0.f, 0.f};
    fma8(y, dpp<BCAST + 0>(ov), wov[0]); fma8(y, dpp<BCAST + 2>(ov), wov[1]); fma8(y, dpp<BCAST + 4>(ov), wov[2]); fma8(y, dpp<BCAST + 6>(ov), wov[3]);
    fma8(y, dpp<BCAST + 8>(ov), wov[4]); fma8(y, dpp<BCAST + 10>(ov), wov[5]); fma8(y, dpp<BCAST + 12>(ov), wov[6]); fma8(y, dpp<BCAST + 14>(ov), wov[7]);
#pragma unroll
    for (int e = 0; e < 8; ++e) y[e] = xrow_sum(y[e]);
    if (r == 0) {
      *(LAS f32x4*)(lds + L_TMP + a * 128 + 8 * c) = f32x4{y[0], y[1], y[2], y[3]};
      *(LAS f32x4*)(lds + L_TMP + a * 128 + 8 * c + 4) = f32x4{y[4], y[5], y[6], y[7]};
    }
  }
  __syncthreads();
  if (a != 0) { STAMP_DUMP(); return; }
  {
    f32x2 v = *(LAS f32x2*)(lds + L_TMP + 2 * lane);
#pragma unroll
    for (int q = 1; q < 4; ++q) v += *(LAS f32x2*)(lds + L_TMP + q * 128 + 2 * lane);
    st16_sc1(rws, lane * 16, WS_Y + (h * 8 + s) * Y_STR, gran(v[0], v[1], tagA, tagB));
  }
  STAMP(7);
  {
    u32x4 g;
    const int voff = (lane >> 1) * (8 * Y_STR) + (lane & 1) * 16;
    for (unsigned sp = 0; sp < SPIN_CAP; ++sp) {
      g = ld16_sc1(rws, voff, WS_Y + s * Y_STR + h * 32);
      if (__all(gok(g, tagA, tagB))) break;
      __builtin_amdgcn_s_sleep(1);
    }
    STAMP(8);
    const float a0 = rbf(parity_sum(__uint_as_float(g[0]))), a1 = rbf(parity_sum(__uint_as_float(g[2])));
    const unsigned o = pack2(rbf(lo2f(xw) + a0) + f0, rbf(hi2f(xw) + a1) + f1v);
    if (lane < 2) *(unsigned*)(out + col0 + 2 * lane) = o;
    if (bid == 0 && lane == 0) __builtin_amdgcn_raw_buffer_store_b64(u32x2{(unsigned)e1, (unsigned)(e1 >> 32)}, rws, 0, WS_EPOCH, SC1);
  }
  STAMP(9);
  STAMP_DUMP();
}

extern "C" void kernel_launch(void* const* d_in, const int* in_sizes, int n_in, void* d_out, int out_size, void* d_ws, size_t ws_size,
                              hipStream_t stream) {
  fused_decode_kernel<<<256, NTHR, 0, stream>>>((const u16*)d_in[0], (const u16*)d_in[1], (const u16*)d_in[2], (const u16*)d_in[3],
                                                 (const u16*)d_in[4], (const u16*)d_in[6], (const u16*)d_in[7], (const u16*)d_in[8],
                                                 (u16*)d_out, d_ws);
}
```

```cpp
#define T_MAIN 7300
#define T_K0 370
#define T_GAP 200
#define T_F1 150
#define T_WO 0
#define ABL_DEFAULT 32768
#include <hip/hip_runtime.h>
#include <stdint.h>

typedef unsigned u32x4 __attribute__((ext_vector_type(4)));
typedef unsigned u32x2 __attribute__((ext_vector_type(2)));
typedef float f32x4 __attribute__((ext_vector_type(4)));
typedef float f32x2 __attribute__((ext_vector_type(2)));
typedef __bf16 bf16x2 __attribute__((ext_vector_type(2)));
typedef __bf16 bf16x8 __attribute__((ext_vector_type(8)));
typedef unsigned short u16;
#define LAS __attribute__((address_space(3)))
typedef __amdgpu_buffer_rsrc_t rsrc_t;

#define T_W2 0
#define PACE_G 2

#define TUN_ARG
#define P_TMAIN T_MAIN
#define P_TK0 T_K0
#define P_TGAP T_GAP
#define P_TW2 T_W2
#define P_TWO T_WO
#define P_STG 0
#define P_TF1 T_F1
#define ABL(b) ((ABL_DEFAULT >> (b)) & 1)
constexpr int D = 1024, H = 32, SK = 32768, DH = 128, F = 3584;
constexpr int NMAIN = 8, NAUX = 4, NWAVES = NMAIN + NAUX, NTHR = NWAVES * 64;
constexpr int NT = 2  , SC1 = 16;
constexpr int OOB = 0x40000000;
constexpr unsigned WS_EPOCH = 0, WS_T0 = 1024, WS_ML = 4096, WS_Q = WS_ML + 256 * 128, WS_PART = WS_Q + 256 * 1024, WS_Y = WS_PART + 256 * 1024,
                   WS_G1 = WS_Y + 256 * 1024, WS_FFN = WS_G1 + 4 * F * 8, WS_END = WS_FFN + 16 * D * 8;
constexpr unsigned WS_STAMP = 8u << 20;
constexpr unsigned WS_DIAG = 6u << 20;
constexpr int L_XN = 0, L_QP = 1024, L_QS = L_QP + 4 * 128, L_MRG = L_QS + 64, L_ML = L_MRG + 8 * 128, L_WG = L_ML + 16, L_FP = L_WG + 8,
              L_H = L_FP + 8 * 64, L_F2 = L_H + 4 * 64, L_OB = L_F2 + 4 * 64, L_CNT = L_OB + 64, L_SC = L_CNT + 16, L_WT = L_SC + 8 * 512,
              WT_STR = 68, L_END = L_WT + 128 * WT_STR;
constexpr int C_Q = 0, C_XN = 1, C_F2 = 2, C_WT = 3, C_ML = 4, C_WG = 5, C_END = 6, C_T0 = 8;

__device__ __forceinline__ float lo2f(unsigned w) { return __builtin_bit_cast(float, w << 16); }
__device__ __forceinline__ float hi2f(unsigned w) { return __builtin_bit_cast(float, w & 0xffff0000u); }
__device__ __forceinline__ float rbf(float x) { return (float)(__bf16)x; }
__device__ __forceinline__ unsigned pack2(float a, float b) { bf16x2 v = {(__bf16)a, (__bf16)b}; return __builtin_bit_cast(unsigned, v); }
__device__ __forceinline__ float dot2(unsigned k, unsigned q, float acc) {
  return __builtin_amdgcn_fdot2_f32_bf16(__builtin_bit_cast(bf16x2, k), __builtin_bit_cast(bf16x2, q), acc, false);
}
template <int CTRL> __device__ __forceinline__ float dpp(float x) {
  return __builtin_bit_cast(float, __builtin_amdgcn_mov_dpp(__builtin_bit_cast(int, x), CTRL, 0xf, 0xf, true));
}
constexpr int XOR1 = 0xB1, XOR2 = 0x4E, XOR7 = 0x141, ROR8 = 0x128, ROR4 = 0x124, BCAST = 0x150;
__device__ __forceinline__ float fold32(float a, float b) {
  auto t = __builtin_amdgcn_permlane32_swap(__float_as_uint(a), __float_as_uint(b), false, false);
  return __uint_as_float(t[0]) + __uint_as_float(t[1]);
}
__device__ __forceinline__ float fold16(float a, float b) {
  auto t = __builtin_amdgcn_permlane16_swap(__float_as_uint(a), __float_as_uint(b), false, false);
  return __uint_as_float(t[0]) + __uint_as_float(t[1]);
}
__device__ __forceinline__ float xrow_sum(float x) { x = fold16(x, x); return fold32(x, x); }
__device__ __forceinline__ float xrow_max(float x) {
  auto s = __builtin_amdgcn_permlane16_swap(__float_as_uint(x), __float_as_uint(x), false, false);
  x = fmaxf(__uint_as_float(s[0]), __uint_as_float(s[1]));
  auto t = __builtin_amdgcn_permlane32_swap(__float_as_uint(x), __float_as_uint(x), false, false);
  return fmaxf(__uint_as_float(t[0]), __uint_as_float(t[1]));
}
__device__ __forceinline__ float parity_sum(float x) {
  x += dpp<XOR2>(x); x += dpp<ROR4>(x); x += dpp<ROR8>(x); return xrow_sum(x);
}
__device__ __forceinline__ float wave_sum(float x) { x += dpp<XOR1>(x); return parity_sum(x); }
__device__ __forceinline__ float max8(float x) { x = fmaxf(x, dpp<XOR1>(x)); x = fmaxf(x, dpp<XOR2>(x)); return fmaxf(x, dpp<XOR7>(x)); }
__device__ __forceinline__ float sum8(float x) { x += dpp<XOR1>(x); x += dpp<XOR2>(x); return x + dpp<XOR7>(x); }
__device__ __forceinline__ float wave_max(float x) { x = max8(x); x = fmaxf(x, dpp<ROR8>(x)); return xrow_max(x); }
__device__ __forceinline__ float exp2_(float x) { return __builtin_amdgcn_exp2f(x); }
__device__ __forceinline__ rsrc_t mkrsrc(const void* p, unsigned bytes) { return __builtin_amdgcn_make_buffer_rsrc((void*)p, 0, (int)bytes, 0x00020000); }
__device__ __forceinline__ u32x4 ld16(rsrc_t r, int voff, int soff) { return __builtin_amdgcn_raw_buffer_load_b128(r, voff, soff, 0); }
__device__ __forceinline__ u32x4 ld16_nt(rsrc_t r, int voff, int soff) { return __builtin_amdgcn_raw_buffer_load_b128(r, voff, soff, NT); }
__device__ __forceinline__ u32x4 ld16_sc1(rsrc_t r, int voff, int soff) { return __builtin_amdgcn_raw_buffer_load_b128(r, voff, soff, SC1); }
__device__ __forceinline__ void st16_sc1(rsrc_t r, int voff, int soff, u32x4 v) { __builtin_amdgcn_raw_buffer_store_b128(v, r, voff + soff, 0, SC1); }
#define SB() __builtin_amdgcn_sched_barrier(0)
#define REREAD() asm volatile("" ::: "memory")
__device__ __forceinline__ void pace_wait(unsigned t0, int due) {
  asm volatile("1:\n\ts_memrealtime s[96:97]\n\ts_waitcnt lgkmcnt(0)\n\ts_sub_u32 s96, s96, %0\n\ts_cmp_lt_i32 s96, %1\n\ts_cbranch_scc0 2f\n\ts_sleep 1\n\ts_branch 1b\n2:"
               :: "s"(t0), "s"(due) : "s96", "s97", "scc", "memory");
}
__device__ __forceinline__ void fma8(float (&acc)[8], float s, u32x4 w) {
#pragma unroll
  for (int t = 0; t < 4; ++t) { acc[2 * t] = fmaf(s, lo2f(w[t]), acc[2 * t]); acc[2 * t + 1] = fmaf(s, hi2f(w[t]), acc[2 * t + 1]); }
}
__device__ __forceinline__ u32x4 gran(float a, float b, unsigned ta, unsigned tb) { return u32x4{__float_as_uint(a), ta, __float_as_uint(b), tb}; }
__device__ __forceinline__ bool gok(u32x4 g, unsigned ta, unsigned tb) { return g[1] == ta && g[3] == tb; }
constexpr unsigned SPIN_CAP = 1u << 20;

#define STAMP(i) do { } while (0)
#define STAMP_DUMP() do { } while (0)

__device__ __forceinline__ void cnt_arrive(LAS float* lds, int which, int lane) {
  asm volatile("s_waitcnt lgkmcnt(0)" ::: "memory");
  if (lane == 0) __hip_atomic_fetch_add((LAS unsigned*)(lds + L_CNT + which), 1u, __ATOMIC_RELAXED, __HIP_MEMORY_SCOPE_WORKGROUP);
}
template <bool SLEEP = true>
__device__ __forceinline__ void cnt_wait(LAS float* lds, int which, unsigned n) {
  for (unsigned sp = 0; sp < SPIN_CAP; ++sp) {
    if (__builtin_amdgcn_readfirstlane(*(volatile LAS unsigned*)(lds + L_CNT + which)) >= n) break;
    if (SLEEP) __builtin_amdgcn_s_sleep(1);
  }
  REREAD();
}

__global__ void __launch_bounds__(NTHR, 3)
fused_decode_kernel(const u16* __restrict__ x, const u16* __restrict__ Kc, const u16* __restrict__ Vc, const u16* __restrict__ wq,
                    const u16* __restrict__ wo, const u16* __restrict__ wg, const u16* __restrict__ wu, const u16* __restrict__ wd,
                    u16* __restrict__ out, void* ws TUN_ARG) {
  __shared__ __attribute__((aligned(16))) float lds_[L_END];
  LAS float* lds = (LAS float*)lds_;
  const unsigned t0 = (unsigned)__builtin_amdgcn_s_memrealtime();
  const int tid = threadIdx.x, lane = tid & 63, wave = __builtin_amdgcn_readfirstlane(tid >> 6);
  const int r = lane >> 4, c = lane & 15;
  const int bid = blockIdx.x, xl = bid & 7, j = bid >> 3;
  const int h = xl * 4 + (j >> 3), s = j & 7;
  const int a = wave - NMAIN;
  const int f1 = xl * 28 + j, mat = f1 / 112, rb = (f1 / 56) & 1, cb = f1 % 56;
  STAMP(0);

  if (wave < NMAIN) {
    const rsrc_t rk = mkrsrc(Kc, (unsigned)H * SK * DH * 2), rv = mkrsrc(Vc, (unsigned)H * SK * DH * 2);
    u32x4 bA[16], bB[16];
    const unsigned kvbase = (unsigned)h * (8u << 20) + (unsigned)s * (1u << 20) + wave * 1024;
    const unsigned rot = 32u * ((h + (h >> 2) + (h >> 4)) & 3);
    const int kvoff = lane * 16;
    const int STEP = ((int)P_TMAIN << 16) / 256 * PACE_G;
    int due = (int)P_TK0 * 65536 - STEP + (P_STG == 0 ? (STEP / 256) * (wave * 32 + j) : (STEP / 32) * j);
#define PACE(T0, I) do { if ((I) % PACE_G == 0) { due += STEP; pace_wait(T0, due >> 16); } } while (0)
    {
      const rsrc_t rg = mkrsrc(mat ? wu : wg, D * F * 2);
      int g_voff = (j < 28 && !ABL(1)) ? (lane >> 3) * (F * 2) + (lane & 7) * 16 : OOB, g_sb = (rb * 512 + wave * 64) * (F * 2) + cb * 128, g_is = 8 * (F * 2);
      __builtin_amdgcn_s_barrier(); SB();
      const int fstep = ((P_TK0 - P_TF1) << 16) / 8;
      int fdue = P_TF1 * 65536 + (fstep / 256) * (wave * 32 + j);
#pragma unroll
      for (int i = 0; i < 8; ++i) { pace_wait(t0, fdue >> 16); fdue += fstep; bB[i] = ld16_nt(rg, g_voff, g_sb + i * g_is); SB(); }
    }
#pragma unroll
    for (int i = 0; i < 16; ++i) { PACE(t0, i); bA[i] = ld16_nt(rk, kvoff, kvbase + (rot + i) * 8192); SB(); if (i == 0) { STAMP(12); SB(); } }
    {
      cnt_wait(lds, C_XN, 1);
      float ag[8] = {0.f, 0.f, 0.f, 0.f, 0.f, 0.f, 0.f, 0.f};
#pragma unroll
      for (int i = 0; i < 8; ++i) {
        const int k = rb * 512 + wave * 64 + 8 * i + (lane >> 3);
        fma8(ag, lds[L_XN + (k & 3) * 256 + (k >> 2)], bB[i]);
      }
#pragma unroll
      for (int e = 0; e < 8; ++e) ag[e] += dpp<ROR8>(ag[e]);
      const float u0 = fold32(ag[0], ag[4]), u1 = fold32(ag[1], ag[5]), u2 = fold32(ag[2], ag[6]), u3 = fold32(ag[3], ag[7]);
      const f32x2 z = {fold16(u0, u2), fold16(u1, u3)};
      if (c < 8) *(LAS f32x2*)(lds + L_FP + wave * 64 + 8 * c + 2 * r) = z;
      SB();
    }
#pragma unroll
    for (int i = 0; i < 16; ++i) { PACE(t0, i); bB[i] = ld16_nt(rk, kvoff, kvbase + (rot + 16 + i) * 8192); SB(); }
    STAMP(1);
    __syncthreads();
    const u32x4 qp = *(LAS u32x4*)(lds + L_QS + 4 * c);
    const unsigned t0m = __builtin_amdgcn_readfirstlane(*(LAS unsigned*)(lds + L_CNT + C_T0));
    STAMP(2);
    constexpr float SCALE = 0.08838834764831845f, LOG2E = 1.4426950408889634f;
    LAS float* scp = lds + L_SC + wave * 512 + lane * 8;
    float mx = -INFINITY;
    auto kgroup = [&](u32x4 (&kb)[16], const bool more, const rsrc_t rn, const unsigned tb, const int g) __attribute__((always_inline)) {
      float sx[16];
#pragma unroll
      for (int i = 0; i < 16; ++i) {
        float d = 0.f;
#pragma unroll
        for (int t = 0; t < 4; ++t) d = dot2(kb[i][t], qp[t], d);
        asm volatile("" : "+v"(d));
        sx[i] = d;
        SB();
        if (more) { PACE(t0m, i); kb[i] = ld16_nt(rn, kvoff, tb + i * 8192); SB(); }
      }
#define TR_STEP(HALF, CTRL, BIT) _Pragma("unroll") for (int hh = 0; hh < (HALF); ++hh) { \
        const float lo_ = sx[hh], hi_ = sx[hh + (HALF)]; const float t1_ = lo_ + dpp<CTRL>(lo_), t2_ = hi_ + dpp<CTRL>(hi_); \
        sx[hh] = (c & (BIT)) ? t2_ : t1_; }
      TR_STEP(8, ROR8, 8) TR_STEP(4, XOR7, 4) TR_STEP(2, XOR2, 2) TR_STEP(1, XOR1, 1)
#undef TR_STEP
      const float sc = rbf(rbf(sx[0]) * SCALE) * LOG2E;
      mx = fmaxf(mx, sc);
      scp[g] = sc;
      SB();
    };
    kgroup(bA, false, rk, 0, 0);
#pragma unroll 1
    for (int g = 1; g < 8; ++g) {
      const bool tov = g == 7;
      due += tov ? (int)P_TGAP * 65536 : 0;
      kgroup(bB, true, mkrsrc(tov ? Vc : Kc, (unsigned)H * SK * DH * 2), kvbase + (((g + 1) * 16 + rot) & 127u) * 8192, g);
    }
    {
      const float m = wave_max(mx);
      f32x4 p0 = *(LAS f32x4*)scp, p1 = *(LAS f32x4*)(scp + 4);
      float l = 0.f;
#pragma unroll
      for (int e = 0; e < 4; ++e) { p0[e] = exp2_(p0[e] - m); p1[e] = exp2_(p1[e] - m); l += p0[e] + p1[e]; }
      *(LAS f32x4*)scp = p0; *(LAS f32x4*)(scp + 4) = p1;
      l = wave_sum(l);
      if (lane == 0) *(LAS f32x2*)(lds + L_ML + 2 * wave) = f32x2{m, l};
      cnt_arrive(lds, C_ML, lane);
    }
    STAMP(11);
    float acc[8] = {0.f, 0.f, 0.f, 0.f, 0.f, 0.f, 0.f, 0.f};
    auto vgroup = [&](const bool more, const unsigned tb, const float p) __attribute__((always_inline)) {
#define PV_STEP(I) { fma8(acc, dpp<BCAST + (I)>(p), bB[I]); \
        asm volatile("" : "+v"(acc[0]), "+v"(acc[1]), "+v"(acc[2]), "+v"(acc[3]), "+v"(acc[4]), "+v"(acc[5]), "+v"(acc[6]), "+v"(acc[7])); SB(); \
        if (more) { PACE(t0m, I); bB[I] = ld16_nt(rv, kvoff, tb + (I) * 8192); SB(); } }
      PV_STEP(0) PV_STEP(1) PV_STEP(2) PV_STEP(3) PV_STEP(4) PV_STEP(5) PV_STEP(6) PV_STEP(7)
      PV_STEP(8) PV_STEP(9) PV_STEP(10) PV_STEP(11) PV_STEP(12) PV_STEP(13) PV_STEP(14) PV_STEP(15)
#undef PV_STEP
    };
#pragma unroll 1
    for (int g = 0; g < 7; ++g) vgroup(true, kvbase + (((g + 1) * 16 + rot) & 127u) * 8192, scp[g]);
    STAMP(3);
    __builtin_amdgcn_s_setprio(3);
    cnt_wait(lds, C_WG, 1);
    {
      const float wgt = lds[L_WG + wave];
#pragma unroll
      for (int e = 0; e < 8; ++e) acc[e] *= wgt;
      vgroup(false, 0, scp[7] * wgt);
    }
    STAMP(4);
    {
      const float u0 = fold32(acc[0], acc[4]), u1 = fold32(acc[1], acc[5]), u2 = fold32(acc[2], acc[6]), u3 = fold32(acc[3], acc[7]);
      *(LAS f32x2*)(lds + L_MRG + wave * 128 + 8 * c + 2 * r) = f32x2{fold16(u0, u2), fold16(u1, u3)};
    }
    cnt_arrive(lds, C_END, lane);
    STAMP_DUMP();
    return;
  }

  const rsrc_t rx = mkrsrc(x, D * 2), rws = mkrsrc(ws, WS_END);
  unsigned tagA, tagB;
  unsigned long long e1;
  {
    u32x4 xa = ld16(rx, lane * 32, 0), xb = ld16(rx, lane * 32 + 16, 0);
    const u32x2 ep = __builtin_amdgcn_raw_buffer_load_b64(rws, 0, WS_EPOCH, SC1);
    u32x4 pt = ABL(14) ? ld16(rws, (a == 0 && ABL(13)) ? lane * 16 : OOB, WS_Q + (h * 8 + s) * 1024) : ld16_sc1(rws, (a == 0 && ABL(13)) ? lane * 16 : OOB, WS_Q + (h * 8 + s) * 1024);
    const rsrc_t rwq = mkrsrc(wq, D * H * DH * 2);
    u32x4 w[8];
    const int voff = r * 8192 + c * 16, sb = (s * 128 + a * 32) * 8192 + h * 256;
#pragma unroll
    for (int i = 0; i < 8; ++i) { w[i] = ld16_nt(rwq, voff, sb + i * 32768); SB(); }
    if (a == 0 && lane < 8) *(LAS unsigned*)(lds + L_CNT + lane) = 0u;
    __syncthreads();
    asm volatile("" : "+v"(xa), "+v"(xb), "+v"(pt));
    STAMP(10);
    float v[16];
#pragma unroll
    for (int i = 0; i < 4; ++i) { v[2 * i] = lo2f(xa[i]); v[2 * i + 1] = hi2f(xa[i]); v[8 + 2 * i] = lo2f(xb[i]); v[9 + 2 * i] = hi2f(xb[i]); }
    float sm = 0.f;
#pragma unroll
    for (int i = 0; i < 16; ++i) sm += v[i];
    const float mean = wave_sum(sm) * (1.f / D);
    float ss = 0.f;
#pragma unroll
    for (int i = 0; i < 16; ++i) { const float d = v[i] - mean; ss = fmaf(d, d, ss); }
    const float var = wave_sum(ss) * (1.f / D);
    const float mb = rbf(mean), den = rbf(sqrtf(rbf(rbf(var) + 1e-5f)));
    const float rinv = 1.0f / den;
#pragma unroll
    for (int i = 0; i < 16; ++i) {
      const float nu = rbf(v[i] - mb), q0 = nu * rinv;
      v[i] = rbf(fmaf(fmaf(-q0, den, nu), rinv, q0));
    }
#pragma unroll
    for (int q = 0; q < 4; ++q) *(LAS f32x4*)(lds + L_XN + q * 256 + 4 * lane) = f32x4{v[q], v[4 + q], v[8 + q], v[12 + q]};
    cnt_arrive(lds, C_XN, lane);
    e1 = (((unsigned long long)__builtin_amdgcn_readfirstlane(ep[1]) << 32) | (unsigned)__builtin_amdgcn_readfirstlane(ep[0])) + 1ull;
    tagA = (unsigned)e1; tagB = (unsigned)(e1 >> 32) + (unsigned)e1 * 0x9E3779B1u;
    float acc[8] = {0.f, 0.f, 0.f, 0.f, 0.f, 0.f, 0.f, 0.f};
#pragma unroll
    for (int q = 0; q < 2; ++q) {
      const f32x4 xv = *(LAS f32x4*)(lds + L_XN + r * 256 + s * 32 + a * 8 + 4 * q);
#pragma unroll
      for (int e = 0; e < 4; ++e) fma8(acc, xv[e], w[4 * q + e]);
    }
    const float u0 = fold32(acc[0], acc[4]), u1 = fold32(acc[1], acc[5]), u2 = fold32(acc[2], acc[6]), u3 = fold32(acc[3], acc[7]);
    *(LAS f32x2*)(lds + L_QP + a * 128 + 8 * c + 2 * r) = f32x2{fold16(u0, u2), fold16(u1, u3)};
  }
  STAMP(1);
  cnt_arrive(lds, C_Q, lane);
  if (a == 0) {
    cnt_wait<false>(lds, C_Q, NAUX);
    STAMP(11);
    f32x2 v = *(LAS f32x2*)(lds + L_QP + 2 * lane);
#pragma unroll
    for (int q = 1; q < 4; ++q) v += *(LAS f32x2*)(lds + L_QP + q * 128 + 2 * lane);
    st16_sc1(rws, lane * 16, WS_Q + (h * 8 + s) * 1024, gran(v[0], v[1], tagA, tagB));
    st16_sc1(rws, (bid == 0 && lane == 0) ? 0 : OOB, WS_T0, u32x4{t0, tagA, t0, tagB});
  }
  if (a == (ABL(15) ? 1 : 0)) {
    if (ABL(15)) cnt_wait<false>(lds, C_Q, NAUX);
    float q0 = 0.f, q1 = 0.f;
    unsigned tg = t0;
    for (unsigned sp = 0; sp < SPIN_CAP; ++sp) {
      REREAD();
      u32x4 g[8];
#pragma unroll
      for (int i = 0; i < 8; ++i) g[i] = ld16_sc1(rws, lane * 16, WS_Q + (h * 8 + i) * 1024);
      const u32x4 gt = ld16_sc1(rws, 0, WS_T0);
      bool ok = gok(gt, tagA, tagB);
      tg = gt[0];
      q0 = 0.f; q1 = 0.f;
#pragma unroll
      for (int i = 0; i < 8; ++i) { ok = ok && gok(g[i], tagA, tagB); q0 += __uint_as_float(g[i][0]); q1 += __uint_as_float(g[i][2]); }
      if (__all(ok)) break;
      __builtin_amdgcn_s_sleep(2);
    }
    STAMP(13);
    *(LAS unsigned*)(lds + L_QS + lane) = pack2(q0, q1);
    if (lane == 0) *(LAS unsigned*)(lds + L_CNT + C_T0) = (ABL(12) || (int)(t0 - tg) < 0 || (int)(t0 - tg) > 300) ? t0 : tg;
  }
  __syncthreads();
  if (a == 0 && j < 28) {
    f32x2 v = *(LAS f32x2*)(lds + L_FP + 2 * (lane & 31));
#pragma unroll
    for (int q = 1; q < 8; ++q) v += *(LAS f32x2*)(lds + L_FP + q * 64 + 2 * (lane & 31));
    st16_sc1(rws, lane < 32 ? lane * 16 : OOB, WS_G1 + ((mat * 2 + rb) * F + cb * 64) * 8, gran(v[0], v[1], tagA, tagB));
  }
  STAMP(12);
  const int ks = xl * 2 + (j >> 4), cg = j & 15;
  const int t_w2 = P_TK0 + P_TMAIN / 2 + P_TW2;
  u32x4 dw[7], wov[8];
  const rsrc_t ro = mkrsrc(wo, H * DH * D * 2);
  const int o_voff = ABL(3) ? OOB : r * 2048 + c * 16, o_sb = (h * 128 + a * 32) * 2048 + s * 256;
  {
    pace_wait(t0, t_w2 - 600);
    u32x4 g[4];
    const int voff = (lane < 28 ? lane : 27) * 16;
    for (unsigned sp = 0; sp < SPIN_CAP; ++sp) {
      REREAD();
#pragma unroll
      for (int k = 0; k < 4; ++k) g[k] = ld16_sc1(rws, voff, WS_G1 + (k * F + ks * 224 + a * 56) * 8);
      if (__all(gok(g[0], tagA, tagB) && gok(g[1], tagA, tagB) && gok(g[2], tagA, tagB) && gok(g[3], tagA, tagB))) break;
      __builtin_amdgcn_s_sleep(32);
    }
    float hh[2];
#pragma unroll
    for (int e = 0; e < 2; ++e) {
      const float gs = rbf(__uint_as_float(g[0][2 * e]) + __uint_as_float(g[1][2 * e]));
      const float us = rbf(__uint_as_float(g[2][2 * e]) + __uint_as_float(g[3][2 * e]));
      const float ge = 0.5f * gs * (1.f + tanhf(0.7978845608028654f * (gs + 0.044715f * gs * gs * gs)));
      hh[e] = rbf(rbf(ge) * us);
    }
    if (lane < 28) *(LAS f32x2*)(lds + L_H + a * 64 + 2 * lane) = f32x2{hh[0], hh[1]};
    const rsrc_t rd = mkrsrc(wd, D * F * 2);
    const int d_voff = ABL(2) ? OOB : (lane >> 3) * 2048 + (lane & 7) * 16, d_sb = (ks * 224 + a * 56) * 2048 + cg * 128;
    const int wstep = (P_TGAP << 16) / (P_TWO == 0 ? 15 : 7);
    int wdue = t_w2 * 65536 + (wstep / 128) * (a * 32 + j);
#pragma unroll
    for (int i = 0; i < 7; ++i) { pace_wait(t0, wdue >> 16); wdue += wstep; dw[i] = ld16_nt(rd, d_voff, d_sb + i * 16384); SB(); }
    if (P_TWO == 0) {
#pragma unroll
      for (int i = 0; i < 8; ++i) { pace_wait(t0, wdue >> 16); wdue += wstep; wov[i] = ld16_nt(ro, o_voff, o_sb + i * 8192); SB(); }
    }
    STAMP(15);
    float y[8] = {0.f, 0.f, 0.f, 0.f, 0.f, 0.f, 0.f, 0.f};
#pragma unroll
    for (int i = 0; i < 7; ++i) fma8(y, lds[L_H + a * 64 + 8 * i + (lane >> 3)], dw[i]);
#pragma unroll
    for (int e = 0; e < 8; ++e) { y[e] += dpp<ROR8>(y[e]); y[e] = xrow_sum(y[e]); }
    if (lane < 8) {
      *(LAS f32x4*)(lds + L_F2 + a * 64 + lane * 8) = f32x4{y[0], y[1], y[2], y[3]};
      *(LAS f32x4*)(lds + L_F2 + a * 64 + lane * 8 + 4) = f32x4{y[4], y[5], y[6], y[7]};
    }
    cnt_arrive(lds, C_F2, lane);
  }
  STAMP(2);
  const int col0 = s * 128 + 4 * h;
  float f0 = 0.f, f1v = 0.f;
  unsigned xw = 0;
  if (a == 0) {
    cnt_wait(lds, C_F2, NAUX);
    f32x2 v = *(LAS f32x2*)(lds + L_F2 + 2 * (lane & 31));
#pragma unroll
    for (int q = 1; q < 4; ++q) v += *(LAS f32x2*)(lds + L_F2 + q * 64 + 2 * (lane & 31));
    st16_sc1(rws, lane < 32 ? lane * 16 : OOB, WS_FFN + (ks * D + cg * 64) * 8, gran(v[0], v[1], tagA, tagB));
    xw = __builtin_amdgcn_raw_buffer_load_b32(rx, (col0 + 2 * (lane & 1)) * 2, 0, 0);
    pace_wait(t0, t_w2 + 1500);
    const int voff = ((lane >> 1) & 15) * (D * 8) + (lane & 1) * 16;
    u32x4 g;
    for (unsigned sp = 0; sp < SPIN_CAP; ++sp) {
      REREAD();
      g = ld16_sc1(rws, voff, WS_FFN + col0 * 8);
      if (__all(gok(g, tagA, tagB))) break;
      __builtin_amdgcn_s_sleep(32);
    }
    f0 = rbf(parity_sum(__uint_as_float(g[0])) * 0.5f); f1v = rbf(parity_sum(__uint_as_float(g[2])) * 0.5f);
  }
  if (a == 1) {
    cnt_wait(lds, C_ML, NMAIN);
    const f32x2 ml = *(LAS f32x2*)(lds + L_ML + 2 * (lane & 7));
    const float Mw = max8(ml[0]), Lw = sum8(exp2_(ml[0] - Mw) * ml[1]);
    st16_sc1(rws, lane == 0 ? 0 : OOB, WS_ML + (h * 8 + s) * 128, gran(Mw, Lw, tagA, tagB));
    pace_wait(t0, t_w2 + 1500);
    u32x4 g;
    for (unsigned sp = 0; sp < SPIN_CAP; ++sp) {
      REREAD();
      g = ld16_sc1(rws, (lane & 7) * 128, WS_ML + h * 8 * 128);
      if (__all(gok(g, tagA, tagB))) break;
      __builtin_amdgcn_s_sleep(32);
    }
    const float M = max8(__uint_as_float(g[0])), L = sum8(exp2_(__uint_as_float(g[0]) - M) * __uint_as_float(g[2]));
    if (lane < 8) lds[L_WG + lane] = exp2_(ml[0] - M) / L;
    cnt_arrive(lds, C_WG, lane);
  }
  u32x4 bfr[2][4];
  int pass = ABL(10) ? 1 : 0;
  asm volatile("" : "+s"(pass));
#pragma unroll 1
  for (; pass < 2; ++pass) {
    const bool real = pass != 0;
    const int st_off = real ? 0 : OOB;
    if (real == (P_TWO != 0) || ABL(10)) {
      if (P_TWO != 0) {
        pace_wait(t0, t_w2 + P_TWO);
#pragma unroll
        for (int i = 0; i < 8; ++i) { wov[i] = ld16_nt(ro, o_voff, o_sb + i * 8192); SB(); }
      }
#pragma unroll
      for (int i = 0; i < 8; ++i) *(LAS u32x4*)(lds + L_WT + (a * 32 + 4 * i + r) * WT_STR + 4 * c) = wov[i];
      cnt_arrive(lds, C_WT, lane);
      cnt_wait(lds, C_WT, NAUX);
#pragma unroll
      for (int q = 0; q < 4; ++q)
#pragma unroll
        for (int d = 0; d < 4; ++d) {
          const unsigned w0 = *(LAS unsigned*)(lds + L_WT + (32 * q + 8 * r + 2 * d) * WT_STR + 16 * a + c);
          const unsigned w1 = *(LAS unsigned*)(lds + L_WT + (32 * q + 8 * r + 2 * d + 1) * WT_STR + 16 * a + c);
          bfr[0][q][d] = __builtin_amdgcn_perm(w1, w0, 0x05040100u);
          bfr[1][q][d] = __builtin_amdgcn_perm(w1, w0, 0x07060302u);
        }
    }
    if (!real) pace_wait(t0, P_TK0 + P_TMAIN + P_TGAP - 500);
    STAMP(3);
    if (a == 0) {
      cnt_wait<false>(lds, C_END, real ? NMAIN : 0);
      STAMP(14);
      f32x2 v = *(LAS f32x2*)(lds + L_MRG + 2 * lane);
#pragma unroll
      for (int w = 1; w < 8; ++w) v += *(LAS f32x2*)(lds + L_MRG + w * 128 + 2 * lane);
      st16_sc1(rws, st_off + lane * 16, WS_PART + (h * 8 + s) * 1024, gran(v[0], v[1], tagA, tagB));
    } else {
      cnt_wait(lds, C_END, real ? NMAIN : 0);
    }
    STAMP(4);
    STAMP(5);
    u32x4 g0, g1;
    const int vo = r * 1024 + (16 * a + c) * 16;
    for (unsigned sp = 0; sp < SPIN_CAP; ++sp) {
      REREAD();
      g0 = ld16_sc1(rws, vo, WS_PART + h * 8 * 1024);
      g1 = ld16_sc1(rws, vo, WS_PART + (h * 8 + 4) * 1024);
      if (__all(gok(g0, tagA, tagB) && gok(g1, tagA, tagB)) || !real) break;
    }
    STAMP(6);
    {
      float o = fold32(__uint_as_float(g0[0]) + __uint_as_float(g1[0]), __uint_as_float(g0[2]) + __uint_as_float(g1[2]));
      o = fold16(o, o);
      if ((r & 1) == 0) *((LAS u16*)(lds + L_OB) + 32 * a + 2 * c + (r >> 1)) = __builtin_bit_cast(u16, (__bf16)o);
    }
    if (real) __syncthreads();
    f32x4 y0 = {0.f, 0.f, 0.f, 0.f}, y1 = y0;
#pragma unroll
    for (int q = 0; q < 4; ++q) {
      const u32x4 af = *(LAS u32x4*)(lds + L_OB + 16 * q + 4 * r);
      y0 = __builtin_amdgcn_mfma_f32_16x16x32_bf16(__builtin_bit_cast(bf16x8, af), __builtin_bit_cast(bf16x8, bfr[0][q]), y0, 0, 0, 0);
      y1 = __builtin_amdgcn_mfma_f32_16x16x32_bf16(__builtin_bit_cast(bf16x8, af), __builtin_bit_cast(bf16x8, bfr[1][q]), y1, 0, 0, 0);
    }
    st16_sc1(rws, lane < 16 ? st_off + (16 * a + lane) * 16 : OOB, WS_Y + (h * 8 + s) * 1024, gran(y0[0], y1[0], tagA, tagB));
    STAMP(7);
    if (a == 0) {
      u32x4 g;
      const int voff = (lane >> 1) * (8 * 1024) + (lane & 1) * 16;
      for (unsigned sp = 0; sp < SPIN_CAP; ++sp) {
        REREAD();
        g = ld16_sc1(rws, voff, WS_Y + s * 1024 + h * 32);
        if (__all(gok(g, tagA, tagB)) || !real) break;
      }
      STAMP(8);
      const float a0 = rbf(parity_sum(__uint_as_float(g[0]))), a1 = rbf(parity_sum(__uint_as_float(g[2])));
      const unsigned o = pack2(rbf(lo2f(xw) + a0) + f0, rbf(hi2f(xw) + a1) + f1v);
      __builtin_amdgcn_raw_buffer_store_b32(o, mkrsrc(out, D * 2), lane < 2 ? st_off + (col0 + 2 * lane) * 2 : OOB, 0, 0);
      __builtin_amdgcn_raw_buffer_store_b64(u32x2{(unsigned)e1, (unsigned)(e1 >> 32)}, rws, (bid == 0 && lane == 0) ? st_off : OOB, WS_EPOCH, SC1);
    }
  }
  if (a != 0) { STAMP_DUMP(); return; }
  STAMP(9);
  STAMP_DUMP();
}

extern "C" void kernel_launch(void* const* d_in, const int* in_sizes, int n_in, void* d_out, int out_size, void* d_ws, size_t ws_size,
                              hipStream_t stream) {
  fused_decode_kernel<<<256, NTHR, 0, stream>>>((const u16*)d_in[0], (const u16*)d_in[1], (const u16*)d_in[2], (const u16*)d_in[3],
                                                 (const u16*)d_in[4], (const u16*)d_in[6], (const u16*)d_in[7], (const u16*)d_in[8],
                                                 (u16*)d_out, d_ws);
}
```

```cpp
#define T_MAIN 7100
#define T_K0 550
#define T_W2 340
#define T_F1 130
#define T_WO 390
#include <hip/hip_runtime.h>
#include <stdint.h>

typedef unsigned u32x4 __attribute__((ext_vector_type(4)));
typedef unsigned u32x2 __attribute__((ext_vector_type(2)));
typedef float f32x4 __attribute__((ext_vector_type(4)));
typedef float f32x2 __attribute__((ext_vector_type(2)));
typedef __bf16 bf16x2 __attribute__((ext_vector_type(2)));
typedef __bf16 bf16x8 __attribute__((ext_vector_type(8)));
typedef unsigned short u16;
#define LAS __attribute__((address_space(3)))
typedef __amdgpu_buffer_rsrc_t rsrc_t;

#define T_GAP 0
#define PACE_G 2
#define ABL_DEFAULT 0

#define TUN_ARG
#define P_TMAIN T_MAIN
#define P_TK0 T_K0
#define P_TGAP T_GAP
#define P_TW2 T_W2
#define P_TWO T_WO
#define P_STG 0
#define P_TF1 T_F1
#define ABL(b) ((ABL_DEFAULT >> (b)) & 1)
constexpr int D = 1024, H = 32, SK = 32768, DH = 128, F = 3584;
constexpr int NMAIN = 8, NAUX = 4, NWAVES = NMAIN + NAUX, NTHR = NWAVES * 64;
constexpr int NT = 2  , SC1 = 16;
constexpr int OOB = 0x40000000;
constexpr unsigned WS_EPOCH = 0, WS_T0 = 1024, WS_ML = 4096, WS_Q = WS_ML + 256 * 128, WS_PART = WS_Q + 256 * 1024, WS_Y = WS_PART + 256 * 1024,
                   WS_G1 = WS_Y + 256 * 1024, WS_FFN = WS_G1 + 4 * F * 8, WS_END = WS_FFN + 16 * D * 8;
constexpr unsigned WS_STAMP = 8u << 20;
constexpr unsigned WS_DIAG = 6u << 20;
constexpr int L_XN = 0, L_QP = 1024, L_QS = L_QP + 4 * 128, L_MRG = L_QS + 64, L_ML = L_MRG + 8 * 128, L_WG = L_ML + 16, L_FP = L_WG + 8,
              L_H = L_FP + 8 * 64, L_F2 = L_H + 4 * 64, L_OB = L_F2 + 4 * 64, L_CNT = L_OB + 64, L_SC = L_CNT + 16, L_WT = L_SC + 8 * 512,
              WT_STR = 68, L_END = L_WT + 128 * WT_STR;
constexpr int C_Q = 0, C_XN = 1, C_F2 = 2, C_WT = 3, C_ML = 4, C_WG = 5, C_END = 6, C_QR = 7, C_F1 = 8, C_NUM = 9, C_T0 = 12;

__device__ __forceinline__ float lo2f(unsigned w) { return __builtin_bit_cast(float, w << 16); }
__device__ __forceinline__ float hi2f(unsigned w) { return __builtin_bit_cast(float, w & 0xffff0000u); }
__device__ __forceinline__ float rbf(float x) { return (float)(__bf16)x; }
__device__ __forceinline__ unsigned pack2(float a, float b) { bf16x2 v = {(__bf16)a, (__bf16)b}; return __builtin_bit_cast(unsigned, v); }
__device__ __forceinline__ float dot2(unsigned k, unsigned q, float acc) {
  return __builtin_amdgcn_fdot2_f32_bf16(__builtin_bit_cast(bf16x2, k), __builtin_bit_cast(bf16x2, q), acc, false);
}
template <int CTRL> __device__ __forceinline__ float dpp(float x) {
  return __builtin_bit_cast(float, __builtin_amdgcn_mov_dpp(__builtin_bit_cast(int, x), CTRL, 0xf, 0xf, true));
}
constexpr int XOR1 = 0xB1, XOR2 = 0x4E, XOR7 = 0x141, ROR8 = 0x128, ROR4 = 0x124, BCAST = 0x150;
__device__ __forceinline__ float fold32(float a, float b) {
  auto t = __builtin_amdgcn_permlane32_swap(__float_as_uint(a), __float_as_uint(b), false, false);
  return __uint_as_float(t[0]) + __uint_as_float(t[1]);
}
__device__ __forceinline__ float fold16(float a, float b) {
  auto t = __builtin_amdgcn_permlane16_swap(__float_as_uint(a), __float_as_uint(b), false, false);
  return __uint_as_float(t[0]) + __uint_as_float(t[1]);
}
__device__ __forceinline__ float xrow_sum(float x) { x = fold16(x, x); return fold32(x, x); }
__device__ __forceinline__ float xrow_max(float x) {
  auto s = __builtin_amdgcn_permlane16_swap(__float_as_uint(x), __float_as_uint(x), false, false);
  x = fmaxf(__uint_as_float(s[0]), __uint_as_float(s[1]));
  auto t = __builtin_amdgcn_permlane32_swap(__float_as_uint(x), __float_as_uint(x), false, false);
  return fmaxf(__uint_as_float(t[0]), __uint_as_float(t[1]));
}
__device__ __forceinline__ float parity_sum(float x) {
  x += dpp<XOR2>(x); x += dpp<ROR4>(x); x += dpp<ROR8>(x); return xrow_sum(x);
}
__device__ __forceinline__ float wave_sum(float x) { x += dpp<XOR1>(x); return parity_sum(x); }
__device__ __forceinline__ float max8(float x) { x = fmaxf(x, dpp<XOR1>(x)); x = fmaxf(x, dpp<XOR2>(x)); return fmaxf(x, dpp<XOR7>(x)); }
__device__ __forceinline__ float sum8(float x) { x += dpp<XOR1>(x); x += dpp<XOR2>(x); return x + dpp<XOR7>(x); }
__device__ __forceinline__ float wave_max(float x) { x = max8(x); x = fmaxf(x, dpp<ROR8>(x)); return xrow_max(x); }
__device__ __forceinline__ float exp2_(float x) { return __builtin_amdgcn_exp2f(x); }
__device__ __forceinline__ rsrc_t mkrsrc(const void* p, unsigned bytes) { return __builtin_amdgcn_make_buffer_rsrc((void*)p, 0, (int)bytes, 0x00020000); }
__device__ __forceinline__ u32x4 ld16(rsrc_t r, int voff, int soff) { return __builtin_amdgcn_raw_buffer_load_b128(r, voff, soff, 0); }
__device__ __forceinline__ u32x4 ld16_nt(rsrc_t r, int voff, int soff) { return __builtin_amdgcn_raw_buffer_load_b128(r, voff, soff, NT); }
__device__ __forceinline__ u32x4 ld16_sc1(rsrc_t r, int voff, int soff) { return __builtin_amdgcn_raw_buffer_load_b128(r, voff, soff, SC1); }
__device__ __forceinline__ void st16_sc1(rsrc_t r, int voff, int soff, u32x4 v) { __builtin_amdgcn_raw_buffer_store_b128(v, r, voff + soff, 0, SC1); }
#define SB() __builtin_amdgcn_sched_barrier(0)
#define REREAD() asm volatile("" ::: "memory")
__device__ __forceinline__ void pace_wait(unsigned t0, int due) {
  asm volatile("1:\n\ts_memrealtime s[96:97]\n\ts_waitcnt lgkmcnt(0)\n\ts_sub_u32 s96, s96, %0\n\ts_cmp_lt_i32 s96, %1\n\ts_cbranch_scc0 2f\n\ts_sleep 1\n\ts_branch 1b\n2:"
               :: "s"(t0), "s"(due) : "s96", "s97", "scc", "memory");
}
__device__ __forceinline__ void fma8(float (&acc)[8], float s, u32x4 w) {
#pragma unroll
  for (int t = 0; t < 4; ++t) { acc[2 * t] = fmaf(s, lo2f(w[t]), acc[2 * t]); acc[2 * t + 1] = fmaf(s, hi2f(w[t]), acc[2 * t + 1]); }
}
__device__ __forceinline__ u32x4 gran(float a, float b, unsigned ta, unsigned tb) { return u32x4{__float_as_uint(a), ta, __float_as_uint(b), tb}; }
__device__ __forceinline__ bool gok(u32x4 g, unsigned ta, unsigned tb) { return g[1] == ta && g[3] == tb; }
constexpr unsigned SPIN_CAP = 1u << 20;

#define STAMP(i) do { } while (0)
#define STAMP_DUMP() do { } while (0)

__device__ __forceinline__ void cnt_arrive(LAS float* lds, int which, int lane) {
  asm volatile("s_waitcnt lgkmcnt(0)" ::: "memory");
  if (lane == 0) __hip_atomic_fetch_add((LAS unsigned*)(lds + L_CNT + which), 1u, __ATOMIC_RELAXED, __HIP_MEMORY_SCOPE_WORKGROUP);
}
template <bool SLEEP = true>
__device__ __forceinline__ void cnt_wait(LAS float* lds, int which, unsigned n) {
  for (unsigned sp = 0; sp < SPIN_CAP; ++sp) {
    if (__builtin_amdgcn_readfirstlane(*(volatile LAS unsigned*)(lds + L_CNT + which)) >= n) break;
    if (SLEEP) __builtin_amdgcn_s_sleep(1);
  }
  REREAD();
}

__global__ void __launch_bounds__(NTHR, 3)
fused_decode_kernel(const u16* __restrict__ x, const u16* __restrict__ Kc, const u16* __restrict__ Vc, const u16* __restrict__ wq,
                    const u16* __restrict__ wo, const u16* __restrict__ wg, const u16* __restrict__ wu, const u16* __restrict__ wd,
                    u16* __restrict__ out, void* ws TUN_ARG) {
  __shared__ __attribute__((aligned(16))) float lds_[L_END];
  LAS float* lds = (LAS float*)lds_;
  const unsigned t0 = (unsigned)__builtin_amdgcn_s_memrealtime();
  const int tid = threadIdx.x, lane = tid & 63, wave = __builtin_amdgcn_readfirstlane(tid >> 6);
  const int r = lane >> 4, c = lane & 15;
  const int bid = blockIdx.x, xl = bid & 7, j = bid >> 3;
  const int h = xl * 4 + (j >> 3), s = j & 7;
  const int a = wave - NMAIN;
  const int f1 = xl * 28 + j, mat = f1 / 112, rb = (f1 / 56) & 1, cb = f1 % 56;
  STAMP(0);

  if (wave < NMAIN) {
    const rsrc_t rk = mkrsrc(Kc, (unsigned)H * SK * DH * 2), rv = mkrsrc(Vc, (unsigned)H * SK * DH * 2);
    u32x4 bA[16], bB[16];
    const unsigned kvbase = (unsigned)h * (8u << 20) + (unsigned)s * (1u << 20) + wave * 1024;
    const unsigned rot = 32u * ((h + (h >> 2) + (h >> 4)) & 3);
    const int kvoff = lane * 16;
    const int STEP = ((int)P_TMAIN << 16) / 256 * PACE_G;
    int due = (int)P_TK0 * 65536 - STEP + (P_STG == 0 ? (STEP / 256) * (wave * 32 + j) : (STEP / 32) * j);
#define PACE(T0, I) do { if ((I) % PACE_G == 0) { due += STEP; pace_wait(T0, due >> 16); } } while (0)
    {
      const rsrc_t rg = mkrsrc(mat ? wu : wg, D * F * 2);
      int g_voff = (j < 28 && !ABL(1)) ? (lane >> 3) * (F * 2) + (lane & 7) * 16 : OOB, g_sb = (rb * 512 + wave * 64) * (F * 2) + cb * 128, g_is = 8 * (F * 2);
      __builtin_amdgcn_s_barrier(); SB();
      const int fstep = ((P_TW2 - P_TF1) << 16) / 8;
      int fdue = P_TF1 * 65536 + (fstep / 256) * (wave * 32 + j);
#pragma unroll
      for (int i = 0; i < 8; ++i) { pace_wait(t0, fdue >> 16); fdue += fstep; bB[i] = ld16_nt(rg, g_voff, g_sb + i * g_is); SB(); }
    }
#pragma unroll
    for (int i = 0; i < 16; ++i) { PACE(t0, i); bA[i] = ld16_nt(rk, kvoff, kvbase + (rot + i) * 8192); SB(); if (i == 0) { STAMP(12); SB(); } }
    {
      cnt_wait(lds, C_XN, 1);
      float ag[8] = {0.f, 0.f, 0.f, 0.f, 0.f, 0.f, 0.f, 0.f};
#pragma unroll
      for (int i = 0; i < 8; ++i) {
        const int k = rb * 512 + wave * 64 + 8 * i + (lane >> 3);
        fma8(ag, lds[L_XN + (k & 3) * 256 + (k >> 2)], bB[i]);
      }
#pragma unroll
      for (int e = 0; e < 8; ++e) ag[e] += dpp<ROR8>(ag[e]);
      const float u0 = fold32(ag[0], ag[4]), u1 = fold32(ag[1], ag[5]), u2 = fold32(ag[2], ag[6]), u3 = fold32(ag[3], ag[7]);
      const f32x2 z = {fold16(u0, u2), fold16(u1, u3)};
      if (c < 8) *(LAS f32x2*)(lds + L_FP + wave * 64 + 8 * c + 2 * r) = z;
      cnt_arrive(lds, C_F1, lane);
      SB();
    }
#pragma unroll
    for (int i = 0; i < 16; ++i) { PACE(t0, i); bB[i] = ld16_nt(rk, kvoff, kvbase + (rot + 16 + i) * 8192); SB(); }
    STAMP(1);
    cnt_wait(lds, C_QR, 1);
    const u32x4 qp = *(LAS u32x4*)(lds + L_QS + 4 * c);
    const unsigned t0m = __builtin_amdgcn_readfirstlane(*(LAS unsigned*)(lds + L_CNT + C_T0));
    STAMP(2);
    constexpr float SCALE = 0.08838834764831845f, LOG2E = 1.4426950408889634f;
    LAS float* scp = lds + L_SC + wave * 512 + lane * 8;
    float mx = -INFINITY;
    auto kgroup = [&](u32x4 (&kb)[16], const bool more, const rsrc_t rn, const unsigned tb, const int g) __attribute__((always_inline)) {
      float sx[16];
#pragma unroll
      for (int i = 0; i < 16; ++i) {
        float d = 0.f;
#pragma unroll
        for (int t = 0; t < 4; ++t) d = dot2(kb[i][t], qp[t], d);
        asm volatile("" : "+v"(d));
        sx[i] = d;
        SB();
        if (more) { PACE(t0m, i); kb[i] = ld16_nt(rn, kvoff, tb + i * 8192); SB(); }
      }
#define TR_STEP(HALF, CTRL, BIT) _Pragma("unroll") for (int hh = 0; hh < (HALF); ++hh) { \
        const float lo_ = sx[hh], hi_ = sx[hh + (HALF)]; const float t1_ = lo_ + dpp<CTRL>(lo_), t2_ = hi_ + dpp<CTRL>(hi_); \
        sx[hh] = (c & (BIT)) ? t2_ : t1_; }
      TR_STEP(8, ROR8, 8) TR_STEP(4, XOR7, 4) TR_STEP(2, XOR2, 2) TR_STEP(1, XOR1, 1)
#undef TR_STEP
      const float sc = rbf(rbf(sx[0]) * SCALE) * LOG2E;
      mx = fmaxf(mx, sc);
      scp[g] = sc;
      SB();
    };
    kgroup(bA, false, rk, 0, 0);
#pragma unroll 1
    for (int g = 1; g < 8; ++g) {
      const bool tov = g == 7;
      due += tov ? (int)P_TGAP * 65536 : 0;
      kgroup(bB, true, mkrsrc(tov ? Vc : Kc, (unsigned)H * SK * DH * 2), kvbase + (((g + 1) * 16 + rot) & 127u) * 8192, g);
    }
    {
      const float m = wave_max(mx);
      f32x4 p0 = *(LAS f32x4*)scp, p1 = *(LAS f32x4*)(scp + 4);
      float l = 0.f;
#pragma unroll
      for (int e = 0; e < 4; ++e) { p0[e] = exp2_(p0[e] - m); p1[e] = exp2_(p1[e] - m); l += p0[e] + p1[e]; }
      *(LAS f32x4*)scp = p0; *(LAS f32x4*)(scp + 4) = p1;
      l = wave_sum(l);
      if (lane == 0) *(LAS f32x2*)(lds + L_ML + 2 * wave) = f32x2{m, l};
      cnt_arrive(lds, C_ML, lane);
    }
    STAMP(11);
    float acc[8] = {0.f, 0.f, 0.f, 0.f, 0.f, 0.f, 0.f, 0.f};
    auto vgroup = [&](const bool more, const unsigned tb, const float p) __attribute__((always_inline)) {
#define PV_STEP(I) { fma8(acc, dpp<BCAST + (I)>(p), bB[I]); \
        asm volatile("" : "+v"(acc[0]), "+v"(acc[1]), "+v"(acc[2]), "+v"(acc[3]), "+v"(acc[4]), "+v"(acc[5]), "+v"(acc[6]), "+v"(acc[7])); SB(); \
        if (more) { PACE(t0m, I); bB[I] = ld16_nt(rv, kvoff, tb + (I) * 8192); SB(); } }
      PV_STEP(0) PV_STEP(1) PV_STEP(2) PV_STEP(3) PV_STEP(4) PV_STEP(5) PV_STEP(6) PV_STEP(7)
      PV_STEP(8) PV_STEP(9) PV_STEP(10) PV_STEP(11) PV_STEP(12) PV_STEP(13) PV_STEP(14) PV_STEP(15)
#undef PV_STEP
    };
#pragma unroll 1
    for (int g = 0; g < 7; ++g) vgroup(true, kvbase + (((g + 1) * 16 + rot) & 127u) * 8192, scp[g]);
    STAMP(3);
    __builtin_amdgcn_s_setprio(3);
    cnt_wait(lds, C_WG, 1);
    {
      const float wgt = lds[L_WG + wave];
#pragma unroll
      for (int e = 0; e < 8; ++e) acc[e] *= wgt;
      vgroup(false, 0, scp[7] * wgt);
    }
    STAMP(4);
    {
      const float u0 = fold32(acc[0], acc[4]), u1 = fold32(acc[1], acc[5]), u2 = fold32(acc[2], acc[6]), u3 = fold32(acc[3], acc[7]);
      *(LAS f32x2*)(lds + L_MRG + wave * 128 + 8 * c + 2 * r) = f32x2{fold16(u0, u2), fold16(u1, u3)};
    }
    cnt_arrive(lds, C_END, lane);
    STAMP_DUMP();
    return;
  }

  const rsrc_t rx = mkrsrc(x, D * 2), rws = mkrsrc(ws, WS_END);
  unsigned tagA, tagB;
  unsigned long long e1;
  {
    u32x4 xa = ld16(rx, lane * 32, 0), xb = ld16(rx, lane * 32 + 16, 0);
    const u32x2 ep = __builtin_amdgcn_raw_buffer_load_b64(rws, 0, WS_EPOCH, SC1);
    u32x4 pt = ABL(14) ? ld16(rws, (a == 0 && ABL(13)) ? lane * 16 : OOB, WS_Q + (h * 8 + s) * 1024) : ld16_sc1(rws, (a == 0 && ABL(13)) ? lane * 16 : OOB, WS_Q + (h * 8 + s) * 1024);
    const rsrc_t rwq = mkrsrc(wq, D * H * DH * 2);
    u32x4 w[8];
    const int voff = r * 8192 + c * 16, sb = (s * 128 + a * 32) * 8192 + h * 256;
#pragma unroll
    for (int i = 0; i < 8; ++i) { w[i] = ld16_nt(rwq, voff, sb + i * 32768); SB(); }
    if (a == 0 && lane < C_NUM) *(LAS unsigned*)(lds + L_CNT + lane) = 0u;
    __syncthreads();
    asm volatile("" : "+v"(xa), "+v"(xb), "+v"(pt));
    STAMP(10);
    float v[16];
#pragma unroll
    for (int i = 0; i < 4; ++i) { v[2 * i] = lo2f(xa[i]); v[2 * i + 1] = hi2f(xa[i]); v[8 + 2 * i] = lo2f(xb[i]); v[9 + 2 * i] = hi2f(xb[i]); }
    float sm = 0.f;
#pragma unroll
    for (int i = 0; i < 16; ++i) sm += v[i];
    const float mean = wave_sum(sm) * (1.f / D);
    float ss = 0.f;
#pragma unroll
    for (int i = 0; i < 16; ++i) { const float d = v[i] - mean; ss = fmaf(d, d, ss); }
    const float var = wave_sum(ss) * (1.f / D);
    const float mb = rbf(mean), den = rbf(sqrtf(rbf(rbf(var) + 1e-5f)));
    const float rinv = 1.0f / den;
#pragma unroll
    for (int i = 0; i < 16; ++i) {
      const float nu = rbf(v[i] - mb), q0 = nu * rinv;
      v[i] = rbf(fmaf(fmaf(-q0, den, nu), rinv, q0));
    }
#pragma unroll
    for (int q = 0; q < 4; ++q) *(LAS f32x4*)(lds + L_XN + q * 256 + 4 * lane) = f32x4{v[q], v[4 + q], v[8 + q], v[12 + q]};
    cnt_arrive(lds, C_XN, lane);
    e1 = (((unsigned long long)__builtin_amdgcn_readfirstlane(ep[1]) << 32) | (unsigned)__builtin_amdgcn_readfirstlane(ep[0])) + 1ull;
    tagA = (unsigned)e1; tagB = (unsigned)(e1 >> 32) + (unsigned)e1 * 0x9E3779B1u;
    float acc[8] = {0.f, 0.f, 0.f, 0.f, 0.f, 0.f, 0.f, 0.f};
#pragma unroll
    for (int q = 0; q < 2; ++q) {
      const f32x4 xv = *(LAS f32x4*)(lds + L_XN + r * 256 + s * 32 + a * 8 + 4 * q);
#pragma unroll
      for (int e = 0; e < 4; ++e) fma8(acc, xv[e], w[4 * q + e]);
    }
    const float u0 = fold32(acc[0], acc[4]), u1 = fold32(acc[1], acc[5]), u2 = fold32(acc[2], acc[6]), u3 = fold32(acc[3], acc[7]);
    *(LAS f32x2*)(lds + L_QP + a * 128 + 8 * c + 2 * r) = f32x2{fold16(u0, u2), fold16(u1, u3)};
  }
  STAMP(1);
  cnt_arrive(lds, C_Q, lane);
  if (a == 0) {
    cnt_wait<false>(lds, C_Q, NAUX);
    STAMP(11);
    f32x2 v = *(LAS f32x2*)(lds + L_QP + 2 * lane);
#pragma unroll
    for (int q = 1; q < 4; ++q) v += *(LAS f32x2*)(lds + L_QP + q * 128 + 2 * lane);
    st16_sc1(rws, lane * 16, WS_Q + (h * 8 + s) * 1024, gran(v[0], v[1], tagA, tagB));
    st16_sc1(rws, (bid == 0 && lane == 0) ? 0 : OOB, WS_T0, u32x4{t0, tagA, t0, tagB});
  }
  if (a == 1) {
    pace_wait(t0, P_TWO);
    float q0 = 0.f, q1 = 0.f;
    unsigned tg = t0;
    for (unsigned sp = 0; sp < SPIN_CAP; ++sp) {
      REREAD();
      u32x4 g[8];
#pragma unroll
      for (int i = 0; i < 8; ++i) g[i] = ld16_sc1(rws, lane * 16, WS_Q + (h * 8 + i) * 1024);
      const u32x4 gt = ld16_sc1(rws, 0, WS_T0);
      bool ok = gok(gt, tagA, tagB);
      tg = gt[0];
      q0 = 0.f; q1 = 0.f;
#pragma unroll
      for (int i = 0; i < 8; ++i) { ok = ok && gok(g[i], tagA, tagB); q0 += __uint_as_float(g[i][0]); q1 += __uint_as_float(g[i][2]); }
      if (__all(ok)) break;
      __builtin_amdgcn_s_sleep(2);
    }
    STAMP(13);
    *(LAS unsigned*)(lds + L_QS + lane) = pack2(q0, q1);
    if (lane == 0) *(LAS unsigned*)(lds + L_CNT + C_T0) = (ABL(12) || (int)(t0 - tg) < 0 || (int)(t0 - tg) > 300) ? t0 : tg;
    cnt_arrive(lds, C_QR, lane);
  }
  const int ks = xl * 2 + (j >> 4), cg = j & 15;
  u32x4 dw[7], wov[8];
  {
    const rsrc_t rd = mkrsrc(wd, D * F * 2), ro = mkrsrc(wo, H * DH * D * 2);
    const int d_voff = ABL(2) ? OOB : (lane >> 3) * 2048 + (lane & 7) * 16, d_sb = (ks * 224 + a * 56) * 2048 + cg * 128;
    const int o_voff = ABL(3) ? OOB : r * 2048 + c * 16, o_sb = (h * 128 + a * 32) * 2048 + s * 256;
    const int wstep = ((P_TK0 - P_TW2) << 16) / 15;
    int wdue = P_TW2 * 65536 + (wstep / 128) * (a * 32 + j);
#pragma unroll
    for (int i = 0; i < 7; ++i) { pace_wait(t0, wdue >> 16); wdue += wstep; dw[i] = ld16_nt(rd, d_voff, d_sb + i * 16384); SB(); }
#pragma unroll
    for (int i = 0; i < 8; ++i) { pace_wait(t0, wdue >> 16); wdue += wstep; wov[i] = ld16_nt(ro, o_voff, o_sb + i * 8192); SB(); }
  }
  STAMP(15);
  if (a == 0 && j < 28) {
    cnt_wait(lds, C_F1, NMAIN);
    f32x2 v = *(LAS f32x2*)(lds + L_FP + 2 * (lane & 31));
#pragma unroll
    for (int q = 1; q < 8; ++q) v += *(LAS f32x2*)(lds + L_FP + q * 64 + 2 * (lane & 31));
    st16_sc1(rws, lane < 32 ? lane * 16 : OOB, WS_G1 + ((mat * 2 + rb) * F + cb * 64) * 8, gran(v[0], v[1], tagA, tagB));
  }
  STAMP(12);
  const int t_mid = P_TK0 + P_TMAIN / 2;
  u32x4 bfr[2][4];
  {
#pragma unroll
    for (int i = 0; i < 8; ++i) *(LAS u32x4*)(lds + L_WT + (a * 32 + 4 * i + r) * WT_STR + 4 * c) = wov[i];
    cnt_arrive(lds, C_WT, lane);
    cnt_wait(lds, C_WT, NAUX);
#pragma unroll
    for (int q = 0; q < 4; ++q)
#pragma unroll
      for (int d = 0; d < 4; ++d) {
        const unsigned w0 = *(LAS unsigned*)(lds + L_WT + (32 * q + 8 * r + 2 * d) * WT_STR + 16 * a + c);
        const unsigned w1 = *(LAS unsigned*)(lds + L_WT + (32 * q + 8 * r + 2 * d + 1) * WT_STR + 16 * a + c);
        bfr[0][q][d] = __builtin_amdgcn_perm(w1, w0, 0x05040100u);
        bfr[1][q][d] = __builtin_amdgcn_perm(w1, w0, 0x07060302u);
      }
  }
  {
    pace_wait(t0, P_TK0 + 2000);
    u32x4 g[4];
    const int voff = (lane < 28 ? lane : 27) * 16;
    for (unsigned sp = 0; sp < SPIN_CAP; ++sp) {
      REREAD();
#pragma unroll
      for (int k = 0; k < 4; ++k) g[k] = ld16_sc1(rws, voff, WS_G1 + (k * F + ks * 224 + a * 56) * 8);
      if (__all(gok(g[0], tagA, tagB) && gok(g[1], tagA, tagB) && gok(g[2], tagA, tagB) && gok(g[3], tagA, tagB))) break;
      __builtin_amdgcn_s_sleep(32);
    }
    float hh[2];
#pragma unroll
    for (int e = 0; e < 2; ++e) {
      const float gs = rbf(__uint_as_float(g[0][2 * e]) + __uint_as_float(g[1][2 * e]));
      const float us = rbf(__uint_as_float(g[2][2 * e]) + __uint_as_float(g[3][2 * e]));
      const float ge = 0.5f * gs * (1.f + tanhf(0.7978845608028654f * (gs + 0.044715f * gs * gs * gs)));
      hh[e] = rbf(rbf(ge) * us);
    }
    if (lane < 28) *(LAS f32x2*)(lds + L_H + a * 64 + 2 * lane) = f32x2{hh[0], hh[1]};
    float y[8] = {0.f, 0.f, 0.f, 0.f, 0.f, 0.f, 0.f, 0.f};
#pragma unroll
    for (int i = 0; i < 7; ++i) fma8(y, lds[L_H + a * 64 + 8 * i + (lane >> 3)], dw[i]);
#pragma unroll
    for (int e = 0; e < 8; ++e) { y[e] += dpp<ROR8>(y[e]); y[e] = xrow_sum(y[e]); }
    if (lane < 8) {
      *(LAS f32x4*)(lds + L_F2 + a * 64 + lane * 8) = f32x4{y[0], y[1], y[2], y[3]};
      *(LAS f32x4*)(lds + L_F2 + a * 64 + lane * 8 + 4) = f32x4{y[4], y[5], y[6], y[7]};
    }
    cnt_arrive(lds, C_F2, lane);
  }
  STAMP(2);
  const int col0 = s * 128 + 4 * h;
  float f0 = 0.f, f1v = 0.f;
  unsigned xw = 0;
  if (a == 0) {
    cnt_wait(lds, C_F2, NAUX);
    f32x2 v = *(LAS f32x2*)(lds + L_F2 + 2 * (lane & 31));
#pragma unroll
    for (int q = 1; q < 4; ++q) v += *(LAS f32x2*)(lds + L_F2 + q * 64 + 2 * (lane & 31));
    st16_sc1(rws, lane < 32 ? lane * 16 : OOB, WS_FFN + (ks * D + cg * 64) * 8, gran(v[0], v[1], tagA, tagB));
    xw = __builtin_amdgcn_raw_buffer_load_b32(rx, (col0 + 2 * (lane & 1)) * 2, 0, 0);
    pace_wait(t0, P_TK0 + 3500);
    const int voff = ((lane >> 1) & 15) * (D * 8) + (lane & 1) * 16;
    u32x4 g;
    for (unsigned sp = 0; sp < SPIN_CAP; ++sp) {
      REREAD();
      g = ld16_sc1(rws, voff, WS_FFN + col0 * 8);
      if (__all(gok(g, tagA, tagB))) break;
      __builtin_amdgcn_s_sleep(32);
    }
    f0 = rbf(parity_sum(__uint_as_float(g[0])) * 0.5f); f1v = rbf(parity_sum(__uint_as_float(g[2])) * 0.5f);
  }
  if (a == 1) {
    cnt_wait(lds, C_ML, NMAIN);
    const f32x2 ml = *(LAS f32x2*)(lds + L_ML + 2 * (lane & 7));
    const float Mw = max8(ml[0]), Lw = sum8(exp2_(ml[0] - Mw) * ml[1]);
    st16_sc1(rws, lane == 0 ? 0 : OOB, WS_ML + (h * 8 + s) * 128, gran(Mw, Lw, tagA, tagB));
    pace_wait(t0, t_mid + 1500);
    u32x4 g;
    for (unsigned sp = 0; sp < SPIN_CAP; ++sp) {
      REREAD();
      g = ld16_sc1(rws, (lane & 7) * 128, WS_ML + h * 8 * 128);
      if (__all(gok(g, tagA, tagB))) break;
      __builtin_amdgcn_s_sleep(32);
    }
    const float M = max8(__uint_as_float(g[0])), L = sum8(exp2_(__uint_as_float(g[0]) - M) * __uint_as_float(g[2]));
    if (lane < 8) lds[L_WG + lane] = exp2_(ml[0] - M) / L;
    cnt_arrive(lds, C_WG, lane);
  }
  int pass = ABL(10) ? 1 : 0;
  asm volatile("" : "+s"(pass));
#pragma unroll 1
  for (; pass < 2; ++pass) {
    const bool real = pass != 0;
    const int st_off = real ? 0 : OOB;
    if (!real) pace_wait(t0, P_TK0 + P_TMAIN + P_TGAP - 500);
    STAMP(3);
    if (a == 0) {
      cnt_wait<false>(lds, C_END, real ? NMAIN : 0);
      STAMP(14);
      f32x2 v = *(LAS f32x2*)(lds + L_MRG + 2 * lane);
#pragma unroll
      for (int w = 1; w < 8; ++w) v += *(LAS f32x2*)(lds + L_MRG + w * 128 + 2 * lane);
      st16_sc1(rws, st_off + lane * 16, WS_PART + (h * 8 + s) * 1024, gran(v[0], v[1], tagA, tagB));
    } else {
      cnt_wait(lds, C_END, real ? NMAIN : 0);
    }
    STAMP(4);
    STAMP(5);
    u32x4 g0, g1;
    const int vo = r * 1024 + (16 * a + c) * 16;
    for (unsigned sp = 0; sp < SPIN_CAP; ++sp) {
      REREAD();
      g0 = ld16_sc1(rws, vo, WS_PART + h * 8 * 1024);
      g1 = ld16_sc1(rws, vo, WS_PART + (h * 8 + 4) * 1024);
      if (__all(gok(g0, tagA, tagB) && gok(g1, tagA, tagB)) || !real) break;
    }
    STAMP(6);
    {
      float o = fold32(__uint_as_float(g0[0]) + __uint_as_float(g1[0]), __uint_as_float(g0[2]) + __uint_as_float(g1[2]));
      o = fold16(o, o);
      if ((r & 1) == 0) *((LAS u16*)(lds + L_OB) + 32 * a + 2 * c + (r >> 1)) = __builtin_bit_cast(u16, (__bf16)o);
    }
    if (real) __syncthreads();
    f32x4 y0 = {0.f, 0.f, 0.f, 0.f}, y1 = y0;
#pragma unroll
    for (int q = 0; q < 4; ++q) {
      const u32x4 af = *(LAS u32x4*)(lds + L_OB + 16 * q + 4 * r);
      y0 = __builtin_amdgcn_mfma_f32_16x16x32_bf16(__builtin_bit_cast(bf16x8, af), __builtin_bit_cast(bf16x8, bfr[0][q]), y0, 0, 0, 0);
      y1 = __builtin_amdgcn_mfma_f32_16x16x32_bf16(__builtin_bit_cast(bf16x8, af), __builtin_bit_cast(bf16x8, bfr[1][q]), y1, 0, 0, 0);
    }
    st16_sc1(rws, lane < 16 ? st_off + (16 * a + lane) * 16 : OOB, WS_Y + (h * 8 + s) * 1024, gran(y0[0], y1[0], tagA, tagB));
    STAMP(7);
    if (a == 0) {
      u32x4 g;
      const int voff = (lane >> 1) * (8 * 1024) + (lane & 1) * 16;
      for (unsigned sp = 0; sp < SPIN_CAP; ++sp) {
        REREAD();
        g = ld16_sc1(rws, voff, WS_Y + s * 1024 + h * 32);
        if (__all(gok(g, tagA, tagB)) || !real) break;
      }
      STAMP(8);
      const float a0 = rbf(parity_sum(__uint_as_float(g[0]))), a1 = rbf(parity_sum(__uint_as_float(g[2])));
      const unsigned o = pack2(rbf(lo2f(xw) + a0) + f0, rbf(hi2f(xw) + a1) + f1v);
      __builtin_amdgcn_raw_buffer_store_b32(o, mkrsrc(out, D * 2), lane < 2 ? st_off + (col0 + 2 * lane) * 2 : OOB, 0, 0);
      __builtin_amdgcn_raw_buffer_store_b64(u32x2{(unsigned)e1, (unsigned)(e1 >> 32)}, rws, (bid == 0 && lane == 0) ? st_off : OOB, WS_EPOCH, SC1);
    }
  }
  if (a != 0) { STAMP_DUMP(); return; }
  STAMP(9);
  STAMP_DUMP();
}

extern "C" void kernel_launch(void* const* d_in, const int* in_sizes, int n_in, void* d_out, int out_size, void* d_ws, size_t ws_size,
                              hipStream_t stream) {
  fused_decode_kernel<<<256, NTHR, 0, stream>>>((const u16*)d_in[0], (const u16*)d_in[1], (const u16*)d_in[2], (const u16*)d_in[3],
                                                 (const u16*)d_in[4], (const u16*)d_in[6], (const u16*)d_in[7], (const u16*)d_in[8],
                                                 (u16*)d_out, d_ws);
}
```

```cpp
#define T_MAIN 7200
#define T_K0 570
#define T_W2 360
#define T_F1 150
#define T_GAP 1000
#define ABL_DEFAULT 0
#include <hip/hip_runtime.h>
#include <stdint.h>

typedef unsigned u32x4 __attribute__((ext_vector_type(4)));
typedef unsigned u32x2 __attribute__((ext_vector_type(2)));
typedef float f32x4 __attribute__((ext_vector_type(4)));
typedef float f32x2 __attribute__((ext_vector_type(2)));
typedef __bf16 bf16x2 __attribute__((ext_vector_type(2)));
typedef __bf16 bf16x8 __attribute__((ext_vector_type(8)));
typedef unsigned short u16;
#define LAS __attribute__((address_space(3)))
typedef __amdgpu_buffer_rsrc_t rsrc_t;

#define T_WO 390
#define PACE_G 2

#define TUN_ARG
#define P_TMAIN T_MAIN
#define P_TK0 T_K0
#define P_TGAP T_GAP
#define P_TW2 T_W2
#define P_TWO T_WO
#define P_STG 0
#define P_TF1 T_F1
#define ABL(b) ((ABL_DEFAULT >> (b)) & 1)
constexpr int D = 1024, H = 32, SK = 32768, DH = 128, F = 3584;
constexpr int NMAIN = 8, NAUX = 4, NWAVES = NMAIN + NAUX, NTHR = NWAVES * 64;
constexpr int NT = 2  , SC1 = 16;
constexpr int OOB = 0x40000000;
constexpr unsigned WS_EPOCH = 0, WS_T0 = 1024, WS_ML = 4096, WS_Q = WS_ML + 256 * 128, WS_PART = WS_Q + 256 * 1024, WS_Y = WS_PART + 256 * 1024,
                   WS_G1 = WS_Y + 256 * 1024, WS_FFN = WS_G1 + 4 * F * 8, WS_END = WS_FFN + 16 * D * 8;
constexpr unsigned WS_STAMP = 8u << 20;
constexpr unsigned WS_DIAG = 6u << 20;
constexpr int L_XN = 0, L_QP = 1024, L_QS = L_QP + 4 * 128, L_MRG = L_QS + 64, L_ML = L_MRG + 8 * 128, L_WG = L_ML + 16, L_FP = L_WG + 8,
              L_H = L_FP + 8 * 64, L_F2 = L_H + 4 * 64, L_OB = L_F2 + 4 * 64, L_CNT = L_OB + 64, L_SC = L_CNT + 16, L_WT = L_SC + 8 * 512,
              WT_STR = 68, L_END = L_WT + 128 * WT_STR;
constexpr int C_Q = 0, C_XN = 1, C_F2 = 2, C_WT = 3, C_ML = 4, C_WG = 5, C_END = 6, C_QR = 7, C_F1 = 8, C_NUM = 9, C_T0 = 12;

__device__ __forceinline__ float lo2f(unsigned w) { return __builtin_bit_cast(float, w << 16); }
__device__ __forceinline__ float hi2f(unsigned w) { return __builtin_bit_cast(float, w & 0xffff0000u); }
__device__ __forceinline__ float rbf(float x) { return (float)(__bf16)x; }
__device__ __forceinline__ unsigned pack2(float a, float b) { bf16x2 v = {(__bf16)a, (__bf16)b}; return __builtin_bit_cast(unsigned, v); }
__device__ __forceinline__ float dot2(unsigned k, unsigned q, float acc) {
  return __builtin_amdgcn_fdot2_f32_bf16(__builtin_bit_cast(bf16x2, k), __builtin_bit_cast(bf16x2, q), acc, false);
}
template <int CTRL> __device__ __forceinline__ float dpp(float x) {
  return __builtin_bit_cast(float, __builtin_amdgcn_mov_dpp(__builtin_bit_cast(int, x), CTRL, 0xf, 0xf, true));
}
constexpr int XOR1 = 0xB1, XOR2 = 0x4E, XOR7 = 0x141, ROR8 = 0x128, ROR4 = 0x124, BCAST = 0x150;
__device__ __forceinline__ float fold32(float a, float b) {
  auto t = __builtin_amdgcn_permlane32_swap(__float_as_uint(a), __float_as_uint(b), false, false);
  return __uint_as_float(t[0]) + __uint_as_float(t[1]);
}
__device__ __forceinline__ float fold16(float a, float b) {
  auto t = __builtin_amdgcn_permlane16_swap(__float_as_uint(a), __float_as_uint(b), false, false);
  return __uint_as_float(t[0]) + __uint_as_float(t[1]);
}
__device__ __forceinline__ float xrow_sum(float x) { x = fold16(x, x); return fold32(x, x); }
__device__ __forceinline__ float xrow_max(float x) {
  auto s = __builtin_amdgcn_permlane16_swap(__float_as_uint(x), __float_as_uint(x), false, false);
  x = fmaxf(__uint_as_float(s[0]), __uint_as_float(s[1]));
  auto t = __builtin_amdgcn_permlane32_swap(__float_as_uint(x), __float_as_uint(x), false, false);
  return fmaxf(__uint_as_float(t[0]), __uint_as_float(t[1]));
}
__device__ __forceinline__ float parity_sum(float x) {
  x += dpp<XOR2>(x); x += dpp<ROR4>(x); x += dpp<ROR8>(x); return xrow_sum(x);
}
__device__ __forceinline__ float wave_sum(float x) { x += dpp<XOR1>(x); return parity_sum(x); }
__device__ __forceinline__ float max8(float x) { x = fmaxf(x, dpp<XOR1>(x)); x = fmaxf(x, dpp<XOR2>(x)); return fmaxf(x, dpp<XOR7>(x)); }
__device__ __forceinline__ float sum8(float x) { x += dpp<XOR1>(x); x += dpp<XOR2>(x); return x + dpp<XOR7>(x); }
__device__ __forceinline__ float wave_max(float x) { x = max8(x); x = fmaxf(x, dpp<ROR8>(x)); return xrow_max(x); }
__device__ __forceinline__ float exp2_(float x) { return __builtin_amdgcn_exp2f(x); }
__device__ __forceinline__ rsrc_t mkrsrc(const void* p, unsigned bytes) { return __builtin_amdgcn_make_buffer_rsrc((void*)p, 0, (int)bytes, 0x00020000); }
__device__ __forceinline__ u32x4 ld16(rsrc_t r, int voff, int soff) { return __builtin_amdgcn_raw_buffer_load_b128(r, voff, soff, 0); }
__device__ __forceinline__ u32x4 ld16_nt(rsrc_t r, int voff, int soff) { return __builtin_amdgcn_raw_buffer_load_b128(r, voff, soff, NT); }
__device__ __forceinline__ u32x4 ld16_sc1(rsrc_t r, int voff, int soff) { return __builtin_amdgcn_raw_buffer_load_b128(r, voff, soff, SC1); }
__device__ __forceinline__ void st16_sc1(rsrc_t r, int voff, int soff, u32x4 v) { __builtin_amdgcn_raw_buffer_store_b128(v, r, voff + soff, 0, SC1); }
#define SB() __builtin_amdgcn_sched_barrier(0)
#define REREAD() asm volatile("" ::: "memory")
__device__ __forceinline__ void pace_wait(unsigned t0, int due) {
  asm volatile("1:\n\ts_memrealtime s[96:97]\n\ts_waitcnt lgkmcnt(0)\n\ts_sub_u32 s96, s96, %0\n\ts_cmp_lt_i32 s96, %1\n\ts_cbranch_scc0 2f\n\ts_sleep 1\n\ts_branch 1b\n2:"
               :: "s"(t0), "s"(due) : "s96", "s97", "scc", "memory");
}
__device__ __forceinline__ void fma8(float (&acc)[8], float s, u32x4 w) {
#pragma unroll
  for (int t = 0; t < 4; ++t) { acc[2 * t] = fmaf(s, lo2f(w[t]), acc[2 * t]); acc[2 * t + 1] = fmaf(s, hi2f(w[t]), acc[2 * t + 1]); }
}
__device__ __forceinline__ u32x4 gran(float a, float b, unsigned ta, unsigned tb) { return u32x4{__float_as_uint(a), ta, __float_as_uint(b), tb}; }
__device__ __forceinline__ bool gok(u32x4 g, unsigned ta, unsigned tb) { return g[1] == ta && g[3] == tb; }
constexpr unsigned SPIN_CAP = 1u << 20;

#define STAMP(i) do { } while (0)
#define STAMP_DUMP() do { } while (0)

__device__ __forceinline__ void cnt_arrive(LAS float* lds, int which, int lane) {
  asm volatile("s_waitcnt lgkmcnt(0)" ::: "memory");
  if (lane == 0) __hip_atomic_fetch_add((LAS unsigned*)(lds + L_CNT + which), 1u, __ATOMIC_RELAXED, __HIP_MEMORY_SCOPE_WORKGROUP);
}
template <bool SLEEP = true>
__device__ __forceinline__ void cnt_wait(LAS float* lds, int which, unsigned n) {
  for (unsigned sp = 0; sp < SPIN_CAP; ++sp) {
    if (__builtin_amdgcn_readfirstlane(*(volatile LAS unsigned*)(lds + L_CNT + which)) >= n) break;
    if (SLEEP) __builtin_amdgcn_s_sleep(1);
  }
  REREAD();
}

__global__ void __launch_bounds__(NTHR, 3)
fused_decode_kernel(const u16* __restrict__ x, const u16* __restrict__ Kc, const u16* __restrict__ Vc, const u16* __restrict__ wq,
                    const u16* __restrict__ wo, const u16* __restrict__ wg, const u16* __restrict__ wu, const u16* __restrict__ wd,
                    u16* __restrict__ out, void* ws TUN_ARG) {
  __shared__ __attribute__((aligned(16))) float lds_[L_END];
  LAS float* lds = (LAS float*)lds_;
  const unsigned t0 = (unsigned)__builtin_amdgcn_s_memrealtime();
  const int tid = threadIdx.x, lane = tid & 63, wave = __builtin_amdgcn_readfirstlane(tid >> 6);
  const int r = lane >> 4, c = lane & 15;
  const int bid = blockIdx.x, xl = bid & 7, j = bid >> 3;
  const int h = xl * 4 + (j >> 3), s = j & 7;
  const int a = wave - NMAIN;
  const int f1 = xl * 28 + j, mat = f1 / 112, rb = (f1 / 56) & 1, cb = f1 % 56;
  STAMP(0);

  if (wave < NMAIN) {
    const rsrc_t rk = mkrsrc(Kc, (unsigned)H * SK * DH * 2), rv = mkrsrc(Vc, (unsigned)H * SK * DH * 2);
    u32x4 bA[16], bB[16];
    const unsigned kvbase = (unsigned)h * (8u << 20) + (unsigned)s * (1u << 20) + wave * 1024;
    const unsigned rot = 32u * ((h + (h >> 2) + (h >> 4)) & 3);
    const int kvoff = lane * 16;
    const int STEP = ((int)P_TMAIN << 16) / 256 * PACE_G;
    int due = (int)P_TK0 * 65536 - STEP + (P_STG == 0 ? (STEP / 256) * (wave * 32 + j) : (STEP / 32) * j);
#define PACE(T0, I) do { if ((I) % PACE_G == 0) { due += STEP; pace_wait(T0, due >> 16); } } while (0)
    {
      const rsrc_t rg = mkrsrc(mat ? wu : wg, D * F * 2);
      int g_voff = (j < 28 && !ABL(1)) ? (lane >> 3) * (F * 2) + (lane & 7) * 16 : OOB, g_sb = (rb * 512 + wave * 64) * (F * 2) + cb * 128, g_is = 8 * (F * 2);
      __builtin_amdgcn_s_barrier(); SB();
      const int fstep = ((P_TW2 - P_TF1) << 16) / 8;
      int fdue = P_TF1 * 65536 + (fstep / 256) * (wave * 32 + j);
#pragma unroll
      for (int i = 0; i < 8; ++i) { pace_wait(t0, fdue >> 16); fdue += fstep; bB[i] = ld16_nt(rg, g_voff, g_sb + i * g_is); SB(); }
    }
#pragma unroll
    for (int i = 0; i < 16; ++i) { PACE(t0, i); bA[i] = ld16_nt(rk, kvoff, kvbase + (rot + i) * 8192); SB(); if (i == 0) { STAMP(12); SB(); } }
    {
      cnt_wait(lds, C_XN, 1);
      float ag[8] = {0.f, 0.f, 0.f, 0.f, 0.f, 0.f, 0.f, 0.f};
#pragma unroll
      for (int i = 0; i < 8; ++i) {
        const int k = rb * 512 + wave * 64 + 8 * i + (lane >> 3);
        fma8(ag, lds[L_XN + (k & 3) * 256 + (k >> 2)], bB[i]);
      }
#pragma unroll
      for (int e = 0; e < 8; ++e) ag[e] += dpp<ROR8>(ag[e]);
      const float u0 = fold32(ag[0], ag[4]), u1 = fold32(ag[1], ag[5]), u2 = fold32(ag[2], ag[6]), u3 = fold32(ag[3], ag[7]);
      const f32x2 z = {fold16(u0, u2), fold16(u1, u3)};
      if (c < 8) *(LAS f32x2*)(lds + L_FP + wave * 64 + 8 * c + 2 * r) = z;
      cnt_arrive(lds, C_F1, lane);
      SB();
    }
#pragma unroll
    for (int i = 0; i < 16; ++i) { PACE(t0, i); bB[i] = ld16_nt(rk, kvoff, kvbase + (rot + 16 + i) * 8192); SB(); }
    STAMP(1);
    cnt_wait(lds, C_QR, 1);
    const u32x4 qp = *(LAS u32x4*)(lds + L_QS + 4 * c);
    const unsigned t0m = __builtin_amdgcn_readfirstlane(*(LAS unsigned*)(lds + L_CNT + C_T0));
    STAMP(2);
    constexpr float SCALE = 0.08838834764831845f, LOG2E = 1.4426950408889634f;
    LAS float* scp = lds + L_SC + wave * 512 + lane * 8;
    float mx = -INFINITY;
    auto kgroup = [&](u32x4 (&kb)[16], const bool more, const rsrc_t rn, const unsigned tb, const int g) __attribute__((always_inline)) {
      float sx[16];
#pragma unroll
      for (int i = 0; i < 16; ++i) {
        float d = 0.f;
#pragma unroll
        for (int t = 0; t < 4; ++t) d = dot2(kb[i][t], qp[t], d);
        asm volatile("" : "+v"(d));
        sx[i] = d;
        SB();
        if (more) { PACE(t0m, i); kb[i] = ld16_nt(rn, kvoff, tb + i * 8192); SB(); }
      }
#define TR_STEP(HALF, CTRL, BIT) _Pragma("unroll") for (int hh = 0; hh < (HALF); ++hh) { \
        const float lo_ = sx[hh], hi_ = sx[hh + (HALF)]; const float t1_ = lo_ + dpp<CTRL>(lo_), t2_ = hi_ + dpp<CTRL>(hi_); \
        sx[hh] = (c & (BIT)) ? t2_ : t1_; }
      TR_STEP(8, ROR8, 8) TR_STEP(4, XOR7, 4) TR_STEP(2, XOR2, 2) TR_STEP(1, XOR1, 1)
#undef TR_STEP
      const float sc = rbf(rbf(sx[0]) * SCALE) * LOG2E;
      mx = fmaxf(mx, sc);
      scp[g] = sc;
      SB();
    };
    kgroup(bA, false, rk, 0, 0);
#pragma unroll 1
    for (int g = 1; g < 8; ++g) {
      const bool tov = g == 7;
      kgroup(bB, true, mkrsrc(tov ? Vc : Kc, (unsigned)H * SK * DH * 2), kvbase + (((g + 1) * 16 + rot) & 127u) * 8192, g);
    }
    {
      const float m = wave_max(mx);
      f32x4 p0 = *(LAS f32x4*)scp, p1 = *(LAS f32x4*)(scp + 4);
      float l = 0.f;
#pragma unroll
      for (int e = 0; e < 4; ++e) { p0[e] = exp2_(p0[e] - m); p1[e] = exp2_(p1[e] - m); l += p0[e] + p1[e]; }
      *(LAS f32x4*)scp = p0; *(LAS f32x4*)(scp + 4) = p1;
      l = wave_sum(l);
      if (lane == 0) *(LAS f32x2*)(lds + L_ML + 2 * wave) = f32x2{m, l};
      cnt_arrive(lds, C_ML, lane);
    }
    STAMP(11);
    float acc[8] = {0.f, 0.f, 0.f, 0.f, 0.f, 0.f, 0.f, 0.f};
    auto vgroup = [&](const bool more, const unsigned tb, const float p) __attribute__((always_inline)) {
#define PV_STEP(I) { fma8(acc, dpp<BCAST + (I)>(p), bB[I]); \
        asm volatile("" : "+v"(acc[0]), "+v"(acc[1]), "+v"(acc[2]), "+v"(acc[3]), "+v"(acc[4]), "+v"(acc[5]), "+v"(acc[6]), "+v"(acc[7])); SB(); \
        if (more) { PACE(t0m, I); bB[I] = ld16_nt(rv, kvoff, tb + (I) * 8192); SB(); } }
      PV_STEP(0) PV_STEP(1) PV_STEP(2) PV_STEP(3) PV_STEP(4) PV_STEP(5) PV_STEP(6) PV_STEP(7)
      PV_STEP(8) PV_STEP(9) PV_STEP(10) PV_STEP(11) PV_STEP(12) PV_STEP(13) PV_STEP(14) PV_STEP(15)
#undef PV_STEP
    };
#pragma unroll 1
    for (int g = 0; g < 7; ++g) vgroup(true, kvbase + (((g + 1) * 16 + rot) & 127u) * 8192, scp[g]);
    STAMP(3);
    __builtin_amdgcn_s_setprio(3);
    cnt_wait(lds, C_WG, 1);
    {
      const float wgt = lds[L_WG + wave];
#pragma unroll
      for (int e = 0; e < 8; ++e) acc[e] *= wgt;
      vgroup(false, 0, scp[7] * wgt);
    }
    STAMP(4);
    {
      const float u0 = fold32(acc[0], acc[4]), u1 = fold32(acc[1], acc[5]), u2 = fold32(acc[2], acc[6]), u3 = fold32(acc[3], acc[7]);
      *(LAS f32x2*)(lds + L_MRG + wave * 128 + 8 * c + 2 * r) = f32x2{fold16(u0, u2), fold16(u1, u3)};
    }
    cnt_arrive(lds, C_END, lane);
    STAMP_DUMP();
    return;
  }

  const rsrc_t rx = mkrsrc(x, D * 2), rws = mkrsrc(ws, WS_END);
  unsigned tagA, tagB;
  unsigned long long e1;
  {
    u32x4 xa = ld16(rx, lane * 32, 0), xb = ld16(rx, lane * 32 + 16, 0);
    const u32x2 ep = __builtin_amdgcn_raw_buffer_load_b64(rws, 0, WS_EPOCH, SC1);
    const rsrc_t rwq = mkrsrc(wq, D * H * DH * 2);
    u32x4 w[8];
    const int voff = r * 8192 + c * 16, sb = (s * 128 + a * 32) * 8192 + h * 256;
#pragma unroll
    for (int i = 0; i < 8; ++i) { w[i] = ld16_nt(rwq, voff, sb + i * 32768); SB(); }
    if (a == 0 && lane < C_NUM) *(LAS unsigned*)(lds + L_CNT + lane) = 0u;
    __syncthreads();
    asm volatile("" : "+v"(xa), "+v"(xb));
    STAMP(10);
    float v[16];
#pragma unroll
    for (int i = 0; i < 4; ++i) { v[2 * i] = lo2f(xa[i]); v[2 * i + 1] = hi2f(xa[i]); v[8 + 2 * i] = lo2f(xb[i]); v[9 + 2 * i] = hi2f(xb[i]); }
    float sm = 0.f;
#pragma unroll
    for (int i = 0; i < 16; ++i) sm += v[i];
    const float mean = wave_sum(sm) * (1.f / D);
    float ss = 0.f;
#pragma unroll
    for (int i = 0; i < 16; ++i) { const float d = v[i] - mean; ss = fmaf(d, d, ss); }
    const float var = wave_sum(ss) * (1.f / D);
    const float mb = rbf(mean), den = rbf(sqrtf(rbf(rbf(var) + 1e-5f)));
    const float rinv = 1.0f / den;
#pragma unroll
    for (int i = 0; i < 16; ++i) {
      const float nu = rbf(v[i] - mb), q0 = nu * rinv;
      v[i] = rbf(fmaf(fmaf(-q0, den, nu), rinv, q0));
    }
#pragma unroll
    for (int q = 0; q < 4; ++q) *(LAS f32x4*)(lds + L_XN + q * 256 + 4 * lane) = f32x4{v[q], v[4 + q], v[8 + q], v[12 + q]};
    cnt_arrive(lds, C_XN, lane);
    e1 = (((unsigned long long)__builtin_amdgcn_readfirstlane(ep[1]) << 32) | (unsigned)__builtin_amdgcn_readfirstlane(ep[0])) + 1ull;
    tagA = (unsigned)e1; tagB = (unsigned)(e1 >> 32) + (unsigned)e1 * 0x9E3779B1u;
    float acc[8] = {0.f, 0.f, 0.f, 0.f, 0.f, 0.f, 0.f, 0.f};
#pragma unroll
    for (int q = 0; q < 2; ++q) {
      const f32x4 xv = *(LAS f32x4*)(lds + L_XN + r * 256 + s * 32 + a * 8 + 4 * q);
#pragma unroll
      for (int e = 0; e < 4; ++e) fma8(acc, xv[e], w[4 * q + e]);
    }
    const float u0 = fold32(acc[0], acc[4]), u1 = fold32(acc[1], acc[5]), u2 = fold32(acc[2], acc[6]), u3 = fold32(acc[3], acc[7]);
    *(LAS f32x2*)(lds + L_QP + a * 128 + 8 * c + 2 * r) = f32x2{fold16(u0, u2), fold16(u1, u3)};
  }
  STAMP(1);
  cnt_arrive(lds, C_Q, lane);
  if (a == 0) {
    cnt_wait<false>(lds, C_Q, NAUX);
    STAMP(11);
    f32x2 v = *(LAS f32x2*)(lds + L_QP + 2 * lane);
#pragma unroll
    for (int q = 1; q < 4; ++q) v += *(LAS f32x2*)(lds + L_QP + q * 128 + 2 * lane);
    st16_sc1(rws, lane * 16, WS_Q + (h * 8 + s) * 1024, gran(v[0], v[1], tagA, tagB));
    st16_sc1(rws, (bid == 0 && lane == 0) ? 0 : OOB, WS_T0, u32x4{t0, tagA, t0, tagB});
  }
  if (a == 1) {
    pace_wait(t0, P_TWO);
    float q0 = 0.f, q1 = 0.f;
    unsigned tg = t0;
    for (unsigned sp = 0; sp < SPIN_CAP; ++sp) {
      REREAD();
      u32x4 g[8];
#pragma unroll
      for (int i = 0; i < 8; ++i) g[i] = ld16_sc1(rws, lane * 16, WS_Q + (h * 8 + i) * 1024);
      const u32x4 gt = ld16_sc1(rws, 0, WS_T0);
      bool ok = gok(gt, tagA, tagB);
      tg = gt[0];
      q0 = 0.f; q1 = 0.f;
#pragma unroll
      for (int i = 0; i < 8; ++i) { ok = ok && gok(g[i], tagA, tagB); q0 += __uint_as_float(g[i][0]); q1 += __uint_as_float(g[i][2]); }
      if (__all(ok)) break;
      __builtin_amdgcn_s_sleep(2);
    }
    STAMP(13);
    *(LAS unsigned*)(lds + L_QS + lane) = pack2(q0, q1);
    if (lane == 0) *(LAS unsigned*)(lds + L_CNT + C_T0) = (ABL(12) || (int)(t0 - tg) < 0 || (int)(t0 - tg) > 300) ? t0 : tg;
    cnt_arrive(lds, C_QR, lane);
  }
  const int ks = xl * 2 + (j >> 4), cg = j & 15;
  u32x4 dw[7], wov[8];
  {
    const rsrc_t rd = mkrsrc(wd, D * F * 2), ro = mkrsrc(wo, H * DH * D * 2);
    const int d_voff = ABL(2) ? OOB : (lane >> 3) * 2048 + (lane & 7) * 16, d_sb = (ks * 224 + a * 56) * 2048 + cg * 128;
    const int o_voff = ABL(3) ? OOB : r * 2048 + c * 16, o_sb = (h * 128 + a * 32) * 2048 + s * 256;
    const int wstep = ((P_TK0 - P_TW2) << 16) / 15;
    int wdue = P_TW2 * 65536 + (wstep / 128) * (a * 32 + j);
#pragma unroll
    for (int i = 0; i < 7; ++i) { pace_wait(t0, wdue >> 16); wdue += wstep; dw[i] = ld16_nt(rd, d_voff, d_sb + i * 16384); SB(); }
#pragma unroll
    for (int i = 0; i < 8; ++i) { pace_wait(t0, wdue >> 16); wdue += wstep; wov[i] = ld16_nt(ro, o_voff, o_sb + i * 8192); SB(); }
  }
  STAMP(15);
  if (a == 3 && !ABL(13)) {
    pace_wait(t0, P_TGAP);
    u32x4 p0 = ld16_sc1(rws, lane * 16, WS_PART + (h * 8 + s) * 1024), p1 = ld16_sc1(rws, lane * 16, WS_Y + (h * 8 + s) * 1024);
    asm volatile("" :: "v"(p0), "v"(p1));
  }
  if (a == 0 && j < 28) {
    cnt_wait(lds, C_F1, NMAIN);
    f32x2 v = *(LAS f32x2*)(lds + L_FP + 2 * (lane & 31));
#pragma unroll
    for (int q = 1; q < 8; ++q) v += *(LAS f32x2*)(lds + L_FP + q * 64 + 2 * (lane & 31));
    st16_sc1(rws, lane < 32 ? lane * 16 : OOB, WS_G1 + ((mat * 2 + rb) * F + cb * 64) * 8, gran(v[0], v[1], tagA, tagB));
  }
  STAMP(12);
  const int t_mid = P_TK0 + P_TMAIN / 2;
  u32x4 bfr[2][4];
  {
#pragma unroll
    for (int i = 0; i < 8; ++i) *(LAS u32x4*)(lds + L_WT + (a * 32 + 4 * i + r) * WT_STR + 4 * c) = wov[i];
    cnt_arrive(lds, C_WT, lane);
    cnt_wait(lds, C_WT, NAUX);
#pragma unroll
    for (int q = 0; q < 4; ++q)
#pragma unroll
      for (int d = 0; d < 4; ++d) {
        const unsigned w0 = *(LAS unsigned*)(lds + L_WT + (32 * q + 8 * r + 2 * d) * WT_STR + 16 * a + c);
        const unsigned w1 = *(LAS unsigned*)(lds + L_WT + (32 * q + 8 * r + 2 * d + 1) * WT_STR + 16 * a + c);
        bfr[0][q][d] = __builtin_amdgcn_perm(w1, w0, 0x05040100u);
        bfr[1][q][d] = __builtin_amdgcn_perm(w1, w0, 0x07060302u);
      }
  }
  {
    pace_wait(t0, P_TK0 + 2000);
    u32x4 g[4];
    const int voff = (lane < 28 ? lane : 27) * 16;
    for (unsigned sp = 0; sp < SPIN_CAP; ++sp) {
      REREAD();
#pragma unroll
      for (int k = 0; k < 4; ++k) g[k] = ld16_sc1(rws, voff, WS_G1 + (k * F + ks * 224 + a * 56) * 8);
      if (__all(gok(g[0], tagA, tagB) && gok(g[1], tagA, tagB) && gok(g[2], tagA, tagB) && gok(g[3], tagA, tagB))) break;
      __builtin_amdgcn_s_sleep(32);
    }
    float hh[2];
#pragma unroll
    for (int e = 0; e < 2; ++e) {
      const float gs = rbf(__uint_as_float(g[0][2 * e]) + __uint_as_float(g[1][2 * e]));
      const float us = rbf(__uint_as_float(g[2][2 * e]) + __uint_as_float(g[3][2 * e]));
      const float ge = 0.5f * gs * (1.f + tanhf(0.7978845608028654f * (gs + 0.044715f * gs * gs * gs)));
      hh[e] = rbf(rbf(ge) * us);
    }
    if (lane < 28) *(LAS f32x2*)(lds + L_H + a * 64 + 2 * lane) = f32x2{hh[0], hh[1]};
    float y[8] = {0.f, 0.f, 0.f, 0.f, 0.f, 0.f, 0.f, 0.f};
#pragma unroll
    for (int i = 0; i < 7; ++i) fma8(y, lds[L_H + a * 64 + 8 * i + (lane >> 3)], dw[i]);
#pragma unroll
    for (int e = 0; e < 8; ++e) { y[e] += dpp<ROR8>(y[e]); y[e] = xrow_sum(y[e]); }
    if (lane < 8) {
      *(LAS f32x4*)(lds + L_F2 + a * 64 + lane * 8) = f32x4{y[0], y[1], y[2], y[3]};
      *(LAS f32x4*)(lds + L_F2 + a * 64 + lane * 8 + 4) = f32x4{y[4], y[5], y[6], y[7]};
    }
    cnt_arrive(lds, C_F2, lane);
  }
  STAMP(2);
  const int col0 = s * 128 + 4 * h;
  float f0 = 0.f, f1v = 0.f;
  unsigned xw = 0;
  if (a == 0) {
    cnt_wait(lds, C_F2, NAUX);
    f32x2 v = *(LAS f32x2*)(lds + L_F2 + 2 * (lane & 31));
#pragma unroll
    for (int q = 1; q < 4; ++q) v += *(LAS f32x2*)(lds + L_F2 + q * 64 + 2 * (lane & 31));
    st16_sc1(rws, lane < 32 ? lane * 16 : OOB, WS_FFN + (ks * D + cg * 64) * 8, gran(v[0], v[1], tagA, tagB));
    xw = __builtin_amdgcn_raw_buffer_load_b32(rx, (col0 + 2 * (lane & 1)) * 2, 0, 0);
    pace_wait(t0, P_TK0 + 3500);
    const int voff = ((lane >> 1) & 15) * (D * 8) + (lane & 1) * 16;
    u32x4 g;
    for (unsigned sp = 0; sp < SPIN_CAP; ++sp) {
      REREAD();
      g = ld16_sc1(rws, voff, WS_FFN + col0 * 8);
      if (__all(gok(g, tagA, tagB))) break;
      __builtin_amdgcn_s_sleep(32);
    }
    f0 = rbf(parity_sum(__uint_as_float(g[0])) * 0.5f); f1v = rbf(parity_sum(__uint_as_float(g[2])) * 0.5f);
  }
  if (a == 1) {
    cnt_wait(lds, C_ML, NMAIN);
    const f32x2 ml = *(LAS f32x2*)(lds + L_ML + 2 * (lane & 7));
    const float Mw = max8(ml[0]), Lw = sum8(exp2_(ml[0] - Mw) * ml[1]);
    st16_sc1(rws, lane == 0 ? 0 : OOB, WS_ML + (h * 8 + s) * 128, gran(Mw, Lw, tagA, tagB));
    pace_wait(t0, t_mid + 1500);
    u32x4 g;
    for (unsigned sp = 0; sp < SPIN_CAP; ++sp) {
      REREAD();
      g = ld16_sc1(rws, (lane & 7) * 128, WS_ML + h * 8 * 128);
      if (__all(gok(g, tagA, tagB))) break;
      __builtin_amdgcn_s_sleep(32);
    }
    const float M = max8(__uint_as_float(g[0])), L = sum8(exp2_(__uint_as_float(g[0]) - M) * __uint_as_float(g[2]));
    if (lane < 8) lds[L_WG + lane] = exp2_(ml[0] - M) / L;
    cnt_arrive(lds, C_WG, lane);
  }
  {
    STAMP(3);
    pace_wait(t0, P_TK0 + P_TMAIN - 100);
    if (a == 0) {
      cnt_wait<false>(lds, C_END, NMAIN);
      STAMP(14);
      f32x2 v = *(LAS f32x2*)(lds + L_MRG + 2 * lane);
#pragma unroll
      for (int w = 1; w < 8; ++w) v += *(LAS f32x2*)(lds + L_MRG + w * 128 + 2 * lane);
      st16_sc1(rws, lane * 16, WS_PART + (h * 8 + s) * 1024, gran(v[0], v[1], tagA, tagB));
    } else {
      cnt_wait(lds, C_END, NMAIN);
    }
    STAMP(4);
    STAMP(5);
    u32x4 g0, g1;
    const int vo = r * 1024 + (16 * a + c) * 16;
    for (unsigned sp = 0; sp < SPIN_CAP; ++sp) {
      REREAD();
      g0 = ld16_sc1(rws, vo, WS_PART + h * 8 * 1024);
      g1 = ld16_sc1(rws, vo, WS_PART + (h * 8 + 4) * 1024);
      if (__all(gok(g0, tagA, tagB) && gok(g1, tagA, tagB))) break;
    }
    STAMP(6);
    {
      float o = fold32(__uint_as_float(g0[0]) + __uint_as_float(g1[0]), __uint_as_float(g0[2]) + __uint_as_float(g1[2]));
      o = fold16(o, o);
      if ((r & 1) == 0) *((LAS u16*)(lds + L_OB) + 32 * a + 2 * c + (r >> 1)) = __builtin_bit_cast(u16, (__bf16)o);
    }
    __syncthreads();
    f32x4 y0 = {0.f, 0.f, 0.f, 0.f}, y1 = y0;
#pragma unroll
    for (int q = 0; q < 4; ++q) {
      const u32x4 af = *(LAS u32x4*)(lds + L_OB + 16 * q + 4 * r);
      y0 = __builtin_amdgcn_mfma_f32_16x16x32_bf16(__builtin_bit_cast(bf16x8, af), __builtin_bit_cast(bf16x8, bfr[0][q]), y0, 0, 0, 0);
      y1 = __builtin_amdgcn_mfma_f32_16x16x32_bf16(__builtin_bit_cast(bf16x8, af), __builtin_bit_cast(bf16x8, bfr[1][q]), y1, 0, 0, 0);
    }
    st16_sc1(rws, lane < 16 ? (16 * a + lane) * 16 : OOB, WS_Y + (h * 8 + s) * 1024, gran(y0[0], y1[0], tagA, tagB));
    STAMP(7);
    if (a == 0) {
      u32x4 g;
      const int voff = (lane >> 1) * (8 * 1024) + (lane & 1) * 16;
      for (unsigned sp = 0; sp < SPIN_CAP; ++sp) {
        REREAD();
        g = ld16_sc1(rws, voff, WS_Y + s * 1024 + h * 32);
        if (__all(gok(g, tagA, tagB))) break;
      }
      STAMP(8);
      const float a0 = rbf(parity_sum(__uint_as_float(g[0]))), a1 = rbf(parity_sum(__uint_as_float(g[2])));
      const unsigned o = pack2(rbf(lo2f(xw) + a0) + f0, rbf(hi2f(xw) + a1) + f1v);
      __builtin_amdgcn_raw_buffer_store_b32(o, mkrsrc(out, D * 2), lane < 2 ? (col0 + 2 * lane) * 2 : OOB, 0, 0);
      __builtin_amdgcn_raw_buffer_store_b64(u32x2{(unsigned)e1, (unsigned)(e1 >> 32)}, rws, (bid == 0 && lane == 0) ? 0 : OOB, WS_EPOCH, SC1);
    }
  }
  if (a != 0) { STAMP_DUMP(); return; }
  STAMP(9);
  STAMP_DUMP();
}

extern "C" void kernel_launch(void* const* d_in, const int* in_sizes, int n_in, void* d_out, int out_size, void* d_ws, size_t ws_size,
                              hipStream_t stream) {
  fused_decode_kernel<<<256, NTHR, 0, stream>>>((const u16*)d_in[0], (const u16*)d_in[1], (const u16*)d_in[2], (const u16*)d_in[3],
                                                 (const u16*)d_in[4], (const u16*)d_in[6], (const u16*)d_in[7], (const u16*)d_in[8],
                                                 (u16*)d_out, d_ws);
}
```

```cpp
#define T_MAIN 7150
#define T_K0 570
#define T_W2 360
#define T_F1 150
#define PACE_G 4
#define PHASE ((wave >> 1) * 64 + j * 2)
#include <hip/hip_runtime.h>
#include <stdint.h>

typedef unsigned u32x4 __attribute__((ext_vector_type(4)));
typedef unsigned u32x2 __attribute__((ext_vector_type(2)));
typedef float f32x4 __attribute__((ext_vector_type(4)));
typedef float f32x2 __attribute__((ext_vector_type(2)));
typedef __bf16 bf16x2 __attribute__((ext_vector_type(2)));
typedef __bf16 bf16x8 __attribute__((ext_vector_type(8)));
typedef unsigned short u16;
#define LAS __attribute__((address_space(3)))
typedef __amdgpu_buffer_rsrc_t rsrc_t;

constexpr int T_QPOLL = 390, T_TOUCH = 1000;

constexpr int D = 1024, H = 32, SK = 32768, DH = 128, F = 3584;
constexpr int NMAIN = 8, NAUX = 4, NTHR = (NMAIN + NAUX) * 64;
constexpr int NT = 2, SC1 = 16;
constexpr int OOB = 0x40000000;
constexpr unsigned WS_EPOCH = 0, WS_T0 = 1024, WS_ML = 4096, WS_Q = WS_ML + 256 * 128, WS_Y = WS_Q + 256 * 1024, WS_G1 = WS_Y + 256 * 1024,
                   WS_FFN = WS_G1 + 4 * F * 8, WS_END = WS_FFN + 16 * D * 8;
constexpr int L_XN = 0, L_QP = 1024, L_QS = L_QP + 4 * 128, L_MRG = L_QS + 64, L_ML = L_MRG + 8 * 128, L_WG = L_ML + 16, L_FP = L_WG + 8,
              L_H = L_FP + 8 * 64, L_F2 = L_H + 4 * 64, L_OB = L_F2 + 4 * 64, L_CNT = L_OB + 64, L_SC = L_CNT + 16, L_WT = L_SC + 8 * 512,
              WT_STR = 68, L_END = L_WT + 128 * WT_STR;
constexpr int C_Q = 0, C_XN = 1, C_F2 = 2, C_WT = 3, C_ML = 4, C_WG = 5, C_END = 6, C_QR = 7, C_F1 = 8, C_NUM = 9, W_T0 = 12;
constexpr unsigned SPIN_MEM = 1u << 18, SPIN_LDS = 1u << 22;

__device__ __forceinline__ float lo2f(unsigned w) { return __builtin_bit_cast(float, w << 16); }
__device__ __forceinline__ float hi2f(unsigned w) { return __builtin_bit_cast(float, w & 0xffff0000u); }
__device__ __forceinline__ float rbf(float x) { return (float)(__bf16)x; }
__device__ __forceinline__ unsigned pack2(float a, float b) { bf16x2 v = {(__bf16)a, (__bf16)b}; return __builtin_bit_cast(unsigned, v); }
__device__ __forceinline__ float dot2(unsigned k, unsigned q, float acc) {
  return __builtin_amdgcn_fdot2_f32_bf16(__builtin_bit_cast(bf16x2, k), __builtin_bit_cast(bf16x2, q), acc, false);
}
template <int CTRL> __device__ __forceinline__ float dpp(float x) {
  return __builtin_bit_cast(float, __builtin_amdgcn_mov_dpp(__builtin_bit_cast(int, x), CTRL, 0xf, 0xf, true));
}
constexpr int XOR1 = 0xB1, XOR2 = 0x4E, XOR7 = 0x141, ROR8 = 0x128, ROR4 = 0x124, BCAST = 0x150;
__device__ __forceinline__ float fold32(float a, float b) {
  auto t = __builtin_amdgcn_permlane32_swap(__float_as_uint(a), __float_as_uint(b), false, false);
  return __uint_as_float(t[0]) + __uint_as_float(t[1]);
}
__device__ __forceinline__ float fold16(float a, float b) {
  auto t = __builtin_amdgcn_permlane16_swap(__float_as_uint(a), __float_as_uint(b), false, false);
  return __uint_as_float(t[0]) + __uint_as_float(t[1]);
}
__device__ __forceinline__ f32x2 fold_rows(const float (&v)[8]) {
  const float u0 = fold32(v[0], v[4]), u1 = fold32(v[1], v[5]), u2 = fold32(v[2], v[6]), u3 = fold32(v[3], v[7]);
  return f32x2{fold16(u0, u2), fold16(u1, u3)};
}
__device__ __forceinline__ float xrow_sum(float x) { x = fold16(x, x); return fold32(x, x); }
__device__ __forceinline__ float xrow_max(float x) {
  auto s = __builtin_amdgcn_permlane16_swap(__float_as_uint(x), __float_as_uint(x), false, false);
  x = fmaxf(__uint_as_float(s[0]), __uint_as_float(s[1]));
  auto t = __builtin_amdgcn_permlane32_swap(__float_as_uint(x), __float_as_uint(x), false, false);
  return fmaxf(__uint_as_float(t[0]), __uint_as_float(t[1]));
}
__device__ __forceinline__ float parity_sum(float x) {
  x += dpp<XOR2>(x); x += dpp<ROR4>(x); x += dpp<ROR8>(x); return xrow_sum(x);
}
__device__ __forceinline__ float wave_sum(float x) { x += dpp<XOR1>(x); return parity_sum(x); }
__device__ __forceinline__ float max8(float x) { x = fmaxf(x, dpp<XOR1>(x)); x = fmaxf(x, dpp<XOR2>(x)); return fmaxf(x, dpp<XOR7>(x)); }
__device__ __forceinline__ float sum8(float x) { x += dpp<XOR1>(x); x += dpp<XOR2>(x); return x + dpp<XOR7>(x); }
__device__ __forceinline__ float wave_max(float x) { x = max8(x); x = fmaxf(x, dpp<ROR8>(x)); return xrow_max(x); }
__device__ __forceinline__ float exp2_(float x) { return __builtin_amdgcn_exp2f(x); }
__device__ __forceinline__ rsrc_t mkrsrc(const void* p, unsigned bytes) { return __builtin_amdgcn_make_buffer_rsrc((void*)p, 0, (int)bytes, 0x00020000); }
__device__ __forceinline__ u32x4 ld16(rsrc_t r, int voff, int soff) { return __builtin_amdgcn_raw_buffer_load_b128(r, voff, soff, 0); }
__device__ __forceinline__ u32x4 ld16_nt(rsrc_t r, int voff, int soff) { return __builtin_amdgcn_raw_buffer_load_b128(r, voff, soff, NT); }
__device__ __forceinline__ u32x4 ld16_sc1(rsrc_t r, int voff, int soff) { return __builtin_amdgcn_raw_buffer_load_b128(r, voff, soff, SC1); }
__device__ __forceinline__ void st16_sc1(rsrc_t r, int off, u32x4 v) { __builtin_amdgcn_raw_buffer_store_b128(v, r, off, 0, SC1); }
#define SB() __builtin_amdgcn_sched_barrier(0)
#define REREAD() asm volatile("" ::: "memory")
__device__ __forceinline__ void pace_wait(unsigned t0, int due) {
  asm volatile("1:\n\ts_memrealtime s[96:97]\n\ts_waitcnt lgkmcnt(0)\n\ts_sub_u32 s96, s96, %0\n\ts_cmp_lt_i32 s96, %1\n\t"
               "s_cbranch_scc0 2f\n\ts_sleep 1\n\ts_branch 1b\n2:"
               :: "s"(t0), "s"(due) : "s96", "s97", "scc", "memory");
}
__device__ __forceinline__ void fma8(float (&acc)[8], float s, u32x4 w) {
#pragma unroll
  for (int t = 0; t < 4; ++t) { acc[2 * t] = fmaf(s, lo2f(w[t]), acc[2 * t]); acc[2 * t + 1] = fmaf(s, hi2f(w[t]), acc[2 * t + 1]); }
}
__device__ __forceinline__ u32x4 rec(float a, float b, unsigned ta, unsigned tb) { return u32x4{__float_as_uint(a), ta, __float_as_uint(b), tb}; }
__device__ __forceinline__ bool rok(u32x4 g, unsigned ta, unsigned tb) { return g[1] == ta && g[3] == tb; }
__device__ __forceinline__ float r0(u32x4 g) { return __uint_as_float(g[0]); }
__device__ __forceinline__ float r1(u32x4 g) { return __uint_as_float(g[2]); }

__device__ __forceinline__ void cnt_arrive(LAS float* lds, int which, int lane) {
  asm volatile("s_waitcnt lgkmcnt(0)" ::: "memory");
  if (lane == 0) __hip_atomic_fetch_add((LAS unsigned*)(lds + L_CNT + which), 1u, __ATOMIC_RELAXED, __HIP_MEMORY_SCOPE_WORKGROUP);
}
template <bool SLEEP = true>
__device__ __forceinline__ void cnt_wait(LAS float* lds, int which, unsigned n) {
#pragma unroll 1
  for (unsigned sp = 0; sp < SPIN_LDS; ++sp) {
    if (__builtin_amdgcn_readfirstlane(*(volatile LAS unsigned*)(lds + L_CNT + which)) >= n) break;
    if (SLEEP) __builtin_amdgcn_s_sleep(1);
  }
  REREAD();
}

__global__ void __launch_bounds__(NTHR, 3)
fused_decode_kernel(const u16* __restrict__ x, const u16* __restrict__ Kc, const u16* __restrict__ Vc, const u16* __restrict__ wq,
                    const u16* __restrict__ wo, const u16* __restrict__ wg, const u16* __restrict__ wu, const u16* __restrict__ wd,
                    u16* __restrict__ out, void* ws) {
  __shared__ __attribute__((aligned(16))) float lds_[L_END];
  LAS float* lds = (LAS float*)lds_;
  const unsigned t0 = (unsigned)__builtin_amdgcn_s_memrealtime();
  const int tid = threadIdx.x, lane = tid & 63, wave = __builtin_amdgcn_readfirstlane(tid >> 6);
  const int r = lane >> 4, c = lane & 15;
  const int bid = blockIdx.x, xl = bid & 7, j = bid >> 3;
  const int h = xl * 4 + (j >> 3), s = j & 7;
  const int f1 = xl * 28 + j, mat = f1 / 112, rb = (f1 / 56) & 1, cb = f1 % 56;

  if (wave < NMAIN) {
    const rsrc_t rk = mkrsrc(Kc, (unsigned)H * SK * DH * 2), rv = mkrsrc(Vc, (unsigned)H * SK * DH * 2);
    u32x4 bA[16], bB[16];
    const unsigned kvbase = (unsigned)h * (8u << 20) + (unsigned)s * (1u << 20) + wave * 1024;
    const unsigned rot = 32u * ((h + (h >> 2) + (h >> 4)) & 3);
    const int kvoff = lane * 16;
    constexpr int STEP = (T_MAIN << 16) / 256 * PACE_G;
    int due = T_K0 * 65536 - STEP + (STEP / 256) * (PHASE);
#define PACE(T0, I) do { if ((I) % PACE_G == 0) { due += STEP; pace_wait(T0, due >> 16); } } while (0)
    __builtin_amdgcn_s_barrier();
    SB();
    {
      const rsrc_t rg = mkrsrc(mat ? wu : wg, D * F * 2);
      const int voff = j < 28 ? (lane >> 3) * (F * 2) + (lane & 7) * 16 : OOB, sb = (rb * 512 + wave * 64) * (F * 2) + cb * 128;
      constexpr int FSTEP = ((T_W2 - T_F1) << 16) / 8;
      int fdue = T_F1 * 65536 + (FSTEP / 256) * (wave * 32 + j);
#pragma unroll
      for (int i = 0; i < 8; ++i) { pace_wait(t0, fdue >> 16); fdue += FSTEP; bB[i] = ld16_nt(rg, voff, sb + i * 8 * (F * 2)); SB(); }
    }
#pragma unroll
    for (int i = 0; i < 16; ++i) { PACE(t0, i); bA[i] = ld16_nt(rk, kvoff, kvbase + (rot + i) * 8192); SB(); }
    {
      cnt_wait(lds, C_XN, 1);
      float ag[8] = {0.f, 0.f, 0.f, 0.f, 0.f, 0.f, 0.f, 0.f};
#pragma unroll
      for (int i = 0; i < 8; ++i) {
        const int k = rb * 512 + wave * 64 + 8 * i + (lane >> 3);
        fma8(ag, lds[L_XN + (k & 3) * 256 + (k >> 2)], bB[i]);
      }
#pragma unroll
      for (int e = 0; e < 8; ++e) ag[e] += dpp<ROR8>(ag[e]);
      const f32x2 z = fold_rows(ag);
      if (c < 8) *(LAS f32x2*)(lds + L_FP + wave * 64 + 8 * c + 2 * r) = z;
      cnt_arrive(lds, C_F1, lane);
      SB();
    }
#pragma unroll
    for (int i = 0; i < 16; ++i) { PACE(t0, i); bB[i] = ld16_nt(rk, kvoff, kvbase + (rot + 16 + i) * 8192); SB(); }
    cnt_wait(lds, C_QR, 1);
    const u32x4 qp = *(LAS u32x4*)(lds + L_QS + 4 * c);
    const unsigned t0m = __builtin_amdgcn_readfirstlane(*(LAS unsigned*)(lds + L_CNT + W_T0));
    constexpr float SCALE = 0.08838834764831845f, LOG2E = 1.4426950408889634f;
    LAS float* scp = lds + L_SC + wave * 512 + lane * 8;
    float mx = -INFINITY;
    auto kgroup = [&](u32x4 (&kb)[16], const bool more, const rsrc_t rn, const unsigned tb, const int g) __attribute__((always_inline)) {
      float sx[16];
#pragma unroll
      for (int i = 0; i < 16; ++i) {
        float d = 0.f;
#pragma unroll
        for (int t = 0; t < 4; ++t) d = dot2(kb[i][t], qp[t], d);
        asm volatile("" : "+v"(d));
        sx[i] = d;
        SB();
        if (more) { PACE(t0m, i); kb[i] = ld16_nt(rn, kvoff, tb + i * 8192); SB(); }
      }
#define TR_STEP(HALF, CTRL, BIT) _Pragma("unroll") for (int hh = 0; hh < (HALF); ++hh) { \
        const float lo_ = sx[hh], hi_ = sx[hh + (HALF)]; const float t1_ = lo_ + dpp<CTRL>(lo_), t2_ = hi_ + dpp<CTRL>(hi_); \
        sx[hh] = (c & (BIT)) ? t2_ : t1_; }
      TR_STEP(8, ROR8, 8) TR_STEP(4, XOR7, 4) TR_STEP(2, XOR2, 2) TR_STEP(1, XOR1, 1)
#undef TR_STEP
      const float sc = rbf(rbf(sx[0]) * SCALE) * LOG2E;
      mx = fmaxf(mx, sc);
      scp[g] = sc;
      SB();
    };
    kgroup(bA, false, rk, 0, 0);
#pragma unroll 1
    for (int g = 1; g < 8; ++g)
      kgroup(bB, true, mkrsrc(g == 7 ? Vc : Kc, (unsigned)H * SK * DH * 2), kvbase + (((g + 1) * 16 + rot) & 127u) * 8192, g);
    {
      const float m = wave_max(mx);
      f32x4 p0 = *(LAS f32x4*)scp, p1 = *(LAS f32x4*)(scp + 4);
      float l = 0.f;
#pragma unroll
      for (int e = 0; e < 4; ++e) { p0[e] = exp2_(p0[e] - m); p1[e] = exp2_(p1[e] - m); l += p0[e] + p1[e]; }
      *(LAS f32x4*)scp = p0; *(LAS f32x4*)(scp + 4) = p1;
      l = wave_sum(l);
      if (lane == 0) *(LAS f32x2*)(lds + L_ML + 2 * wave) = f32x2{m, l};
      cnt_arrive(lds, C_ML, lane);
    }
    float acc[8] = {0.f, 0.f, 0.f, 0.f, 0.f, 0.f, 0.f, 0.f};
    auto vgroup = [&](const bool more, const unsigned tb, const float p) __attribute__((always_inline)) {
#define PV_STEP(I) { fma8(acc, dpp<BCAST + (I)>(p), bB[I]); \
        asm volatile("" : "+v"(acc[0]), "+v"(acc[1]), "+v"(acc[2]), "+v"(acc[3]), "+v"(acc[4]), "+v"(acc[5]), "+v"(acc[6]), "+v"(acc[7])); SB(); \
        if (more) { PACE(t0m, I); bB[I] = ld16_nt(rv, kvoff, tb + (I) * 8192); SB(); } }
      PV_STEP(0) PV_STEP(1) PV_STEP(2) PV_STEP(3) PV_STEP(4) PV_STEP(5) PV_STEP(6) PV_STEP(7)
      PV_STEP(8) PV_STEP(9) PV_STEP(10) PV_STEP(11) PV_STEP(12) PV_STEP(13) PV_STEP(14) PV_STEP(15)
#undef PV_STEP
    };
#pragma unroll 1
    for (int g = 0; g < 7; ++g) vgroup(true, kvbase + (((g + 1) * 16 + rot) & 127u) * 8192, scp[g]);
    __builtin_amdgcn_s_setprio(3);
    cnt_wait(lds, C_WG, 1);
    {
      const float wgt = lds[L_WG + wave];
#pragma unroll
      for (int e = 0; e < 8; ++e) acc[e] *= wgt;
      vgroup(false, 0, scp[7] * wgt);
    }
    *(LAS f32x2*)(lds + L_MRG + wave * 128 + 8 * c + 2 * r) = fold_rows(acc);
    cnt_arrive(lds, C_END, lane);
    return;
#undef PACE
  }

  const int a = wave - NMAIN;
  const rsrc_t rx = mkrsrc(x, D * 2), rws = mkrsrc(ws, WS_END);
  unsigned tagA, tagB, tagC, tagD;
  unsigned long long e2;
  {
    u32x4 xa = ld16(rx, lane * 32, 0), xb = ld16(rx, lane * 32 + 16, 0);
    const u32x2 ep = __builtin_amdgcn_raw_buffer_load_b64(rws, 0, WS_EPOCH, SC1);
    const rsrc_t rwq = mkrsrc(wq, D * H * DH * 2);
    u32x4 w[8];
    const int voff = r * 8192 + c * 16, sb = (s * 128 + a * 32) * 8192 + h * 256;
#pragma unroll
    for (int i = 0; i < 8; ++i) { w[i] = ld16_nt(rwq, voff, sb + i * 32768); SB(); }
    if (a == 0 && lane < C_NUM) *(LAS unsigned*)(lds + L_CNT + lane) = 0u;
    __syncthreads();
    asm volatile("" : "+v"(xa), "+v"(xb));
    float v[16];
#pragma unroll
    for (int i = 0; i < 4; ++i) { v[2 * i] = lo2f(xa[i]); v[2 * i + 1] = hi2f(xa[i]); v[8 + 2 * i] = lo2f(xb[i]); v[9 + 2 * i] = hi2f(xb[i]); }
    float sm = 0.f;
#pragma unroll
    for (int i = 0; i < 16; ++i) sm += v[i];
    const float mean = wave_sum(sm) * (1.f / D);
    float ss = 0.f;
#pragma unroll
    for (int i = 0; i < 16; ++i) { const float d = v[i] - mean; ss = fmaf(d, d, ss); }
    const float var = wave_sum(ss) * (1.f / D);
    const float mb = rbf(mean), den = rbf(sqrtf(rbf(rbf(var) + 1e-5f)));
    const float rinv = 1.0f / den;
#pragma unroll
    for (int i = 0; i < 16; ++i) {
      const float nu = rbf(v[i] - mb), q0 = nu * rinv;
      v[i] = rbf(fmaf(fmaf(-q0, den, nu), rinv, q0));
    }
#pragma unroll
    for (int q = 0; q < 4; ++q) *(LAS f32x4*)(lds + L_XN + q * 256 + 4 * lane) = f32x4{v[q], v[4 + q], v[8 + q], v[12 + q]};
    cnt_arrive(lds, C_XN, lane);
    const unsigned long long e1 = (((unsigned long long)__builtin_amdgcn_readfirstlane(ep[1]) << 32) | (unsigned)__builtin_amdgcn_readfirstlane(ep[0])) + 1ull;
    e2 = e1 + 1ull;
    tagA = (unsigned)e1; tagB = (unsigned)(e1 >> 32) + (unsigned)e1 * 0x9E3779B1u;
    tagC = (unsigned)e2; tagD = (unsigned)(e2 >> 32) + (unsigned)e2 * 0x9E3779B1u;
    float acc[8] = {0.f, 0.f, 0.f, 0.f, 0.f, 0.f, 0.f, 0.f};
#pragma unroll
    for (int q = 0; q < 2; ++q) {
      const f32x4 xv = *(LAS f32x4*)(lds + L_XN + r * 256 + s * 32 + a * 8 + 4 * q);
#pragma unroll
      for (int e = 0; e < 4; ++e) fma8(acc, xv[e], w[4 * q + e]);
    }
    *(LAS f32x2*)(lds + L_QP + a * 128 + 8 * c + 2 * r) = fold_rows(acc);
  }
  cnt_arrive(lds, C_Q, lane);
  if (a == 0) {
    cnt_wait<false>(lds, C_Q, NAUX);
    f32x2 v = *(LAS f32x2*)(lds + L_QP + 2 * lane);
#pragma unroll
    for (int q = 1; q < 4; ++q) v += *(LAS f32x2*)(lds + L_QP + q * 128 + 2 * lane);
    st16_sc1(rws, WS_Q + (h * 8 + s) * 1024 + lane * 16, rec(v[0], v[1], tagA, tagB));
    st16_sc1(rws, (bid == 0 && lane == 0) ? WS_T0 : OOB, u32x4{t0, tagA, t0, tagB});
  }
  if (a == 1) {
    pace_wait(t0, T_QPOLL);
    float q0 = 0.f, q1 = 0.f;
    unsigned tg = t0;
#pragma unroll 1
    for (unsigned sp = 0; sp < SPIN_MEM; ++sp) {
      REREAD();
      u32x4 g[8];
#pragma unroll
      for (int i = 0; i < 8; ++i) g[i] = ld16_sc1(rws, lane * 16, WS_Q + (h * 8 + i) * 1024);
      const u32x4 gt = ld16_sc1(rws, 0, WS_T0);
      bool ok = rok(gt, tagA, tagB);
      tg = gt[0];
      q0 = 0.f; q1 = 0.f;
#pragma unroll
      for (int i = 0; i < 8; ++i) { ok = ok && rok(g[i], tagA, tagB); q0 += r0(g[i]); q1 += r1(g[i]); }
      if (__all(ok)) break;
      __builtin_amdgcn_s_sleep(2);
    }
    *(LAS unsigned*)(lds + L_QS + lane) = pack2(q0, q1);
    if (lane == 0) *(LAS unsigned*)(lds + L_CNT + W_T0) = ((int)(t0 - tg) < 0 || (int)(t0 - tg) > 300) ? t0 : tg;
    cnt_arrive(lds, C_QR, lane);
  }
  const int ks = xl * 2 + (j >> 4), cg = j & 15;
  u32x4 dw[7], wov[8];
  {
    const rsrc_t rd = mkrsrc(wd, D * F * 2), ro = mkrsrc(wo, H * DH * D * 2);
    const int d_voff = (lane >> 3) * 2048 + (lane & 7) * 16, d_sb = (ks * 224 + a * 56) * 2048 + cg * 128;
    const int o_voff = r * 2048 + c * 16, o_sb = (h * 128 + a * 32) * 2048 + s * 256;
    constexpr int WSTEP = ((T_K0 - T_W2) << 16) / 15;
    int wdue = T_W2 * 65536 + (WSTEP / 128) * (a * 32 + j);
#pragma unroll
    for (int i = 0; i < 7; ++i) { pace_wait(t0, wdue >> 16); wdue += WSTEP; dw[i] = ld16_nt(rd, d_voff, d_sb + i * 16384); SB(); }
#pragma unroll
    for (int i = 0; i < 8; ++i) { pace_wait(t0, wdue >> 16); wdue += WSTEP; wov[i] = ld16_nt(ro, o_voff, o_sb + i * 8192); SB(); }
  }
  if (a == 3) {
    pace_wait(t0, T_TOUCH);
    const u32x4 p = ld16_sc1(rws, lane * 16, WS_Y + (h * 8 + s) * 1024);
    asm volatile("" :: "v"(p));
  }
  if (a == 0 && j < 28) {
    cnt_wait(lds, C_F1, NMAIN);
    f32x2 v = *(LAS f32x2*)(lds + L_FP + 2 * (lane & 31));
#pragma unroll
    for (int q = 1; q < 8; ++q) v += *(LAS f32x2*)(lds + L_FP + q * 64 + 2 * (lane & 31));
    st16_sc1(rws, lane < 32 ? WS_G1 + ((mat * 2 + rb) * F + cb * 64) * 8 + lane * 16 : OOB, rec(v[0], v[1], tagA, tagB));
  }
  u32x4 bfr[2][4];
  {
#pragma unroll
    for (int i = 0; i < 8; ++i) *(LAS u32x4*)(lds + L_WT + (a * 32 + 4 * i + r) * WT_STR + 4 * c) = wov[i];
    cnt_arrive(lds, C_WT, lane);
    cnt_wait(lds, C_WT, NAUX);
#pragma unroll
    for (int q = 0; q < 4; ++q)
#pragma unroll
      for (int d = 0; d < 4; ++d) {
        const unsigned w0 = *(LAS unsigned*)(lds + L_WT + (32 * q + 8 * r + 2 * d) * WT_STR + 16 * a + c);
        const unsigned w1 = *(LAS unsigned*)(lds + L_WT + (32 * q + 8 * r + 2 * d + 1) * WT_STR + 16 * a + c);
        bfr[0][q][d] = __builtin_amdgcn_perm(w1, w0, 0x05040100u);
        bfr[1][q][d] = __builtin_amdgcn_perm(w1, w0, 0x07060302u);
      }
  }
  {
    pace_wait(t0, T_K0 + 2000);
    u32x4 g[4];
    const int voff = (lane < 28 ? lane : 27) * 16;
#pragma unroll 1
    for (unsigned sp = 0; sp < SPIN_MEM; ++sp) {
      REREAD();
#pragma unroll
      for (int k = 0; k < 4; ++k) g[k] = ld16_sc1(rws, voff, WS_G1 + (k * F + ks * 224 + a * 56) * 8);
      if (__all(rok(g[0], tagA, tagB) && rok(g[1], tagA, tagB) && rok(g[2], tagA, tagB) && rok(g[3], tagA, tagB))) break;
      __builtin_amdgcn_s_sleep(32);
    }
    float hh[2];
#pragma unroll
    for (int e = 0; e < 2; ++e) {
      const float gs = rbf(__uint_as_float(g[0][2 * e]) + __uint_as_float(g[1][2 * e]));
      const float us = rbf(__uint_as_float(g[2][2 * e]) + __uint_as_float(g[3][2 * e]));
      const float ge = 0.5f * gs * (1.f + tanhf(0.7978845608028654f * (gs + 0.044715f * gs * gs * gs)));
      hh[e] = rbf(rbf(ge) * us);
    }
    if (lane < 28) *(LAS f32x2*)(lds + L_H + a * 64 + 2 * lane) = f32x2{hh[0], hh[1]};
    float y[8] = {0.f, 0.f, 0.f, 0.f, 0.f, 0.f, 0.f, 0.f};
#pragma unroll
    for (int i = 0; i < 7; ++i) fma8(y, lds[L_H + a * 64 + 8 * i + (lane >> 3)], dw[i]);
#pragma unroll
    for (int e = 0; e < 8; ++e) y[e] += dpp<ROR8>(y[e]);
    const f32x2 z = fold_rows(y);
    if (c < 8) *(LAS f32x2*)(lds + L_F2 + a * 64 + 8 * c + 2 * r) = z;
    cnt_arrive(lds, C_F2, lane);
  }
  const int col0 = s * 128 + 4 * h;
  float f0 = 0.f, f1v = 0.f;
  unsigned xw = 0;
  if (a == 0) {
    cnt_wait(lds, C_F2, NAUX);
    f32x2 v = *(LAS f32x2*)(lds + L_F2 + 2 * (lane & 31));
#pragma unroll
    for (int q = 1; q < 4; ++q) v += *(LAS f32x2*)(lds + L_F2 + q * 64 + 2 * (lane & 31));
    st16_sc1(rws, lane < 32 ? WS_FFN + (ks * D + cg * 64) * 8 + lane * 16 : OOB, rec(v[0], v[1], tagA, tagB));
    xw = __builtin_amdgcn_raw_buffer_load_b32(rx, (col0 + 2 * (lane & 1)) * 2, 0, 0);
    pace_wait(t0, T_K0 + 3500);
    u32x4 g;
#pragma unroll 1
    for (unsigned sp = 0; sp < SPIN_MEM; ++sp) {
      REREAD();
      g = ld16_sc1(rws, ((lane >> 1) & 15) * (D * 8) + (lane & 1) * 16, WS_FFN + col0 * 8);
      if (__all(rok(g, tagA, tagB))) break;
      __builtin_amdgcn_s_sleep(32);
    }
    f0 = rbf(parity_sum(r0(g)) * 0.5f); f1v = rbf(parity_sum(r1(g)) * 0.5f);
  }
  if (a == 1) {
    cnt_wait(lds, C_ML, NMAIN);
    const f32x2 ml = *(LAS f32x2*)(lds + L_ML + 2 * (lane & 7));
    const float Mw = max8(ml[0]), Lw = sum8(exp2_(ml[0] - Mw) * ml[1]);
    st16_sc1(rws, lane == 0 ? WS_ML + (h * 8 + s) * 128 : OOB, rec(Mw, Lw, tagA, tagB));
    pace_wait(t0, T_K0 + T_MAIN / 2 + 1500);
    u32x4 g;
#pragma unroll 1
    for (unsigned sp = 0; sp < SPIN_MEM; ++sp) {
      REREAD();
      g = ld16_sc1(rws, (lane & 7) * 128, WS_ML + h * 8 * 128);
      if (__all(rok(g, tagA, tagB))) break;
      __builtin_amdgcn_s_sleep(32);
    }
    const float M = max8(r0(g)), L = sum8(exp2_(r0(g) - M) * r1(g));
    if (lane < 8) lds[L_WG + lane] = exp2_(ml[0] - M) / L;
    cnt_arrive(lds, C_WG, lane);
  }
  pace_wait(t0, T_K0 + T_MAIN - 100);
  if (a == 0) {
    cnt_wait<false>(lds, C_END, NMAIN);
    f32x2 v = *(LAS f32x2*)(lds + L_MRG + 2 * lane);
#pragma unroll
    for (int w = 1; w < 8; ++w) v += *(LAS f32x2*)(lds + L_MRG + w * 128 + 2 * lane);
    st16_sc1(rws, WS_Q + (h * 8 + s) * 1024 + lane * 16, rec(v[0], v[1], tagC, tagD));
  } else {
    cnt_wait(lds, C_END, NMAIN);
  }
  {
    u32x4 g0, g1;
    const int vo = r * 1024 + (16 * a + c) * 16;
#pragma unroll 1
    for (unsigned sp = 0; sp < SPIN_MEM; ++sp) {
      REREAD();
      g0 = ld16_sc1(rws, vo, WS_Q + h * 8 * 1024);
      g1 = ld16_sc1(rws, vo, WS_Q + (h * 8 + 4) * 1024);
      if (__all(rok(g0, tagC, tagD) && rok(g1, tagC, tagD))) break;
    }
    float o = fold32(r0(g0) + r0(g1), r1(g0) + r1(g1));
    o = fold16(o, o);
    if ((r & 1) == 0) *((LAS u16*)(lds + L_OB) + 32 * a + 2 * c + (r >> 1)) = __builtin_bit_cast(u16, (__bf16)o);
  }
  __syncthreads();
  {
    f32x4 y0 = {0.f, 0.f, 0.f, 0.f}, y1 = y0;
#pragma unroll
    for (int q = 0; q < 4; ++q) {
      const u32x4 af = *(LAS u32x4*)(lds + L_OB + 16 * q + 4 * r);
      y0 = __builtin_amdgcn_mfma_f32_16x16x32_bf16(__builtin_bit_cast(bf16x8, af), __builtin_bit_cast(bf16x8, bfr[0][q]), y0, 0, 0, 0);
      y1 = __builtin_amdgcn_mfma_f32_16x16x32_bf16(__builtin_bit_cast(bf16x8, af), __builtin_bit_cast(bf16x8, bfr[1][q]), y1, 0, 0, 0);
    }
    st16_sc1(rws, lane < 16 ? WS_Y + (h * 8 + s) * 1024 + (16 * a + lane) * 16 : OOB, rec(y0[0], y1[0], tagA, tagB));
  }
  if (a == 0) {
    u32x4 g;
#pragma unroll 1
    for (unsigned sp = 0; sp < SPIN_MEM; ++sp) {
      REREAD();
      g = ld16_sc1(rws, (lane >> 1) * (8 * 1024) + (lane & 1) * 16, WS_Y + s * 1024 + h * 32);
      if (__all(rok(g, tagA, tagB))) break;
    }
    const float a0 = rbf(parity_sum(r0(g))), a1 = rbf(parity_sum(r1(g)));
    const unsigned o = pack2(rbf(lo2f(xw) + a0) + f0, rbf(hi2f(xw) + a1) + f1v);
    __builtin_amdgcn_raw_buffer_store_b32(o, mkrsrc(out, D * 2), lane < 2 ? (col0 + 2 * lane) * 2 : OOB, 0, 0);
    __builtin_amdgcn_raw_buffer_store_b64(u32x2{(unsigned)e2, (unsigned)(e2 >> 32)}, rws, (bid == 0 && lane == 0) ? WS_EPOCH : OOB, 0, SC1);
  }
}

extern "C" void kernel_launch(void* const* d_in, const int* in_sizes, int n_in, void* d_out, int out_size, void* d_ws, size_t ws_size,
                              hipStream_t stream) {
  fused_decode_kernel<<<256, NTHR, 0, stream>>>((const u16*)d_in[0], (const u16*)d_in[1], (const u16*)d_in[2], (const u16*)d_in[3],
                                                 (const u16*)d_in[4], (const u16*)d_in[6], (const u16*)d_in[7], (const u16*)d_in[8],
                                                 (u16*)d_out, d_ws);
}
```

```cpp
#define T_MAIN 7230
#define T_K0 460
#define PACE_G 2
#define PHASE (wave * 32 + j)
#include <hip/hip_runtime.h>
#include <stdint.h>

typedef unsigned u32x4 __attribute__((ext_vector_type(4)));
typedef unsigned u32x2 __attribute__((ext_vector_type(2)));
typedef float f32x4 __attribute__((ext_vector_type(4)));
typedef float f32x2 __attribute__((ext_vector_type(2)));
typedef __bf16 bf16x2 __attribute__((ext_vector_type(2)));
typedef __bf16 bf16x8 __attribute__((ext_vector_type(8)));
typedef unsigned short u16;
#define LAS __attribute__((address_space(3)))
typedef __amdgpu_buffer_rsrc_t rsrc_t;

#define T_F1 150
#define T_W2 360
constexpr int T_QPOLL = 390, T_TOUCH = 1000;

constexpr int D = 1024, H = 32, SK = 32768, DH = 128, F = 3584;
constexpr int NMAIN = 8, NAUX = 4, NTHR = (NMAIN + NAUX) * 64;
constexpr int NT = 2, SC1 = 16;
constexpr int OOB = 0x40000000;
constexpr unsigned WS_EPOCH = 0, WS_T0 = 1024, WS_ML = 4096, WS_Q = WS_ML + 256 * 128, WS_Y = WS_Q + 256 * 1024, WS_G1 = WS_Y + 256 * 1024,
                   WS_FFN = WS_G1 + 4 * F * 8, WS_END = WS_FFN + 16 * D * 8;
constexpr int L_XN = 0, L_QP = 1024, L_QS = L_QP + 4 * 128, L_MRG = L_QS + 64, L_ML = L_MRG + 8 * 128, L_WG = L_ML + 16, L_FP = L_WG + 8,
              L_H = L_FP + 8 * 64, L_F2 = L_H + 4 * 64, L_OB = L_F2 + 4 * 64, L_CNT = L_OB + 64, L_SC = L_CNT + 16, L_WT = L_SC + 8 * 512,
              WT_STR = 68, L_END = L_WT + 128 * WT_STR;
constexpr int C_Q = 0, C_XN = 1, C_F2 = 2, C_WT = 3, C_ML = 4, C_WG = 5, C_END = 6, C_QR = 7, C_F1 = 8, C_OB = 9, C_NUM = 10, W_T0 = 12;
constexpr unsigned SPIN_MEM = 1u << 18, SPIN_LDS = 1u << 22;

__device__ __forceinline__ float lo2f(unsigned w) { return __builtin_bit_cast(float, w << 16); }
__device__ __forceinline__ float hi2f(unsigned w) { return __builtin_bit_cast(float, w & 0xffff0000u); }
__device__ __forceinline__ float rbf(float x) { return (float)(__bf16)x; }
__device__ __forceinline__ unsigned pack2(float a, float b) { bf16x2 v = {(__bf16)a, (__bf16)b}; return __builtin_bit_cast(unsigned, v); }
__device__ __forceinline__ float dot2(unsigned k, unsigned q, float acc) {
  return __builtin_amdgcn_fdot2_f32_bf16(__builtin_bit_cast(bf16x2, k), __builtin_bit_cast(bf16x2, q), acc, false);
}
template <int CTRL> __device__ __forceinline__ float dpp(float x) {
  return __builtin_bit_cast(float, __builtin_amdgcn_mov_dpp(__builtin_bit_cast(int, x), CTRL, 0xf, 0xf, true));
}
constexpr int XOR1 = 0xB1, XOR2 = 0x4E, XOR7 = 0x141, ROR8 = 0x128, ROR4 = 0x124, BCAST = 0x150;
__device__ __forceinline__ float fold32(float a, float b) {
  auto t = __builtin_amdgcn_permlane32_swap(__float_as_uint(a), __float_as_uint(b), false, false);
  return __uint_as_float(t[0]) + __uint_as_float(t[1]);
}
__device__ __forceinline__ float fold16(float a, float b) {
  auto t = __builtin_amdgcn_permlane16_swap(__float_as_uint(a), __float_as_uint(b), false, false);
  return __uint_as_float(t[0]) + __uint_as_float(t[1]);
}
__device__ __forceinline__ f32x2 fold_rows(const float (&v)[8]) {
  const float u0 = fold32(v[0], v[4]), u1 = fold32(v[1], v[5]), u2 = fold32(v[2], v[6]), u3 = fold32(v[3], v[7]);
  return f32x2{fold16(u0, u2), fold16(u1, u3)};
}
__device__ __forceinline__ float xrow_sum(float x) { x = fold16(x, x); return fold32(x, x); }
__device__ __forceinline__ float xrow_max(float x) {
  auto s = __builtin_amdgcn_permlane16_swap(__float_as_uint(x), __float_as_uint(x), false, false);
  x = fmaxf(__uint_as_float(s[0]), __uint_as_float(s[1]));
  auto t = __builtin_amdgcn_permlane32_swap(__float_as_uint(x), __float_as_uint(x), false, false);
  return fmaxf(__uint_as_float(t[0]), __uint_as_float(t[1]));
}
__device__ __forceinline__ float parity_sum(float x) {
  x += dpp<XOR2>(x); x += dpp<ROR4>(x); x += dpp<ROR8>(x); return xrow_sum(x);
}
__device__ __forceinline__ float wave_sum(float x) { x += dpp<XOR1>(x); return parity_sum(x); }
__device__ __forceinline__ float max8(float x) { x = fmaxf(x, dpp<XOR1>(x)); x = fmaxf(x, dpp<XOR2>(x)); return fmaxf(x, dpp<XOR7>(x)); }
__device__ __forceinline__ float sum8(float x) { x += dpp<XOR1>(x); x += dpp<XOR2>(x); return x + dpp<XOR7>(x); }
__device__ __forceinline__ float wave_max(float x) { x = max8(x); x = fmaxf(x, dpp<ROR8>(x)); return xrow_max(x); }
__device__ __forceinline__ float exp2_(float x) { return __builtin_amdgcn_exp2f(x); }
__device__ __forceinline__ rsrc_t mkrsrc(const void* p, unsigned bytes) { return __builtin_amdgcn_make_buffer_rsrc((void*)p, 0, (int)bytes, 0x00020000); }
__device__ __forceinline__ u32x4 ld16(rsrc_t r, int voff, int soff) { return __builtin_amdgcn_raw_buffer_load_b128(r, voff, soff, 0); }
__device__ __forceinline__ u32x4 ld16_nt(rsrc_t r, int voff, int soff) { return __builtin_amdgcn_raw_buffer_load_b128(r, voff, soff, NT); }
__device__ __forceinline__ u32x4 ld16_sc1(rsrc_t r, int voff, int soff) { return __builtin_amdgcn_raw_buffer_load_b128(r, voff, soff, SC1); }
__device__ __forceinline__ void st16_sc1(rsrc_t r, int off, u32x4 v) { __builtin_amdgcn_raw_buffer_store_b128(v, r, off, 0, SC1); }
#define SB() __builtin_amdgcn_sched_barrier(0)
#define REREAD() asm volatile("" ::: "memory")
__device__ __forceinline__ void pace_wait(unsigned t0, int due) {
  asm volatile("1:\n\ts_memrealtime s[96:97]\n\ts_waitcnt lgkmcnt(0)\n\ts_sub_u32 s96, s96, %0\n\ts_cmp_lt_i32 s96, %1\n\t"
               "s_cbranch_scc0 2f\n\ts_sleep 1\n\ts_branch 1b\n2:"
               :: "s"(t0), "s"(due) : "s96", "s97", "scc", "memory");
}
__device__ __forceinline__ void fma8(float (&acc)[8], float s, u32x4 w) {
#pragma unroll
  for (int t = 0; t < 4; ++t) { acc[2 * t] = fmaf(s, lo2f(w[t]), acc[2 * t]); acc[2 * t + 1] = fmaf(s, hi2f(w[t]), acc[2 * t + 1]); }
}
__device__ __forceinline__ u32x4 rec(float a, float b, unsigned ta, unsigned tb) { return u32x4{__float_as_uint(a), ta, __float_as_uint(b), tb}; }
__device__ __forceinline__ bool rok(u32x4 g, unsigned ta, unsigned tb) { return g[1] == ta && g[3] == tb; }
__device__ __forceinline__ float r0(u32x4 g) { return __uint_as_float(g[0]); }
__device__ __forceinline__ float r1(u32x4 g) { return __uint_as_float(g[2]); }

__device__ __forceinline__ void cnt_arrive(LAS float* lds, int which, int lane) {
  asm volatile("s_waitcnt lgkmcnt(0)" ::: "memory");
  if (lane == 0) __hip_atomic_fetch_add((LAS unsigned*)(lds + L_CNT + which), 1u, __ATOMIC_RELAXED, __HIP_MEMORY_SCOPE_WORKGROUP);
}
template <bool SLEEP = true>
__device__ __forceinline__ void cnt_wait(LAS float* lds, int which, unsigned n) {
#pragma unroll 1
  for (unsigned sp = 0; sp < SPIN_LDS; ++sp) {
    if (__builtin_amdgcn_readfirstlane(*(volatile LAS unsigned*)(lds + L_CNT + which)) >= n) break;
    if (SLEEP) __builtin_amdgcn_s_sleep(1);
  }
  REREAD();
}

__global__ void __launch_bounds__(NTHR, 3)
fused_decode_kernel(const u16* __restrict__ x, const u16* __restrict__ Kc, const u16* __restrict__ Vc, const u16* __restrict__ wq,
                    const u16* __restrict__ wo, const u16* __restrict__ wg, const u16* __restrict__ wu, const u16* __restrict__ wd,
                    u16* __restrict__ out, void* ws) {
  __shared__ __attribute__((aligned(16))) float lds_[L_END];
  LAS float* lds = (LAS float*)lds_;
  const unsigned t0 = (unsigned)__builtin_amdgcn_s_memrealtime();
  const int tid = threadIdx.x, lane = tid & 63, wave = __builtin_amdgcn_readfirstlane(tid >> 6);
  const int r = lane >> 4, c = lane & 15;
  const int bid = blockIdx.x, xl = bid & 7, j = bid >> 3;
  const int h = xl * 4 + (j >> 3), s = j & 7;
  const int f1 = xl * 28 + j, mat = f1 / 112, rb = (f1 / 56) & 1, cb = f1 % 56;

  if (wave < NMAIN) {
    const rsrc_t rk = mkrsrc(Kc, (unsigned)H * SK * DH * 2), rv = mkrsrc(Vc, (unsigned)H * SK * DH * 2);
    u32x4 bA[16], bB[16];
    const unsigned kvbase = (unsigned)h * (8u << 20) + (unsigned)s * (1u << 20) + wave * 1024;
    const unsigned rot = 32u * ((h + (h >> 2) + (h >> 4)) & 3);
    const int kvoff = lane * 16;
    constexpr int STEP = (T_MAIN << 16) / 256 * PACE_G;
    int due = T_K0 * 65536 - STEP + (STEP / 256) * (PHASE);
#define PACE(T0, I) do { if ((I) % PACE_G == 0) { due += STEP; pace_wait(T0, due >> 16); } } while (0)
    __builtin_amdgcn_s_barrier();
    SB();
    {
      const rsrc_t rg = mkrsrc(mat ? wu : wg, D * F * 2);
      const int voff = j < 28 ? (lane >> 3) * (F * 2) + (lane & 7) * 16 : OOB, sb = (rb * 512 + wave * 64) * (F * 2) + cb * 128;
      constexpr int FSTEP = ((T_W2 - T_F1) << 16) / 8;
      int fdue = T_F1 * 65536 + (FSTEP / 256) * (wave * 32 + j);
#pragma unroll
      for (int i = 0; i < 8; ++i) { pace_wait(t0, fdue >> 16); fdue += FSTEP; bB[i] = ld16_nt(rg, voff, sb + i * 8 * (F * 2)); SB(); }
    }
#pragma unroll
    for (int i = 0; i < 16; ++i) { PACE(t0, i); bA[i] = ld16_nt(rk, kvoff, kvbase + (rot + i) * 8192); SB(); }
    {
      cnt_wait(lds, C_XN, 1);
      float ag[8] = {0.f, 0.f, 0.f, 0.f, 0.f, 0.f, 0.f, 0.f};
#pragma unroll
      for (int i = 0; i < 8; ++i) {
        const int k = rb * 512 + wave * 64 + 8 * i + (lane >> 3);
        fma8(ag, lds[L_XN + (k & 3) * 256 + (k >> 2)], bB[i]);
      }
#pragma unroll
      for (int e = 0; e < 8; ++e) ag[e] += dpp<ROR8>(ag[e]);
      const f32x2 z = fold_rows(ag);
      if (c < 8) *(LAS f32x2*)(lds + L_FP + wave * 64 + 8 * c + 2 * r) = z;
      cnt_arrive(lds, C_F1, lane);
      SB();
    }
#pragma unroll
    for (int i = 0; i < 16; ++i) { PACE(t0, i); bB[i] = ld16_nt(rk, kvoff, kvbase + (rot + 16 + i) * 8192); SB(); }
    cnt_wait(lds, C_QR, 1);
    const u32x4 qp = *(LAS u32x4*)(lds + L_QS + 4 * c);
    const unsigned t0m = __builtin_amdgcn_readfirstlane(*(LAS unsigned*)(lds + L_CNT + W_T0));
    constexpr float SCALE = 0.08838834764831845f, LOG2E = 1.4426950408889634f;
    LAS float* scp = lds + L_SC + wave * 512 + lane * 8;
    float mx = -INFINITY;
    auto kgroup = [&](u32x4 (&kb)[16], const bool more, const rsrc_t rn, const unsigned tb, const int g) __attribute__((always_inline)) {
      float sx[16];
#pragma unroll
      for (int i = 0; i < 16; ++i) {
        float d = 0.f;
#pragma unroll
        for (int t = 0; t < 4; ++t) d = dot2(kb[i][t], qp[t], d);
        asm volatile("" : "+v"(d));
        sx[i] = d;
        SB();
        if (more) { PACE(t0m, i); kb[i] = ld16_nt(rn, kvoff, tb + i * 8192); SB(); }
      }
#define TR_STEP(HALF, CTRL, BIT) _Pragma("unroll") for (int hh = 0; hh < (HALF); ++hh) { \
        const float lo_ = sx[hh], hi_ = sx[hh + (HALF)]; const float t1_ = lo_ + dpp<CTRL>(lo_), t2_ = hi_ + dpp<CTRL>(hi_); \
        sx[hh] = (c & (BIT)) ? t2_ : t1_; }
      TR_STEP(8, ROR8, 8) TR_STEP(4, XOR7, 4) TR_STEP(2, XOR2, 2) TR_STEP(1, XOR1, 1)
#undef TR_STEP
      const float sc = rbf(rbf(sx[0]) * SCALE) * LOG2E;
      mx = fmaxf(mx, sc);
      scp[g] = sc;
      SB();
    };
    kgroup(bA, false, rk, 0, 0);
#pragma unroll 1
    for (int g = 1; g < 8; ++g)
      kgroup(bB, true, mkrsrc(g == 7 ? Vc : Kc, (unsigned)H * SK * DH * 2), kvbase + (((g + 1) * 16 + rot) & 127u) * 8192, g);
    {
      const float m = wave_max(mx);
      f32x4 p0 = *(LAS f32x4*)scp, p1 = *(LAS f32x4*)(scp + 4);
      float l = 0.f;
#pragma unroll
      for (int e = 0; e < 4; ++e) { p0[e] = exp2_(p0[e] - m); p1[e] = exp2_(p1[e] - m); l += p0[e] + p1[e]; }
      *(LAS f32x4*)scp = p0; *(LAS f32x4*)(scp + 4) = p1;
      l = wave_sum(l);
      if (lane == 0) *(LAS f32x2*)(lds + L_ML + 2 * wave) = f32x2{m, l};
      cnt_arrive(lds, C_ML, lane);
    }
    float acc[8] = {0.f, 0.f, 0.f, 0.f, 0.f, 0.f, 0.f, 0.f};
    auto vgroup = [&](const bool more, const unsigned tb, const float p) __attribute__((always_inline)) {
#define PV_STEP(I) { fma8(acc, dpp<BCAST + (I)>(p), bB[I]); \
        asm volatile("" : "+v"(acc[0]), "+v"(acc[1]), "+v"(acc[2]), "+v"(acc[3]), "+v"(acc[4]), "+v"(acc[5]), "+v"(acc[6]), "+v"(acc[7])); SB(); \
        if (more) { PACE(t0m, I); bB[I] = ld16_nt(rv, kvoff, tb + (I) * 8192); SB(); } \
        else if ((I) == 11) {   \
          pace_wait(t0m, T_K0 + T_MAIN); \
          _Pragma("unroll") for (int u = 0; u < 4; ++u) { \
            bB[u] = ld16_nt(mkrsrc(wo, H * DH * D * 2), r * 2048 + c * 16, (h * 128 + wave * 16 + 4 * u) * 2048 + s * 256); SB(); } } }
      PV_STEP(0) PV_STEP(1) PV_STEP(2) PV_STEP(3) PV_STEP(4) PV_STEP(5) PV_STEP(6) PV_STEP(7)
      PV_STEP(8) PV_STEP(9) PV_STEP(10) PV_STEP(11) PV_STEP(12) PV_STEP(13) PV_STEP(14) PV_STEP(15)
#undef PV_STEP
    };
#pragma unroll 1
    for (int g = 0; g < 7; ++g) vgroup(true, kvbase + (((g + 1) * 16 + rot) & 127u) * 8192, scp[g]);
    __builtin_amdgcn_s_setprio(3);
    cnt_wait(lds, C_WG, 1);
    {
      const float wgt = lds[L_WG + wave];
#pragma unroll
      for (int e = 0; e < 8; ++e) acc[e] *= wgt;
      vgroup(false, 0, scp[7] * wgt);
    }
    *(LAS f32x2*)(lds + L_MRG + wave * 128 + 8 * c + 2 * r) = fold_rows(acc);
    cnt_arrive(lds, C_END, lane);
#pragma unroll
    for (int u = 0; u < 4; ++u) *(LAS u32x4*)(lds + L_WT + (wave * 16 + 4 * u + r) * WT_STR + 4 * c) = bB[u];
    cnt_arrive(lds, C_WT, lane);
    return;
#undef PACE
  }

  const int a = wave - NMAIN;
  const rsrc_t rx = mkrsrc(x, D * 2), rws = mkrsrc(ws, WS_END);
  unsigned tagA, tagB, tagC, tagD;
  unsigned long long e2;
  {
    u32x4 xa = ld16(rx, lane * 32, 0), xb = ld16(rx, lane * 32 + 16, 0);
    const u32x2 ep = __builtin_amdgcn_raw_buffer_load_b64(rws, 0, WS_EPOCH, SC1);
    const rsrc_t rwq = mkrsrc(wq, D * H * DH * 2);
    u32x4 w[8];
    const int voff = r * 8192 + c * 16, sb = (s * 128 + a * 32) * 8192 + h * 256;
#pragma unroll
    for (int i = 0; i < 8; ++i) { w[i] = ld16_nt(rwq, voff, sb + i * 32768); SB(); }
    if (a == 0 && lane < C_NUM) *(LAS unsigned*)(lds + L_CNT + lane) = 0u;
    __syncthreads();
    asm volatile("" : "+v"(xa), "+v"(xb));
    float v[16];
#pragma unroll
    for (int i = 0; i < 4; ++i) { v[2 * i] = lo2f(xa[i]); v[2 * i + 1] = hi2f(xa[i]); v[8 + 2 * i] = lo2f(xb[i]); v[9 + 2 * i] = hi2f(xb[i]); }
    float sm = 0.f;
#pragma unroll
    for (int i = 0; i < 16; ++i) sm += v[i];
    const float mean = wave_sum(sm) * (1.f / D);
    float ss = 0.f;
#pragma unroll
    for (int i = 0; i < 16; ++i) { const float d = v[i] - mean; ss = fmaf(d, d, ss); }
    const float var = wave_sum(ss) * (1.f / D);
    const float mb = rbf(mean), den = rbf(sqrtf(rbf(rbf(var) + 1e-5f)));
    const float rinv = 1.0f / den;
#pragma unroll
    for (int i = 0; i < 16; ++i) {
      const float nu = rbf(v[i] - mb), q0 = nu * rinv;
      v[i] = rbf(fmaf(fmaf(-q0, den, nu), rinv, q0));
    }
#pragma unroll
    for (int q = 0; q < 4; ++q) *(LAS f32x4*)(lds + L_XN + q * 256 + 4 * lane) = f32x4{v[q], v[4 + q], v[8 + q], v[12 + q]};
    cnt_arrive(lds, C_XN, lane);
    const unsigned long long e1 = (((unsigned long long)__builtin_amdgcn_readfirstlane(ep[1]) << 32) | (unsigned)__builtin_amdgcn_readfirstlane(ep[0])) + 1ull;
    e2 = e1 + 1ull;
    tagA = (unsigned)e1; tagB = (unsigned)(e1 >> 32) + (unsigned)e1 * 0x9E3779B1u;
    tagC = (unsigned)e2; tagD = (unsigned)(e2 >> 32) + (unsigned)e2 * 0x9E3779B1u;
    float acc[8] = {0.f, 0.f, 0.f, 0.f, 0.f, 0.f, 0.f, 0.f};
#pragma unroll
    for (int q = 0; q < 2; ++q) {
      const f32x4 xv = *(LAS f32x4*)(lds + L_XN + r * 256 + s * 32 + a * 8 + 4 * q);
#pragma unroll
      for (int e = 0; e < 4; ++e) fma8(acc, xv[e], w[4 * q + e]);
    }
    *(LAS f32x2*)(lds + L_QP + a * 128 + 8 * c + 2 * r) = fold_rows(acc);
  }
  cnt_arrive(lds, C_Q, lane);
  if (a == 0) {
    cnt_wait<false>(lds, C_Q, NAUX);
    f32x2 v = *(LAS f32x2*)(lds + L_QP + 2 * lane);
#pragma unroll
    for (int q = 1; q < 4; ++q) v += *(LAS f32x2*)(lds + L_QP + q * 128 + 2 * lane);
    st16_sc1(rws, WS_Q + (h * 8 + s) * 1024 + lane * 16, rec(v[0], v[1], tagA, tagB));
    st16_sc1(rws, (bid == 0 && lane == 0) ? WS_T0 : OOB, u32x4{t0, tagA, t0, tagB});
  }
  if (a == 1) {
    pace_wait(t0, T_QPOLL);
    float q0 = 0.f, q1 = 0.f;
    unsigned tg = t0;
#pragma unroll 1
    for (unsigned sp = 0; sp < SPIN_MEM; ++sp) {
      REREAD();
      u32x4 g[8];
#pragma unroll
      for (int i = 0; i < 8; ++i) g[i] = ld16_sc1(rws, lane * 16, WS_Q + (h * 8 + i) * 1024);
      const u32x4 gt = ld16_sc1(rws, 0, WS_T0);
      bool ok = rok(gt, tagA, tagB);
      tg = gt[0];
      q0 = 0.f; q1 = 0.f;
#pragma unroll
      for (int i = 0; i < 8; ++i) { ok = ok && rok(g[i], tagA, tagB); q0 += r0(g[i]); q1 += r1(g[i]); }
      if (__all(ok)) break;
      __builtin_amdgcn_s_sleep(2);
    }
    *(LAS unsigned*)(lds + L_QS + lane) = pack2(q0, q1);
    if (lane == 0) *(LAS unsigned*)(lds + L_CNT + W_T0) = ((int)(t0 - tg) < 0 || (int)(t0 - tg) > 300) ? t0 : tg;
    cnt_arrive(lds, C_QR, lane);
  }
  const int ks = xl * 2 + (j >> 4), cg = j & 15;
  u32x4 dw[7];
  {
    const rsrc_t rd = mkrsrc(wd, D * F * 2);
    const int voff = (lane >> 3) * 2048 + (lane & 7) * 16, sb = (ks * 224 + a * 56) * 2048 + cg * 128;
    constexpr int WSTEP = ((T_K0 - T_W2) << 16) / 7;
    int wdue = T_W2 * 65536 + (WSTEP / 128) * (a * 32 + j);
#pragma unroll
    for (int i = 0; i < 7; ++i) { pace_wait(t0, wdue >> 16); wdue += WSTEP; dw[i] = ld16_nt(rd, voff, sb + i * 16384); SB(); }
  }
  if (a == 3) {
    pace_wait(t0, T_TOUCH);
    const u32x4 p = ld16_sc1(rws, lane * 16, WS_Y + (h * 8 + s) * 1024);
    asm volatile("" :: "v"(p));
  }
  if (a == 0 && j < 28) {
    cnt_wait(lds, C_F1, NMAIN);
    f32x2 v = *(LAS f32x2*)(lds + L_FP + 2 * (lane & 31));
#pragma unroll
    for (int q = 1; q < 8; ++q) v += *(LAS f32x2*)(lds + L_FP + q * 64 + 2 * (lane & 31));
    st16_sc1(rws, lane < 32 ? WS_G1 + ((mat * 2 + rb) * F + cb * 64) * 8 + lane * 16 : OOB, rec(v[0], v[1], tagA, tagB));
  }
  {
    pace_wait(t0, T_K0 + 2000);
    u32x4 g[4];
    const int voff = (lane < 28 ? lane : 27) * 16;
#pragma unroll 1
    for (unsigned sp = 0; sp < SPIN_MEM; ++sp) {
      REREAD();
#pragma unroll
      for (int k = 0; k < 4; ++k) g[k] = ld16_sc1(rws, voff, WS_G1 + (k * F + ks * 224 + a * 56) * 8);
      if (__all(rok(g[0], tagA, tagB) && rok(g[1], tagA, tagB) && rok(g[2], tagA, tagB) && rok(g[3], tagA, tagB))) break;
      __builtin_amdgcn_s_sleep(32);
    }
    float hh[2];
#pragma unroll
    for (int e = 0; e < 2; ++e) {
      const float gs = rbf(__uint_as_float(g[0][2 * e]) + __uint_as_float(g[1][2 * e]));
      const float us = rbf(__uint_as_float(g[2][2 * e]) + __uint_as_float(g[3][2 * e]));
      const float ge = 0.5f * gs * (1.f + tanhf(0.7978845608028654f * (gs + 0.044715f * gs * gs * gs)));
      hh[e] = rbf(rbf(ge) * us);
    }
    if (lane < 28) *(LAS f32x2*)(lds + L_H + a * 64 + 2 * lane) = f32x2{hh[0], hh[1]};
    float y[8] = {0.f, 0.f, 0.f, 0.f, 0.f, 0.f, 0.f, 0.f};
#pragma unroll
    for (int i = 0; i < 7; ++i) fma8(y, lds[L_H + a * 64 + 8 * i + (lane >> 3)], dw[i]);
#pragma unroll
    for (int e = 0; e < 8; ++e) y[e] += dpp<ROR8>(y[e]);
    const f32x2 z = fold_rows(y);
    if (c < 8) *(LAS f32x2*)(lds + L_F2 + a * 64 + 8 * c + 2 * r) = z;
    cnt_arrive(lds, C_F2, lane);
  }
  const int col0 = s * 128 + 4 * h;
  float f0 = 0.f, f1v = 0.f;
  unsigned xw = 0;
  if (a == 0) {
    cnt_wait(lds, C_F2, NAUX);
    f32x2 v = *(LAS f32x2*)(lds + L_F2 + 2 * (lane & 31));
#pragma unroll
    for (int q = 1; q < 4; ++q) v += *(LAS f32x2*)(lds + L_F2 + q * 64 + 2 * (lane & 31));
    st16_sc1(rws, lane < 32 ? WS_FFN + (ks * D + cg * 64) * 8 + lane * 16 : OOB, rec(v[0], v[1], tagA, tagB));
    xw = __builtin_amdgcn_raw_buffer_load_b32(rx, (col0 + 2 * (lane & 1)) * 2, 0, 0);
    pace_wait(t0, T_K0 + 3500);
    u32x4 g;
#pragma unroll 1
    for (unsigned sp = 0; sp < SPIN_MEM; ++sp) {
      REREAD();
      g = ld16_sc1(rws, ((lane >> 1) & 15) * (D * 8) + (lane & 1) * 16, WS_FFN + col0 * 8);
      if (__all(rok(g, tagA, tagB))) break;
      __builtin_amdgcn_s_sleep(32);
    }
    f0 = rbf(parity_sum(r0(g)) * 0.5f); f1v = rbf(parity_sum(r1(g)) * 0.5f);
  }
  if (a == 1) {
    cnt_wait(lds, C_ML, NMAIN);
    const f32x2 ml = *(LAS f32x2*)(lds + L_ML + 2 * (lane & 7));
    const float Mw = max8(ml[0]), Lw = sum8(exp2_(ml[0] - Mw) * ml[1]);
    st16_sc1(rws, lane == 0 ? WS_ML + (h * 8 + s) * 128 : OOB, rec(Mw, Lw, tagA, tagB));
    pace_wait(t0, T_K0 + T_MAIN / 2 + 1500);
    u32x4 g;
#pragma unroll 1
    for (unsigned sp = 0; sp < SPIN_MEM; ++sp) {
      REREAD();
      g = ld16_sc1(rws, (lane & 7) * 128, WS_ML + h * 8 * 128);
      if (__all(rok(g, tagA, tagB))) break;
      __builtin_amdgcn_s_sleep(32);
    }
    const float M = max8(r0(g)), L = sum8(exp2_(r0(g) - M) * r1(g));
    if (lane < 8) lds[L_WG + lane] = exp2_(ml[0] - M) / L;
    cnt_arrive(lds, C_WG, lane);
  }
  pace_wait(t0, T_K0 + T_MAIN - 100);
  if (a == 0) {
    cnt_wait<false>(lds, C_END, NMAIN);
    f32x2 v = *(LAS f32x2*)(lds + L_MRG + 2 * lane);
#pragma unroll
    for (int w = 1; w < 8; ++w) v += *(LAS f32x2*)(lds + L_MRG + w * 128 + 2 * lane);
    st16_sc1(rws, WS_Q + (h * 8 + s) * 1024 + lane * 16, rec(v[0], v[1], tagC, tagD));
  } else {
    cnt_wait(lds, C_END, NMAIN);
  }
  u32x4 bfr[2][4];
  cnt_wait<false>(lds, C_WT, NMAIN);
#pragma unroll
  for (int q = 0; q < 4; ++q)
#pragma unroll
    for (int d = 0; d < 4; ++d) {
      const unsigned w0 = *(LAS unsigned*)(lds + L_WT + (32 * q + 8 * r + 2 * d) * WT_STR + 16 * a + c);
      const unsigned w1 = *(LAS unsigned*)(lds + L_WT + (32 * q + 8 * r + 2 * d + 1) * WT_STR + 16 * a + c);
      bfr[0][q][d] = __builtin_amdgcn_perm(w1, w0, 0x05040100u);
      bfr[1][q][d] = __builtin_amdgcn_perm(w1, w0, 0x07060302u);
    }
  {
    u32x4 g0, g1;
    const int vo = r * 1024 + (16 * a + c) * 16;
#pragma unroll 1
    for (unsigned sp = 0; sp < SPIN_MEM; ++sp) {
      REREAD();
      g0 = ld16_sc1(rws, vo, WS_Q + h * 8 * 1024);
      g1 = ld16_sc1(rws, vo, WS_Q + (h * 8 + 4) * 1024);
      if (__all(rok(g0, tagC, tagD) && rok(g1, tagC, tagD))) break;
    }
    float o = fold32(r0(g0) + r0(g1), r1(g0) + r1(g1));
    o = fold16(o, o);
    if ((r & 1) == 0) *((LAS u16*)(lds + L_OB) + 32 * a + 2 * c + (r >> 1)) = __builtin_bit_cast(u16, (__bf16)o);
  }
  cnt_arrive(lds, C_OB, lane);
  cnt_wait<false>(lds, C_OB, NAUX);
  {
    f32x4 y0 = {0.f, 0.f, 0.f, 0.f}, y1 = y0;
#pragma unroll
    for (int q = 0; q < 4; ++q) {
      const u32x4 af = *(LAS u32x4*)(lds + L_OB + 16 * q + 4 * r);
      y0 = __builtin_amdgcn_mfma_f32_16x16x32_bf16(__builtin_bit_cast(bf16x8, af), __builtin_bit_cast(bf16x8, bfr[0][q]), y0, 0, 0, 0);
      y1 = __builtin_amdgcn_mfma_f32_16x16x32_bf16(__builtin_bit_cast(bf16x8, af), __builtin_bit_cast(bf16x8, bfr[1][q]), y1, 0, 0, 0);
    }
    st16_sc1(rws, lane < 16 ? WS_Y + (h * 8 + s) * 1024 + (16 * a + lane) * 16 : OOB, rec(y0[0], y1[0], tagA, tagB));
  }
  if (a == 0) {
    u32x4 g;
#pragma unroll 1
    for (unsigned sp = 0; sp < SPIN_MEM; ++sp) {
      REREAD();
      g = ld16_sc1(rws, (lane >> 1) * (8 * 1024) + (lane & 1) * 16, WS_Y + s * 1024 + h * 32);
      if (__all(rok(g, tagA, tagB))) break;
    }
    const float a0 = rbf(parity_sum(r0(g))), a1 = rbf(parity_sum(r1(g)));
    const unsigned o = pack2(rbf(lo2f(xw) + a0) + f0, rbf(hi2f(xw) + a1) + f1v);
    __builtin_amdgcn_raw_buffer_store_b32(o, mkrsrc(out, D * 2), lane < 2 ? (col0 + 2 * lane) * 2 : OOB, 0, 0);
    __builtin_amdgcn_raw_buffer_store_b64(u32x2{(unsigned)e2, (unsigned)(e2 >> 32)}, rws, (bid == 0 && lane == 0) ? WS_EPOCH : OOB, 0, SC1);
  }
}

extern "C" void kernel_launch(void* const* d_in, const int* in_sizes, int n_in, void* d_out, int out_size, void* d_ws, size_t ws_size,
                              hipStream_t stream) {
  fused_decode_kernel<<<256, NTHR, 0, stream>>>((const u16*)d_in[0], (const u16*)d_in[1], (const u16*)d_in[2], (const u16*)d_in[3],
                                                 (const u16*)d_in[4], (const u16*)d_in[6], (const u16*)d_in[7], (const u16*)d_in[8],
                                                 (u16*)d_out, d_ws);
}
```

```cpp
#define T_MAIN 7230
#define T_K0 460
#define CODE_TOUCH 0
#include <hip/hip_runtime.h>
#include <stdint.h>

typedef unsigned u32x4 __attribute__((ext_vector_type(4)));
typedef unsigned u32x2 __attribute__((ext_vector_type(2)));
typedef float f32x4 __attribute__((ext_vector_type(4)));
typedef float f32x2 __attribute__((ext_vector_type(2)));
typedef __bf16 bf16x2 __attribute__((ext_vector_type(2)));
typedef __bf16 bf16x8 __attribute__((ext_vector_type(8)));
typedef unsigned short u16;
#define LAS __attribute__((address_space(3)))
typedef __amdgpu_buffer_rsrc_t rsrc_t;

#define T_F1 150
#define T_W2 360
#define PACE_G 2
#define PHASE (wave * 32 + j)
constexpr int T_QPOLL = 390, T_TOUCH = 1000;

constexpr int D = 1024, H = 32, SK = 32768, DH = 128, F = 3584;
constexpr int NMAIN = 8, NAUX = 4, NTHR = (NMAIN + NAUX) * 64;
constexpr int NT = 2, SC1 = 16;
constexpr int OOB = 0x40000000;
constexpr unsigned WS_EPOCH = 0, WS_T0 = 1024, WS_ML = 4096, WS_Q = WS_ML + 256 * 128, WS_Y = WS_Q + 256 * 1024, WS_G1 = WS_Y + 256 * 1024,
                   WS_FFN = WS_G1 + 4 * F * 8, WS_END = WS_FFN + 16 * D * 8;
constexpr int L_XN = 0, L_QP = 1024, L_QS = L_QP + 4 * 128, L_MRG = L_QS + 64, L_ML = L_MRG + 8 * 128, L_WG = L_ML + 16, L_FP = L_WG + 8,
              L_H = L_FP + 8 * 64, L_F2 = L_H + 4 * 64, L_OB = L_F2 + 4 * 64, L_CNT = L_OB + 64, L_SC = L_CNT + 16, L_WT = L_SC + 8 * 512,
              WT_STR = 68, L_END = L_WT + 128 * WT_STR;
constexpr int C_Q = 0, C_XN = 1, C_F2 = 2, C_WT = 3, C_ML = 4, C_WG = 5, C_END = 6, C_QR = 7, C_F1 = 8, C_OB = 9, C_NUM = 10, W_T0 = 12;
constexpr unsigned SPIN_MEM = 1u << 18, SPIN_LDS = 1u << 22;

__device__ __forceinline__ float lo2f(unsigned w) { return __builtin_bit_cast(float, w << 16); }
__device__ __forceinline__ float hi2f(unsigned w) { return __builtin_bit_cast(float, w & 0xffff0000u); }
__device__ __forceinline__ float rbf(float x) { return (float)(__bf16)x; }
__device__ __forceinline__ unsigned pack2(float a, float b) { bf16x2 v = {(__bf16)a, (__bf16)b}; return __builtin_bit_cast(unsigned, v); }
__device__ __forceinline__ float dot2(unsigned k, unsigned q, float acc) {
  return __builtin_amdgcn_fdot2_f32_bf16(__builtin_bit_cast(bf16x2, k), __builtin_bit_cast(bf16x2, q), acc, false);
}
template <int CTRL> __device__ __forceinline__ float dpp(float x) {
  return __builtin_bit_cast(float, __builtin_amdgcn_mov_dpp(__builtin_bit_cast(int, x), CTRL, 0xf, 0xf, true));
}
constexpr int XOR1 = 0xB1, XOR2 = 0x4E, XOR7 = 0x141, ROR8 = 0x128, ROR4 = 0x124, BCAST = 0x150;
__device__ __forceinline__ float fold32(float a, float b) {
  auto t = __builtin_amdgcn_permlane32_swap(__float_as_uint(a), __float_as_uint(b), false, false);
  return __uint_as_float(t[0]) + __uint_as_float(t[1]);
}
__device__ __forceinline__ float fold16(float a, float b) {
  auto t = __builtin_amdgcn_permlane16_swap(__float_as_uint(a), __float_as_uint(b), false, false);
  return __uint_as_float(t[0]) + __uint_as_float(t[1]);
}
__device__ __forceinline__ f32x2 fold_rows(const float (&v)[8]) {
  const float u0 = fold32(v[0], v[4]), u1 = fold32(v[1], v[5]), u2 = fold32(v[2], v[6]), u3 = fold32(v[3], v[7]);
  return f32x2{fold16(u0, u2), fold16(u1, u3)};
}
__device__ __forceinline__ float xrow_sum(float x) { x = fold16(x, x); return fold32(x, x); }
__device__ __forceinline__ float xrow_max(float x) {
  auto s = __builtin_amdgcn_permlane16_swap(__float_as_uint(x), __float_as_uint(x), false, false);
  x = fmaxf(__uint_as_float(s[0]), __uint_as_float(s[1]));
  auto t = __builtin_amdgcn_permlane32_swap(__float_as_uint(x), __float_as_uint(x), false, false);
  return fmaxf(__uint_as_float(t[0]), __uint_as_float(t[1]));
}
__device__ __forceinline__ float parity_sum(float x) {
  x += dpp<XOR2>(x); x += dpp<ROR4>(x); x += dpp<ROR8>(x); return xrow_sum(x);
}
__device__ __forceinline__ float wave_sum(float x) { x += dpp<XOR1>(x); return parity_sum(x); }
__device__ __forceinline__ float max8(float x) { x = fmaxf(x, dpp<XOR1>(x)); x = fmaxf(x, dpp<XOR2>(x)); return fmaxf(x, dpp<XOR7>(x)); }
__device__ __forceinline__ float sum8(float x) { x += dpp<XOR1>(x); x += dpp<XOR2>(x); return x + dpp<XOR7>(x); }
__device__ __forceinline__ float wave_max(float x) { x = max8(x); x = fmaxf(x, dpp<ROR8>(x)); return xrow_max(x); }
__device__ __forceinline__ float exp2_(float x) { return __builtin_amdgcn_exp2f(x); }
__device__ __forceinline__ rsrc_t mkrsrc(const void* p, unsigned bytes) { return __builtin_amdgcn_make_buffer_rsrc((void*)p, 0, (int)bytes, 0x00020000); }
__device__ __forceinline__ u32x4 ld16(rsrc_t r, int voff, int soff) { return __builtin_amdgcn_raw_buffer_load_b128(r, voff, soff, 0); }
__device__ __forceinline__ u32x4 ld16_nt(rsrc_t r, int voff, int soff) { return __builtin_amdgcn_raw_buffer_load_b128(r, voff, soff, NT); }
__device__ __forceinline__ u32x4 ld16_sc1(rsrc_t r, int voff, int soff) { return __builtin_amdgcn_raw_buffer_load_b128(r, voff, soff, SC1); }
__device__ __forceinline__ void st16_sc1(rsrc_t r, int off, u32x4 v) { __builtin_amdgcn_raw_buffer_store_b128(v, r, off, 0, SC1); }
#define SB() __builtin_amdgcn_sched_barrier(0)
#define REREAD() asm volatile("" ::: "memory")
__device__ __forceinline__ void pace_wait(unsigned t0, int due) {
  asm volatile("1:\n\ts_memrealtime s[96:97]\n\ts_waitcnt lgkmcnt(0)\n\ts_sub_u32 s96, s96, %0\n\ts_cmp_lt_i32 s96, %1\n\t"
               "s_cbranch_scc0 2f\n\ts_sleep 1\n\ts_branch 1b\n2:"
               :: "s"(t0), "s"(due) : "s96", "s97", "scc", "memory");
}
__device__ __forceinline__ void fma8(float (&acc)[8], float s, u32x4 w) {
#pragma unroll
  for (int t = 0; t < 4; ++t) { acc[2 * t] = fmaf(s, lo2f(w[t]), acc[2 * t]); acc[2 * t + 1] = fmaf(s, hi2f(w[t]), acc[2 * t + 1]); }
}
__device__ __forceinline__ u32x4 rec(float a, float b, unsigned ta, unsigned tb) { return u32x4{__float_as_uint(a), ta, __float_as_uint(b), tb}; }
__device__ __forceinline__ bool rok(u32x4 g, unsigned ta, unsigned tb) { return g[1] == ta && g[3] == tb; }
__device__ __forceinline__ float r0(u32x4 g) { return __uint_as_float(g[0]); }
__device__ __forceinline__ float r1(u32x4 g) { return __uint_as_float(g[2]); }

__device__ __forceinline__ void cnt_arrive(LAS float* lds, int which, int lane) {
  asm volatile("s_waitcnt lgkmcnt(0)" ::: "memory");
  if (lane == 0) __hip_atomic_fetch_add((LAS unsigned*)(lds + L_CNT + which), 1u, __ATOMIC_RELAXED, __HIP_MEMORY_SCOPE_WORKGROUP);
}
template <bool SLEEP = true>
__device__ __forceinline__ void cnt_wait(LAS float* lds, int which, unsigned n) {
#pragma unroll 1
  for (unsigned sp = 0; sp < SPIN_LDS; ++sp) {
    if (__builtin_amdgcn_readfirstlane(*(volatile LAS unsigned*)(lds + L_CNT + which)) >= n) break;
    if (SLEEP) __builtin_amdgcn_s_sleep(1);
  }
  REREAD();
}

__device__ __forceinline__ void code_touch(int a, int lane) {
  unsigned long long pc; unsigned len;
  asm volatile("s_getpc_b64 %0\n3:\n\ts_mov_b32 %1, .Lfunc_end0-3b" : "=s"(pc), "=s"(len));
  const unsigned v = __builtin_amdgcn_raw_buffer_load_b32(mkrsrc((const void*)pc, len & ~3u), (a * 64 + lane) * 128, 0, 0);
  asm volatile("" :: "v"(v));
}

__global__ void __launch_bounds__(NTHR, 3)
fused_decode_kernel(const u16* __restrict__ x, const u16* __restrict__ Kc, const u16* __restrict__ Vc, const u16* __restrict__ wq,
                    const u16* __restrict__ wo, const u16* __restrict__ wg, const u16* __restrict__ wu, const u16* __restrict__ wd,
                    u16* __restrict__ out, void* ws) {
  __shared__ __attribute__((aligned(16))) float lds_[L_END];
  LAS float* lds = (LAS float*)lds_;
  const unsigned t0 = (unsigned)__builtin_amdgcn_s_memrealtime();
  const int tid = threadIdx.x, lane = tid & 63, wave = __builtin_amdgcn_readfirstlane(tid >> 6);
  const int r = lane >> 4, c = lane & 15;
  const int bid = blockIdx.x, xl = bid & 7, j = bid >> 3;
  const int h = xl * 4 + (j >> 3), s = j & 7;
  const int f1 = xl * 28 + j, mat = f1 / 112, rb = (f1 / 56) & 1, cb = f1 % 56;

  if (wave < NMAIN) {
    const rsrc_t rk = mkrsrc(Kc, (unsigned)H * SK * DH * 2), rv = mkrsrc(Vc, (unsigned)H * SK * DH * 2);
    u32x4 bA[16], bB[16];
    const unsigned kvbase = (unsigned)h * (8u << 20) + (unsigned)s * (1u << 20) + wave * 1024;
    const unsigned rot = 32u * ((h + (h >> 2) + (h >> 4)) & 3);
    const int kvoff = lane * 16;
    constexpr int STEP = (T_MAIN << 16) / 256 * PACE_G;
    int due = T_K0 * 65536 - STEP + (STEP / 256) * (PHASE);
#define PACE(T0, I) do { if ((I) % PACE_G == 0) { due += STEP; pace_wait(T0, due >> 16); } } while (0)
    __builtin_amdgcn_s_barrier();
    SB();
    {
      const rsrc_t rg = mkrsrc(mat ? wu : wg, D * F * 2);
      const int voff = j < 28 ? (lane >> 3) * (F * 2) + (lane & 7) * 16 : OOB, sb = (rb * 512 + wave * 64) * (F * 2) + cb * 128;
      constexpr int FSTEP = ((T_W2 - T_F1) << 16) / 8;
      int fdue = T_F1 * 65536 + (FSTEP / 256) * (wave * 32 + j);
#pragma unroll
      for (int i = 0; i < 8; ++i) { pace_wait(t0, fdue >> 16); fdue += FSTEP; bB[i] = ld16_nt(rg, voff, sb + i * 8 * (F * 2)); SB(); }
    }
#pragma unroll
    for (int i = 0; i < 16; ++i) { PACE(t0, i); bA[i] = ld16_nt(rk, kvoff, kvbase + (rot + i) * 8192); SB(); }
    {
      cnt_wait(lds, C_XN, 1);
      float ag[8] = {0.f, 0.f, 0.f, 0.f, 0.f, 0.f, 0.f, 0.f};
#pragma unroll
      for (int i = 0; i < 8; ++i) {
        const int k = rb * 512 + wave * 64 + 8 * i + (lane >> 3);
        fma8(ag, lds[L_XN + (k & 3) * 256 + (k >> 2)], bB[i]);
      }
#pragma unroll
      for (int e = 0; e < 8; ++e) ag[e] += dpp<ROR8>(ag[e]);
      const f32x2 z = fold_rows(ag);
      if (c < 8) *(LAS f32x2*)(lds + L_FP + wave * 64 + 8 * c + 2 * r) = z;
      cnt_arrive(lds, C_F1, lane);
      SB();
    }
#pragma unroll
    for (int i = 0; i < 16; ++i) { PACE(t0, i); bB[i] = ld16_nt(rk, kvoff, kvbase + (rot + 16 + i) * 8192); SB(); }
    cnt_wait(lds, C_QR, 1);
    const u32x4 qp = *(LAS u32x4*)(lds + L_QS + 4 * c);
    const unsigned t0m = __builtin_amdgcn_readfirstlane(*(LAS unsigned*)(lds + L_CNT + W_T0));
    constexpr float SCALE = 0.08838834764831845f, LOG2E = 1.4426950408889634f;
    LAS float* scp = lds + L_SC + wave * 512 + lane * 8;
    float mx = -INFINITY;
    auto kgroup = [&](u32x4 (&kb)[16], const bool more, const rsrc_t rn, const unsigned tb, const int g) __attribute__((always_inline)) {
      float sx[16];
#pragma unroll
      for (int i = 0; i < 16; ++i) {
        float d = 0.f;
#pragma unroll
        for (int t = 0; t < 4; ++t) d = dot2(kb[i][t], qp[t], d);
        asm volatile("" : "+v"(d));
        sx[i] = d;
        SB();
        if (more) { PACE(t0m, i); kb[i] = ld16_nt(rn, kvoff, tb + i * 8192); SB(); }
      }
#define TR_STEP(HALF, CTRL, BIT) _Pragma("unroll") for (int hh = 0; hh < (HALF); ++hh) { \
        const float lo_ = sx[hh], hi_ = sx[hh + (HALF)]; const float t1_ = lo_ + dpp<CTRL>(lo_), t2_ = hi_ + dpp<CTRL>(hi_); \
        sx[hh] = (c & (BIT)) ? t2_ : t1_; }
      TR_STEP(8, ROR8, 8) TR_STEP(4, XOR7, 4) TR_STEP(2, XOR2, 2) TR_STEP(1, XOR1, 1)
#undef TR_STEP
      const float sc = rbf(rbf(sx[0]) * SCALE) * LOG2E;
      mx = fmaxf(mx, sc);
      scp[g] = sc;
      SB();
    };
    kgroup(bA, false, rk, 0, 0);
#pragma unroll 1
    for (int g = 1; g < 8; ++g)
      kgroup(bB, true, mkrsrc(g == 7 ? Vc : Kc, (unsigned)H * SK * DH * 2), kvbase + (((g + 1) * 16 + rot) & 127u) * 8192, g);
    {
      const float m = wave_max(mx);
      f32x4 p0 = *(LAS f32x4*)scp, p1 = *(LAS f32x4*)(scp + 4);
      float l = 0.f;
#pragma unroll
      for (int e = 0; e < 4; ++e) { p0[e] = exp2_(p0[e] - m); p1[e] = exp2_(p1[e] - m); l += p0[e] + p1[e]; }
      *(LAS f32x4*)scp = p0; *(LAS f32x4*)(scp + 4) = p1;
      l = wave_sum(l);
      if (lane == 0) *(LAS f32x2*)(lds + L_ML + 2 * wave) = f32x2{m, l};
      cnt_arrive(lds, C_ML, lane);
    }
    float acc[8] = {0.f, 0.f, 0.f, 0.f, 0.f, 0.f, 0.f, 0.f};
    auto vgroup = [&](const bool more, const unsigned tb, const float p) __attribute__((always_inline)) {
#define PV_STEP(I) { fma8(acc, dpp<BCAST + (I)>(p), bB[I]); \
        asm volatile("" : "+v"(acc[0]), "+v"(acc[1]), "+v"(acc[2]), "+v"(acc[3]), "+v"(acc[4]), "+v"(acc[5]), "+v"(acc[6]), "+v"(acc[7])); SB(); \
        if (more) { PACE(t0m, I); bB[I] = ld16_nt(rv, kvoff, tb + (I) * 8192); SB(); } \
        else if ((I) == 11) {   \
          pace_wait(t0m, T_K0 + T_MAIN); \
          _Pragma("unroll") for (int u = 0; u < 4; ++u) { \
            bB[u] = ld16_nt(mkrsrc(wo, H * DH * D * 2), r * 2048 + c * 16, (h * 128 + wave * 16 + 4 * u) * 2048 + s * 256); SB(); } } }
      PV_STEP(0) PV_STEP(1) PV_STEP(2) PV_STEP(3) PV_STEP(4) PV_STEP(5) PV_STEP(6) PV_STEP(7)
      PV_STEP(8) PV_STEP(9) PV_STEP(10) PV_STEP(11) PV_STEP(12) PV_STEP(13) PV_STEP(14) PV_STEP(15)
#undef PV_STEP
    };
#pragma unroll 1
    for (int g = 0; g < 7; ++g) vgroup(true, kvbase + (((g + 1) * 16 + rot) & 127u) * 8192, scp[g]);
    __builtin_amdgcn_s_setprio(3);
    cnt_wait(lds, C_WG, 1);
    {
      const float wgt = lds[L_WG + wave];
#pragma unroll
      for (int e = 0; e < 8; ++e) acc[e] *= wgt;
      vgroup(false, 0, scp[7] * wgt);
    }
    *(LAS f32x2*)(lds + L_MRG + wave * 128 + 8 * c + 2 * r) = fold_rows(acc);
    cnt_arrive(lds, C_END, lane);
#pragma unroll
    for (int u = 0; u < 4; ++u) *(LAS u32x4*)(lds + L_WT + (wave * 16 + 4 * u + r) * WT_STR + 4 * c) = bB[u];
    cnt_arrive(lds, C_WT, lane);
    return;
#undef PACE
  }

  const int a = wave - NMAIN;
  const rsrc_t rx = mkrsrc(x, D * 2), rws = mkrsrc(ws, WS_END);
  unsigned tagA, tagB, tagC, tagD;
  unsigned long long e2;
  {
    u32x4 xa = ld16(rx, lane * 32, 0), xb = ld16(rx, lane * 32 + 16, 0);
    const u32x2 ep = __builtin_amdgcn_raw_buffer_load_b64(rws, 0, WS_EPOCH, SC1);
    const rsrc_t rwq = mkrsrc(wq, D * H * DH * 2);
    u32x4 w[8];
    const int voff = r * 8192 + c * 16, sb = (s * 128 + a * 32) * 8192 + h * 256;
#pragma unroll
    for (int i = 0; i < 8; ++i) { w[i] = ld16_nt(rwq, voff, sb + i * 32768); SB(); }
    if (a == 0 && lane < C_NUM) *(LAS unsigned*)(lds + L_CNT + lane) = 0u;
    __syncthreads();
    asm volatile("" : "+v"(xa), "+v"(xb));
    float v[16];
#pragma unroll
    for (int i = 0; i < 4; ++i) { v[2 * i] = lo2f(xa[i]); v[2 * i + 1] = hi2f(xa[i]); v[8 + 2 * i] = lo2f(xb[i]); v[9 + 2 * i] = hi2f(xb[i]); }
    float sm = 0.f;
#pragma unroll
    for (int i = 0; i < 16; ++i) sm += v[i];
    const float mean = wave_sum(sm) * (1.f / D);
    float ss = 0.f;
#pragma unroll
    for (int i = 0; i < 16; ++i) { const float d = v[i] - mean; ss = fmaf(d, d, ss); }
    const float var = wave_sum(ss) * (1.f / D);
    const float mb = rbf(mean), den = rbf(sqrtf(rbf(rbf(var) + 1e-5f)));
    const float rinv = 1.0f / den;
#pragma unroll
    for (int i = 0; i < 16; ++i) {
      const float nu = rbf(v[i] - mb), q0 = nu * rinv;
      v[i] = rbf(fmaf(fmaf(-q0, den, nu), rinv, q0));
    }
#pragma unroll
    for (int q = 0; q < 4; ++q) *(LAS f32x4*)(lds + L_XN + q * 256 + 4 * lane) = f32x4{v[q], v[4 + q], v[8 + q], v[12 + q]};
    cnt_arrive(lds, C_XN, lane);
    const unsigned long long e1 = (((unsigned long long)__builtin_amdgcn_readfirstlane(ep[1]) << 32) | (unsigned)__builtin_amdgcn_readfirstlane(ep[0])) + 1ull;
    e2 = e1 + 1ull;
    tagA = (unsigned)e1; tagB = (unsigned)(e1 >> 32) + (unsigned)e1 * 0x9E3779B1u;
    tagC = (unsigned)e2; tagD = (unsigned)(e2 >> 32) + (unsigned)e2 * 0x9E3779B1u;
    float acc[8] = {0.f, 0.f, 0.f, 0.f, 0.f, 0.f, 0.f, 0.f};
#pragma unroll
    for (int q = 0; q < 2; ++q) {
      const f32x4 xv = *(LAS f32x4*)(lds + L_XN + r * 256 + s * 32 + a * 8 + 4 * q);
#pragma unroll
      for (int e = 0; e < 4; ++e) fma8(acc, xv[e], w[4 * q + e]);
    }
    *(LAS f32x2*)(lds + L_QP + a * 128 + 8 * c + 2 * r) = fold_rows(acc);
  }
  cnt_arrive(lds, C_Q, lane);
  if (a == 0) {
    cnt_wait<false>(lds, C_Q, NAUX);
    f32x2 v = *(LAS f32x2*)(lds + L_QP + 2 * lane);
#pragma unroll
    for (int q = 1; q < 4; ++q) v += *(LAS f32x2*)(lds + L_QP + q * 128 + 2 * lane);
    st16_sc1(rws, WS_Q + (h * 8 + s) * 1024 + lane * 16, rec(v[0], v[1], tagA, tagB));
    st16_sc1(rws, (bid == 0 && lane == 0) ? WS_T0 : OOB, u32x4{t0, tagA, t0, tagB});
  }
  if (a == 1) {
    pace_wait(t0, T_QPOLL);
    float q0 = 0.f, q1 = 0.f;
    unsigned tg = t0;
#pragma unroll 1
    for (unsigned sp = 0; sp < SPIN_MEM; ++sp) {
      REREAD();
      u32x4 g[8];
#pragma unroll
      for (int i = 0; i < 8; ++i) g[i] = ld16_sc1(rws, lane * 16, WS_Q + (h * 8 + i) * 1024);
      const u32x4 gt = ld16_sc1(rws, 0, WS_T0);
      bool ok = rok(gt, tagA, tagB);
      tg = gt[0];
      q0 = 0.f; q1 = 0.f;
#pragma unroll
      for (int i = 0; i < 8; ++i) { ok = ok && rok(g[i], tagA, tagB); q0 += r0(g[i]); q1 += r1(g[i]); }
      if (__all(ok)) break;
      __builtin_amdgcn_s_sleep(2);
    }
    *(LAS unsigned*)(lds + L_QS + lane) = pack2(q0, q1);
    if (lane == 0) *(LAS unsigned*)(lds + L_CNT + W_T0) = ((int)(t0 - tg) < 0 || (int)(t0 - tg) > 300) ? t0 : tg;
    cnt_arrive(lds, C_QR, lane);
  }
  const int ks = xl * 2 + (j >> 4), cg = j & 15;
  u32x4 dw[7];
  {
    const rsrc_t rd = mkrsrc(wd, D * F * 2);
    const int voff = (lane >> 3) * 2048 + (lane & 7) * 16, sb = (ks * 224 + a * 56) * 2048 + cg * 128;
    constexpr int WSTEP = ((T_K0 - T_W2) << 16) / 7;
    int wdue = T_W2 * 65536 + (WSTEP / 128) * (a * 32 + j);
#pragma unroll
    for (int i = 0; i < 7; ++i) { pace_wait(t0, wdue >> 16); wdue += WSTEP; dw[i] = ld16_nt(rd, voff, sb + i * 16384); SB(); }
  }
  if (a == 3) {
    pace_wait(t0, T_TOUCH);
    const u32x4 p = ld16_sc1(rws, lane * 16, WS_Y + (h * 8 + s) * 1024);
    asm volatile("" :: "v"(p));
  }
  if (a == 0 && j < 28) {
    cnt_wait(lds, C_F1, NMAIN);
    f32x2 v = *(LAS f32x2*)(lds + L_FP + 2 * (lane & 31));
#pragma unroll
    for (int q = 1; q < 8; ++q) v += *(LAS f32x2*)(lds + L_FP + q * 64 + 2 * (lane & 31));
    st16_sc1(rws, lane < 32 ? WS_G1 + ((mat * 2 + rb) * F + cb * 64) * 8 + lane * 16 : OOB, rec(v[0], v[1], tagA, tagB));
  }
  {
    pace_wait(t0, T_K0 + 2000);
    u32x4 g[4];
    const int voff = (lane < 28 ? lane : 27) * 16;
#pragma unroll 1
    for (unsigned sp = 0; sp < SPIN_MEM; ++sp) {
      REREAD();
#pragma unroll
      for (int k = 0; k < 4; ++k) g[k] = ld16_sc1(rws, voff, WS_G1 + (k * F + ks * 224 + a * 56) * 8);
      if (__all(rok(g[0], tagA, tagB) && rok(g[1], tagA, tagB) && rok(g[2], tagA, tagB) && rok(g[3], tagA, tagB))) break;
      __builtin_amdgcn_s_sleep(32);
    }
    float hh[2];
#pragma unroll
    for (int e = 0; e < 2; ++e) {
      const float gs = rbf(__uint_as_float(g[0][2 * e]) + __uint_as_float(g[1][2 * e]));
      const float us = rbf(__uint_as_float(g[2][2 * e]) + __uint_as_float(g[3][2 * e]));
      const float ge = 0.5f * gs * (1.f + tanhf(0.7978845608028654f * (gs + 0.044715f * gs * gs * gs)));
      hh[e] = rbf(rbf(ge) * us);
    }
    if (lane < 28) *(LAS f32x2*)(lds + L_H + a * 64 + 2 * lane) = f32x2{hh[0], hh[1]};
    float y[8] = {0.f, 0.f, 0.f, 0.f, 0.f, 0.f, 0.f, 0.f};
#pragma unroll
    for (int i = 0; i < 7; ++i) fma8(y, lds[L_H + a * 64 + 8 * i + (lane >> 3)], dw[i]);
#pragma unroll
    for (int e = 0; e < 8; ++e) y[e] += dpp<ROR8>(y[e]);
    const f32x2 z = fold_rows(y);
    if (c < 8) *(LAS f32x2*)(lds + L_F2 + a * 64 + 8 * c + 2 * r) = z;
    cnt_arrive(lds, C_F2, lane);
  }
  const int col0 = s * 128 + 4 * h;
  float f0 = 0.f, f1v = 0.f;
  unsigned xw = 0;
  if (a == 0) {
    cnt_wait(lds, C_F2, NAUX);
    f32x2 v = *(LAS f32x2*)(lds + L_F2 + 2 * (lane & 31));
#pragma unroll
    for (int q = 1; q < 4; ++q) v += *(LAS f32x2*)(lds + L_F2 + q * 64 + 2 * (lane & 31));
    st16_sc1(rws, lane < 32 ? WS_FFN + (ks * D + cg * 64) * 8 + lane * 16 : OOB, rec(v[0], v[1], tagA, tagB));
    xw = __builtin_amdgcn_raw_buffer_load_b32(rx, (col0 + 2 * (lane & 1)) * 2, 0, 0);
    pace_wait(t0, T_K0 + 3500);
    u32x4 g;
#pragma unroll 1
    for (unsigned sp = 0; sp < SPIN_MEM; ++sp) {
      REREAD();
      g = ld16_sc1(rws, ((lane >> 1) & 15) * (D * 8) + (lane & 1) * 16, WS_FFN + col0 * 8);
      if (__all(rok(g, tagA, tagB))) break;
      __builtin_amdgcn_s_sleep(32);
    }
    f0 = rbf(parity_sum(r0(g)) * 0.5f); f1v = rbf(parity_sum(r1(g)) * 0.5f);
  }
  if (a == 1) {
    cnt_wait(lds, C_ML, NMAIN);
    const f32x2 ml = *(LAS f32x2*)(lds + L_ML + 2 * (lane & 7));
    const float Mw = max8(ml[0]), Lw = sum8(exp2_(ml[0] - Mw) * ml[1]);
    st16_sc1(rws, lane == 0 ? WS_ML + (h * 8 + s) * 128 : OOB, rec(Mw, Lw, tagA, tagB));
    pace_wait(t0, T_K0 + T_MAIN / 2 + 1500);
    u32x4 g;
#pragma unroll 1
    for (unsigned sp = 0; sp < SPIN_MEM; ++sp) {
      REREAD();
      g = ld16_sc1(rws, (lane & 7) * 128, WS_ML + h * 8 * 128);
      if (__all(rok(g, tagA, tagB))) break;
      __builtin_amdgcn_s_sleep(32);
    }
    const float M = max8(r0(g)), L = sum8(exp2_(r0(g) - M) * r1(g));
    if (lane < 8) lds[L_WG + lane] = exp2_(ml[0] - M) / L;
    cnt_arrive(lds, C_WG, lane);
  }
  pace_wait(t0, T_K0 + T_MAIN - 100);
  if (a == 0) {
    cnt_wait<false>(lds, C_END, NMAIN);
    f32x2 v = *(LAS f32x2*)(lds + L_MRG + 2 * lane);
#pragma unroll
    for (int w = 1; w < 8; ++w) v += *(LAS f32x2*)(lds + L_MRG + w * 128 + 2 * lane);
    st16_sc1(rws, WS_Q + (h * 8 + s) * 1024 + lane * 16, rec(v[0], v[1], tagC, tagD));
  } else {
    cnt_wait(lds, C_END, NMAIN);
  }
  u32x4 bfr[2][4];
  cnt_wait<false>(lds, C_WT, NMAIN);
#pragma unroll
  for (int q = 0; q < 4; ++q)
#pragma unroll
    for (int d = 0; d < 4; ++d) {
      const unsigned w0 = *(LAS unsigned*)(lds + L_WT + (32 * q + 8 * r + 2 * d) * WT_STR + 16 * a + c);
      const unsigned w1 = *(LAS unsigned*)(lds + L_WT + (32 * q + 8 * r + 2 * d + 1) * WT_STR + 16 * a + c);
      bfr[0][q][d] = __builtin_amdgcn_perm(w1, w0, 0x05040100u);
      bfr[1][q][d] = __builtin_amdgcn_perm(w1, w0, 0x07060302u);
    }
  {
    u32x4 g0, g1;
    const int vo = r * 1024 + (16 * a + c) * 16;
#pragma unroll 1
    for (unsigned sp = 0; sp < SPIN_MEM; ++sp) {
      REREAD();
      g0 = ld16_sc1(rws, vo, WS_Q + h * 8 * 1024);
      g1 = ld16_sc1(rws, vo, WS_Q + (h * 8 + 4) * 1024);
      if (__all(rok(g0, tagC, tagD) && rok(g1, tagC, tagD))) break;
    }
    float o = fold32(r0(g0) + r0(g1), r1(g0) + r1(g1));
    o = fold16(o, o);
    if ((r & 1) == 0) *((LAS u16*)(lds + L_OB) + 32 * a + 2 * c + (r >> 1)) = __builtin_bit_cast(u16, (__bf16)o);
  }
  cnt_arrive(lds, C_OB, lane);
  cnt_wait<false>(lds, C_OB, NAUX);
  {
    f32x4 y0 = {0.f, 0.f, 0.f, 0.f}, y1 = y0;
#pragma unroll
    for (int q = 0; q < 4; ++q) {
      const u32x4 af = *(LAS u32x4*)(lds + L_OB + 16 * q + 4 * r);
      y0 = __builtin_amdgcn_mfma_f32_16x16x32_bf16(__builtin_bit_cast(bf16x8, af), __builtin_bit_cast(bf16x8, bfr[0][q]), y0, 0, 0, 0);
      y1 = __builtin_amdgcn_mfma_f32_16x16x32_bf16(__builtin_bit_cast(bf16x8, af), __builtin_bit_cast(bf16x8, bfr[1][q]), y1, 0, 0, 0);
    }
    st16_sc1(rws, lane < 16 ? WS_Y + (h * 8 + s) * 1024 + (16 * a + lane) * 16 : OOB, rec(y0[0], y1[0], tagA, tagB));
  }
  if (a == 0) {
    u32x4 g;
#pragma unroll 1
    for (unsigned sp = 0; sp < SPIN_MEM; ++sp) {
      REREAD();
      g = ld16_sc1(rws, (lane >> 1) * (8 * 1024) + (lane & 1) * 16, WS_Y + s * 1024 + h * 32);
      if (__all(rok(g, tagA, tagB))) break;
    }
    const float a0 = rbf(parity_sum(r0(g))), a1 = rbf(parity_sum(r1(g)));
    const unsigned o = pack2(rbf(lo2f(xw) + a0) + f0, rbf(hi2f(xw) + a1) + f1v);
    __builtin_amdgcn_raw_buffer_store_b32(o, mkrsrc(out, D * 2), lane < 2 ? (col0 + 2 * lane) * 2 : OOB, 0, 0);
    __builtin_amdgcn_raw_buffer_store_b64(u32x2{(unsigned)e2, (unsigned)(e2 >> 32)}, rws, (bid == 0 && lane == 0) ? WS_EPOCH : OOB, 0, SC1);
  }
}

extern "C" void kernel_launch(void* const* d_in, const int* in_sizes, int n_in, void* d_out, int out_size, void* d_ws, size_t ws_size,
                              hipStream_t stream) {
  fused_decode_kernel<<<256, NTHR, 0, stream>>>((const u16*)d_in[0], (const u16*)d_in[1], (const u16*)d_in[2], (const u16*)d_in[3],
                                                 (const u16*)d_in[4], (const u16*)d_in[6], (const u16*)d_in[7], (const u16*)d_in[8],
                                                 (u16*)d_out, d_ws);
}
```

```cpp
#define T_MAIN 7230
#define T_K0 460
#define CODE_TOUCH 2
#include <hip/hip_runtime.h>
#include <stdint.h>

typedef unsigned u32x4 __attribute__((ext_vector_type(4)));
typedef unsigned u32x2 __attribute__((ext_vector_type(2)));
typedef float f32x4 __attribute__((ext_vector_type(4)));
typedef float f32x2 __attribute__((ext_vector_type(2)));
typedef __bf16 bf16x2 __attribute__((ext_vector_type(2)));
typedef __bf16 bf16x8 __attribute__((ext_vector_type(8)));
typedef unsigned short u16;
#define LAS __attribute__((address_space(3)))
typedef __amdgpu_buffer_rsrc_t rsrc_t;

#define T_F1 150
#define T_W2 360
#define PACE_G 2
#define PHASE (wave * 32 + j)
constexpr int T_QPOLL = 390, T_TOUCH = 1000;

constexpr int D = 1024, H = 32, SK = 32768, DH = 128, F = 3584;
constexpr int NMAIN = 8, NAUX = 4, NTHR = (NMAIN + NAUX) * 64;
constexpr int NT = 2, SC1 = 16;
constexpr int OOB = 0x40000000;
constexpr unsigned WS_EPOCH = 0, WS_T0 = 1024, WS_ML = 4096, WS_Q = WS_ML + 256 * 128, WS_Y = WS_Q + 256 * 1024, WS_G1 = WS_Y + 256 * 1024,
                   WS_FFN = WS_G1 + 4 * F * 8, WS_END = WS_FFN + 16 * D * 8;
constexpr int L_XN = 0, L_QP = 1024, L_QS = L_QP + 4 * 128, L_MRG = L_QS + 64, L_ML = L_MRG + 8 * 128, L_WG = L_ML + 16, L_FP = L_WG + 8,
              L_H = L_FP + 8 * 64, L_F2 = L_H + 4 * 64, L_OB = L_F2 + 4 * 64, L_CNT = L_OB + 64, L_SC = L_CNT + 16, L_WT = L_SC + 8 * 512,
              WT_STR = 68, L_END = L_WT + 128 * WT_STR;
constexpr int C_Q = 0, C_XN = 1, C_F2 = 2, C_WT = 3, C_ML = 4, C_WG = 5, C_END = 6, C_QR = 7, C_F1 = 8, C_OB = 9, C_NUM = 10, W_T0 = 12;
constexpr unsigned SPIN_MEM = 1u << 18, SPIN_LDS = 1u << 22;

__device__ __forceinline__ float lo2f(unsigned w) { return __builtin_bit_cast(float, w << 16); }
__device__ __forceinline__ float hi2f(unsigned w) { return __builtin_bit_cast(float, w & 0xffff0000u); }
__device__ __forceinline__ float rbf(float x) { return (float)(__bf16)x; }
__device__ __forceinline__ unsigned pack2(float a, float b) { bf16x2 v = {(__bf16)a, (__bf16)b}; return __builtin_bit_cast(unsigned, v); }
__device__ __forceinline__ float dot2(unsigned k, unsigned q, float acc) {
  return __builtin_amdgcn_fdot2_f32_bf16(__builtin_bit_cast(bf16x2, k), __builtin_bit_cast(bf16x2, q), acc, false);
}
template <int CTRL> __device__ __forceinline__ float dpp(float x) {
  return __builtin_bit_cast(float, __builtin_amdgcn_mov_dpp(__builtin_bit_cast(int, x), CTRL, 0xf, 0xf, true));
}
constexpr int XOR1 = 0xB1, XOR2 = 0x4E, XOR7 = 0x141, ROR8 = 0x128, ROR4 = 0x124, BCAST = 0x150;
__device__ __forceinline__ float fold32(float a, float b) {
  auto t = __builtin_amdgcn_permlane32_swap(__float_as_uint(a), __float_as_uint(b), false, false);
  return __uint_as_float(t[0]) + __uint_as_float(t[1]);
}
__device__ __forceinline__ float fold16(float a, float b) {
  auto t = __builtin_amdgcn_permlane16_swap(__float_as_uint(a), __float_as_uint(b), false, false);
  return __uint_as_float(t[0]) + __uint_as_float(t[1]);
}
__device__ __forceinline__ f32x2 fold_rows(const float (&v)[8]) {
  const float u0 = fold32(v[0], v[4]), u1 = fold32(v[1], v[5]), u2 = fold32(v[2], v[6]), u3 = fold32(v[3], v[7]);
  return f32x2{fold16(u0, u2), fold16(u1, u3)};
}
__device__ __forceinline__ float xrow_sum(float x) { x = fold16(x, x); return fold32(x, x); }
__device__ __forceinline__ float xrow_max(float x) {
  auto s = __builtin_amdgcn_permlane16_swap(__float_as_uint(x), __float_as_uint(x), false, false);
  x = fmaxf(__uint_as_float(s[0]), __uint_as_float(s[1]));
  auto t = __builtin_amdgcn_permlane32_swap(__float_as_uint(x), __float_as_uint(x), false, false);
  return fmaxf(__uint_as_float(t[0]), __uint_as_float(t[1]));
}
__device__ __forceinline__ float parity_sum(float x) {
  x += dpp<XOR2>(x); x += dpp<ROR4>(x); x += dpp<ROR8>(x); return xrow_sum(x);
}
__device__ __forceinline__ float wave_sum(float x) { x += dpp<XOR1>(x); return parity_sum(x); }
__device__ __forceinline__ float max8(float x) { x = fmaxf(x, dpp<XOR1>(x)); x = fmaxf(x, dpp<XOR2>(x)); return fmaxf(x, dpp<XOR7>(x)); }
__device__ __forceinline__ float sum8(float x) { x += dpp<XOR1>(x); x += dpp<XOR2>(x); return x + dpp<XOR7>(x); }
__device__ __forceinline__ float wave_max(float x) { x = max8(x); x = fmaxf(x, dpp<ROR8>(x)); return xrow_max(x); }
__device__ __forceinline__ float exp2_(float x) { return __builtin_amdgcn_exp2f(x); }
__device__ __forceinline__ rsrc_t mkrsrc(const void* p, unsigned bytes) { return __builtin_amdgcn_make_buffer_rsrc((void*)p, 0, (int)bytes, 0x00020000); }
__device__ __forceinline__ u32x4 ld16(rsrc_t r, int voff, int soff) { return __builtin_amdgcn_raw_buffer_load_b128(r, voff, soff, 0); }
__device__ __forceinline__ u32x4 ld16_nt(rsrc_t r, int voff, int soff) { return __builtin_amdgcn_raw_buffer_load_b128(r, voff, soff, NT); }
__device__ __forceinline__ u32x4 ld16_sc1(rsrc_t r, int voff, int soff) { return __builtin_amdgcn_raw_buffer_load_b128(r, voff, soff, SC1); }
__device__ __forceinline__ void st16_sc1(rsrc_t r, int off, u32x4 v) { __builtin_amdgcn_raw_buffer_store_b128(v, r, off, 0, SC1); }
#define SB() __builtin_amdgcn_sched_barrier(0)
#define REREAD() asm volatile("" ::: "memory")
__device__ __forceinline__ void pace_wait(unsigned t0, int due) {
  asm volatile("1:\n\ts_memrealtime s[96:97]\n\ts_waitcnt lgkmcnt(0)\n\ts_sub_u32 s96, s96, %0\n\ts_cmp_lt_i32 s96, %1\n\t"
               "s_cbranch_scc0 2f\n\ts_sleep 1\n\ts_branch 1b\n2:"
               :: "s"(t0), "s"(due) : "s96", "s97", "scc", "memory");
}
__device__ __forceinline__ void fma8(float (&acc)[8], float s, u32x4 w) {
#pragma unroll
  for (int t = 0; t < 4; ++t) { acc[2 * t] = fmaf(s, lo2f(w[t]), acc[2 * t]); acc[2 * t + 1] = fmaf(s, hi2f(w[t]), acc[2 * t + 1]); }
}
__device__ __forceinline__ u32x4 rec(float a, float b, unsigned ta, unsigned tb) { return u32x4{__float_as_uint(a), ta, __float_as_uint(b), tb}; }
__device__ __forceinline__ bool rok(u32x4 g, unsigned ta, unsigned tb) { return g[1] == ta && g[3] == tb; }
__device__ __forceinline__ float r0(u32x4 g) { return __uint_as_float(g[0]); }
__device__ __forceinline__ float r1(u32x4 g) { return __uint_as_float(g[2]); }

__device__ __forceinline__ void cnt_arrive(LAS float* lds, int which, int lane) {
  asm volatile("s_waitcnt lgkmcnt(0)" ::: "memory");
  if (lane == 0) __hip_atomic_fetch_add((LAS unsigned*)(lds + L_CNT + which), 1u, __ATOMIC_RELAXED, __HIP_MEMORY_SCOPE_WORKGROUP);
}
template <bool SLEEP = true>
__device__ __forceinline__ void cnt_wait(LAS float* lds, int which, unsigned n) {
#pragma unroll 1
  for (unsigned sp = 0; sp < SPIN_LDS; ++sp) {
    if (__builtin_amdgcn_readfirstlane(*(volatile LAS unsigned*)(lds + L_CNT + which)) >= n) break;
    if (SLEEP) __builtin_amdgcn_s_sleep(1);
  }
  REREAD();
}

__device__ __forceinline__ void code_touch(int a, int lane) {
  unsigned long long pc; unsigned len;
  asm volatile("s_getpc_b64 %0\n3:\n\ts_mov_b32 %1, .Lfdk_last-3b" : "=s"(pc), "=s"(len));
  len = len > 32768u ? 0u : len;
  const unsigned v = __builtin_amdgcn_raw_buffer_load_b32(mkrsrc((const void*)pc, len & ~3u), (a * 64 + lane) * 128, 0, 0);
  asm volatile("" :: "v"(v));
}

__global__ void __launch_bounds__(NTHR, 3)
fused_decode_kernel(const u16* __restrict__ x, const u16* __restrict__ Kc, const u16* __restrict__ Vc, const u16* __restrict__ wq,
                    const u16* __restrict__ wo, const u16* __restrict__ wg, const u16* __restrict__ wu, const u16* __restrict__ wd,
                    u16* __restrict__ out, void* ws) {
  __shared__ __attribute__((aligned(16))) float lds_[L_END];
  LAS float* lds = (LAS float*)lds_;
  const unsigned t0 = (unsigned)__builtin_amdgcn_s_memrealtime();
  const int tid = threadIdx.x, lane = tid & 63, wave = __builtin_amdgcn_readfirstlane(tid >> 6);
  const int r = lane >> 4, c = lane & 15;
  const int bid = blockIdx.x, xl = bid & 7, j = bid >> 3;
  const int h = xl * 4 + (j >> 3), s = j & 7;
  const int f1 = xl * 28 + j, mat = f1 / 112, rb = (f1 / 56) & 1, cb = f1 % 56;

  if (wave < NMAIN) {
    const rsrc_t rk = mkrsrc(Kc, (unsigned)H * SK * DH * 2), rv = mkrsrc(Vc, (unsigned)H * SK * DH * 2);
    u32x4 bA[16], bB[16];
    const unsigned kvbase = (unsigned)h * (8u << 20) + (unsigned)s * (1u << 20) + wave * 1024;
    const unsigned rot = 32u * ((h + (h >> 2) + (h >> 4)) & 3);
    const int kvoff = lane * 16;
    constexpr int STEP = (T_MAIN << 16) / 256 * PACE_G;
    int due = T_K0 * 65536 - STEP + (STEP / 256) * (PHASE);
#define PACE(T0, I) do { if ((I) % PACE_G == 0) { due += STEP; pace_wait(T0, due >> 16); } } while (0)
    __builtin_amdgcn_s_barrier();
    SB();
    {
      const rsrc_t rg = mkrsrc(mat ? wu : wg, D * F * 2);
      const int voff = j < 28 ? (lane >> 3) * (F * 2) + (lane & 7) * 16 : OOB, sb = (rb * 512 + wave * 64) * (F * 2) + cb * 128;
      constexpr int FSTEP = ((T_W2 - T_F1) << 16) / 8;
      int fdue = T_F1 * 65536 + (FSTEP / 256) * (wave * 32 + j);
#pragma unroll
      for (int i = 0; i < 8; ++i) { pace_wait(t0, fdue >> 16); fdue += FSTEP; bB[i] = ld16_nt(rg, voff, sb + i * 8 * (F * 2)); SB(); }
    }
#pragma unroll
    for (int i = 0; i < 16; ++i) { PACE(t0, i); bA[i] = ld16_nt(rk, kvoff, kvbase + (rot + i) * 8192); SB(); }
    {
      cnt_wait(lds, C_XN, 1);
      float ag[8] = {0.f, 0.f, 0.f, 0.f, 0.f, 0.f, 0.f, 0.f};
#pragma unroll
      for (int i = 0; i < 8; ++i) {
        const int k = rb * 512 + wave * 64 + 8 * i + (lane >> 3);
        fma8(ag, lds[L_XN + (k & 3) * 256 + (k >> 2)], bB[i]);
      }
#pragma unroll
      for (int e = 0; e < 8; ++e) ag[e] += dpp<ROR8>(ag[e]);
      const f32x2 z = fold_rows(ag);
      if (c < 8) *(LAS f32x2*)(lds + L_FP + wave * 64 + 8 * c + 2 * r) = z;
      cnt_arrive(lds, C_F1, lane);
      SB();
    }
#pragma unroll
    for (int i = 0; i < 16; ++i) { PACE(t0, i); bB[i] = ld16_nt(rk, kvoff, kvbase + (rot + 16 + i) * 8192); SB(); }
    cnt_wait(lds, C_QR, 1);
    const u32x4 qp = *(LAS u32x4*)(lds + L_QS + 4 * c);
    const unsigned t0m = __builtin_amdgcn_readfirstlane(*(LAS unsigned*)(lds + L_CNT + W_T0));
    constexpr float SCALE = 0.08838834764831845f, LOG2E = 1.4426950408889634f;
    LAS float* scp = lds + L_SC + wave * 512 + lane * 8;
    float mx = -INFINITY;
    auto kgroup = [&](u32x4 (&kb)[16], const bool more, const rsrc_t rn, const unsigned tb, const int g) __attribute__((always_inline)) {
      float sx[16];
#pragma unroll
      for (int i = 0; i < 16; ++i) {
        float d = 0.f;
#pragma unroll
        for (int t = 0; t < 4; ++t) d = dot2(kb[i][t], qp[t], d);
        asm volatile("" : "+v"(d));
        sx[i] = d;
        SB();
        if (more) { PACE(t0m, i); kb[i] = ld16_nt(rn, kvoff, tb + i * 8192); SB(); }
      }
#define TR_STEP(HALF, CTRL, BIT) _Pragma("unroll") for (int hh = 0; hh < (HALF); ++hh) { \
        const float lo_ = sx[hh], hi_ = sx[hh + (HALF)]; const float t1_ = lo_ + dpp<CTRL>(lo_), t2_ = hi_ + dpp<CTRL>(hi_); \
        sx[hh] = (c & (BIT)) ? t2_ : t1_; }
      TR_STEP(8, ROR8, 8) TR_STEP(4, XOR7, 4) TR_STEP(2, XOR2, 2) TR_STEP(1, XOR1, 1)
#undef TR_STEP
      const float sc = rbf(rbf(sx[0]) * SCALE) * LOG2E;
      mx = fmaxf(mx, sc);
      scp[g] = sc;
      SB();
    };
    kgroup(bA, false, rk, 0, 0);
#pragma unroll 1
    for (int g = 1; g < 8; ++g)
      kgroup(bB, true, mkrsrc(g == 7 ? Vc : Kc, (unsigned)H * SK * DH * 2), kvbase + (((g + 1) * 16 + rot) & 127u) * 8192, g);
    {
      const float m = wave_max(mx);
      f32x4 p0 = *(LAS f32x4*)scp, p1 = *(LAS f32x4*)(scp + 4);
      float l = 0.f;
#pragma unroll
      for (int e = 0; e < 4; ++e) { p0[e] = exp2_(p0[e] - m); p1[e] = exp2_(p1[e] - m); l += p0[e] + p1[e]; }
      *(LAS f32x4*)scp = p0; *(LAS f32x4*)(scp + 4) = p1;
      l = wave_sum(l);
      if (lane == 0) *(LAS f32x2*)(lds + L_ML + 2 * wave) = f32x2{m, l};
      cnt_arrive(lds, C_ML, lane);
    }
    float acc[8] = {0.f, 0.f, 0.f, 0.f, 0.f, 0.f, 0.f, 0.f};
    auto vgroup = [&](const bool more, const unsigned tb, const float p) __attribute__((always_inline)) {
#define PV_STEP(I) { fma8(acc, dpp<BCAST + (I)>(p), bB[I]); \
        asm volatile("" : "+v"(acc[0]), "+v"(acc[1]), "+v"(acc[2]), "+v"(acc[3]), "+v"(acc[4]), "+v"(acc[5]), "+v"(acc[6]), "+v"(acc[7])); SB(); \
        if (more) { PACE(t0m, I); bB[I] = ld16_nt(rv, kvoff, tb + (I) * 8192); SB(); } \
        else if ((I) == 11) {   \
          pace_wait(t0m, T_K0 + T_MAIN); \
          _Pragma("unroll") for (int u = 0; u < 4; ++u) { \
            bB[u] = ld16_nt(mkrsrc(wo, H * DH * D * 2), r * 2048 + c * 16, (h * 128 + wave * 16 + 4 * u) * 2048 + s * 256); SB(); } } }
      PV_STEP(0) PV_STEP(1) PV_STEP(2) PV_STEP(3) PV_STEP(4) PV_STEP(5) PV_STEP(6) PV_STEP(7)
      PV_STEP(8) PV_STEP(9) PV_STEP(10) PV_STEP(11) PV_STEP(12) PV_STEP(13) PV_STEP(14) PV_STEP(15)
#undef PV_STEP
    };
#pragma unroll 1
    for (int g = 0; g < 7; ++g) vgroup(true, kvbase + (((g + 1) * 16 + rot) & 127u) * 8192, scp[g]);
    __builtin_amdgcn_s_setprio(3);
    cnt_wait(lds, C_WG, 1);
    {
      const float wgt = lds[L_WG + wave];
#pragma unroll
      for (int e = 0; e < 8; ++e) acc[e] *= wgt;
      vgroup(false, 0, scp[7] * wgt);
    }
    *(LAS f32x2*)(lds + L_MRG + wave * 128 + 8 * c + 2 * r) = fold_rows(acc);
    cnt_arrive(lds, C_END, lane);
#pragma unroll
    for (int u = 0; u < 4; ++u) *(LAS u32x4*)(lds + L_WT + (wave * 16 + 4 * u + r) * WT_STR + 4 * c) = bB[u];
    cnt_arrive(lds, C_WT, lane);
    asm volatile(".Lfdk_last:");
    return;
#undef PACE
  }

  const int a = wave - NMAIN;
  const rsrc_t rx = mkrsrc(x, D * 2), rws = mkrsrc(ws, WS_END);
  unsigned tagA, tagB, tagC, tagD;
  unsigned long long e2;
  {
    u32x4 xa = ld16(rx, lane * 32, 0), xb = ld16(rx, lane * 32 + 16, 0);
    const u32x2 ep = __builtin_amdgcn_raw_buffer_load_b64(rws, 0, WS_EPOCH, SC1);
    const rsrc_t rwq = mkrsrc(wq, D * H * DH * 2);
    u32x4 w[8];
    const int voff = r * 8192 + c * 16, sb = (s * 128 + a * 32) * 8192 + h * 256;
#pragma unroll
    for (int i = 0; i < 8; ++i) { w[i] = ld16_nt(rwq, voff, sb + i * 32768); SB(); }
    if (a == 0 && lane < C_NUM) *(LAS unsigned*)(lds + L_CNT + lane) = 0u;
    __syncthreads();
    asm volatile("" : "+v"(xa), "+v"(xb));
    float v[16];
#pragma unroll
    for (int i = 0; i < 4; ++i) { v[2 * i] = lo2f(xa[i]); v[2 * i + 1] = hi2f(xa[i]); v[8 + 2 * i] = lo2f(xb[i]); v[9 + 2 * i] = hi2f(xb[i]); }
    float sm = 0.f;
#pragma unroll
    for (int i = 0; i < 16; ++i) sm += v[i];
    const float mean = wave_sum(sm) * (1.f / D);
    float ss = 0.f;
#pragma unroll
    for (int i = 0; i < 16; ++i) { const float d = v[i] - mean; ss = fmaf(d, d, ss); }
    const float var = wave_sum(ss) * (1.f / D);
    const float mb = rbf(mean), den = rbf(sqrtf(rbf(rbf(var) + 1e-5f)));
    const float rinv = 1.0f / den;
#pragma unroll
    for (int i = 0; i < 16; ++i) {
      const float nu = rbf(v[i] - mb), q0 = nu * rinv;
      v[i] = rbf(fmaf(fmaf(-q0, den, nu), rinv, q0));
    }
#pragma unroll
    for (int q = 0; q < 4; ++q) *(LAS f32x4*)(lds + L_XN + q * 256 + 4 * lane) = f32x4{v[q], v[4 + q], v[8 + q], v[12 + q]};
    cnt_arrive(lds, C_XN, lane);
    const unsigned long long e1 = (((unsigned long long)__builtin_amdgcn_readfirstlane(ep[1]) << 32) | (unsigned)__builtin_amdgcn_readfirstlane(ep[0])) + 1ull;
    e2 = e1 + 1ull;
    tagA = (unsigned)e1; tagB = (unsigned)(e1 >> 32) + (unsigned)e1 * 0x9E3779B1u;
    tagC = (unsigned)e2; tagD = (unsigned)(e2 >> 32) + (unsigned)e2 * 0x9E3779B1u;
    float acc[8] = {0.f, 0.f, 0.f, 0.f, 0.f, 0.f, 0.f, 0.f};
#pragma unroll
    for (int q = 0; q < 2; ++q) {
      const f32x4 xv = *(LAS f32x4*)(lds + L_XN + r * 256 + s * 32 + a * 8 + 4 * q);
#pragma unroll
      for (int e = 0; e < 4; ++e) fma8(acc, xv[e], w[4 * q + e]);
    }
    *(LAS f32x2*)(lds + L_QP + a * 128 + 8 * c + 2 * r) = fold_rows(acc);
  }
  cnt_arrive(lds, C_Q, lane);
  if (a == 0) {
    cnt_wait<false>(lds, C_Q, NAUX);
    f32x2 v = *(LAS f32x2*)(lds + L_QP + 2 * lane);
#pragma unroll
    for (int q = 1; q < 4; ++q) v += *(LAS f32x2*)(lds + L_QP + q * 128 + 2 * lane);
    st16_sc1(rws, WS_Q + (h * 8 + s) * 1024 + lane * 16, rec(v[0], v[1], tagA, tagB));
    st16_sc1(rws, (bid == 0 && lane == 0) ? WS_T0 : OOB, u32x4{t0, tagA, t0, tagB});
  }
  if (a == 1) {
    pace_wait(t0, T_QPOLL);
    float q0 = 0.f, q1 = 0.f;
    unsigned tg = t0;
#pragma unroll 1
    for (unsigned sp = 0; sp < SPIN_MEM; ++sp) {
      REREAD();
      u32x4 g[8];
#pragma unroll
      for (int i = 0; i < 8; ++i) g[i] = ld16_sc1(rws, lane * 16, WS_Q + (h * 8 + i) * 1024);
      const u32x4 gt = ld16_sc1(rws, 0, WS_T0);
      bool ok = rok(gt, tagA, tagB);
      tg = gt[0];
      q0 = 0.f; q1 = 0.f;
#pragma unroll
      for (int i = 0; i < 8; ++i) { ok = ok && rok(g[i], tagA, tagB); q0 += r0(g[i]); q1 += r1(g[i]); }
      if (__all(ok)) break;
      __builtin_amdgcn_s_sleep(2);
    }
    *(LAS unsigned*)(lds + L_QS + lane) = pack2(q0, q1);
    if (lane == 0) *(LAS unsigned*)(lds + L_CNT + W_T0) = ((int)(t0 - tg) < 0 || (int)(t0 - tg) > 300) ? t0 : tg;
    cnt_arrive(lds, C_QR, lane);
  }
  const int ks = xl * 2 + (j >> 4), cg = j & 15;
  u32x4 dw[7];
  {
    const rsrc_t rd = mkrsrc(wd, D * F * 2);
    const int voff = (lane >> 3) * 2048 + (lane & 7) * 16, sb = (ks * 224 + a * 56) * 2048 + cg * 128;
    constexpr int WSTEP = ((T_K0 - T_W2) << 16) / 7;
    int wdue = T_W2 * 65536 + (WSTEP / 128) * (a * 32 + j);
#pragma unroll
    for (int i = 0; i < 7; ++i) { pace_wait(t0, wdue >> 16); wdue += WSTEP; dw[i] = ld16_nt(rd, voff, sb + i * 16384); SB(); }
  }
  code_touch(a, lane);
  if (a == 3) {
    pace_wait(t0, T_TOUCH);
    const u32x4 p = ld16_sc1(rws, lane * 16, WS_Y + (h * 8 + s) * 1024);
    asm volatile("" :: "v"(p));
  }
  if (a == 0 && j < 28) {
    cnt_wait(lds, C_F1, NMAIN);
    f32x2 v = *(LAS f32x2*)(lds + L_FP + 2 * (lane & 31));
#pragma unroll
    for (int q = 1; q < 8; ++q) v += *(LAS f32x2*)(lds + L_FP + q * 64 + 2 * (lane & 31));
    st16_sc1(rws, lane < 32 ? WS_G1 + ((mat * 2 + rb) * F + cb * 64) * 8 + lane * 16 : OOB, rec(v[0], v[1], tagA, tagB));
  }
  {
    pace_wait(t0, T_K0 + 2000);
    u32x4 g[4];
    const int voff = (lane < 28 ? lane : 27) * 16;
#pragma unroll 1
    for (unsigned sp = 0; sp < SPIN_MEM; ++sp) {
      REREAD();
#pragma unroll
      for (int k = 0; k < 4; ++k) g[k] = ld16_sc1(rws, voff, WS_G1 + (k * F + ks * 224 + a * 56) * 8);
      if (__all(rok(g[0], tagA, tagB) && rok(g[1], tagA, tagB) && rok(g[2], tagA, tagB) && rok(g[3], tagA, tagB))) break;
      __builtin_amdgcn_s_sleep(32);
    }
    float hh[2];
#pragma unroll
    for (int e = 0; e < 2; ++e) {
      const float gs = rbf(__uint_as_float(g[0][2 * e]) + __uint_as_float(g[1][2 * e]));
      const float us = rbf(__uint_as_float(g[2][2 * e]) + __uint_as_float(g[3][2 * e]));
      const float ge = 0.5f * gs * (1.f + tanhf(0.7978845608028654f * (gs + 0.044715f * gs * gs * gs)));
      hh[e] = rbf(rbf(ge) * us);
    }
    if (lane < 28) *(LAS f32x2*)(lds + L_H + a * 64 + 2 * lane) = f32x2{hh[0], hh[1]};
    float y[8] = {0.f, 0.f, 0.f, 0.f, 0.f, 0.f, 0.f, 0.f};
#pragma unroll
    for (int i = 0; i < 7; ++i) fma8(y, lds[L_H + a * 64 + 8 * i + (lane >> 3)], dw[i]);
#pragma unroll
    for (int e = 0; e < 8; ++e) y[e] += dpp<ROR8>(y[e]);
    const f32x2 z = fold_rows(y);
    if (c < 8) *(LAS f32x2*)(lds + L_F2 + a * 64 + 8 * c + 2 * r) = z;
    cnt_arrive(lds, C_F2, lane);
  }
  const int col0 = s * 128 + 4 * h;
  float f0 = 0.f, f1v = 0.f;
  unsigned xw = 0;
  if (a == 0) {
    cnt_wait(lds, C_F2, NAUX);
    f32x2 v = *(LAS f32x2*)(lds + L_F2 + 2 * (lane & 31));
#pragma unroll
    for (int q = 1; q < 4; ++q) v += *(LAS f32x2*)(lds + L_F2 + q * 64 + 2 * (lane & 31));
    st16_sc1(rws, lane < 32 ? WS_FFN + (ks * D + cg * 64) * 8 + lane * 16 : OOB, rec(v[0], v[1], tagA, tagB));
    xw = __builtin_amdgcn_raw_buffer_load_b32(rx, (col0 + 2 * (lane & 1)) * 2, 0, 0);
    pace_wait(t0, T_K0 + 3500);
    u32x4 g;
#pragma unroll 1
    for (unsigned sp = 0; sp < SPIN_MEM; ++sp) {
      REREAD();
      g = ld16_sc1(rws, ((lane >> 1) & 15) * (D * 8) + (lane & 1) * 16, WS_FFN + col0 * 8);
      if (__all(rok(g, tagA, tagB))) break;
      __builtin_amdgcn_s_sleep(32);
    }
    f0 = rbf(parity_sum(r0(g)) * 0.5f); f1v = rbf(parity_sum(r1(g)) * 0.5f);
  }
  if (a == 1) {
    cnt_wait(lds, C_ML, NMAIN);
    const f32x2 ml = *(LAS f32x2*)(lds + L_ML + 2 * (lane & 7));
    const float Mw = max8(ml[0]), Lw = sum8(exp2_(ml[0] - Mw) * ml[1]);
    st16_sc1(rws, lane == 0 ? WS_ML + (h * 8 + s) * 128 : OOB, rec(Mw, Lw, tagA, tagB));
    pace_wait(t0, T_K0 + T_MAIN / 2 + 1500);
    u32x4 g;
#pragma unroll 1
    for (unsigned sp = 0; sp < SPIN_MEM; ++sp) {
      REREAD();
      g = ld16_sc1(rws, (lane & 7) * 128, WS_ML + h * 8 * 128);
      if (__all(rok(g, tagA, tagB))) break;
      __builtin_amdgcn_s_sleep(32);
    }
    const float M = max8(r0(g)), L = sum8(exp2_(r0(g) - M) * r1(g));
    if (lane < 8) lds[L_WG + lane] = exp2_(ml[0] - M) / L;
    cnt_arrive(lds, C_WG, lane);
  }
  pace_wait(t0, T_K0 + T_MAIN - 100);
  if (a == 0) {
    cnt_wait<false>(lds, C_END, NMAIN);
    f32x2 v = *(LAS f32x2*)(lds + L_MRG + 2 * lane);
#pragma unroll
    for (int w = 1; w < 8; ++w) v += *(LAS f32x2*)(lds + L_MRG + w * 128 + 2 * lane);
    st16_sc1(rws, WS_Q + (h * 8 + s) * 1024 + lane * 16, rec(v[0], v[1], tagC, tagD));
  } else {
    cnt_wait(lds, C_END, NMAIN);
  }
  u32x4 bfr[2][4];
  cnt_wait<false>(lds, C_WT, NMAIN);
#pragma unroll
  for (int q = 0; q < 4; ++q)
#pragma unroll
    for (int d = 0; d < 4; ++d) {
      const unsigned w0 = *(LAS unsigned*)(lds + L_WT + (32 * q + 8 * r + 2 * d) * WT_STR + 16 * a + c);
      const unsigned w1 = *(LAS unsigned*)(lds + L_WT + (32 * q + 8 * r + 2 * d + 1) * WT_STR + 16 * a + c);
      bfr[0][q][d] = __builtin_amdgcn_perm(w1, w0, 0x05040100u);
      bfr[1][q][d] = __builtin_amdgcn_perm(w1, w0, 0x07060302u);
    }
  {
    u32x4 g0, g1;
    const int vo = r * 1024 + (16 * a + c) * 16;
#pragma unroll 1
    for (unsigned sp = 0; sp < SPIN_MEM; ++sp) {
      REREAD();
      g0 = ld16_sc1(rws, vo, WS_Q + h * 8 * 1024);
      g1 = ld16_sc1(rws, vo, WS_Q + (h * 8 + 4) * 1024);
      if (__all(rok(g0, tagC, tagD) && rok(g1, tagC, tagD))) break;
    }
    float o = fold32(r0(g0) + r0(g1), r1(g0) + r1(g1));
    o = fold16(o, o);
    if ((r & 1) == 0) *((LAS u16*)(lds + L_OB) + 32 * a + 2 * c + (r >> 1)) = __builtin_bit_cast(u16, (__bf16)o);
  }
  cnt_arrive(lds, C_OB, lane);
  cnt_wait<false>(lds, C_OB, NAUX);
  {
    f32x4 y0 = {0.f, 0.f, 0.f, 0.f}, y1 = y0;
#pragma unroll
    for (int q = 0; q < 4; ++q) {
      const u32x4 af = *(LAS u32x4*)(lds + L_OB + 16 * q + 4 * r);
      y0 = __builtin_amdgcn_mfma_f32_16x16x32_bf16(__builtin_bit_cast(bf16x8, af), __builtin_bit_cast(bf16x8, bfr[0][q]), y0, 0, 0, 0);
      y1 = __builtin_amdgcn_mfma_f32_16x16x32_bf16(__builtin_bit_cast(bf16x8, af), __builtin_bit_cast(bf16x8, bfr[1][q]), y1, 0, 0, 0);
    }
    st16_sc1(rws, lane < 16 ? WS_Y + (h * 8 + s) * 1024 + (16 * a + lane) * 16 : OOB, rec(y0[0], y1[0], tagA, tagB));
  }
  if (a == 0) {
    u32x4 g;
#pragma unroll 1
    for (unsigned sp = 0; sp < SPIN_MEM; ++sp) {
      REREAD();
      g = ld16_sc1(rws, (lane >> 1) * (8 * 1024) + (lane & 1) * 16, WS_Y + s * 1024 + h * 32);
      if (__all(rok(g, tagA, tagB))) break;
    }
    const float a0 = rbf(parity_sum(r0(g))), a1 = rbf(parity_sum(r1(g)));
    const unsigned o = pack2(rbf(lo2f(xw) + a0) + f0, rbf(hi2f(xw) + a1) + f1v);
    __builtin_amdgcn_raw_buffer_store_b32(o, mkrsrc(out, D * 2), lane < 2 ? (col0 + 2 * lane) * 2 : OOB, 0, 0);
    __builtin_amdgcn_raw_buffer_store_b64(u32x2{(unsigned)e2, (unsigned)(e2 >> 32)}, rws, (bid == 0 && lane == 0) ? WS_EPOCH : OOB, 0, SC1);
  }
}

extern "C" void kernel_launch(void* const* d_in, const int* in_sizes, int n_in, void* d_out, int out_size, void* d_ws, size_t ws_size,
                              hipStream_t stream) {
  fused_decode_kernel<<<256, NTHR, 0, stream>>>((const u16*)d_in[0], (const u16*)d_in[1], (const u16*)d_in[2], (const u16*)d_in[3],
                                                 (const u16*)d_in[4], (const u16*)d_in[6], (const u16*)d_in[7], (const u16*)d_in[8],
                                                 (u16*)d_out, d_ws);
}
```

```cpp
#define DM 0
#define T_MAIN 7300
#include <hip/hip_runtime.h>
#include <stdint.h>

typedef unsigned u32x4 __attribute__((ext_vector_type(4)));
typedef unsigned u32x2 __attribute__((ext_vector_type(2)));
typedef float f32x4 __attribute__((ext_vector_type(4)));
typedef float f32x2 __attribute__((ext_vector_type(2)));
typedef __bf16 bf16x2 __attribute__((ext_vector_type(2)));
typedef __bf16 bf16x8 __attribute__((ext_vector_type(8)));
typedef unsigned short u16;
#define LAS __attribute__((address_space(3)))
typedef __amdgpu_buffer_rsrc_t rsrc_t;

#define T_F1 150
#define T_W2 360
#define T_K0 460
#define PACE_G 2
#define PHASE (wave * 32 + j)
constexpr int T_QPOLL = 390, T_TOUCH = 1000;

constexpr int D = 1024, H = 32, SK = 32768, DH = 128, F = 3584;
constexpr int NMAIN = 8, NAUX = 4, NTHR = (NMAIN + NAUX) * 64;
constexpr int NT = 2, SC1 = 16;
constexpr int OOB = 0x40000000;
constexpr unsigned WS_EPOCH = 0, WS_T0 = 1024, WS_ML = 4096, WS_Q = WS_ML + 256 * 128, WS_Y = WS_Q + 256 * 1024, WS_G1 = WS_Y + 256 * 1024,
                   WS_FFN = WS_G1 + 4 * F * 8, WS_END = WS_FFN + 16 * D * 8;
constexpr int L_XN = 0, L_QP = 1024, L_QS = L_QP + 4 * 128, L_MRG = L_QS + 64, L_ML = L_MRG + 8 * 128, L_WG = L_ML + 16, L_FP = L_WG + 8,
              L_H = L_FP + 8 * 64, L_F2 = L_H + 4 * 64, L_OB = L_F2 + 4 * 64, L_CNT = L_OB + 64, L_SC = L_CNT + 16, L_WT = L_SC + 8 * 512,
              WT_STR = 68, L_END = L_WT + 128 * WT_STR;
constexpr int C_Q = 0, C_XN = 1, C_F2 = 2, C_WT = 3, C_ML = 4, C_WG = 5, C_END = 6, C_QR = 7, C_F1 = 8, C_OB = 9, C_NUM = 10, W_T0 = 12;
constexpr unsigned SPIN_MEM = 1u << 18, SPIN_LDS = 1u << 22;

__device__ __forceinline__ float lo2f(unsigned w) { return __builtin_bit_cast(float, w << 16); }
__device__ __forceinline__ float hi2f(unsigned w) { return __builtin_bit_cast(float, w & 0xffff0000u); }
__device__ __forceinline__ float rbf(float x) { return (float)(__bf16)x; }
__device__ __forceinline__ unsigned pack2(float a, float b) { bf16x2 v = {(__bf16)a, (__bf16)b}; return __builtin_bit_cast(unsigned, v); }
__device__ __forceinline__ float dot2(unsigned k, unsigned q, float acc) {
  return __builtin_amdgcn_fdot2_f32_bf16(__builtin_bit_cast(bf16x2, k), __builtin_bit_cast(bf16x2, q), acc, false);
}
template <int CTRL> __device__ __forceinline__ float dpp(float x) {
  return __builtin_bit_cast(float, __builtin_amdgcn_mov_dpp(__builtin_bit_cast(int, x), CTRL, 0xf, 0xf, true));
}
constexpr int XOR1 = 0xB1, XOR2 = 0x4E, XOR7 = 0x141, ROR8 = 0x128, ROR4 = 0x124, BCAST = 0x150;
__device__ __forceinline__ float fold32(float a, float b) {
  auto t = __builtin_amdgcn_permlane32_swap(__float_as_uint(a), __float_as_uint(b), false, false);
  return __uint_as_float(t[0]) + __uint_as_float(t[1]);
}
__device__ __forceinline__ float fold16(float a, float b) {
  auto t = __builtin_amdgcn_permlane16_swap(__float_as_uint(a), __float_as_uint(b), false, false);
  return __uint_as_float(t[0]) + __uint_as_float(t[1]);
}
__device__ __forceinline__ f32x2 fold_rows(const float (&v)[8]) {
  const float u0 = fold32(v[0], v[4]), u1 = fold32(v[1], v[5]), u2 = fold32(v[2], v[6]), u3 = fold32(v[3], v[7]);
  return f32x2{fold16(u0, u2), fold16(u1, u3)};
}
__device__ __forceinline__ float xrow_sum(float x) { x = fold16(x, x); return fold32(x, x); }
__device__ __forceinline__ float xrow_max(float x) {
  auto s = __builtin_amdgcn_permlane16_swap(__float_as_uint(x), __float_as_uint(x), false, false);
  x = fmaxf(__uint_as_float(s[0]), __uint_as_float(s[1]));
  auto t = __builtin_amdgcn_permlane32_swap(__float_as_uint(x), __float_as_uint(x), false, false);
  return fmaxf(__uint_as_float(t[0]), __uint_as_float(t[1]));
}
__device__ __forceinline__ float parity_sum(float x) {
  x += dpp<XOR2>(x); x += dpp<ROR4>(x); x += dpp<ROR8>(x); return xrow_sum(x);
}
__device__ __forceinline__ float wave_sum(float x) { x += dpp<XOR1>(x); return parity_sum(x); }
__device__ __forceinline__ float max8(float x) { x = fmaxf(x, dpp<XOR1>(x)); x = fmaxf(x, dpp<XOR2>(x)); return fmaxf(x, dpp<XOR7>(x)); }
__device__ __forceinline__ float sum8(float x) { x += dpp<XOR1>(x); x += dpp<XOR2>(x); return x + dpp<XOR7>(x); }
__device__ __forceinline__ float wave_max(float x) { x = max8(x); x = fmaxf(x, dpp<ROR8>(x)); return xrow_max(x); }
__device__ __forceinline__ float exp2_(float x) { return __builtin_amdgcn_exp2f(x); }
__device__ __forceinline__ rsrc_t mkrsrc(const void* p, unsigned bytes) { return __builtin_amdgcn_make_buffer_rsrc((void*)p, 0, (int)bytes, 0x00020000); }
__device__ __forceinline__ u32x4 ld16(rsrc_t r, int voff, int soff) { return __builtin_amdgcn_raw_buffer_load_b128(r, voff, soff, 0); }
__device__ __forceinline__ u32x4 ld16_nt(rsrc_t r, int voff, int soff) { return __builtin_amdgcn_raw_buffer_load_b128(r, voff, soff, NT); }
__device__ __forceinline__ u32x4 ld16_sc1(rsrc_t r, int voff, int soff) { return __builtin_amdgcn_raw_buffer_load_b128(r, voff, soff, SC1); }
__device__ __forceinline__ void st16_sc1(rsrc_t r, int off, u32x4 v) { __builtin_amdgcn_raw_buffer_store_b128(v, r, off, 0, SC1); }
#define SB() __builtin_amdgcn_sched_barrier(0)
#define REREAD() asm volatile("" ::: "memory")
__device__ __forceinline__ void pace_wait(unsigned t0, int due) {
  asm volatile("1:\n\ts_memrealtime s[96:97]\n\ts_waitcnt lgkmcnt(0)\n\ts_sub_u32 s96, s96, %0\n\ts_cmp_lt_i32 s96, %1\n\t"
               "s_cbranch_scc0 2f\n\ts_sleep 1\n\ts_branch 1b\n2:"
               :: "s"(t0), "s"(due) : "s96", "s97", "scc", "memory");
}
__device__ __forceinline__ void fma8(float (&acc)[8], float s, u32x4 w) {
#pragma unroll
  for (int t = 0; t < 4; ++t) { acc[2 * t] = fmaf(s, lo2f(w[t]), acc[2 * t]); acc[2 * t + 1] = fmaf(s, hi2f(w[t]), acc[2 * t + 1]); }
}
__device__ __forceinline__ u32x4 rec(float a, float b, unsigned ta, unsigned tb) { return u32x4{__float_as_uint(a), ta, __float_as_uint(b), tb}; }
__device__ __forceinline__ bool rok(u32x4 g, unsigned ta, unsigned tb) { return g[1] == ta && g[3] == tb; }
__device__ __forceinline__ float r0(u32x4 g) { return __uint_as_float(g[0]); }
__device__ __forceinline__ float r1(u32x4 g) { return __uint_as_float(g[2]); }

__device__ __forceinline__ void cnt_arrive(LAS float* lds, int which, int lane) {
  asm volatile("s_waitcnt lgkmcnt(0)" ::: "memory");
  if (lane == 0) __hip_atomic_fetch_add((LAS unsigned*)(lds + L_CNT + which), 1u, __ATOMIC_RELAXED, __HIP_MEMORY_SCOPE_WORKGROUP);
}
template <bool SLEEP = true>
__device__ __forceinline__ void cnt_wait(LAS float* lds, int which, unsigned n) {
#pragma unroll 1
  for (unsigned sp = 0; sp < SPIN_LDS; ++sp) {
    if (__builtin_amdgcn_readfirstlane(*(volatile LAS unsigned*)(lds + L_CNT + which)) >= n) break;
    if (SLEEP) __builtin_amdgcn_s_sleep(1);
  }
  REREAD();
}

__global__ void __launch_bounds__(NTHR, 3)
fused_decode_kernel(const u16* __restrict__ x, const u16* __restrict__ Kc, const u16* __restrict__ Vc, const u16* __restrict__ wq,
                    const u16* __restrict__ wo, const u16* __restrict__ wg, const u16* __restrict__ wu, const u16* __restrict__ wd,
                    u16* __restrict__ out, void* ws) {
  __shared__ __attribute__((aligned(16))) float lds_[L_END];
  LAS float* lds = (LAS float*)lds_;
  const unsigned t0 = (unsigned)__builtin_amdgcn_s_memrealtime();
  const int tid = threadIdx.x, lane = tid & 63, wave = __builtin_amdgcn_readfirstlane(tid >> 6);
  const int r = lane >> 4, c = lane & 15;
  const int bid = blockIdx.x, xl = bid & 7, j = bid >> 3;
  const int h = xl * 4 + (j >> 3), s = j & 7;
  const int f1 = xl * 28 + j, mat = f1 / 112, rb = (f1 / 56) & 1, cb = f1 % 56;

  if (wave < NMAIN) {
    const rsrc_t rk = mkrsrc(Kc, (unsigned)H * SK * DH * 2), rv = mkrsrc(Vc, (unsigned)H * SK * DH * 2);
    u32x4 bA[16], bB[16];
    const unsigned kvbase = (unsigned)h * (8u << 20) + (unsigned)s * (1u << 20) + wave * 1024;
    const unsigned rot = 32u * ((h + (h >> 2) + (h >> 4)) & 3);
    const int kvoff = lane * 16;
    constexpr int STEP = (T_MAIN << 16) / 256 * PACE_G;
    int due = T_K0 * 65536 - STEP + (STEP / 256) * (PHASE);
#define PACE(T0, I) do { if ((I) % PACE_G == 0) { due += STEP; pace_wait(T0, due >> 16); } } while (0)
    __builtin_amdgcn_s_barrier();
    SB();
    {
      const rsrc_t rg = mkrsrc(mat ? wu : wg, D * F * 2);
      const int voff = j < 28 ? (lane >> 3) * (F * 2) + (lane & 7) * 16 : OOB, sb = (rb * 512 + wave * 64) * (F * 2) + cb * 128;
      constexpr int FSTEP = ((T_W2 - T_F1) << 16) / 8;
      int fdue = T_F1 * 65536 + (FSTEP / 256) * (wave * 32 + j);
#pragma unroll
      for (int i = 0; i < 8; ++i) { pace_wait(t0, fdue >> 16); fdue += FSTEP; bB[i] = ld16_nt(rg, voff, sb + i * 8 * (F * 2)); SB(); }
    }
#pragma unroll
    for (int i = 0; i < 16; ++i) { PACE(t0, i); bA[i] = ld16_nt(rk, kvoff, kvbase + (rot + i) * 8192); SB(); }
    {
      cnt_wait(lds, C_XN, 1);
      float ag[8] = {0.f, 0.f, 0.f, 0.f, 0.f, 0.f, 0.f, 0.f};
#pragma unroll
      for (int i = 0; i < 8; ++i) {
        const int k = rb * 512 + wave * 64 + 8 * i + (lane >> 3);
        fma8(ag, lds[L_XN + (k & 3) * 256 + (k >> 2)], bB[i]);
      }
#pragma unroll
      for (int e = 0; e < 8; ++e) ag[e] += dpp<ROR8>(ag[e]);
      const f32x2 z = fold_rows(ag);
      if (c < 8) *(LAS f32x2*)(lds + L_FP + wave * 64 + 8 * c + 2 * r) = z;
      cnt_arrive(lds, C_F1, lane);
      SB();
    }
#pragma unroll
    for (int i = 0; i < 16; ++i) { PACE(t0, i); bB[i] = ld16_nt(rk, kvoff, kvbase + (rot + 16 + i) * 8192); SB(); }
    cnt_wait(lds, C_QR, 1);
    const u32x4 qp = *(LAS u32x4*)(lds + L_QS + 4 * c);
    const unsigned t0m = __builtin_amdgcn_readfirstlane(*(LAS unsigned*)(lds + L_CNT + W_T0));
    constexpr float SCALE = 0.08838834764831845f, LOG2E = 1.4426950408889634f;
    LAS float* scp = lds + L_SC + wave * 512 + lane * 8;
    float mx = -INFINITY;
    auto kgroup = [&](u32x4 (&kb)[16], const bool more, const rsrc_t rn, const unsigned tb, const int g) __attribute__((always_inline)) {
      float sx[16];
#pragma unroll
      for (int i = 0; i < 16; ++i) {
        float d = 0.f;
#pragma unroll
        for (int t = 0; t < 4; ++t) d = dot2(kb[i][t], qp[t], d);
        asm volatile("" : "+v"(d));
        sx[i] = d;
        SB();
        if (more) { PACE(t0m, i); kb[i] = ld16_nt(rn, kvoff, tb + i * 8192); SB(); }
      }
#define TR_STEP(HALF, CTRL, BIT) _Pragma("unroll") for (int hh = 0; hh < (HALF); ++hh) { \
        const float lo_ = sx[hh], hi_ = sx[hh + (HALF)]; const float t1_ = lo_ + dpp<CTRL>(lo_), t2_ = hi_ + dpp<CTRL>(hi_); \
        sx[hh] = (c & (BIT)) ? t2_ : t1_; }
      TR_STEP(8, ROR8, 8) TR_STEP(4, XOR7, 4) TR_STEP(2, XOR2, 2) TR_STEP(1, XOR1, 1)
#undef TR_STEP
      const float sc = rbf(rbf(sx[0]) * SCALE) * LOG2E;
      mx = fmaxf(mx, sc);
      scp[g] = sc;
      SB();
    };
    kgroup(bA, false, rk, 0, 0);
#pragma unroll 1
    for (int g = 1; g < 8; ++g)
      kgroup(bB, true, mkrsrc(g == 7 ? Vc : Kc, (unsigned)H * SK * DH * 2), kvbase + (((g + 1) * 16 + rot) & 127u) * 8192, g);
    {
      const float m = wave_max(mx);
      f32x4 p0 = *(LAS f32x4*)scp, p1 = *(LAS f32x4*)(scp + 4);
      float l = 0.f;
#pragma unroll
      for (int e = 0; e < 4; ++e) { p0[e] = exp2_(p0[e] - m); p1[e] = exp2_(p1[e] - m); l += p0[e] + p1[e]; }
      *(LAS f32x4*)scp = p0; *(LAS f32x4*)(scp + 4) = p1;
      l = wave_sum(l);
      if (lane == 0) *(LAS f32x2*)(lds + L_ML + 2 * wave) = f32x2{m, l};
      cnt_arrive(lds, C_ML, lane);
    }
    float acc[8] = {0.f, 0.f, 0.f, 0.f, 0.f, 0.f, 0.f, 0.f};
    auto vgroup = [&](const bool more, const unsigned tb, const float p) __attribute__((always_inline)) {
#define PV_STEP(I) { fma8(acc, dpp<BCAST + (I)>(p), bB[I]); \
        asm volatile("" : "+v"(acc[0]), "+v"(acc[1]), "+v"(acc[2]), "+v"(acc[3]), "+v"(acc[4]), "+v"(acc[5]), "+v"(acc[6]), "+v"(acc[7])); SB(); \
        if (more) { PACE(t0m, I); bB[I] = ld16_nt(rv, kvoff, tb + (I) * 8192); SB(); } \
        else if ((I) == 11) {   \
          pace_wait(t0m, T_K0 + T_MAIN); \
          _Pragma("unroll") for (int u = 0; u < 4; ++u) { \
            bB[u] = ld16_nt(mkrsrc(wo, H * DH * D * 2), r * 2048 + c * 16, (h * 128 + wave * 16 + 4 * u) * 2048 + s * 256); SB(); } } }
      PV_STEP(0) PV_STEP(1) PV_STEP(2) PV_STEP(3) PV_STEP(4) PV_STEP(5) PV_STEP(6) PV_STEP(7)
      PV_STEP(8) PV_STEP(9) PV_STEP(10) PV_STEP(11) PV_STEP(12) PV_STEP(13) PV_STEP(14) PV_STEP(15)
#undef PV_STEP
    };
#pragma unroll 1
    for (int g = 0; g < 7; ++g) vgroup(true, kvbase + (((g + 1) * 16 + rot) & 127u) * 8192, scp[g]);
    __builtin_amdgcn_s_setprio(3);
    cnt_wait(lds, C_WG, 1);
    {
      const float wgt = lds[L_WG + wave];
#pragma unroll
      for (int e = 0; e < 8; ++e) acc[e] *= wgt;
      vgroup(false, 0, scp[7] * wgt);
    }
    *(LAS f32x2*)(lds + L_MRG + wave * 128 + 8 * c + 2 * r) = fold_rows(acc);
    cnt_arrive(lds, C_END, lane);
#pragma unroll
    for (int u = 0; u < 4; ++u) *(LAS u32x4*)(lds + L_WT + (wave * 16 + 4 * u + r) * WT_STR + 4 * c) = bB[u];
    cnt_arrive(lds, C_WT, lane);
    return;
#undef PACE
  }

  const int a = wave - NMAIN;
  const rsrc_t rx = mkrsrc(x, D * 2), rws = mkrsrc(ws, WS_END);
  unsigned tagA, tagB, tagC, tagD;
  unsigned long long e2;
  {
    u32x4 xa = ld16(rx, lane * 32, 0), xb = ld16(rx, lane * 32 + 16, 0);
    const u32x2 ep = __builtin_amdgcn_raw_buffer_load_b64(rws, 0, WS_EPOCH, SC1);
    const rsrc_t rwq = mkrsrc(wq, D * H * DH * 2);
    u32x4 w[8];
    const int voff = r * 8192 + c * 16, sb = (s * 128 + a * 32) * 8192 + h * 256;
#pragma unroll
    for (int i = 0; i < 8; ++i) { w[i] = ld16_nt(rwq, voff, sb + i * 32768); SB(); }
    if (a == 0 && lane < C_NUM) *(LAS unsigned*)(lds + L_CNT + lane) = 0u;
    __syncthreads();
    asm volatile("" : "+v"(xa), "+v"(xb));
    float v[16];
#pragma unroll
    for (int i = 0; i < 4; ++i) { v[2 * i] = lo2f(xa[i]); v[2 * i + 1] = hi2f(xa[i]); v[8 + 2 * i] = lo2f(xb[i]); v[9 + 2 * i] = hi2f(xb[i]); }
    float sm = 0.f;
#pragma unroll
    for (int i = 0; i < 16; ++i) sm += v[i];
    const float mean = wave_sum(sm) * (1.f / D);
    float ss = 0.f;
#pragma unroll
    for (int i = 0; i < 16; ++i) { const float d = v[i] - mean; ss = fmaf(d, d, ss); }
    const float var = wave_sum(ss) * (1.f / D);
    const float mb = rbf(mean), den = rbf(sqrtf(rbf(rbf(var) + 1e-5f)));
    const float rinv = 1.0f / den;
#pragma unroll
    for (int i = 0; i < 16; ++i) {
      const float nu = rbf(v[i] - mb), q0 = nu * rinv;
      v[i] = rbf(fmaf(fmaf(-q0, den, nu), rinv, q0));
    }
#pragma unroll
    for (int q = 0; q < 4; ++q) *(LAS f32x4*)(lds + L_XN + q * 256 + 4 * lane) = f32x4{v[q], v[4 + q], v[8 + q], v[12 + q]};
    cnt_arrive(lds, C_XN, lane);
    const unsigned long long e1 = (((unsigned long long)__builtin_amdgcn_readfirstlane(ep[1]) << 32) | (unsigned)__builtin_amdgcn_readfirstlane(ep[0])) + 1ull;
    e2 = e1 + 1ull;
    tagA = (unsigned)e1; tagB = (unsigned)(e1 >> 32) + (unsigned)e1 * 0x9E3779B1u;
    tagC = (unsigned)e2; tagD = (unsigned)(e2 >> 32) + (unsigned)e2 * 0x9E3779B1u;
    float acc[8] = {0.f, 0.f, 0.f, 0.f, 0.f, 0.f, 0.f, 0.f};
#pragma unroll
    for (int q = 0; q < 2; ++q) {
      const f32x4 xv = *(LAS f32x4*)(lds + L_XN + r * 256 + s * 32 + a * 8 + 4 * q);
#pragma unroll
      for (int e = 0; e < 4; ++e) fma8(acc, xv[e], w[4 * q + e]);
    }
    *(LAS f32x2*)(lds + L_QP + a * 128 + 8 * c + 2 * r) = fold_rows(acc);
  }
  cnt_arrive(lds, C_Q, lane);
  if (a == 0) {
    cnt_wait<false>(lds, C_Q, NAUX);
    f32x2 v = *(LAS f32x2*)(lds + L_QP + 2 * lane);
#pragma unroll
    for (int q = 1; q < 4; ++q) v += *(LAS f32x2*)(lds + L_QP + q * 128 + 2 * lane);
    st16_sc1(rws, WS_Q + (h * 8 + s) * 1024 + lane * 16, rec(v[0], v[1], tagA, tagB));
    st16_sc1(rws, (bid == 0 && lane == 0) ? WS_T0 : OOB, u32x4{t0, tagA, t0, tagB});
  }
  if (a == 1) {
    pace_wait(t0, T_QPOLL);
    float q0 = 0.f, q1 = 0.f;
    unsigned tg = t0;
#pragma unroll 1
    for (unsigned sp = 0; sp < SPIN_MEM; ++sp) {
      REREAD();
      u32x4 g[8];
#pragma unroll
      for (int i = 0; i < 8; ++i) g[i] = ld16_sc1(rws, lane * 16, WS_Q + (h * 8 + i) * 1024);
      const u32x4 gt = ld16_sc1(rws, 0, WS_T0);
      bool ok = rok(gt, tagA, tagB);
      tg = gt[0];
      q0 = 0.f; q1 = 0.f;
#pragma unroll
      for (int i = 0; i < 8; ++i) { ok = ok && rok(g[i], tagA, tagB); q0 += r0(g[i]); q1 += r1(g[i]); }
      if (__all(ok)) break;
      __builtin_amdgcn_s_sleep(2);
    }
    *(LAS unsigned*)(lds + L_QS + lane) = pack2(q0, q1);
    if (lane == 0) *(LAS unsigned*)(lds + L_CNT + W_T0) = ((int)(t0 - tg) < 0 || (int)(t0 - tg) > 300) ? t0 : tg;
    cnt_arrive(lds, C_QR, lane);
  }
  const int ks = xl * 2 + (j >> 4), cg = j & 15;
  u32x4 dw[7];
  {
    const rsrc_t rd = mkrsrc(wd, D * F * 2);
    const int voff = (lane >> 3) * 2048 + (lane & 7) * 16, sb = (ks * 224 + a * 56) * 2048 + cg * 128;
    constexpr int WSTEP = ((T_K0 - T_W2) << 16) / 7;
    int wdue = T_W2 * 65536 + (WSTEP / 128) * (a * 32 + j);
#pragma unroll
    for (int i = 0; i < 7; ++i) { pace_wait(t0, wdue >> 16); wdue += WSTEP; dw[i] = ld16_nt(rd, voff, sb + i * 16384); SB(); }
  }
  if (a == 3) {
    pace_wait(t0, T_TOUCH);
    const u32x4 p = ld16_sc1(rws, lane * 16, WS_Y + (h * 8 + s) * 1024);
    asm volatile("" :: "v"(p));
  }
  if (a == 0 && j < 28) {
    cnt_wait(lds, C_F1, NMAIN);
    f32x2 v = *(LAS f32x2*)(lds + L_FP + 2 * (lane & 31));
#pragma unroll
    for (int q = 1; q < 8; ++q) v += *(LAS f32x2*)(lds + L_FP + q * 64 + 2 * (lane & 31));
    st16_sc1(rws, lane < 32 ? WS_G1 + ((mat * 2 + rb) * F + cb * 64) * 8 + lane * 16 : OOB, rec(v[0], v[1], tagA, tagB));
  }
  {
    pace_wait(t0, T_K0 + 2000);
    u32x4 g[4];
    const int voff = (lane < 28 ? lane : 27) * 16;
#pragma unroll 1
    for (unsigned sp = 0; sp < SPIN_MEM; ++sp) {
      REREAD();
#pragma unroll
      for (int k = 0; k < 4; ++k) g[k] = ld16_sc1(rws, voff, WS_G1 + (k * F + ks * 224 + a * 56) * 8);
      if (__all(rok(g[0], tagA, tagB) && rok(g[1], tagA, tagB) && rok(g[2], tagA, tagB) && rok(g[3], tagA, tagB))) break;
      __builtin_amdgcn_s_sleep(32);
    }
    float hh[2];
#pragma unroll
    for (int e = 0; e < 2; ++e) {
      const float gs = rbf(__uint_as_float(g[0][2 * e]) + __uint_as_float(g[1][2 * e]));
      const float us = rbf(__uint_as_float(g[2][2 * e]) + __uint_as_float(g[3][2 * e]));
      const float ge = 0.5f * gs * (1.f + tanhf(0.7978845608028654f * (gs + 0.044715f * gs * gs * gs)));
      hh[e] = rbf(rbf(ge) * us);
    }
    if (lane < 28) *(LAS f32x2*)(lds + L_H + a * 64 + 2 * lane) = f32x2{hh[0], hh[1]};
    float y[8] = {0.f, 0.f, 0.f, 0.f, 0.f, 0.f, 0.f, 0.f};
#pragma unroll
    for (int i = 0; i < 7; ++i) fma8(y, lds[L_H + a * 64 + 8 * i + (lane >> 3)], dw[i]);
#pragma unroll
    for (int e = 0; e < 8; ++e) y[e] += dpp<ROR8>(y[e]);
    const f32x2 z = fold_rows(y);
    if (c < 8) *(LAS f32x2*)(lds + L_F2 + a * 64 + 8 * c + 2 * r) = z;
    cnt_arrive(lds, C_F2, lane);
  }
  const int col0 = s * 128 + 4 * h;
  float f0 = 0.f, f1v = 0.f;
  unsigned xw = 0;
  if (a == 0) {
    cnt_wait(lds, C_F2, NAUX);
    f32x2 v = *(LAS f32x2*)(lds + L_F2 + 2 * (lane & 31));
#pragma unroll
    for (int q = 1; q < 4; ++q) v += *(LAS f32x2*)(lds + L_F2 + q * 64 + 2 * (lane & 31));
    st16_sc1(rws, lane < 32 ? WS_FFN + (ks * D + cg * 64) * 8 + lane * 16 : OOB, rec(v[0], v[1], tagA, tagB));
    xw = __builtin_amdgcn_raw_buffer_load_b32(rx, (col0 + 2 * (lane & 1)) * 2, 0, 0);
    pace_wait(t0, T_K0 + 3500);
    u32x4 g;
#pragma unroll 1
    for (unsigned sp = 0; sp < SPIN_MEM; ++sp) {
      REREAD();
      g = ld16_sc1(rws, ((lane >> 1) & 15) * (D * 8) + (lane & 1) * 16, WS_FFN + col0 * 8);
      if (__all(rok(g, tagA, tagB))) break;
      __builtin_amdgcn_s_sleep(32);
    }
    f0 = rbf(parity_sum(r0(g)) * 0.5f); f1v = rbf(parity_sum(r1(g)) * 0.5f);
  }
  if (a == 1) {
    cnt_wait(lds, C_ML, NMAIN);
    const f32x2 ml = *(LAS f32x2*)(lds + L_ML + 2 * (lane & 7));
    const float Mw = max8(ml[0]), Lw = sum8(exp2_(ml[0] - Mw) * ml[1]);
    st16_sc1(rws, lane == 0 ? WS_ML + (h * 8 + s) * 128 : OOB, rec(Mw, Lw, tagA, tagB));
    pace_wait(t0, T_K0 + T_MAIN / 2 + 1500);
    u32x4 g;
#pragma unroll 1
    for (unsigned sp = 0; sp < SPIN_MEM; ++sp) {
      REREAD();
      g = ld16_sc1(rws, (lane & 7) * 128, WS_ML + h * 8 * 128);
      if (__all(rok(g, tagA, tagB))) break;
      __builtin_amdgcn_s_sleep(32);
    }
    const float M = max8(r0(g)), L = sum8(exp2_(r0(g) - M) * r1(g));
    if (lane < 8) lds[L_WG + lane] = exp2_(ml[0] - M) / L;
    cnt_arrive(lds, C_WG, lane);
  }
  pace_wait(t0, T_K0 + T_MAIN - 100);
  if (a == 0) {
    cnt_wait<false>(lds, C_END, NMAIN);
    f32x2 v = *(LAS f32x2*)(lds + L_MRG + 2 * lane);
#pragma unroll
    for (int w = 1; w < 8; ++w) v += *(LAS f32x2*)(lds + L_MRG + w * 128 + 2 * lane);
    st16_sc1(rws, WS_Q + (h * 8 + s) * 1024 + lane * 16, rec(v[0], v[1], tagC, tagD));
  } else {
    cnt_wait(lds, C_END, NMAIN);
  }
  const unsigned tA = (unsigned)__builtin_amdgcn_s_memrealtime();
  u32x4 bfr[2][4];
  cnt_wait<false>(lds, C_WT, NMAIN);
#pragma unroll
  for (int q = 0; q < 4; ++q)
#pragma unroll
    for (int d = 0; d < 4; ++d) {
      const unsigned w0 = *(LAS unsigned*)(lds + L_WT + (32 * q + 8 * r + 2 * d) * WT_STR + 16 * a + c);
      const unsigned w1 = *(LAS unsigned*)(lds + L_WT + (32 * q + 8 * r + 2 * d + 1) * WT_STR + 16 * a + c);
      bfr[0][q][d] = __builtin_amdgcn_perm(w1, w0, 0x05040100u);
      bfr[1][q][d] = __builtin_amdgcn_perm(w1, w0, 0x07060302u);
    }
  asm volatile("s_waitcnt lgkmcnt(0)" ::: "memory");
  const unsigned tB = (unsigned)__builtin_amdgcn_s_memrealtime();
  unsigned tC;
  {
    u32x4 g0, g1;
    const int vo = r * 1024 + (16 * a + c) * 16;
#pragma unroll 1
    for (unsigned sp = 0; sp < SPIN_MEM; ++sp) {
      REREAD();
      g0 = ld16_sc1(rws, vo, WS_Q + h * 8 * 1024);
      g1 = ld16_sc1(rws, vo, WS_Q + (h * 8 + 4) * 1024);
      if (__all(rok(g0, tagC, tagD) && rok(g1, tagC, tagD))) break;
    }
    tC = (unsigned)__builtin_amdgcn_s_memrealtime();
    float o = fold32(r0(g0) + r0(g1), r1(g0) + r1(g1));
    o = fold16(o, o);
    if ((r & 1) == 0) *((LAS u16*)(lds + L_OB) + 32 * a + 2 * c + (r >> 1)) = __builtin_bit_cast(u16, (__bf16)o);
  }
  cnt_arrive(lds, C_OB, lane);
  cnt_wait<false>(lds, C_OB, NAUX);
  {
    f32x4 y0 = {0.f, 0.f, 0.f, 0.f}, y1 = y0;
#pragma unroll
    for (int q = 0; q < 4; ++q) {
      const u32x4 af = *(LAS u32x4*)(lds + L_OB + 16 * q + 4 * r);
      y0 = __builtin_amdgcn_mfma_f32_16x16x32_bf16(__builtin_bit_cast(bf16x8, af), __builtin_bit_cast(bf16x8, bfr[0][q]), y0, 0, 0, 0);
      y1 = __builtin_amdgcn_mfma_f32_16x16x32_bf16(__builtin_bit_cast(bf16x8, af), __builtin_bit_cast(bf16x8, bfr[1][q]), y1, 0, 0, 0);
    }
    st16_sc1(rws, lane < 16 ? WS_Y + (h * 8 + s) * 1024 + (16 * a + lane) * 16 : OOB, rec(y0[0], y1[0], tagA, tagB));
  }
  const unsigned tD = (unsigned)__builtin_amdgcn_s_memrealtime();
  const rsrc_t rdg = mkrsrc((char*)ws + (6u << 20), 8192);
  if (a == 2) st16_sc1(rdg, lane == 0 ? bid * 16 : OOB, u32x4{t0, tagA, DM == 1 ? tA : DM == 2 ? tB : DM == 3 ? tC : tD, tagB});
  if (a == 0) {
    u32x4 g;
#pragma unroll 1
    for (unsigned sp = 0; sp < SPIN_MEM; ++sp) {
      REREAD();
      g = ld16_sc1(rws, (lane >> 1) * (8 * 1024) + (lane & 1) * 16, WS_Y + s * 1024 + h * 32);
      if (__all(rok(g, tagA, tagB))) break;
    }
    const float a0 = rbf(parity_sum(r0(g))), a1 = rbf(parity_sum(r1(g)));
    const unsigned o = pack2(rbf(lo2f(xw) + a0) + f0, rbf(hi2f(xw) + a1) + f1v);
    __builtin_amdgcn_raw_buffer_store_b32(o, mkrsrc(out, D * 2), lane < 2 ? (col0 + 2 * lane) * 2 : OOB, 0, 0);
    __builtin_amdgcn_raw_buffer_store_b64(u32x2{(unsigned)e2, (unsigned)(e2 >> 32)}, rws, (bid == 0 && lane == 0) ? WS_EPOCH : OOB, 0, SC1);
    if (bid == 0) {
      const unsigned tend = (unsigned)__builtin_amdgcn_s_memrealtime();
      u32x4 gg[4];
      for (unsigned sp = 0; sp < SPIN_MEM; ++sp) { REREAD();
        for (int k = 0; k < 4; ++k) gg[k] = ld16_sc1(rdg, lane * 16 + k * 1024, 0);
        if (__all(rok(gg[0], tagA, tagB) && rok(gg[1], tagA, tagB) && rok(gg[2], tagA, tagB) && rok(gg[3], tagA, tagB))) break; }
      int m2 = -0x7fffffff;
      for (int k = 0; k < 4; ++k) m2 = max(m2, (int)(gg[k][2] - t0));
      for (int o = 1; o < 64; o <<= 1) m2 = max(m2, __shfl_xor(m2, o));
      int v = DM == 0 ? 0 : DM == 5 ? (int)(tend - t0) - (T_K0 + T_MAIN) : m2 - (T_K0 + T_MAIN);
      v = v < 0 ? 0 : v > 1000 ? 1000 : v;
      pace_wait(__builtin_amdgcn_readfirstlane(tend), __builtin_amdgcn_readfirstlane(100 + v * 10));
    }
  }
}

extern "C" void kernel_launch(void* const* d_in, const int* in_sizes, int n_in, void* d_out, int out_size, void* d_ws, size_t ws_size,
                              hipStream_t stream) {
  fused_decode_kernel<<<256, NTHR, 0, stream>>>((const u16*)d_in[0], (const u16*)d_in[1], (const u16*)d_in[2], (const u16*)d_in[3],
                                                 (const u16*)d_in[4], (const u16*)d_in[6], (const u16*)d_in[7], (const u16*)d_in[8],
                                                 (u16*)d_out, d_ws);
}
```

```cpp
#define T_MAIN 7230
#include <hip/hip_runtime.h>
#include <stdint.h>

typedef unsigned u32x4 __attribute__((ext_vector_type(4)));
typedef unsigned u32x2 __attribute__((ext_vector_type(2)));
typedef float f32x4 __attribute__((ext_vector_type(4)));
typedef float f32x2 __attribute__((ext_vector_type(2)));
typedef __bf16 bf16x2 __attribute__((ext_vector_type(2)));
typedef __bf16 bf16x8 __attribute__((ext_vector_type(8)));
typedef unsigned short u16;
#define LAS __attribute__((address_space(3)))
typedef __amdgpu_buffer_rsrc_t rsrc_t;

#define T_F1 150
#define T_W2 360
#define T_K0 460
#define PACE_G 2
#define PHASE (wave * 32 + j)
constexpr int T_QPOLL = 390, T_TOUCH = 1000;

constexpr int D = 1024, H = 32, SK = 32768, DH = 128, F = 3584;
constexpr int NMAIN = 8, NAUX = 4, NTHR = (NMAIN + NAUX) * 64;
constexpr int NT = 2, SC1 = 16;
constexpr int OOB = 0x40000000;
constexpr unsigned WS_EPOCH = 0, WS_T0 = 1024, WS_ML = 4096, WS_Q = WS_ML + 256 * 128, WS_Y = WS_Q + 256 * 1024, WS_G1 = WS_Y + 256 * 1024,
                   WS_FFN = WS_G1 + 4 * F * 8, WS_END = WS_FFN + 16 * D * 8;
constexpr int L_XN = 0, L_QP = 1024, L_QS = L_QP + 4 * 128, L_MRG = L_QS + 64, L_ML = L_MRG + 8 * 128, L_WG = L_ML + 16, L_FP = L_WG + 8,
              L_H = L_FP + 8 * 64, L_F2 = L_H + 4 * 64, L_OB = L_F2 + 4 * 64, L_CNT = L_OB + 64, L_SC = L_CNT + 16, L_WT = L_SC + 8 * 512,
              WT_STR = 68, L_END = L_WT + 128 * WT_STR;
constexpr int C_Q = 0, C_XN = 1, C_F2 = 2, C_WT = 3, C_ML = 4, C_WG = 5, C_END = 6, C_QR = 7, C_F1 = 8, C_OB = 9, C_NUM = 10, W_T0 = 12;
constexpr unsigned SPIN_MEM = 1u << 18, SPIN_LDS = 1u << 22;

__device__ __forceinline__ float lo2f(unsigned w) { return __builtin_bit_cast(float, w << 16); }
__device__ __forceinline__ float hi2f(unsigned w) { return __builtin_bit_cast(float, w & 0xffff0000u); }
__device__ __forceinline__ float rbf(float x) { return (float)(__bf16)x; }
__device__ __forceinline__ unsigned pack2(float a, float b) { bf16x2 v = {(__bf16)a, (__bf16)b}; return __builtin_bit_cast(unsigned, v); }
__device__ __forceinline__ float dot2(unsigned k, unsigned q, float acc) {
  return __builtin_amdgcn_fdot2_f32_bf16(__builtin_bit_cast(bf16x2, k), __builtin_bit_cast(bf16x2, q), acc, false);
}
template <int CTRL> __device__ __forceinline__ float dpp(float x) {
  return __builtin_bit_cast(float, __builtin_amdgcn_mov_dpp(__builtin_bit_cast(int, x), CTRL, 0xf, 0xf, true));
}
constexpr int XOR1 = 0xB1, XOR2 = 0x4E, XOR7 = 0x141, ROR8 = 0x128, ROR4 = 0x124, BCAST = 0x150;
__device__ __forceinline__ float fold32(float a, float b) {
  auto t = __builtin_amdgcn_permlane32_swap(__float_as_uint(a), __float_as_uint(b), false, false);
  return __uint_as_float(t[0]) + __uint_as_float(t[1]);
}
__device__ __forceinline__ float fold16(float a, float b) {
  auto t = __builtin_amdgcn_permlane16_swap(__float_as_uint(a), __float_as_uint(b), false, false);
  return __uint_as_float(t[0]) + __uint_as_float(t[1]);
}
__device__ __forceinline__ f32x2 fold_rows(const float (&v)[8]) {
  const float u0 = fold32(v[0], v[4]), u1 = fold32(v[1], v[5]), u2 = fold32(v[2], v[6]), u3 = fold32(v[3], v[7]);
  return f32x2{fold16(u0, u2), fold16(u1, u3)};
}
__device__ __forceinline__ float xrow_sum(float x) { x = fold16(x, x); return fold32(x, x); }
__device__ __forceinline__ float xrow_max(float x) {
  auto s = __builtin_amdgcn_permlane16_swap(__float_as_uint(x), __float_as_uint(x), false, false);
  x = fmaxf(__uint_as_float(s[0]), __uint_as_float(s[1]));
  auto t = __builtin_amdgcn_permlane32_swap(__float_as_uint(x), __float_as_uint(x), false, false);
  return fmaxf(__uint_as_float(t[0]), __uint_as_float(t[1]));
}
__device__ __forceinline__ float parity_sum(float x) {
  x += dpp<XOR2>(x); x += dpp<ROR4>(x); x += dpp<ROR8>(x); return xrow_sum(x);
}
__device__ __forceinline__ float wave_sum(float x) { x += dpp<XOR1>(x); return parity_sum(x); }
__device__ __forceinline__ float max8(float x) { x = fmaxf(x, dpp<XOR1>(x)); x = fmaxf(x, dpp<XOR2>(x)); return fmaxf(x, dpp<XOR7>(x)); }
__device__ __forceinline__ float sum8(float x) { x += dpp<XOR1>(x); x += dpp<XOR2>(x); return x + dpp<XOR7>(x); }
__device__ __forceinline__ float wave_max(float x) { x = max8(x); x = fmaxf(x, dpp<ROR8>(x)); return xrow_max(x); }
__device__ __forceinline__ float exp2_(float x) { return __builtin_amdgcn_exp2f(x); }
__device__ __forceinline__ rsrc_t mkrsrc(const void* p, unsigned bytes) { return __builtin_amdgcn_make_buffer_rsrc((void*)p, 0, (int)bytes, 0x00020000); }
__device__ __forceinline__ u32x4 ld16(rsrc_t r, int voff, int soff) { return __builtin_amdgcn_raw_buffer_load_b128(r, voff, soff, 0); }
__device__ __forceinline__ u32x4 ld16_nt(rsrc_t r, int voff, int soff) { return __builtin_amdgcn_raw_buffer_load_b128(r, voff, soff, NT); }
__device__ __forceinline__ u32x4 ld16_sc1(rsrc_t r, int voff, int soff) { return __builtin_amdgcn_raw_buffer_load_b128(r, voff, soff, SC1); }
__device__ __forceinline__ void st16_sc1(rsrc_t r, int off, u32x4 v) { __builtin_amdgcn_raw_buffer_store_b128(v, r, off, 0, SC1); }
#define SB() __builtin_amdgcn_sched_barrier(0)
#define REREAD() asm volatile("" ::: "memory")
__device__ __forceinline__ void pace_wait(unsigned t0, int due) {
  asm volatile("1:\n\ts_memrealtime s[96:97]\n\ts_waitcnt lgkmcnt(0)\n\ts_sub_u32 s96, s96, %0\n\ts_cmp_lt_i32 s96, %1\n\t"
               "s_cbranch_scc0 2f\n\ts_sleep 1\n\ts_branch 1b\n2:"
               :: "s"(t0), "s"(due) : "s96", "s97", "scc", "memory");
}
__device__ __forceinline__ void fma8(float (&acc)[8], float s, u32x4 w) {
#pragma unroll
  for (int t = 0; t < 4; ++t) { acc[2 * t] = fmaf(s, lo2f(w[t]), acc[2 * t]); acc[2 * t + 1] = fmaf(s, hi2f(w[t]), acc[2 * t + 1]); }
}
__device__ __forceinline__ u32x4 rec(float a, float b, unsigned ta, unsigned tb) { return u32x4{__float_as_uint(a), ta, __float_as_uint(b), tb}; }
__device__ __forceinline__ bool rok(u32x4 g, unsigned ta, unsigned tb) { return g[1] == ta && g[3] == tb; }
__device__ __forceinline__ float r0(u32x4 g) { return __uint_as_float(g[0]); }
__device__ __forceinline__ float r1(u32x4 g) { return __uint_as_float(g[2]); }

__device__ __forceinline__ void cnt_arrive(LAS float* lds, int which, int lane) {
  asm volatile("s_waitcnt lgkmcnt(0)" ::: "memory");
  if (lane == 0) __hip_atomic_fetch_add((LAS unsigned*)(lds + L_CNT + which), 1u, __ATOMIC_RELAXED, __HIP_MEMORY_SCOPE_WORKGROUP);
}
template <bool SLEEP = true>
__device__ __forceinline__ void cnt_wait(LAS float* lds, int which, unsigned n) {
#pragma unroll 1
  for (unsigned sp = 0; sp < SPIN_LDS; ++sp) {
    if (__builtin_amdgcn_readfirstlane(*(volatile LAS unsigned*)(lds + L_CNT + which)) >= n) break;
    if (SLEEP) __builtin_amdgcn_s_sleep(1);
  }
  REREAD();
}

__global__ void __launch_bounds__(NTHR, 3)
fused_decode_kernel(const u16* __restrict__ x, const u16* __restrict__ Kc, const u16* __restrict__ Vc, const u16* __restrict__ wq,
                    const u16* __restrict__ wo, const u16* __restrict__ wg, const u16* __restrict__ wu, const u16* __restrict__ wd,
                    u16* __restrict__ out, void* ws) {
  __shared__ __attribute__((aligned(16))) float lds_[L_END];
  LAS float* lds = (LAS float*)lds_;
  const unsigned t0 = (unsigned)__builtin_amdgcn_s_memrealtime();
  const int tid = threadIdx.x, lane = tid & 63, wave = __builtin_amdgcn_readfirstlane(tid >> 6);
  const int r = lane >> 4, c = lane & 15;
  const int bid = blockIdx.x, xl = bid & 7, j = bid >> 3;
  const int h = xl * 4 + (j >> 3), s = j & 7;
  const int f1 = xl * 28 + j, mat = f1 / 112, rb = (f1 / 56) & 1, cb = f1 % 56;

  if (wave < NMAIN) {
    const rsrc_t rk = mkrsrc(Kc, (unsigned)H * SK * DH * 2), rv = mkrsrc(Vc, (unsigned)H * SK * DH * 2);
    u32x4 bA[16], bB[16];
    const unsigned kvbase = (unsigned)h * (8u << 20) + (unsigned)s * (1u << 20) + wave * 1024;
    const unsigned rot = 32u * ((h + (h >> 2) + (h >> 4)) & 3);
    const int kvoff = lane * 16;
    constexpr int STEP = (T_MAIN << 16) / 256 * PACE_G;
    int due = T_K0 * 65536 - STEP + (STEP / 256) * (PHASE);
#define PACE(T0, I) do { if ((I) % PACE_G == 0) { due += STEP; pace_wait(T0, due >> 16); } } while (0)
    __builtin_amdgcn_s_barrier();
    SB();
    {
      const rsrc_t rg = mkrsrc(mat ? wu : wg, D * F * 2);
      const int voff = j < 28 ? (lane >> 3) * (F * 2) + (lane & 7) * 16 : OOB, sb = (rb * 512 + wave * 64) * (F * 2) + cb * 128;
      constexpr int FSTEP = ((T_W2 - T_F1) << 16) / 8;
      int fdue = T_F1 * 65536 + (FSTEP / 256) * (wave * 32 + j);
#pragma unroll
      for (int i = 0; i < 8; ++i) { pace_wait(t0, fdue >> 16); fdue += FSTEP; bB[i] = ld16_nt(rg, voff, sb + i * 8 * (F * 2)); SB(); }
    }
#pragma unroll
    for (int i = 0; i < 16; ++i) { PACE(t0, i); bA[i] = ld16_nt(rk, kvoff, kvbase + (rot + i) * 8192); SB(); }
    {
      cnt_wait(lds, C_XN, 1);
      float ag[8] = {0.f, 0.f, 0.f, 0.f, 0.f, 0.f, 0.f, 0.f};
#pragma unroll
      for (int i = 0; i < 8; ++i) {
        const int k = rb * 512 + wave * 64 + 8 * i + (lane >> 3);
        fma8(ag, lds[L_XN + (k & 3) * 256 + (k >> 2)], bB[i]);
      }
#pragma unroll
      for (int e = 0; e < 8; ++e) ag[e] += dpp<ROR8>(ag[e]);
      const f32x2 z = fold_rows(ag);
      if (c < 8) *(LAS f32x2*)(lds + L_FP + wave * 64 + 8 * c + 2 * r) = z;
      cnt_arrive(lds, C_F1, lane);
      SB();
    }
#pragma unroll
    for (int i = 0; i < 16; ++i) { PACE(t0, i); bB[i] = ld16_nt(rk, kvoff, kvbase + (rot + 16 + i) * 8192); SB(); }
    cnt_wait(lds, C_QR, 1);
    const u32x4 qp = *(LAS u32x4*)(lds + L_QS + 4 * c);
    const unsigned t0m = __builtin_amdgcn_readfirstlane(*(LAS unsigned*)(lds + L_CNT + W_T0));
    constexpr float SCALE = 0.08838834764831845f, LOG2E = 1.4426950408889634f;
    LAS float* scp = lds + L_SC + wave * 512 + lane * 8;
    float mx = -INFINITY;
    auto kgroup = [&](u32x4 (&kb)[16], const bool more, const rsrc_t rn, const unsigned tb, const int g) __attribute__((always_inline)) {
      float sx[16];
#pragma unroll
      for (int i = 0; i < 16; ++i) {
        float d = 0.f;
#pragma unroll
        for (int t = 0; t < 4; ++t) d = dot2(kb[i][t], qp[t], d);
        asm volatile("" : "+v"(d));
        sx[i] = d;
        SB();
        if (more) { PACE(t0m, i); kb[i] = ld16_nt(rn, kvoff, tb + i * 8192); SB(); }
      }
#define TR_STEP(HALF, CTRL, BIT) _Pragma("unroll") for (int hh = 0; hh < (HALF); ++hh) { \
        const float lo_ = sx[hh], hi_ = sx[hh + (HALF)]; const float t1_ = lo_ + dpp<CTRL>(lo_), t2_ = hi_ + dpp<CTRL>(hi_); \
        sx[hh] = (c & (BIT)) ? t2_ : t1_; }
      TR_STEP(8, ROR8, 8) TR_STEP(4, XOR7, 4) TR_STEP(2, XOR2, 2) TR_STEP(1, XOR1, 1)
#undef TR_STEP
      const float sc = rbf(rbf(sx[0]) * SCALE) * LOG2E;
      mx = fmaxf(mx, sc);
      scp[g] = sc;
      SB();
    };
    kgroup(bA, false, rk, 0, 0);
#pragma unroll 1
    for (int g = 1; g < 8; ++g)
      kgroup(bB, true, mkrsrc(g == 7 ? Vc : Kc, (unsigned)H * SK * DH * 2), kvbase + (((g + 1) * 16 + rot) & 127u) * 8192, g);
    {
      const float m = wave_max(mx);
      f32x4 p0 = *(LAS f32x4*)scp, p1 = *(LAS f32x4*)(scp + 4);
      float l = 0.f;
#pragma unroll
      for (int e = 0; e < 4; ++e) { p0[e] = exp2_(p0[e] - m); p1[e] = exp2_(p1[e] - m); l += p0[e] + p1[e]; }
      *(LAS f32x4*)scp = p0; *(LAS f32x4*)(scp + 4) = p1;
      l = wave_sum(l);
      if (lane == 0) *(LAS f32x2*)(lds + L_ML + 2 * wave) = f32x2{m, l};
      cnt_arrive(lds, C_ML, lane);
    }
    float acc[8] = {0.f, 0.f, 0.f, 0.f, 0.f, 0.f, 0.f, 0.f};
    auto vgroup = [&](const bool more, const unsigned tb, const float p) __attribute__((always_inline)) {
#define PV_STEP(I) { fma8(acc, dpp<BCAST + (I)>(p), bB[I]); \
        asm volatile("" : "+v"(acc[0]), "+v"(acc[1]), "+v"(acc[2]), "+v"(acc[3]), "+v"(acc[4]), "+v"(acc[5]), "+v"(acc[6]), "+v"(acc[7])); SB(); \
        if (more) { PACE(t0m, I); bB[I] = ld16_nt(rv, kvoff, tb + (I) * 8192); SB(); } \
        else if ((I) == 11) {   \
          pace_wait(t0m, T_K0 + T_MAIN); \
          _Pragma("unroll") for (int u = 0; u < 4; ++u) { \
            bB[u] = ld16_nt(mkrsrc(wo, H * DH * D * 2), r * 2048 + c * 16, (h * 128 + wave * 16 + 4 * u) * 2048 + s * 256); SB(); } } }
      PV_STEP(0) PV_STEP(1) PV_STEP(2) PV_STEP(3) PV_STEP(4) PV_STEP(5) PV_STEP(6) PV_STEP(7)
      PV_STEP(8) PV_STEP(9) PV_STEP(10) PV_STEP(11) PV_STEP(12) PV_STEP(13) PV_STEP(14) PV_STEP(15)
#undef PV_STEP
    };
#pragma unroll 1
    for (int g = 0; g < 7; ++g) vgroup(true, kvbase + (((g + 1) * 16 + rot) & 127u) * 8192, scp[g]);
    __builtin_amdgcn_s_setprio(3);
    cnt_wait(lds, C_WG, 1);
    {
      const float wgt = lds[L_WG + wave];
#pragma unroll
      for (int e = 0; e < 8; ++e) acc[e] *= wgt;
      vgroup(false, 0, scp[7] * wgt);
    }
    *(LAS f32x2*)(lds + L_MRG + wave * 128 + 8 * c + 2 * r) = fold_rows(acc);
    cnt_arrive(lds, C_END, lane);
#pragma unroll
    for (int u = 0; u < 4; ++u) *(LAS u32x4*)(lds + L_WT + (wave * 16 + 4 * u + r) * WT_STR + 4 * c) = bB[u];
    cnt_arrive(lds, C_WT, lane);
    return;
#undef PACE
  }

  const int a = wave - NMAIN;
  const rsrc_t rx = mkrsrc(x, D * 2), rws = mkrsrc(ws, WS_END);
  unsigned tagA, tagB, tagC, tagD;
  unsigned long long e2;
  {
    u32x4 xa = ld16(rx, lane * 32, 0), xb = ld16(rx, lane * 32 + 16, 0);
    const u32x2 ep = __builtin_amdgcn_raw_buffer_load_b64(rws, 0, WS_EPOCH, SC1);
    const rsrc_t rwq = mkrsrc(wq, D * H * DH * 2);
    u32x4 w[8];
    const int voff = r * 8192 + c * 16, sb = (s * 128 + a * 32) * 8192 + h * 256;
#pragma unroll
    for (int i = 0; i < 8; ++i) { w[i] = ld16_nt(rwq, voff, sb + i * 32768); SB(); }
    if (a == 0 && lane < C_NUM) *(LAS unsigned*)(lds + L_CNT + lane) = 0u;
    __syncthreads();
    asm volatile("" : "+v"(xa), "+v"(xb));
    float v[16];
#pragma unroll
    for (int i = 0; i < 4; ++i) { v[2 * i] = lo2f(xa[i]); v[2 * i + 1] = hi2f(xa[i]); v[8 + 2 * i] = lo2f(xb[i]); v[9 + 2 * i] = hi2f(xb[i]); }
    float sm = 0.f;
#pragma unroll
    for (int i = 0; i < 16; ++i) sm += v[i];
    const float mean = wave_sum(sm) * (1.f / D);
    float ss = 0.f;
#pragma unroll
    for (int i = 0; i < 16; ++i) { const float d = v[i] - mean; ss = fmaf(d, d, ss); }
    const float var = wave_sum(ss) * (1.f / D);
    const float mb = rbf(mean), den = rbf(sqrtf(rbf(rbf(var) + 1e-5f)));
    const float rinv = 1.0f / den;
#pragma unroll
    for (int i = 0; i < 16; ++i) {
      const float nu = rbf(v[i] - mb), q0 = nu * rinv;
      v[i] = rbf(fmaf(fmaf(-q0, den, nu), rinv, q0));
    }
#pragma unroll
    for (int q = 0; q < 4; ++q) *(LAS f32x4*)(lds + L_XN + q * 256 + 4 * lane) = f32x4{v[q], v[4 + q], v[8 + q], v[12 + q]};
    cnt_arrive(lds, C_XN, lane);
    const unsigned long long e1 = (((unsigned long long)__builtin_amdgcn_readfirstlane(ep[1]) << 32) | (unsigned)__builtin_amdgcn_readfirstlane(ep[0])) + 1ull;
    e2 = e1 + 1ull;
    tagA = (unsigned)e1; tagB = (unsigned)(e1 >> 32) + (unsigned)e1 * 0x9E3779B1u;
    tagC = (unsigned)e2; tagD = (unsigned)(e2 >> 32) + (unsigned)e2 * 0x9E3779B1u;
    float acc[8] = {0.f, 0.f, 0.f, 0.f, 0.f, 0.f, 0.f, 0.f};
#pragma unroll
    for (int q = 0; q < 2; ++q) {
      const f32x4 xv = *(LAS f32x4*)(lds + L_XN + r * 256 + s * 32 + a * 8 + 4 * q);
#pragma unroll
      for (int e = 0; e < 4; ++e) fma8(acc, xv[e], w[4 * q + e]);
    }
    *(LAS f32x2*)(lds + L_QP + a * 128 + 8 * c + 2 * r) = fold_rows(acc);
  }
  cnt_arrive(lds, C_Q, lane);
  if (a == 0) {
    cnt_wait<false>(lds, C_Q, NAUX);
    f32x2 v = *(LAS f32x2*)(lds + L_QP + 2 * lane);
#pragma unroll
    for (int q = 1; q < 4; ++q) v += *(LAS f32x2*)(lds + L_QP + q * 128 + 2 * lane);
    st16_sc1(rws, WS_Q + (h * 8 + s) * 1024 + lane * 16, rec(v[0], v[1], tagA, tagB));
    st16_sc1(rws, (bid == 0 && lane == 0) ? WS_T0 : OOB, u32x4{t0, tagA, t0, tagB});
  }
  if (a == 1) {
    pace_wait(t0, T_QPOLL);
    float q0 = 0.f, q1 = 0.f;
    unsigned tg = t0;
#pragma unroll 1
    for (unsigned sp = 0; sp < SPIN_MEM; ++sp) {
      REREAD();
      u32x4 g[8];
#pragma unroll
      for (int i = 0; i < 8; ++i) g[i] = ld16_sc1(rws, lane * 16, WS_Q + (h * 8 + i) * 1024);
      const u32x4 gt = ld16_sc1(rws, 0, WS_T0);
      bool ok = rok(gt, tagA, tagB);
      tg = gt[0];
      q0 = 0.f; q1 = 0.f;
#pragma unroll
      for (int i = 0; i < 8; ++i) { ok = ok && rok(g[i], tagA, tagB); q0 += r0(g[i]); q1 += r1(g[i]); }
      if (__all(ok)) break;
      __builtin_amdgcn_s_sleep(2);
    }
    *(LAS unsigned*)(lds + L_QS + lane) = pack2(q0, q1);
    if (lane == 0) *(LAS unsigned*)(lds + L_CNT + W_T0) = ((int)(t0 - tg) < 0 || (int)(t0 - tg) > 300) ? t0 : tg;
    cnt_arrive(lds, C_QR, lane);
  }
  const int ks = xl * 2 + (j >> 4), cg = j & 15;
  u32x4 dw[7];
  {
    const rsrc_t rd = mkrsrc(wd, D * F * 2);
    const int voff = (lane >> 3) * 2048 + (lane & 7) * 16, sb = (ks * 224 + a * 56) * 2048 + cg * 128;
    constexpr int WSTEP = ((T_K0 - T_W2) << 16) / 7;
    int wdue = T_W2 * 65536 + (WSTEP / 128) * (a * 32 + j);
#pragma unroll
    for (int i = 0; i < 7; ++i) { pace_wait(t0, wdue >> 16); wdue += WSTEP; dw[i] = ld16_nt(rd, voff, sb + i * 16384); SB(); }
  }
  if (a == 3) {
    pace_wait(t0, T_TOUCH);
    const u32x4 p = ld16_sc1(rws, lane * 16, WS_Y + (h * 8 + s) * 1024);
    asm volatile("" :: "v"(p));
  }
  if (a == 0 && j < 28) {
    cnt_wait(lds, C_F1, NMAIN);
    f32x2 v = *(LAS f32x2*)(lds + L_FP + 2 * (lane & 31));
#pragma unroll
    for (int q = 1; q < 8; ++q) v += *(LAS f32x2*)(lds + L_FP + q * 64 + 2 * (lane & 31));
    st16_sc1(rws, lane < 32 ? WS_G1 + ((mat * 2 + rb) * F + cb * 64) * 8 + lane * 16 : OOB, rec(v[0], v[1], tagA, tagB));
  }
  {
    pace_wait(t0, T_K0 + 2000);
    u32x4 g[4];
    const int voff = (lane < 28 ? lane : 27) * 16;
#pragma unroll 1
    for (unsigned sp = 0; sp < SPIN_MEM; ++sp) {
      REREAD();
#pragma unroll
      for (int k = 0; k < 4; ++k) g[k] = ld16_sc1(rws, voff, WS_G1 + (k * F + ks * 224 + a * 56) * 8);
      if (__all(rok(g[0], tagA, tagB) && rok(g[1], tagA, tagB) && rok(g[2], tagA, tagB) && rok(g[3], tagA, tagB))) break;
      __builtin_amdgcn_s_sleep(32);
    }
    float hh[2];
#pragma unroll
    for (int e = 0; e < 2; ++e) {
      const float gs = rbf(__uint_as_float(g[0][2 * e]) + __uint_as_float(g[1][2 * e]));
      const float us = rbf(__uint_as_float(g[2][2 * e]) + __uint_as_float(g[3][2 * e]));
      const float ge = 0.5f * gs * (1.f + tanhf(0.7978845608028654f * (gs + 0.044715f * gs * gs * gs)));
      hh[e] = rbf(rbf(ge) * us);
    }
    if (lane < 28) *(LAS f32x2*)(lds + L_H + a * 64 + 2 * lane) = f32x2{hh[0], hh[1]};
    float y[8] = {0.f, 0.f, 0.f, 0.f, 0.f, 0.f, 0.f, 0.f};
#pragma unroll
    for (int i = 0; i < 7; ++i) fma8(y, lds[L_H + a * 64 + 8 * i + (lane >> 3)], dw[i]);
#pragma unroll
    for (int e = 0; e < 8; ++e) y[e] += dpp<ROR8>(y[e]);
    const f32x2 z = fold_rows(y);
    if (c < 8) *(LAS f32x2*)(lds + L_F2 + a * 64 + 8 * c + 2 * r) = z;
    cnt_arrive(lds, C_F2, lane);
  }
  const int col0 = s * 128 + 4 * h;
  float f0 = 0.f, f1v = 0.f;
  unsigned xw = 0;
  if (a == 0) {
    cnt_wait(lds, C_F2, NAUX);
    f32x2 v = *(LAS f32x2*)(lds + L_F2 + 2 * (lane & 31));
#pragma unroll
    for (int q = 1; q < 4; ++q) v += *(LAS f32x2*)(lds + L_F2 + q * 64 + 2 * (lane & 31));
    st16_sc1(rws, lane < 32 ? WS_FFN + (ks * D + cg * 64) * 8 + lane * 16 : OOB, rec(v[0], v[1], tagA, tagB));
    xw = __builtin_amdgcn_raw_buffer_load_b32(rx, (col0 + 2 * (lane & 1)) * 2, 0, 0);
    pace_wait(t0, T_K0 + 3500);
    u32x4 g;
#pragma unroll 1
    for (unsigned sp = 0; sp < SPIN_MEM; ++sp) {
      REREAD();
      g = ld16_sc1(rws, ((lane >> 1) & 15) * (D * 8) + (lane & 1) * 16, WS_FFN + col0 * 8);
      if (__all(rok(g, tagA, tagB))) break;
      __builtin_amdgcn_s_sleep(32);
    }
    f0 = rbf(parity_sum(r0(g)) * 0.5f); f1v = rbf(parity_sum(r1(g)) * 0.5f);
  }
  if (a == 1) {
    cnt_wait(lds, C_ML, NMAIN);
    const f32x2 ml = *(LAS f32x2*)(lds + L_ML + 2 * (lane & 7));
    const float Mw = max8(ml[0]), Lw = sum8(exp2_(ml[0] - Mw) * ml[1]);
    st16_sc1(rws, lane == 0 ? WS_ML + (h * 8 + s) * 128 : OOB, rec(Mw, Lw, tagA, tagB));
    pace_wait(t0, T_K0 + T_MAIN / 2 + 1500);
    u32x4 g;
#pragma unroll 1
    for (unsigned sp = 0; sp < SPIN_MEM; ++sp) {
      REREAD();
      g = ld16_sc1(rws, (lane & 7) * 128, WS_ML + h * 8 * 128);
      if (__all(rok(g, tagA, tagB))) break;
      __builtin_amdgcn_s_sleep(32);
    }
    const float M = max8(r0(g)), L = sum8(exp2_(r0(g) - M) * r1(g));
    if (lane < 8) lds[L_WG + lane] = exp2_(ml[0] - M) / L;
    cnt_arrive(lds, C_WG, lane);
  }
  pace_wait(t0, T_K0 + T_MAIN - 300);
  if (a == 0) { const unsigned po = __builtin_amdgcn_raw_buffer_load_b32(mkrsrc(out, D * 2), (col0 + 2 * (lane & 1)) * 2, 0, 16); asm volatile("" :: "v"(po)); }
  pace_wait(t0, T_K0 + T_MAIN - 100);
  if (a == 0) {
    cnt_wait<false>(lds, C_END, NMAIN);
    f32x2 v = *(LAS f32x2*)(lds + L_MRG + 2 * lane);
#pragma unroll
    for (int w = 1; w < 8; ++w) v += *(LAS f32x2*)(lds + L_MRG + w * 128 + 2 * lane);
    st16_sc1(rws, WS_Q + (h * 8 + s) * 1024 + lane * 16, rec(v[0], v[1], tagC, tagD));
  } else {
    cnt_wait(lds, C_END, NMAIN);
  }
  u32x4 bfr[2][4];
  cnt_wait<false>(lds, C_WT, NMAIN);
#pragma unroll
  for (int q = 0; q < 4; ++q)
#pragma unroll
    for (int d = 0; d < 4; ++d) {
      const unsigned w0 = *(LAS unsigned*)(lds + L_WT + (32 * q + 8 * r + 2 * d) * WT_STR + 16 * a + c);
      const unsigned w1 = *(LAS unsigned*)(lds + L_WT + (32 * q + 8 * r + 2 * d + 1) * WT_STR + 16 * a + c);
      bfr[0][q][d] = __builtin_amdgcn_perm(w1, w0, 0x05040100u);
      bfr[1][q][d] = __builtin_amdgcn_perm(w1, w0, 0x07060302u);
    }
  {
    u32x4 g0, g1;
    const int vo = r * 1024 + (16 * a + c) * 16;
#pragma unroll 1
    for (unsigned sp = 0; sp < SPIN_MEM; ++sp) {
      REREAD();
      g0 = ld16_sc1(rws, vo, WS_Q + h * 8 * 1024);
      g1 = ld16_sc1(rws, vo, WS_Q + (h * 8 + 4) * 1024);
      if (__all(rok(g0, tagC, tagD) && rok(g1, tagC, tagD))) break;
    }
    float o = fold32(r0(g0) + r0(g1), r1(g0) + r1(g1));
    o = fold16(o, o);
    if ((r & 1) == 0) *((LAS u16*)(lds + L_OB) + 32 * a + 2 * c + (r >> 1)) = __builtin_bit_cast(u16, (__bf16)o);
  }
  cnt_arrive(lds, C_OB, lane);
  cnt_wait<false>(lds, C_OB, NAUX);
  {
    f32x4 y0 = {0.f, 0.f, 0.f, 0.f}, y1 = y0;
#pragma unroll
    for (int q = 0; q < 4; ++q) {
      const u32x4 af = *(LAS u32x4*)(lds + L_OB + 16 * q + 4 * r);
      y0 = __builtin_amdgcn_mfma_f32_16x16x32_bf16(__builtin_bit_cast(bf16x8, af), __builtin_bit_cast(bf16x8, bfr[0][q]), y0, 0, 0, 0);
      y1 = __builtin_amdgcn_mfma_f32_16x16x32_bf16(__builtin_bit_cast(bf16x8, af), __builtin_bit_cast(bf16x8, bfr[1][q]), y1, 0, 0, 0);
    }
    st16_sc1(rws, lane < 16 ? WS_Y + (h * 8 + s) * 1024 + (16 * a + lane) * 16 : OOB, rec(y0[0], y1[0], tagA, tagB));
  }
  if (a == 0) {
    u32x4 g;
#pragma unroll 1
    for (unsigned sp = 0; sp < SPIN_MEM; ++sp) {
      REREAD();
      g = ld16_sc1(rws, (lane >> 1) * (8 * 1024) + (lane & 1) * 16, WS_Y + s * 1024 + h * 32);
      if (__all(rok(g, tagA, tagB))) break;
    }
    const float a0 = rbf(parity_sum(r0(g))), a1 = rbf(parity_sum(r1(g)));
    const unsigned o = pack2(rbf(lo2f(xw) + a0) + f0, rbf(hi2f(xw) + a1) + f1v);
    __builtin_amdgcn_raw_buffer_store_b32(o, mkrsrc(out, D * 2), lane < 2 ? (col0 + 2 * lane) * 2 : OOB, 0, 16);
    __builtin_amdgcn_raw_buffer_store_b64(u32x2{(unsigned)e2, (unsigned)(e2 >> 32)}, rws, (bid == 0 && lane == 0) ? WS_EPOCH : OOB, 0, SC1);
  }
}

extern "C" void kernel_launch(void* const* d_in, const int* in_sizes, int n_in, void* d_out, int out_size, void* d_ws, size_t ws_size,
                              hipStream_t stream) {
  fused_decode_kernel<<<256, NTHR, 0, stream>>>((const u16*)d_in[0], (const u16*)d_in[1], (const u16*)d_in[2], (const u16*)d_in[3],
                                                 (const u16*)d_in[4], (const u16*)d_in[6], (const u16*)d_in[7], (const u16*)d_in[8],
                                                 (u16*)d_out, d_ws);
}
```

```cpp
#define T_MAIN 7230
#define T_D1 20
#define T_D2 30
#include <hip/hip_runtime.h>
#include <stdint.h>

typedef unsigned u32x4 __attribute__((ext_vector_type(4)));
typedef unsigned u32x2 __attribute__((ext_vector_type(2)));
typedef float f32x4 __attribute__((ext_vector_type(4)));
typedef float f32x2 __attribute__((ext_vector_type(2)));
typedef __bf16 bf16x2 __attribute__((ext_vector_type(2)));
typedef __bf16 bf16x8 __attribute__((ext_vector_type(8)));
typedef unsigned short u16;
#define LAS __attribute__((address_space(3)))
typedef __amdgpu_buffer_rsrc_t rsrc_t;

#define T_F1 150
#define T_W2 360
#define T_K0 460
#define PACE_G 2
#define PHASE (wave * 32 + j)
constexpr int T_QPOLL = 390, T_TOUCH = 1000;

constexpr int D = 1024, H = 32, SK = 32768, DH = 128, F = 3584;
constexpr int NMAIN = 8, NAUX = 4, NTHR = (NMAIN + NAUX) * 64;
constexpr int NT = 2, SC1 = 16;
constexpr int OOB = 0x40000000;
constexpr unsigned WS_EPOCH = 0, WS_T0 = 1024, WS_ML = 4096, WS_Q = WS_ML + 256 * 128, WS_Y = WS_Q + 256 * 1024, WS_G1 = WS_Y + 256 * 1024,
                   WS_FFN = WS_G1 + 4 * F * 8, WS_END = WS_FFN + 16 * D * 8;
constexpr int L_XN = 0, L_QP = 1024, L_QS = L_QP + 4 * 128, L_MRG = L_QS + 64, L_ML = L_MRG + 8 * 128, L_WG = L_ML + 16, L_FP = L_WG + 8,
              L_H = L_FP + 8 * 64, L_F2 = L_H + 4 * 64, L_OB = L_F2 + 4 * 64, L_CNT = L_OB + 64, L_SC = L_CNT + 16, L_WT = L_SC + 8 * 512,
              WT_STR = 68, L_END = L_WT + 128 * WT_STR;
constexpr int C_Q = 0, C_XN = 1, C_F2 = 2, C_WT = 3, C_ML = 4, C_WG = 5, C_END = 6, C_QR = 7, C_F1 = 8, C_OB = 9, C_NUM = 10, W_T0 = 12;
constexpr unsigned SPIN_MEM = 1u << 18, SPIN_LDS = 1u << 22;

__device__ __forceinline__ float lo2f(unsigned w) { return __builtin_bit_cast(float, w << 16); }
__device__ __forceinline__ float hi2f(unsigned w) { return __builtin_bit_cast(float, w & 0xffff0000u); }
__device__ __forceinline__ float rbf(float x) { return (float)(__bf16)x; }
__device__ __forceinline__ unsigned pack2(float a, float b) { bf16x2 v = {(__bf16)a, (__bf16)b}; return __builtin_bit_cast(unsigned, v); }
__device__ __forceinline__ float dot2(unsigned k, unsigned q, float acc) {
  return __builtin_amdgcn_fdot2_f32_bf16(__builtin_bit_cast(bf16x2, k), __builtin_bit_cast(bf16x2, q), acc, false);
}
template <int CTRL> __device__ __forceinline__ float dpp(float x) {
  return __builtin_bit_cast(float, __builtin_amdgcn_mov_dpp(__builtin_bit_cast(int, x), CTRL, 0xf, 0xf, true));
}
constexpr int XOR1 = 0xB1, XOR2 = 0x4E, XOR7 = 0x141, ROR8 = 0x128, ROR4 = 0x124, BCAST = 0x150;
__device__ __forceinline__ float fold32(float a, float b) {
  auto t = __builtin_amdgcn_permlane32_swap(__float_as_uint(a), __float_as_uint(b), false, false);
  return __uint_as_float(t[0]) + __uint_as_float(t[1]);
}
__device__ __forceinline__ float fold16(float a, float b) {
  auto t = __builtin_amdgcn_permlane16_swap(__float_as_uint(a), __float_as_uint(b), false, false);
  return __uint_as_float(t[0]) + __uint_as_float(t[1]);
}
__device__ __forceinline__ f32x2 fold_rows(const float (&v)[8]) {
  const float u0 = fold32(v[0], v[4]), u1 = fold32(v[1], v[5]), u2 = fold32(v[2], v[6]), u3 = fold32(v[3], v[7]);
  return f32x2{fold16(u0, u2), fold16(u1, u3)};
}
__device__ __forceinline__ float xrow_sum(float x) { x = fold16(x, x); return fold32(x, x); }
__device__ __forceinline__ float xrow_max(float x) {
  auto s = __builtin_amdgcn_permlane16_swap(__float_as_uint(x), __float_as_uint(x), false, false);
  x = fmaxf(__uint_as_float(s[0]), __uint_as_float(s[1]));
  auto t = __builtin_amdgcn_permlane32_swap(__float_as_uint(x), __float_as_uint(x), false, false);
  return fmaxf(__uint_as_float(t[0]), __uint_as_float(t[1]));
}
__device__ __forceinline__ float parity_sum(float x) {
  x += dpp<XOR2>(x); x += dpp<ROR4>(x); x += dpp<ROR8>(x); return xrow_sum(x);
}
__device__ __forceinline__ float wave_sum(float x) { x += dpp<XOR1>(x); return parity_sum(x); }
__device__ __forceinline__ float max8(float x) { x = fmaxf(x, dpp<XOR1>(x)); x = fmaxf(x, dpp<XOR2>(x)); return fmaxf(x, dpp<XOR7>(x)); }
__device__ __forceinline__ float sum8(float x) { x += dpp<XOR1>(x); x += dpp<XOR2>(x); return x + dpp<XOR7>(x); }
__device__ __forceinline__ float wave_max(float x) { x = max8(x); x = fmaxf(x, dpp<ROR8>(x)); return xrow_max(x); }
__device__ __forceinline__ float exp2_(float x) { return __builtin_amdgcn_exp2f(x); }
__device__ __forceinline__ rsrc_t mkrsrc(const void* p, unsigned bytes) { return __builtin_amdgcn_make_buffer_rsrc((void*)p, 0, (int)bytes, 0x00020000); }
__device__ __forceinline__ u32x4 ld16(rsrc_t r, int voff, int soff) { return __builtin_amdgcn_raw_buffer_load_b128(r, voff, soff, 0); }
__device__ __forceinline__ u32x4 ld16_nt(rsrc_t r, int voff, int soff) { return __builtin_amdgcn_raw_buffer_load_b128(r, voff, soff, NT); }
__device__ __forceinline__ u32x4 ld16_sc1(rsrc_t r, int voff, int soff) { return __builtin_amdgcn_raw_buffer_load_b128(r, voff, soff, SC1); }
__device__ __forceinline__ void st16_sc1(rsrc_t r, int off, u32x4 v) { __builtin_amdgcn_raw_buffer_store_b128(v, r, off, 0, SC1); }
#define SB() __builtin_amdgcn_sched_barrier(0)
#define REREAD() asm volatile("" ::: "memory")
__device__ __forceinline__ void pace_wait(unsigned t0, int due) {
  asm volatile("1:\n\ts_memrealtime s[96:97]\n\ts_waitcnt lgkmcnt(0)\n\ts_sub_u32 s96, s96, %0\n\ts_cmp_lt_i32 s96, %1\n\t"
               "s_cbranch_scc0 2f\n\ts_sleep 1\n\ts_branch 1b\n2:"
               :: "s"(t0), "s"(due) : "s96", "s97", "scc", "memory");
}
__device__ __forceinline__ void fma8(float (&acc)[8], float s, u32x4 w) {
#pragma unroll
  for (int t = 0; t < 4; ++t) { acc[2 * t] = fmaf(s, lo2f(w[t]), acc[2 * t]); acc[2 * t + 1] = fmaf(s, hi2f(w[t]), acc[2 * t + 1]); }
}
__device__ __forceinline__ u32x4 rec(float a, float b, unsigned ta, unsigned tb) { return u32x4{__float_as_uint(a), ta, __float_as_uint(b), tb}; }
__device__ __forceinline__ bool rok(u32x4 g, unsigned ta, unsigned tb) { return g[1] == ta && g[3] == tb; }
__device__ __forceinline__ float r0(u32x4 g) { return __uint_as_float(g[0]); }
__device__ __forceinline__ float r1(u32x4 g) { return __uint_as_float(g[2]); }

__device__ __forceinline__ void cnt_arrive(LAS float* lds, int which, int lane) {
  asm volatile("s_waitcnt lgkmcnt(0)" ::: "memory");
  if (lane == 0) __hip_atomic_fetch_add((LAS unsigned*)(lds + L_CNT + which), 1u, __ATOMIC_RELAXED, __HIP_MEMORY_SCOPE_WORKGROUP);
}
template <bool SLEEP = true>
__device__ __forceinline__ void cnt_wait(LAS float* lds, int which, unsigned n) {
#pragma unroll 1
  for (unsigned sp = 0; sp < SPIN_LDS; ++sp) {
    if (__builtin_amdgcn_readfirstlane(*(volatile LAS unsigned*)(lds + L_CNT + which)) >= n) break;
    if (SLEEP) __builtin_amdgcn_s_sleep(1);
  }
  REREAD();
}

__global__ void __launch_bounds__(NTHR, 3)
fused_decode_kernel(const u16* __restrict__ x, const u16* __restrict__ Kc, const u16* __restrict__ Vc, const u16* __restrict__ wq,
                    const u16* __restrict__ wo, const u16* __restrict__ wg, const u16* __restrict__ wu, const u16* __restrict__ wd,
                    u16* __restrict__ out, void* ws) {
  __shared__ __attribute__((aligned(16))) float lds_[L_END];
  LAS float* lds = (LAS float*)lds_;
  const unsigned t0 = (unsigned)__builtin_amdgcn_s_memrealtime();
  const int tid = threadIdx.x, lane = tid & 63, wave = __builtin_amdgcn_readfirstlane(tid >> 6);
  const int r = lane >> 4, c = lane & 15;
  const int bid = blockIdx.x, xl = bid & 7, j = bid >> 3;
  const int h = xl * 4 + (j >> 3), s = j & 7;
  const int f1 = xl * 28 + j, mat = f1 / 112, rb = (f1 / 56) & 1, cb = f1 % 56;

  if (wave < NMAIN) {
    const rsrc_t rk = mkrsrc(Kc, (unsigned)H * SK * DH * 2), rv = mkrsrc(Vc, (unsigned)H * SK * DH * 2);
    u32x4 bA[16], bB[16];
    const unsigned kvbase = (unsigned)h * (8u << 20) + (unsigned)s * (1u << 20) + wave * 1024;
    const unsigned rot = 32u * ((h + (h >> 2) + (h >> 4)) & 3);
    const int kvoff = lane * 16;
    constexpr int STEP = (T_MAIN << 16) / 256 * PACE_G;
    int due = T_K0 * 65536 - STEP + (STEP / 256) * (PHASE);
#define PACE(T0, I) do { if ((I) % PACE_G == 0) { due += STEP; pace_wait(T0, due >> 16); } } while (0)
    __builtin_amdgcn_s_barrier();
    SB();
    {
      const rsrc_t rg = mkrsrc(mat ? wu : wg, D * F * 2);
      const int voff = j < 28 ? (lane >> 3) * (F * 2) + (lane & 7) * 16 : OOB, sb = (rb * 512 + wave * 64) * (F * 2) + cb * 128;
      constexpr int FSTEP = ((T_W2 - T_F1) << 16) / 8;
      int fdue = T_F1 * 65536 + (FSTEP / 256) * (wave * 32 + j);
#pragma unroll
      for (int i = 0; i < 8; ++i) { pace_wait(t0, fdue >> 16); fdue += FSTEP; bB[i] = ld16_nt(rg, voff, sb + i * 8 * (F * 2)); SB(); }
    }
#pragma unroll
    for (int i = 0; i < 16; ++i) { PACE(t0, i); bA[i] = ld16_nt(rk, kvoff, kvbase + (rot + i) * 8192); SB(); }
    {
      cnt_wait(lds, C_XN, 1);
      float ag[8] = {0.f, 0.f, 0.f, 0.f, 0.f, 0.f, 0.f, 0.f};
#pragma unroll
      for (int i = 0; i < 8; ++i) {
        const int k = rb * 512 + wave * 64 + 8 * i + (lane >> 3);
        fma8(ag, lds[L_XN + (k & 3) * 256 + (k >> 2)], bB[i]);
      }
#pragma unroll
      for (int e = 0; e < 8; ++e) ag[e] += dpp<ROR8>(ag[e]);
      const f32x2 z = fold_rows(ag);
      if (c < 8) *(LAS f32x2*)(lds + L_FP + wave * 64 + 8 * c + 2 * r) = z;
      cnt_arrive(lds, C_F1, lane);
      SB();
    }
#pragma unroll
    for (int i = 0; i < 16; ++i) { PACE(t0, i); bB[i] = ld16_nt(rk, kvoff, kvbase + (rot + 16 + i) * 8192); SB(); }
    cnt_wait(lds, C_QR, 1);
    const u32x4 qp = *(LAS u32x4*)(lds + L_QS + 4 * c);
    const unsigned t0m = __builtin_amdgcn_readfirstlane(*(LAS unsigned*)(lds + L_CNT + W_T0));
    constexpr float SCALE = 0.08838834764831845f, LOG2E = 1.4426950408889634f;
    LAS float* scp = lds + L_SC + wave * 512 + lane * 8;
    float mx = -INFINITY;
    auto kgroup = [&](u32x4 (&kb)[16], const bool more, const rsrc_t rn, const unsigned tb, const int g) __attribute__((always_inline)) {
      float sx[16];
#pragma unroll
      for (int i = 0; i < 16; ++i) {
        float d = 0.f;
#pragma unroll
        for (int t = 0; t < 4; ++t) d = dot2(kb[i][t], qp[t], d);
        asm volatile("" : "+v"(d));
        sx[i] = d;
        SB();
        if (more) { PACE(t0m, i); kb[i] = ld16_nt(rn, kvoff, tb + i * 8192); SB(); }
      }
#define TR_STEP(HALF, CTRL, BIT) _Pragma("unroll") for (int hh = 0; hh < (HALF); ++hh) { \
        const float lo_ = sx[hh], hi_ = sx[hh + (HALF)]; const float t1_ = lo_ + dpp<CTRL>(lo_), t2_ = hi_ + dpp<CTRL>(hi_); \
        sx[hh] = (c & (BIT)) ? t2_ : t1_; }
      TR_STEP(8, ROR8, 8) TR_STEP(4, XOR7, 4) TR_STEP(2, XOR2, 2) TR_STEP(1, XOR1, 1)
#undef TR_STEP
      const float sc = rbf(rbf(sx[0]) * SCALE) * LOG2E;
      mx = fmaxf(mx, sc);
      scp[g] = sc;
      SB();
    };
    kgroup(bA, false, rk, 0, 0);
#pragma unroll 1
    for (int g = 1; g < 8; ++g)
      kgroup(bB, true, mkrsrc(g == 7 ? Vc : Kc, (unsigned)H * SK * DH * 2), kvbase + (((g + 1) * 16 + rot) & 127u) * 8192, g);
    {
      const float m = wave_max(mx);
      f32x4 p0 = *(LAS f32x4*)scp, p1 = *(LAS f32x4*)(scp + 4);
      float l = 0.f;
#pragma unroll
      for (int e = 0; e < 4; ++e) { p0[e] = exp2_(p0[e] - m); p1[e] = exp2_(p1[e] - m); l += p0[e] + p1[e]; }
      *(LAS f32x4*)scp = p0; *(LAS f32x4*)(scp + 4) = p1;
      l = wave_sum(l);
      if (lane == 0) *(LAS f32x2*)(lds + L_ML + 2 * wave) = f32x2{m, l};
      cnt_arrive(lds, C_ML, lane);
    }
    float acc[8] = {0.f, 0.f, 0.f, 0.f, 0.f, 0.f, 0.f, 0.f};
    auto vgroup = [&](const bool more, const unsigned tb, const float p) __attribute__((always_inline)) {
#define PV_STEP(I) { fma8(acc, dpp<BCAST + (I)>(p), bB[I]); \
        asm volatile("" : "+v"(acc[0]), "+v"(acc[1]), "+v"(acc[2]), "+v"(acc[3]), "+v"(acc[4]), "+v"(acc[5]), "+v"(acc[6]), "+v"(acc[7])); SB(); \
        if (more) { PACE(t0m, I); bB[I] = ld16_nt(rv, kvoff, tb + (I) * 8192); SB(); } \
        else if ((I) == 11) {   \
          pace_wait(t0m, T_K0 + T_MAIN); \
          _Pragma("unroll") for (int u = 0; u < 4; ++u) { \
            bB[u] = ld16_nt(mkrsrc(wo, H * DH * D * 2), r * 2048 + c * 16, (h * 128 + wave * 16 + 4 * u) * 2048 + s * 256); SB(); } } }
      PV_STEP(0) PV_STEP(1) PV_STEP(2) PV_STEP(3) PV_STEP(4) PV_STEP(5) PV_STEP(6) PV_STEP(7)
      PV_STEP(8) PV_STEP(9) PV_STEP(10) PV_STEP(11) PV_STEP(12) PV_STEP(13) PV_STEP(14) PV_STEP(15)
#undef PV_STEP
    };
#pragma unroll 1
    for (int g = 0; g < 7; ++g) vgroup(true, kvbase + (((g + 1) * 16 + rot) & 127u) * 8192, scp[g]);
    __builtin_amdgcn_s_setprio(3);
    cnt_wait(lds, C_WG, 1);
    {
      const float wgt = lds[L_WG + wave];
#pragma unroll
      for (int e = 0; e < 8; ++e) acc[e] *= wgt;
      vgroup(false, 0, scp[7] * wgt);
    }
    *(LAS f32x2*)(lds + L_MRG + wave * 128 + 8 * c + 2 * r) = fold_rows(acc);
    cnt_arrive(lds, C_END, lane);
#pragma unroll
    for (int u = 0; u < 4; ++u) *(LAS u32x4*)(lds + L_WT + (wave * 16 + 4 * u + r) * WT_STR + 4 * c) = bB[u];
    cnt_arrive(lds, C_WT, lane);
    return;
#undef PACE
  }

  const int a = wave - NMAIN;
  const rsrc_t rx = mkrsrc(x, D * 2), rws = mkrsrc(ws, WS_END);
  unsigned tagA, tagB, tagC, tagD;
  unsigned long long e2;
  {
    u32x4 xa = ld16(rx, lane * 32, 0), xb = ld16(rx, lane * 32 + 16, 0);
    const u32x2 ep = __builtin_amdgcn_raw_buffer_load_b64(rws, 0, WS_EPOCH, SC1);
    const rsrc_t rwq = mkrsrc(wq, D * H * DH * 2);
    u32x4 w[8];
    const int voff = r * 8192 + c * 16, sb = (s * 128 + a * 32) * 8192 + h * 256;
#pragma unroll
    for (int i = 0; i < 8; ++i) { w[i] = ld16_nt(rwq, voff, sb + i * 32768); SB(); }
    if (a == 0 && lane < C_NUM) *(LAS unsigned*)(lds + L_CNT + lane) = 0u;
    __syncthreads();
    asm volatile("" : "+v"(xa), "+v"(xb));
    float v[16];
#pragma unroll
    for (int i = 0; i < 4; ++i) { v[2 * i] = lo2f(xa[i]); v[2 * i + 1] = hi2f(xa[i]); v[8 + 2 * i] = lo2f(xb[i]); v[9 + 2 * i] = hi2f(xb[i]); }
    float sm = 0.f;
#pragma unroll
    for (int i = 0; i < 16; ++i) sm += v[i];
    const float mean = wave_sum(sm) * (1.f / D);
    float ss = 0.f;
#pragma unroll
    for (int i = 0; i < 16; ++i) { const float d = v[i] - mean; ss = fmaf(d, d, ss); }
    const float var = wave_sum(ss) * (1.f / D);
    const float mb = rbf(mean), den = rbf(sqrtf(rbf(rbf(var) + 1e-5f)));
    const float rinv = 1.0f / den;
#pragma unroll
    for (int i = 0; i < 16; ++i) {
      const float nu = rbf(v[i] - mb), q0 = nu * rinv;
      v[i] = rbf(fmaf(fmaf(-q0, den, nu), rinv, q0));
    }
#pragma unroll
    for (int q = 0; q < 4; ++q) *(LAS f32x4*)(lds + L_XN + q * 256 + 4 * lane) = f32x4{v[q], v[4 + q], v[8 + q], v[12 + q]};
    cnt_arrive(lds, C_XN, lane);
    const unsigned long long e1 = (((unsigned long long)__builtin_amdgcn_readfirstlane(ep[1]) << 32) | (unsigned)__builtin_amdgcn_readfirstlane(ep[0])) + 1ull;
    e2 = e1 + 1ull;
    tagA = (unsigned)e1; tagB = (unsigned)(e1 >> 32) + (unsigned)e1 * 0x9E3779B1u;
    tagC = (unsigned)e2; tagD = (unsigned)(e2 >> 32) + (unsigned)e2 * 0x9E3779B1u;
    float acc[8] = {0.f, 0.f, 0.f, 0.f, 0.f, 0.f, 0.f, 0.f};
#pragma unroll
    for (int q = 0; q < 2; ++q) {
      const f32x4 xv = *(LAS f32x4*)(lds + L_XN + r * 256 + s * 32 + a * 8 + 4 * q);
#pragma unroll
      for (int e = 0; e < 4; ++e) fma8(acc, xv[e], w[4 * q + e]);
    }
    *(LAS f32x2*)(lds + L_QP + a * 128 + 8 * c + 2 * r) = fold_rows(acc);
  }
  cnt_arrive(lds, C_Q, lane);
  if (a == 0) {
    cnt_wait<false>(lds, C_Q, NAUX);
    f32x2 v = *(LAS f32x2*)(lds + L_QP + 2 * lane);
#pragma unroll
    for (int q = 1; q < 4; ++q) v += *(LAS f32x2*)(lds + L_QP + q * 128 + 2 * lane);
    st16_sc1(rws, WS_Q + (h * 8 + s) * 1024 + lane * 16, rec(v[0], v[1], tagA, tagB));
    st16_sc1(rws, (bid == 0 && lane == 0) ? WS_T0 : OOB, u32x4{t0, tagA, t0, tagB});
  }
  if (a == 1) {
    pace_wait(t0, T_QPOLL);
    float q0 = 0.f, q1 = 0.f;
    unsigned tg = t0;
#pragma unroll 1
    for (unsigned sp = 0; sp < SPIN_MEM; ++sp) {
      REREAD();
      u32x4 g[8];
#pragma unroll
      for (int i = 0; i < 8; ++i) g[i] = ld16_sc1(rws, lane * 16, WS_Q + (h * 8 + i) * 1024);
      const u32x4 gt = ld16_sc1(rws, 0, WS_T0);
      bool ok = rok(gt, tagA, tagB);
      tg = gt[0];
      q0 = 0.f; q1 = 0.f;
#pragma unroll
      for (int i = 0; i < 8; ++i) { ok = ok && rok(g[i], tagA, tagB); q0 += r0(g[i]); q1 += r1(g[i]); }
      if (__all(ok)) break;
      __builtin_amdgcn_s_sleep(2);
    }
    *(LAS unsigned*)(lds + L_QS + lane) = pack2(q0, q1);
    if (lane == 0) *(LAS unsigned*)(lds + L_CNT + W_T0) = ((int)(t0 - tg) < 0 || (int)(t0 - tg) > 300) ? t0 : tg;
    cnt_arrive(lds, C_QR, lane);
  }
  const int ks = xl * 2 + (j >> 4), cg = j & 15;
  u32x4 dw[7];
  {
    const rsrc_t rd = mkrsrc(wd, D * F * 2);
    const int voff = (lane >> 3) * 2048 + (lane & 7) * 16, sb = (ks * 224 + a * 56) * 2048 + cg * 128;
    constexpr int WSTEP = ((T_K0 - T_W2) << 16) / 7;
    int wdue = T_W2 * 65536 + (WSTEP / 128) * (a * 32 + j);
#pragma unroll
    for (int i = 0; i < 7; ++i) { pace_wait(t0, wdue >> 16); wdue += WSTEP; dw[i] = ld16_nt(rd, voff, sb + i * 16384); SB(); }
  }
  if (a == 3) {
    pace_wait(t0, T_TOUCH);
    const u32x4 p = ld16_sc1(rws, lane * 16, WS_Y + (h * 8 + s) * 1024);
    asm volatile("" :: "v"(p));
  }
  if (a == 0 && j < 28) {
    cnt_wait(lds, C_F1, NMAIN);
    f32x2 v = *(LAS f32x2*)(lds + L_FP + 2 * (lane & 31));
#pragma unroll
    for (int q = 1; q < 8; ++q) v += *(LAS f32x2*)(lds + L_FP + q * 64 + 2 * (lane & 31));
    st16_sc1(rws, lane < 32 ? WS_G1 + ((mat * 2 + rb) * F + cb * 64) * 8 + lane * 16 : OOB, rec(v[0], v[1], tagA, tagB));
  }
  {
    pace_wait(t0, T_K0 + 2000);
    u32x4 g[4];
    const int voff = (lane < 28 ? lane : 27) * 16;
#pragma unroll 1
    for (unsigned sp = 0; sp < SPIN_MEM; ++sp) {
      REREAD();
#pragma unroll
      for (int k = 0; k < 4; ++k) g[k] = ld16_sc1(rws, voff, WS_G1 + (k * F + ks * 224 + a * 56) * 8);
      if (__all(rok(g[0], tagA, tagB) && rok(g[1], tagA, tagB) && rok(g[2], tagA, tagB) && rok(g[3], tagA, tagB))) break;
      __builtin_amdgcn_s_sleep(32);
    }
    float hh[2];
#pragma unroll
    for (int e = 0; e < 2; ++e) {
      const float gs = rbf(__uint_as_float(g[0][2 * e]) + __uint_as_float(g[1][2 * e]));
      const float us = rbf(__uint_as_float(g[2][2 * e]) + __uint_as_float(g[3][2 * e]));
      const float ge = 0.5f * gs * (1.f + tanhf(0.7978845608028654f * (gs + 0.044715f * gs * gs * gs)));
      hh[e] = rbf(rbf(ge) * us);
    }
    if (lane < 28) *(LAS f32x2*)(lds + L_H + a * 64 + 2 * lane) = f32x2{hh[0], hh[1]};
    float y[8] = {0.f, 0.f, 0.f, 0.f, 0.f, 0.f, 0.f, 0.f};
#pragma unroll
    for (int i = 0; i < 7; ++i) fma8(y, lds[L_H + a * 64 + 8 * i + (lane >> 3)], dw[i]);
#pragma unroll
    for (int e = 0; e < 8; ++e) y[e] += dpp<ROR8>(y[e]);
    const f32x2 z = fold_rows(y);
    if (c < 8) *(LAS f32x2*)(lds + L_F2 + a * 64 + 8 * c + 2 * r) = z;
    cnt_arrive(lds, C_F2, lane);
  }
  const int col0 = s * 128 + 4 * h;
  float f0 = 0.f, f1v = 0.f;
  unsigned xw = 0;
  if (a == 0) {
    cnt_wait(lds, C_F2, NAUX);
    f32x2 v = *(LAS f32x2*)(lds + L_F2 + 2 * (lane & 31));
#pragma unroll
    for (int q = 1; q < 4; ++q) v += *(LAS f32x2*)(lds + L_F2 + q * 64 + 2 * (lane & 31));
    st16_sc1(rws, lane < 32 ? WS_FFN + (ks * D + cg * 64) * 8 + lane * 16 : OOB, rec(v[0], v[1], tagA, tagB));
    xw = __builtin_amdgcn_raw_buffer_load_b32(rx, (col0 + 2 * (lane & 1)) * 2, 0, 0);
    pace_wait(t0, T_K0 + 3500);
    u32x4 g;
#pragma unroll 1
    for (unsigned sp = 0; sp < SPIN_MEM; ++sp) {
      REREAD();
      g = ld16_sc1(rws, ((lane >> 1) & 15) * (D * 8) + (lane & 1) * 16, WS_FFN + col0 * 8);
      if (__all(rok(g, tagA, tagB))) break;
      __builtin_amdgcn_s_sleep(32);
    }
    f0 = rbf(parity_sum(r0(g)) * 0.5f); f1v = rbf(parity_sum(r1(g)) * 0.5f);
  }
  if (a == 1) {
    cnt_wait(lds, C_ML, NMAIN);
    const f32x2 ml = *(LAS f32x2*)(lds + L_ML + 2 * (lane & 7));
    const float Mw = max8(ml[0]), Lw = sum8(exp2_(ml[0] - Mw) * ml[1]);
    st16_sc1(rws, lane == 0 ? WS_ML + (h * 8 + s) * 128 : OOB, rec(Mw, Lw, tagA, tagB));
    pace_wait(t0, T_K0 + T_MAIN / 2 + 1500);
    u32x4 g;
#pragma unroll 1
    for (unsigned sp = 0; sp < SPIN_MEM; ++sp) {
      REREAD();
      g = ld16_sc1(rws, (lane & 7) * 128, WS_ML + h * 8 * 128);
      if (__all(rok(g, tagA, tagB))) break;
      __builtin_amdgcn_s_sleep(32);
    }
    const float M = max8(r0(g)), L = sum8(exp2_(r0(g) - M) * r1(g));
    if (lane < 8) lds[L_WG + lane] = exp2_(ml[0] - M) / L;
    cnt_arrive(lds, C_WG, lane);
  }
  pace_wait(t0, T_K0 + T_MAIN - 300);
  if (a == 0) {
    const unsigned po = __builtin_amdgcn_raw_buffer_load_b32(mkrsrc(out, D * 2), (col0 + 2 * (lane & 1)) * 2, 0, SC1);
    asm volatile("" :: "v"(po));
  }
  pace_wait(t0, T_K0 + T_MAIN - 100);
  if (a == 0) {
    cnt_wait<false>(lds, C_END, NMAIN);
    f32x2 v = *(LAS f32x2*)(lds + L_MRG + 2 * lane);
#pragma unroll
    for (int w = 1; w < 8; ++w) v += *(LAS f32x2*)(lds + L_MRG + w * 128 + 2 * lane);
    st16_sc1(rws, WS_Q + (h * 8 + s) * 1024 + lane * 16, rec(v[0], v[1], tagC, tagD));
  } else {
    cnt_wait(lds, C_END, NMAIN);
  }
  u32x4 bfr[2][4];
  cnt_wait<false>(lds, C_WT, NMAIN);
#pragma unroll
  for (int q = 0; q < 4; ++q)
#pragma unroll
    for (int d = 0; d < 4; ++d) {
      const unsigned w0 = *(LAS unsigned*)(lds + L_WT + (32 * q + 8 * r + 2 * d) * WT_STR + 16 * a + c);
      const unsigned w1 = *(LAS unsigned*)(lds + L_WT + (32 * q + 8 * r + 2 * d + 1) * WT_STR + 16 * a + c);
      bfr[0][q][d] = __builtin_amdgcn_perm(w1, w0, 0x05040100u);
      bfr[1][q][d] = __builtin_amdgcn_perm(w1, w0, 0x07060302u);
    }
  {
    u32x4 g0, g1;
    const int vo = r * 1024 + (16 * a + c) * 16;
    if (T_D1 > 0) pace_wait((unsigned)__builtin_amdgcn_s_memrealtime(), T_D1);
#pragma unroll 1
    for (unsigned sp = 0; sp < SPIN_MEM; ++sp) {
      REREAD();
      g0 = ld16_sc1(rws, vo, WS_Q + h * 8 * 1024);
      g1 = ld16_sc1(rws, vo, WS_Q + (h * 8 + 4) * 1024);
      if (__all(rok(g0, tagC, tagD) && rok(g1, tagC, tagD))) break;
    }
    float o = fold32(r0(g0) + r0(g1), r1(g0) + r1(g1));
    o = fold16(o, o);
    if ((r & 1) == 0) *((LAS u16*)(lds + L_OB) + 32 * a + 2 * c + (r >> 1)) = __builtin_bit_cast(u16, (__bf16)o);
  }
  cnt_arrive(lds, C_OB, lane);
  cnt_wait<false>(lds, C_OB, NAUX);
  {
    f32x4 y0 = {0.f, 0.f, 0.f, 0.f}, y1 = y0;
#pragma unroll
    for (int q = 0; q < 4; ++q) {
      const u32x4 af = *(LAS u32x4*)(lds + L_OB + 16 * q + 4 * r);
      y0 = __builtin_amdgcn_mfma_f32_16x16x32_bf16(__builtin_bit_cast(bf16x8, af), __builtin_bit_cast(bf16x8, bfr[0][q]), y0, 0, 0, 0);
      y1 = __builtin_amdgcn_mfma_f32_16x16x32_bf16(__builtin_bit_cast(bf16x8, af), __builtin_bit_cast(bf16x8, bfr[1][q]), y1, 0, 0, 0);
    }
    st16_sc1(rws, lane < 16 ? WS_Y + (h * 8 + s) * 1024 + (16 * a + lane) * 16 : OOB, rec(y0[0], y1[0], tagA, tagB));
  }
  if (a == 0) {
    u32x4 g;
    if (T_D2 > 0) pace_wait((unsigned)__builtin_amdgcn_s_memrealtime(), T_D2);
#pragma unroll 1
    for (unsigned sp = 0; sp < SPIN_MEM; ++sp) {
      REREAD();
      g = ld16_sc1(rws, (lane >> 1) * (8 * 1024) + (lane & 1) * 16, WS_Y + s * 1024 + h * 32);
      if (__all(rok(g, tagA, tagB))) break;
    }
    const float a0 = rbf(parity_sum(r0(g))), a1 = rbf(parity_sum(r1(g)));
    const unsigned o = pack2(rbf(lo2f(xw) + a0) + f0, rbf(hi2f(xw) + a1) + f1v);
    __builtin_amdgcn_raw_buffer_store_b32(o, mkrsrc(out, D * 2), lane < 2 ? (col0 + 2 * lane) * 2 : OOB, 0, SC1);
    __builtin_amdgcn_raw_buffer_store_b64(u32x2{(unsigned)e2, (unsigned)(e2 >> 32)}, rws, (bid == 0 && lane == 0) ? WS_EPOCH : OOB, 0, SC1);
  }
}

extern "C" void kernel_launch(void* const* d_in, const int* in_sizes, int n_in, void* d_out, int out_size, void* d_ws, size_t ws_size,
                              hipStream_t stream) {
  fused_decode_kernel<<<256, NTHR, 0, stream>>>((const u16*)d_in[0], (const u16*)d_in[1], (const u16*)d_in[2], (const u16*)d_in[3],
                                                 (const u16*)d_in[4], (const u16*)d_in[6], (const u16*)d_in[7], (const u16*)d_in[8],
                                                 (u16*)d_out, d_ws);
}
```

```cpp
#define T_MAIN 7200
#define T_K0 460
#include <hip/hip_runtime.h>
#include <stdint.h>

typedef unsigned u32x4 __attribute__((ext_vector_type(4)));
typedef unsigned u32x2 __attribute__((ext_vector_type(2)));
typedef float f32x4 __attribute__((ext_vector_type(4)));
typedef float f32x2 __attribute__((ext_vector_type(2)));
typedef __bf16 bf16x2 __attribute__((ext_vector_type(2)));
typedef __bf16 bf16x8 __attribute__((ext_vector_type(8)));
typedef unsigned short u16;
#define LAS __attribute__((address_space(3)))
typedef __amdgpu_buffer_rsrc_t rsrc_t;

#define T_F1 150
#define T_W2 360
#define PACE_G 2
#define PHASE (wave * 32 + j)
constexpr int T_QPOLL = 390, T_TOUCH = 1000;

constexpr int D = 1024, H = 32, SK = 32768, DH = 128, F = 3584;
constexpr int NMAIN = 8, NAUX = 4, NTHR = (NMAIN + NAUX) * 64;
constexpr int NT = 2, SC1 = 16;
constexpr int OOB = 0x40000000;
constexpr unsigned WS_EPOCH = 0, WS_T0 = 1024, WS_ML = 4096, WS_Q = WS_ML + 256 * 128, WS_Y = WS_Q + 256 * 1024, WS_G1 = WS_Y + 256 * 1024,
                   WS_FFN = WS_G1 + 4 * F * 8, WS_END = WS_FFN + 16 * D * 8;
constexpr int L_XN = 0, L_QP = 1024, L_QS = L_QP + 4 * 128, L_MRG = L_QS + 64, L_ML = L_MRG + 8 * 128, L_WG = L_ML + 16, L_FP = L_WG + 8,
              L_H = L_FP + 8 * 64, L_F2 = L_H + 4 * 64, L_OB = L_F2 + 4 * 64, L_CNT = L_OB + 64, L_SC = L_CNT + 16, L_WT = L_SC + 8 * 512,
              WT_STR = 68, L_END = L_WT + 128 * WT_STR;
constexpr int C_Q = 0, C_XN = 1, C_F2 = 2, C_WT = 3, C_ML = 4, C_WG = 5, C_END = 6, C_QR = 7, C_F1 = 8, C_OB = 9, C_NUM = 10, W_T0 = 12;
constexpr unsigned SPIN_MEM = 1u << 18, SPIN_LDS = 1u << 22;

__device__ __forceinline__ float lo2f(unsigned w) { return __builtin_bit_cast(float, w << 16); }
__device__ __forceinline__ float hi2f(unsigned w) { return __builtin_bit_cast(float, w & 0xffff0000u); }
__device__ __forceinline__ float rbf(float x) { return (float)(__bf16)x; }
__device__ __forceinline__ unsigned pack2(float a, float b) { bf16x2 v = {(__bf16)a, (__bf16)b}; return __builtin_bit_cast(unsigned, v); }
__device__ __forceinline__ float dot2(unsigned k, unsigned q, float acc) {
  return __builtin_amdgcn_fdot2_f32_bf16(__builtin_bit_cast(bf16x2, k), __builtin_bit_cast(bf16x2, q), acc, false);
}
template <int CTRL> __device__ __forceinline__ float dpp(float x) {
  return __builtin_bit_cast(float, __builtin_amdgcn_mov_dpp(__builtin_bit_cast(int, x), CTRL, 0xf, 0xf, true));
}
constexpr int XOR1 = 0xB1, XOR2 = 0x4E, XOR7 = 0x141, ROR8 = 0x128, ROR4 = 0x124, BCAST = 0x150;
__device__ __forceinline__ float fold32(float a, float b) {
  auto t = __builtin_amdgcn_permlane32_swap(__float_as_uint(a), __float_as_uint(b), false, false);
  return __uint_as_float(t[0]) + __uint_as_float(t[1]);
}
__device__ __forceinline__ float fold16(float a, float b) {
  auto t = __builtin_amdgcn_permlane16_swap(__float_as_uint(a), __float_as_uint(b), false, false);
  return __uint_as_float(t[0]) + __uint_as_float(t[1]);
}
__device__ __forceinline__ f32x2 fold_rows(const float (&v)[8]) {
  const float u0 = fold32(v[0], v[4]), u1 = fold32(v[1], v[5]), u2 = fold32(v[2], v[6]), u3 = fold32(v[3], v[7]);
  return f32x2{fold16(u0, u2), fold16(u1, u3)};
}
__device__ __forceinline__ float xrow_sum(float x) { x = fold16(x, x); return fold32(x, x); }
__device__ __forceinline__ float xrow_max(float x) {
  auto s = __builtin_amdgcn_permlane16_swap(__float_as_uint(x), __float_as_uint(x), false, false);
  x = fmaxf(__uint_as_float(s[0]), __uint_as_float(s[1]));
  auto t = __builtin_amdgcn_permlane32_swap(__float_as_uint(x), __float_as_uint(x), false, false);
  return fmaxf(__uint_as_float(t[0]), __uint_as_float(t[1]));
}
__device__ __forceinline__ float parity_sum(float x) {
  x += dpp<XOR2>(x); x += dpp<ROR4>(x); x += dpp<ROR8>(x); return xrow_sum(x);
}
__device__ __forceinline__ float wave_sum(float x) { x += dpp<XOR1>(x); return parity_sum(x); }
__device__ __forceinline__ float max8(float x) { x = fmaxf(x, dpp<XOR1>(x)); x = fmaxf(x, dpp<XOR2>(x)); return fmaxf(x, dpp<XOR7>(x)); }
__device__ __forceinline__ float sum8(float x) { x += dpp<XOR1>(x); x += dpp<XOR2>(x); return x + dpp<XOR7>(x); }
__device__ __forceinline__ float wave_max(float x) { x = max8(x); x = fmaxf(x, dpp<ROR8>(x)); return xrow_max(x); }
__device__ __forceinline__ float exp2_(float x) { return __builtin_amdgcn_exp2f(x); }
__device__ __forceinline__ rsrc_t mkrsrc(const void* p, unsigned bytes) { return __builtin_amdgcn_make_buffer_rsrc((void*)p, 0, (int)bytes, 0x00020000); }
__device__ __forceinline__ u32x4 ld16(rsrc_t r, int voff, int soff) { return __builtin_amdgcn_raw_buffer_load_b128(r, voff, soff, 0); }
__device__ __forceinline__ u32x4 ld16_nt(rsrc_t r, int voff, int soff) { return __builtin_amdgcn_raw_buffer_load_b128(r, voff, soff, NT); }
__device__ __forceinline__ u32x4 ld16_sc1(rsrc_t r, int voff, int soff) { return __builtin_amdgcn_raw_buffer_load_b128(r, voff, soff, SC1); }
__device__ __forceinline__ void st16_sc1(rsrc_t r, int off, u32x4 v) { __builtin_amdgcn_raw_buffer_store_b128(v, r, off, 0, SC1); }
#define SB() __builtin_amdgcn_sched_barrier(0)
#define REREAD() asm volatile("" ::: "memory")
__device__ __forceinline__ void pace_wait(unsigned t0, int due) {
  asm volatile("1:\n\ts_memrealtime s[96:97]\n\ts_waitcnt lgkmcnt(0)\n\ts_sub_u32 s96, s96, %0\n\ts_cmp_lt_i32 s96, %1\n\t"
               "s_cbranch_scc0 2f\n\ts_sleep 1\n\ts_branch 1b\n2:"
               :: "s"(t0), "s"(due) : "s96", "s97", "scc", "memory");
}
__device__ __forceinline__ void fma8(float (&acc)[8], float s, u32x4 w) {
#pragma unroll
  for (int t = 0; t < 4; ++t) { acc[2 * t] = fmaf(s, lo2f(w[t]), acc[2 * t]); acc[2 * t + 1] = fmaf(s, hi2f(w[t]), acc[2 * t + 1]); }
}
__device__ __forceinline__ u32x4 rec(float a, float b, unsigned ta, unsigned tb) { return u32x4{__float_as_uint(a), ta, __float_as_uint(b), tb}; }
__device__ __forceinline__ bool rok(u32x4 g, unsigned ta, unsigned tb) { return g[1] == ta && g[3] == tb; }
__device__ __forceinline__ float r0(u32x4 g) { return __uint_as_float(g[0]); }
__device__ __forceinline__ float r1(u32x4 g) { return __uint_as_float(g[2]); }

__device__ __forceinline__ void cnt_arrive(LAS float* lds, int which, int lane) {
  asm volatile("s_waitcnt lgkmcnt(0)" ::: "memory");
  if (lane == 0) __hip_atomic_fetch_add((LAS unsigned*)(lds + L_CNT + which), 1u, __ATOMIC_RELAXED, __HIP_MEMORY_SCOPE_WORKGROUP);
}
template <bool SLEEP = true>
__device__ __forceinline__ void cnt_wait(LAS float* lds, int which, unsigned n) {
#pragma unroll 1
  for (unsigned sp = 0; sp < SPIN_LDS; ++sp) {
    if (__builtin_amdgcn_readfirstlane(*(volatile LAS unsigned*)(lds + L_CNT + which)) >= n) break;
    if (SLEEP) __builtin_amdgcn_s_sleep(1);
  }
  REREAD();
}

__global__ void __launch_bounds__(NTHR, 3)
fused_decode_kernel(const u16* __restrict__ x, const u16* __restrict__ Kc, const u16* __restrict__ Vc, const u16* __restrict__ wq,
                    const u16* __restrict__ wo, const u16* __restrict__ wg, const u16* __restrict__ wu, const u16* __restrict__ wd,
                    u16* __restrict__ out, void* ws) {
  __shared__ __attribute__((aligned(16))) float lds_[L_END];
  LAS float* lds = (LAS float*)lds_;
  const unsigned t0 = (unsigned)__builtin_amdgcn_s_memrealtime();
  const int tid = threadIdx.x, lane = tid & 63, wave = __builtin_amdgcn_readfirstlane(tid >> 6);
  const int r = lane >> 4, c = lane & 15;
  const int bid = blockIdx.x, xl = bid & 7, j = bid >> 3;
  const int h = xl * 4 + (j >> 3), s = j & 7;
  const int f1 = xl * 28 + j, mat = f1 / 112, rb = (f1 / 56) & 1, cb = f1 % 56;

  if (wave < NMAIN) {
    const rsrc_t rk = mkrsrc(Kc, (unsigned)H * SK * DH * 2), rv = mkrsrc(Vc, (unsigned)H * SK * DH * 2);
    u32x4 bA[16], bB[16];
    const unsigned kvbase = (unsigned)h * (8u << 20) + (unsigned)s * (1u << 20) + wave * 1024;
    const unsigned rot = 32u * ((h + (h >> 2) + (h >> 4)) & 3);
    const int kvoff = lane * 16;
    constexpr int STEP = (T_MAIN << 16) / 256 * PACE_G;
    int due = T_K0 * 65536 - STEP + (STEP / 256) * (PHASE);
#define PACE(T0, I) do { if ((I) % PACE_G == 0) { due += STEP; pace_wait(T0, due >> 16); } } while (0)
    __builtin_amdgcn_s_barrier();
    SB();
    {
      const rsrc_t rg = mkrsrc(mat ? wu : wg, D * F * 2);
      const int voff = j < 28 ? (lane >> 3) * (F * 2) + (lane & 7) * 16 : OOB, sb = (rb * 512 + wave * 64) * (F * 2) + cb * 128;
      constexpr int FSTEP = ((T_W2 - T_F1) << 16) / 8;
      int fdue = T_F1 * 65536 + (FSTEP / 256) * (wave * 32 + j);
#pragma unroll
      for (int i = 0; i < 8; ++i) { pace_wait(t0, fdue >> 16); fdue += FSTEP; bB[i] = ld16_nt(rg, voff, sb + i * 8 * (F * 2)); SB(); }
    }
#pragma unroll
    for (int i = 0; i < 16; ++i) { PACE(t0, i); bA[i] = ld16_nt(rk, kvoff, kvbase + (rot + i) * 8192); SB(); }
    {
      cnt_wait(lds, C_XN, 1);
      float ag[8] = {0.f, 0.f, 0.f, 0.f, 0.f, 0.f, 0.f, 0.f};
#pragma unroll
      for (int i = 0; i < 8; ++i) {
        const int k = rb * 512 + wave * 64 + 8 * i + (lane >> 3);
        fma8(ag, lds[L_XN + (k & 3) * 256 + (k >> 2)], bB[i]);
      }
#pragma unroll
      for (int e = 0; e < 8; ++e) ag[e] += dpp<ROR8>(ag[e]);
      const f32x2 z = fold_rows(ag);
      if (c < 8) *(LAS f32x2*)(lds + L_FP + wave * 64 + 8 * c + 2 * r) = z;
      cnt_arrive(lds, C_F1, lane);
      SB();
    }
#pragma unroll
    for (int i = 0; i < 16; ++i) { PACE(t0, i); bB[i] = ld16_nt(rk, kvoff, kvbase + (rot + 16 + i) * 8192); SB(); }
    cnt_wait(lds, C_QR, 1);
    const u32x4 qp = *(LAS u32x4*)(lds + L_QS + 4 * c);
    const unsigned t0m = __builtin_amdgcn_readfirstlane(*(LAS unsigned*)(lds + L_CNT + W_T0));
    constexpr float SCALE = 0.08838834764831845f, LOG2E = 1.4426950408889634f;
    LAS float* scp = lds + L_SC + wave * 512 + lane * 8;
    float mx = -INFINITY;
    auto kgroup = [&](u32x4 (&kb)[16], const bool more, const rsrc_t rn, const unsigned tb, const int g) __attribute__((always_inline)) {
      float sx[16];
#pragma unroll
      for (int i = 0; i < 16; ++i) {
        float d = 0.f;
#pragma unroll
        for (int t = 0; t < 4; ++t) d = dot2(kb[i][t], qp[t], d);
        asm volatile("" : "+v"(d));
        sx[i] = d;
        SB();
        if (more) { PACE(t0m, i); kb[i] = ld16_nt(rn, kvoff, tb + i * 8192); SB(); }
      }
#define TR_STEP(HALF, CTRL, BIT) _Pragma("unroll") for (int hh = 0; hh < (HALF); ++hh) { \
        const float lo_ = sx[hh], hi_ = sx[hh + (HALF)]; const float t1_ = lo_ + dpp<CTRL>(lo_), t2_ = hi_ + dpp<CTRL>(hi_); \
        sx[hh] = (c & (BIT)) ? t2_ : t1_; }
      TR_STEP(8, ROR8, 8) TR_STEP(4, XOR7, 4) TR_STEP(2, XOR2, 2) TR_STEP(1, XOR1, 1)
#undef TR_STEP
      const float sc = rbf(rbf(sx[0]) * SCALE) * LOG2E;
      mx = fmaxf(mx, sc);
      scp[g] = sc;
      SB();
    };
    kgroup(bA, false, rk, 0, 0);
#pragma unroll 1
    for (int g = 1; g < 8; ++g)
      kgroup(bB, true, mkrsrc(g == 7 ? Vc : Kc, (unsigned)H * SK * DH * 2), kvbase + (((g + 1) * 16 + rot) & 127u) * 8192, g);
    {
      const float m = wave_max(mx);
      f32x4 p0 = *(LAS f32x4*)scp, p1 = *(LAS f32x4*)(scp + 4);
      float l = 0.f;
#pragma unroll
      for (int e = 0; e < 4; ++e) { p0[e] = exp2_(p0[e] - m); p1[e] = exp2_(p1[e] - m); l += p0[e] + p1[e]; }
      *(LAS f32x4*)scp = p0; *(LAS f32x4*)(scp + 4) = p1;
      l = wave_sum(l);
      if (lane == 0) *(LAS f32x2*)(lds + L_ML + 2 * wave) = f32x2{m, l};
      cnt_arrive(lds, C_ML, lane);
    }
    float acc[8] = {0.f, 0.f, 0.f, 0.f, 0.f, 0.f, 0.f, 0.f};
    auto vgroup = [&](const bool more, const unsigned tb, const float p) __attribute__((always_inline)) {
#define PV_STEP(I) { fma8(acc, dpp<BCAST + (I)>(p), bB[I]); \
        asm volatile("" : "+v"(acc[0]), "+v"(acc[1]), "+v"(acc[2]), "+v"(acc[3]), "+v"(acc[4]), "+v"(acc[5]), "+v"(acc[6]), "+v"(acc[7])); SB(); \
        if (more) { PACE(t0m, I); bB[I] = ld16_nt(rv, kvoff, tb + (I) * 8192); SB(); } \
        else if ((I) == 11) {   \
          pace_wait(t0m, T_K0 + T_MAIN); \
          _Pragma("unroll") for (int u = 0; u < 4; ++u) { \
            bB[u] = ld16_nt(mkrsrc(wo, H * DH * D * 2), r * 2048 + c * 16, (h * 128 + wave * 16 + 4 * u) * 2048 + s * 256); SB(); } } }
      PV_STEP(0) PV_STEP(1) PV_STEP(2) PV_STEP(3) PV_STEP(4) PV_STEP(5) PV_STEP(6) PV_STEP(7)
      PV_STEP(8) PV_STEP(9) PV_STEP(10) PV_STEP(11) PV_STEP(12) PV_STEP(13) PV_STEP(14) PV_STEP(15)
#undef PV_STEP
    };
#pragma unroll 1
    for (int g = 0; g < 7; ++g) vgroup(true, kvbase + (((g + 1) * 16 + rot) & 127u) * 8192, scp[g]);
    __builtin_amdgcn_s_setprio(3);
    cnt_wait(lds, C_WG, 1);
    {
      const float wgt = lds[L_WG + wave];
#pragma unroll
      for (int e = 0; e < 8; ++e) acc[e] *= wgt;
      vgroup(false, 0, scp[7] * wgt);
    }
    *(LAS f32x2*)(lds + L_MRG + wave * 128 + 8 * c + 2 * r) = fold_rows(acc);
    cnt_arrive(lds, C_END, lane);
#pragma unroll
    for (int u = 0; u < 4; ++u) *(LAS u32x4*)(lds + L_WT + (wave * 16 + 4 * u + r) * WT_STR + 4 * c) = bB[u];
    cnt_arrive(lds, C_WT, lane);
    return;
#undef PACE
  }

  const int a = wave - NMAIN;
  const rsrc_t rx = mkrsrc(x, D * 2), rws = mkrsrc(ws, WS_END);
  unsigned tagA, tagB, tagC, tagD;
  unsigned long long e2;
  {
    u32x4 xa = ld16(rx, lane * 32, 0), xb = ld16(rx, lane * 32 + 16, 0);
    const u32x2 ep = __builtin_amdgcn_raw_buffer_load_b64(rws, 0, WS_EPOCH, SC1);
    const rsrc_t rwq = mkrsrc(wq, D * H * DH * 2);
    u32x4 w[8];
    const int voff = r * 8192 + c * 16, sb = (s * 128 + a * 32) * 8192 + h * 256;
#pragma unroll
    for (int i = 0; i < 8; ++i) { w[i] = ld16_nt(rwq, voff, sb + i * 32768); SB(); }
    if (a == 0 && lane < C_NUM) *(LAS unsigned*)(lds + L_CNT + lane) = 0u;
    __syncthreads();
    asm volatile("" : "+v"(xa), "+v"(xb));
    float v[16];
#pragma unroll
    for (int i = 0; i < 4; ++i) { v[2 * i] = lo2f(xa[i]); v[2 * i + 1] = hi2f(xa[i]); v[8 + 2 * i] = lo2f(xb[i]); v[9 + 2 * i] = hi2f(xb[i]); }
    float sm = 0.f;
#pragma unroll
    for (int i = 0; i < 16; ++i) sm += v[i];
    const float mean = wave_sum(sm) * (1.f / D);
    float ss = 0.f;
#pragma unroll
    for (int i = 0; i < 16; ++i) { const float d = v[i] - mean; ss = fmaf(d, d, ss); }
    const float var = wave_sum(ss) * (1.f / D);
    const float mb = rbf(mean), den = rbf(sqrtf(rbf(rbf(var) + 1e-5f)));
    const float rinv = 1.0f / den;
#pragma unroll
    for (int i = 0; i < 16; ++i) {
      const float nu = rbf(v[i] - mb), q0 = nu * rinv;
      v[i] = rbf(fmaf(fmaf(-q0, den, nu), rinv, q0));
    }
#pragma unroll
    for (int q = 0; q < 4; ++q) *(LAS f32x4*)(lds + L_XN + q * 256 + 4 * lane) = f32x4{v[q], v[4 + q], v[8 + q], v[12 + q]};
    cnt_arrive(lds, C_XN, lane);
    const unsigned long long e1 = (((unsigned long long)__builtin_amdgcn_readfirstlane(ep[1]) << 32) | (unsigned)__builtin_amdgcn_readfirstlane(ep[0])) + 1ull;
    e2 = e1 + 1ull;
    tagA = (unsigned)e1; tagB = (unsigned)(e1 >> 32) + (unsigned)e1 * 0x9E3779B1u;
    tagC = (unsigned)e2; tagD = (unsigned)(e2 >> 32) + (unsigned)e2 * 0x9E3779B1u;
    float acc[8] = {0.f, 0.f, 0.f, 0.f, 0.f, 0.f, 0.f, 0.f};
#pragma unroll
    for (int q = 0; q < 2; ++q) {
      const f32x4 xv = *(LAS f32x4*)(lds + L_XN + r * 256 + s * 32 + a * 8 + 4 * q);
#pragma unroll
      for (int e = 0; e < 4; ++e) fma8(acc, xv[e], w[4 * q + e]);
    }
    *(LAS f32x2*)(lds + L_QP + a * 128 + 8 * c + 2 * r) = fold_rows(acc);
  }
  cnt_arrive(lds, C_Q, lane);
  if (a == 0) {
    cnt_wait<false>(lds, C_Q, NAUX);
    f32x2 v = *(LAS f32x2*)(lds + L_QP + 2 * lane);
#pragma unroll
    for (int q = 1; q < 4; ++q) v += *(LAS f32x2*)(lds + L_QP + q * 128 + 2 * lane);
    st16_sc1(rws, WS_Q + (h * 8 + s) * 1024 + lane * 16, rec(v[0], v[1], tagA, tagB));
    st16_sc1(rws, (bid == 0 && lane == 0) ? WS_T0 : OOB, u32x4{t0, tagA, t0, tagB});
  }
  if (a == 1) {
    pace_wait(t0, T_QPOLL);
    float q0 = 0.f, q1 = 0.f;
    unsigned tg = t0;
#pragma unroll 1
    for (unsigned sp = 0; sp < SPIN_MEM; ++sp) {
      REREAD();
      u32x4 g[8];
#pragma unroll
      for (int i = 0; i < 8; ++i) g[i] = ld16_sc1(rws, lane * 16, WS_Q + (h * 8 + i) * 1024);
      const u32x4 gt = ld16_sc1(rws, 0, WS_T0);
      bool ok = rok(gt, tagA, tagB);
      tg = gt[0];
      q0 = 0.f; q1 = 0.f;
#pragma unroll
      for (int i = 0; i < 8; ++i) { ok = ok && rok(g[i], tagA, tagB); q0 += r0(g[i]); q1 += r1(g[i]); }
      if (__all(ok)) break;
      __builtin_amdgcn_s_sleep(2);
    }
    *(LAS unsigned*)(lds + L_QS + lane) = pack2(q0, q1);
    if (lane == 0) *(LAS unsigned*)(lds + L_CNT + W_T0) = ((int)(t0 - tg) < 0 || (int)(t0 - tg) > 300) ? t0 : tg;
    cnt_arrive(lds, C_QR, lane);
  }
  const int ks = xl * 2 + (j >> 4), cg = j & 15;
  u32x4 dw[7];
  {
    const rsrc_t rd = mkrsrc(wd, D * F * 2);
    const int voff = (lane >> 3) * 2048 + (lane & 7) * 16, sb = (ks * 224 + a * 56) * 2048 + cg * 128;
    constexpr int WSTEP = ((T_K0 - T_W2) << 16) / 7;
    int wdue = T_W2 * 65536 + (WSTEP / 128) * (a * 32 + j);
#pragma unroll
    for (int i = 0; i < 7; ++i) { pace_wait(t0, wdue >> 16); wdue += WSTEP; dw[i] = ld16_nt(rd, voff, sb + i * 16384); SB(); }
  }
  if (a == 3) {
    pace_wait(t0, T_TOUCH);
    const u32x4 p = ld16_sc1(rws, lane * 16, WS_Y + (h * 8 + s) * 1024);
    asm volatile("" :: "v"(p));
  }
  if (a == 0 && j < 28) {
    cnt_wait(lds, C_F1, NMAIN);
    f32x2 v = *(LAS f32x2*)(lds + L_FP + 2 * (lane & 31));
#pragma unroll
    for (int q = 1; q < 8; ++q) v += *(LAS f32x2*)(lds + L_FP + q * 64 + 2 * (lane & 31));
    st16_sc1(rws, lane < 32 ? WS_G1 + ((mat * 2 + rb) * F + cb * 64) * 8 + lane * 16 : OOB, rec(v[0], v[1], tagA, tagB));
  }
  {
    pace_wait(t0, T_K0 + 2000);
    u32x4 g[4];
    const int voff = (lane < 28 ? lane : 27) * 16;
#pragma unroll 1
    for (unsigned sp = 0; sp < SPIN_MEM; ++sp) {
      REREAD();
#pragma unroll
      for (int k = 0; k < 4; ++k) g[k] = ld16_sc1(rws, voff, WS_G1 + (k * F + ks * 224 + a * 56) * 8);
      if (__all(rok(g[0], tagA, tagB) && rok(g[1], tagA, tagB) && rok(g[2], tagA, tagB) && rok(g[3], tagA, tagB))) break;
      __builtin_amdgcn_s_sleep(32);
    }
    float hh[2];
#pragma unroll
    for (int e = 0; e < 2; ++e) {
      const float gs = rbf(__uint_as_float(g[0][2 * e]) + __uint_as_float(g[1][2 * e]));
      const float us = rbf(__uint_as_float(g[2][2 * e]) + __uint_as_float(g[3][2 * e]));
      const float ge = 0.5f * gs * (1.f + tanhf(0.7978845608028654f * (gs + 0.044715f * gs * gs * gs)));
      hh[e] = rbf(rbf(ge) * us);
    }
    if (lane < 28) *(LAS f32x2*)(lds + L_H + a * 64 + 2 * lane) = f32x2{hh[0], hh[1]};
    float y[8] = {0.f, 0.f, 0.f, 0.f, 0.f, 0.f, 0.f, 0.f};
#pragma unroll
    for (int i = 0; i < 7; ++i) fma8(y, lds[L_H + a * 64 + 8 * i + (lane >> 3)], dw[i]);
#pragma unroll
    for (int e = 0; e < 8; ++e) y[e] += dpp<ROR8>(y[e]);
    const f32x2 z = fold_rows(y);
    if (c < 8) *(LAS f32x2*)(lds + L_F2 + a * 64 + 8 * c + 2 * r) = z;
    cnt_arrive(lds, C_F2, lane);
  }
  const int col0 = s * 128 + 4 * h;
  float f0 = 0.f, f1v = 0.f;
  unsigned xw = 0;
  if (a == 0) {
    cnt_wait(lds, C_F2, NAUX);
    f32x2 v = *(LAS f32x2*)(lds + L_F2 + 2 * (lane & 31));
#pragma unroll
    for (int q = 1; q < 4; ++q) v += *(LAS f32x2*)(lds + L_F2 + q * 64 + 2 * (lane & 31));
    st16_sc1(rws, lane < 32 ? WS_FFN + (ks * D + cg * 64) * 8 + lane * 16 : OOB, rec(v[0], v[1], tagA, tagB));
    xw = __builtin_amdgcn_raw_buffer_load_b32(rx, (col0 + 2 * (lane & 1)) * 2, 0, 0);
    pace_wait(t0, T_K0 + 3500);
    u32x4 g;
#pragma unroll 1
    for (unsigned sp = 0; sp < SPIN_MEM; ++sp) {
      REREAD();
      g = ld16_sc1(rws, ((lane >> 1) & 15) * (D * 8) + (lane & 1) * 16, WS_FFN + col0 * 8);
      if (__all(rok(g, tagA, tagB))) break;
      __builtin_amdgcn_s_sleep(32);
    }
    f0 = rbf(parity_sum(r0(g)) * 0.5f); f1v = rbf(parity_sum(r1(g)) * 0.5f);
  }
  if (a == 1) {
    cnt_wait(lds, C_ML, NMAIN);
    const f32x2 ml = *(LAS f32x2*)(lds + L_ML + 2 * (lane & 7));
    const float Mw = max8(ml[0]), Lw = sum8(exp2_(ml[0] - Mw) * ml[1]);
    st16_sc1(rws, lane == 0 ? WS_ML + (h * 8 + s) * 128 : OOB, rec(Mw, Lw, tagA, tagB));
    pace_wait(t0, T_K0 + T_MAIN / 2 + 1500);
    u32x4 g;
#pragma unroll 1
    for (unsigned sp = 0; sp < SPIN_MEM; ++sp) {
      REREAD();
      g = ld16_sc1(rws, (lane & 7) * 128, WS_ML + h * 8 * 128);
      if (__all(rok(g, tagA, tagB))) break;
      __builtin_amdgcn_s_sleep(32);
    }
    const float M = max8(r0(g)), L = sum8(exp2_(r0(g) - M) * r1(g));
    if (lane < 8) lds[L_WG + lane] = exp2_(ml[0] - M) / L;
    cnt_arrive(lds, C_WG, lane);
  }
  pace_wait(t0, T_K0 + T_MAIN - 300);
  if (a == 0) {
    const unsigned po = __builtin_amdgcn_raw_buffer_load_b32(mkrsrc(out, D * 2), (col0 + 2 * (lane & 1)) * 2, 0, SC1);
    asm volatile("" :: "v"(po));
  }
  pace_wait(t0, T_K0 + T_MAIN - 100);
  __builtin_amdgcn_s_setprio(3);
  if (a == 0) {
    cnt_wait<false>(lds, C_END, NMAIN);
    f32x2 v = *(LAS f32x2*)(lds + L_MRG + 2 * lane);
#pragma unroll
    for (int w = 1; w < 8; ++w) v += *(LAS f32x2*)(lds + L_MRG + w * 128 + 2 * lane);
    st16_sc1(rws, WS_Q + (h * 8 + s) * 1024 + lane * 16, rec(v[0], v[1], tagC, tagD));
  } else {
    cnt_wait(lds, C_END, NMAIN);
  }
  u32x4 bfr[2][4];
  cnt_wait<false>(lds, C_WT, NMAIN);
#pragma unroll
  for (int q = 0; q < 4; ++q)
#pragma unroll
    for (int d = 0; d < 4; ++d) {
      const unsigned w0 = *(LAS unsigned*)(lds + L_WT + (32 * q + 8 * r + 2 * d) * WT_STR + 16 * a + c);
      const unsigned w1 = *(LAS unsigned*)(lds + L_WT + (32 * q + 8 * r + 2 * d + 1) * WT_STR + 16 * a + c);
      bfr[0][q][d] = __builtin_amdgcn_perm(w1, w0, 0x05040100u);
      bfr[1][q][d] = __builtin_amdgcn_perm(w1, w0, 0x07060302u);
    }
  {
    u32x4 g0, g1;
    const int vo = r * 1024 + (16 * a + c) * 16;
#pragma unroll 1
    for (unsigned sp = 0; sp < SPIN_MEM; ++sp) {
      REREAD();
      g0 = ld16_sc1(rws, vo, WS_Q + h * 8 * 1024);
      g1 = ld16_sc1(rws, vo, WS_Q + (h * 8 + 4) * 1024);
      if (__all(rok(g0, tagC, tagD) && rok(g1, tagC, tagD))) break;
    }
    float o = fold32(r0(g0) + r0(g1), r1(g0) + r1(g1));
    o = fold16(o, o);
    if ((r & 1) == 0) *((LAS u16*)(lds + L_OB) + 32 * a + 2 * c + (r >> 1)) = __builtin_bit_cast(u16, (__bf16)o);
  }
  cnt_arrive(lds, C_OB, lane);
  cnt_wait<false>(lds, C_OB, NAUX);
  {
    f32x4 y0 = {0.f, 0.f, 0.f, 0.f}, y1 = y0;
#pragma unroll
    for (int q = 0; q < 4; ++q) {
      const u32x4 af = *(LAS u32x4*)(lds + L_OB + 16 * q + 4 * r);
      y0 = __builtin_amdgcn_mfma_f32_16x16x32_bf16(__builtin_bit_cast(bf16x8, af), __builtin_bit_cast(bf16x8, bfr[0][q]), y0, 0, 0, 0);
      y1 = __builtin_amdgcn_mfma_f32_16x16x32_bf16(__builtin_bit_cast(bf16x8, af), __builtin_bit_cast(bf16x8, bfr[1][q]), y1, 0, 0, 0);
    }
    st16_sc1(rws, lane < 16 ? WS_Y + (h * 8 + s) * 1024 + (16 * a + lane) * 16 : OOB, rec(y0[0], y1[0], tagA, tagB));
  }
  if (a == 0) {
    u32x4 g;
    pace_wait((unsigned)__builtin_amdgcn_s_memrealtime(), 30);
#pragma unroll 1
    for (unsigned sp = 0; sp < SPIN_MEM; ++sp) {
      REREAD();
      g = ld16_sc1(rws, (lane >> 1) * (8 * 1024) + (lane & 1) * 16, WS_Y + s * 1024 + h * 32);
      if (__all(rok(g, tagA, tagB))) break;
    }
    const float a0 = rbf(parity_sum(r0(g))), a1 = rbf(parity_sum(r1(g)));
    const unsigned o = pack2(rbf(lo2f(xw) + a0) + f0, rbf(hi2f(xw) + a1) + f1v);
    __builtin_amdgcn_raw_buffer_store_b32(o, mkrsrc(out, D * 2), lane < 2 ? (col0 + 2 * lane) * 2 : OOB, 0, SC1);
    __builtin_amdgcn_raw_buffer_store_b64(u32x2{(unsigned)e2, (unsigned)(e2 >> 32)}, rws, (bid == 0 && lane == 0) ? WS_EPOCH : OOB, 0, SC1);
  }
}

extern "C" void kernel_launch(void* const* d_in, const int* in_sizes, int n_in, void* d_out, int out_size, void* d_ws, size_t ws_size,
                              hipStream_t stream) {
  fused_decode_kernel<<<256, NTHR, 0, stream>>>((const u16*)d_in[0], (const u16*)d_in[1], (const u16*)d_in[2], (const u16*)d_in[3],
                                                 (const u16*)d_in[4], (const u16*)d_in[6], (const u16*)d_in[7], (const u16*)d_in[8],
                                                 (u16*)d_out, d_ws);
}
```

```cpp
#define T_MAIN 7150
#define T_K0 460
#define Y_TOUCH 0
#include <hip/hip_runtime.h>
#include <stdint.h>

typedef unsigned u32x4 __attribute__((ext_vector_type(4)));
typedef unsigned u32x2 __attribute__((ext_vector_type(2)));
typedef float f32x4 __attribute__((ext_vector_type(4)));
typedef float f32x2 __attribute__((ext_vector_type(2)));
typedef __bf16 bf16x2 __attribute__((ext_vector_type(2)));
typedef __bf16 bf16x8 __attribute__((ext_vector_type(8)));
typedef unsigned short u16;
#define LAS __attribute__((address_space(3)))
typedef __amdgpu_buffer_rsrc_t rsrc_t;

#define T_F1 150
#define T_W2 360
#define PACE_G 2
#define PHASE (wave * 32 + j)
constexpr int T_QPOLL = 390, T_TOUCH = 1000;

constexpr int D = 1024, H = 32, SK = 32768, DH = 128, F = 3584;
constexpr int NMAIN = 8, NAUX = 4, NTHR = (NMAIN + NAUX) * 64;
constexpr int NT = 2, SC1 = 16;
constexpr int OOB = 0x40000000;
constexpr unsigned WS_EPOCH = 0, WS_T0 = 1024, WS_ML = 4096, WS_Q = WS_ML + 256 * 128, WS_Y = WS_Q + 256 * 1024, WS_G1 = WS_Y + 256 * 1024,
                   WS_FFN = WS_G1 + 4 * F * 8, WS_END = WS_FFN + 16 * D * 8;
constexpr int L_XN = 0, L_QP = 1024, L_QS = L_QP + 4 * 128, L_MRG = L_QS + 64, L_ML = L_MRG + 8 * 128, L_WG = L_ML + 16, L_FP = L_WG + 8,
              L_H = L_FP + 8 * 64, L_F2 = L_H + 4 * 64, L_OB = L_F2 + 4 * 64, L_CNT = L_OB + 64, L_SC = L_CNT + 16, L_WT = L_SC + 8 * 512,
              WT_STR = 68, L_END = L_WT + 128 * WT_STR;
constexpr int C_Q = 0, C_XN = 1, C_F2 = 2, C_WT = 3, C_ML = 4, C_WG = 5, C_END = 6, C_QR = 7, C_F1 = 8, C_OB = 9, C_NUM = 10, W_T0 = 12;
constexpr unsigned SPIN_MEM = 1u << 20, SPIN_LDS = 1u << 23;

__device__ __forceinline__ float lo2f(unsigned w) { return __builtin_bit_cast(float, w << 16); }
__device__ __forceinline__ float hi2f(unsigned w) { return __builtin_bit_cast(float, w & 0xffff0000u); }
__device__ __forceinline__ float rbf(float x) { return (float)(__bf16)x; }
__device__ __forceinline__ unsigned pack2(float a, float b) { bf16x2 v = {(__bf16)a, (__bf16)b}; return __builtin_bit_cast(unsigned, v); }
__device__ __forceinline__ float dot2(unsigned k, unsigned q, float acc) {
  return __builtin_amdgcn_fdot2_f32_bf16(__builtin_bit_cast(bf16x2, k), __builtin_bit_cast(bf16x2, q), acc, false);
}
template <int CTRL> __device__ __forceinline__ float dpp(float x) {
  return __builtin_bit_cast(float, __builtin_amdgcn_mov_dpp(__builtin_bit_cast(int, x), CTRL, 0xf, 0xf, true));
}
constexpr int XOR1 = 0xB1, XOR2 = 0x4E, XOR7 = 0x141, ROR8 = 0x128, ROR4 = 0x124, BCAST = 0x150;
__device__ __forceinline__ float fold32(float a, float b) {
  auto t = __builtin_amdgcn_permlane32_swap(__float_as_uint(a), __float_as_uint(b), false, false);
  return __uint_as_float(t[0]) + __uint_as_float(t[1]);
}
__device__ __forceinline__ float fold16(float a, float b) {
  auto t = __builtin_amdgcn_permlane16_swap(__float_as_uint(a), __float_as_uint(b), false, false);
  return __uint_as_float(t[0]) + __uint_as_float(t[1]);
}
__device__ __forceinline__ f32x2 fold_rows(const float (&v)[8]) {
  const float u0 = fold32(v[0], v[4]), u1 = fold32(v[1], v[5]), u2 = fold32(v[2], v[6]), u3 = fold32(v[3], v[7]);
  return f32x2{fold16(u0, u2), fold16(u1, u3)};
}
__device__ __forceinline__ float xrow_sum(float x) { x = fold16(x, x); return fold32(x, x); }
__device__ __forceinline__ float xrow_max(float x) {
  auto s = __builtin_amdgcn_permlane16_swap(__float_as_uint(x), __float_as_uint(x), false, false);
  x = fmaxf(__uint_as_float(s[0]), __uint_as_float(s[1]));
  auto t = __builtin_amdgcn_permlane32_swap(__float_as_uint(x), __float_as_uint(x), false, false);
  return fmaxf(__uint_as_float(t[0]), __uint_as_float(t[1]));
}
__device__ __forceinline__ float parity_sum(float x) {
  x += dpp<XOR2>(x); x += dpp<ROR4>(x); x += dpp<ROR8>(x); return xrow_sum(x);
}
__device__ __forceinline__ float wave_sum(float x) { x += dpp<XOR1>(x); return parity_sum(x); }
__device__ __forceinline__ float max8(float x) { x = fmaxf(x, dpp<XOR1>(x)); x = fmaxf(x, dpp<XOR2>(x)); return fmaxf(x, dpp<XOR7>(x)); }
__device__ __forceinline__ float sum8(float x) { x += dpp<XOR1>(x); x += dpp<XOR2>(x); return x + dpp<XOR7>(x); }
__device__ __forceinline__ float wave_max(float x) { x = max8(x); x = fmaxf(x, dpp<ROR8>(x)); return xrow_max(x); }
__device__ __forceinline__ float exp2_(float x) { return __builtin_amdgcn_exp2f(x); }
__device__ __forceinline__ rsrc_t mkrsrc(const void* p, unsigned bytes) { return __builtin_amdgcn_make_buffer_rsrc((void*)p, 0, (int)bytes, 0x00020000); }
__device__ __forceinline__ u32x4 ld16(rsrc_t r, int voff, int soff) { return __builtin_amdgcn_raw_buffer_load_b128(r, voff, soff, 0); }
__device__ __forceinline__ u32x4 ld16_nt(rsrc_t r, int voff, int soff) { return __builtin_amdgcn_raw_buffer_load_b128(r, voff, soff, NT); }
__device__ __forceinline__ u32x4 ld16_sc1(rsrc_t r, int voff, int soff) { return __builtin_amdgcn_raw_buffer_load_b128(r, voff, soff, SC1); }
__device__ __forceinline__ void st16_sc1(rsrc_t r, int off, u32x4 v) { __builtin_amdgcn_raw_buffer_store_b128(v, r, off, 0, SC1); }
#define SB() __builtin_amdgcn_sched_barrier(0)
#define REREAD() asm volatile("" ::: "memory")
__device__ __forceinline__ void pace_wait(unsigned t0, int due) {
  asm volatile("1:\n\ts_memrealtime s[96:97]\n\ts_waitcnt lgkmcnt(0)\n\ts_sub_u32 s96, s96, %0\n\ts_cmp_lt_i32 s96, %1\n\t"
               "s_cbranch_scc0 2f\n\ts_sleep 1\n\ts_branch 1b\n2:"
               :: "s"(t0), "s"(due) : "s96", "s97", "scc", "memory");
}
__device__ __forceinline__ void fma8(float (&acc)[8], float s, u32x4 w) {
#pragma unroll
  for (int t = 0; t < 4; ++t) { acc[2 * t] = fmaf(s, lo2f(w[t]), acc[2 * t]); acc[2 * t + 1] = fmaf(s, hi2f(w[t]), acc[2 * t + 1]); }
}
__device__ __forceinline__ u32x4 rec(float a, float b, unsigned ta, unsigned tb) { return u32x4{__float_as_uint(a), ta, __float_as_uint(b), tb}; }
__device__ __forceinline__ bool rok(u32x4 g, unsigned ta, unsigned tb) { return g[1] == ta && g[3] == tb; }
__device__ __forceinline__ float r0(u32x4 g) { return __uint_as_float(g[0]); }
__device__ __forceinline__ float r1(u32x4 g) { return __uint_as_float(g[2]); }

__device__ __forceinline__ void cnt_arrive(LAS float* lds, int which, int lane) {
  asm volatile("s_waitcnt lgkmcnt(0)" ::: "memory");
  if (lane == 0) __hip_atomic_fetch_add((LAS unsigned*)(lds + L_CNT + which), 1u, __ATOMIC_RELAXED, __HIP_MEMORY_SCOPE_WORKGROUP);
}
template <bool SLEEP = true>
__device__ __forceinline__ void cnt_wait(LAS float* lds, int which, unsigned n) {
#pragma unroll 1
  for (unsigned sp = 0; sp < SPIN_LDS; ++sp) {
    if (__builtin_amdgcn_readfirstlane(*(volatile LAS unsigned*)(lds + L_CNT + which)) >= n) break;
    if (SLEEP) __builtin_amdgcn_s_sleep(1);
  }
  REREAD();
}

__global__ void __launch_bounds__(NTHR, 3)
fused_decode_kernel(const u16* __restrict__ x, const u16* __restrict__ Kc, const u16* __restrict__ Vc, const u16* __restrict__ wq,
                    const u16* __restrict__ wo, const u16* __restrict__ wg, const u16* __restrict__ wu, const u16* __restrict__ wd,
                    u16* __restrict__ out, void* ws) {
  __shared__ __attribute__((aligned(16))) float lds_[L_END];
  LAS float* lds = (LAS float*)lds_;
  const unsigned t0 = (unsigned)__builtin_amdgcn_s_memrealtime();
  const int tid = threadIdx.x, lane = tid & 63, wave = __builtin_amdgcn_readfirstlane(tid >> 6);
  const int r = lane >> 4, c = lane & 15;
  const int bid = blockIdx.x, xl = bid & 7, j = bid >> 3;
  const int h = xl * 4 + (j >> 3), s = j & 7;
  const int f1 = xl * 28 + j, mat = f1 / 112, rb = (f1 / 56) & 1, cb = f1 % 56;

  if (wave < NMAIN) {
    const rsrc_t rk = mkrsrc(Kc, (unsigned)H * SK * DH * 2), rv = mkrsrc(Vc, (unsigned)H * SK * DH * 2);
    u32x4 bA[16], bB[16];
    const unsigned kvbase = (unsigned)h * (8u << 20) + (unsigned)s * (1u << 20) + wave * 1024;
    const unsigned rot = 32u * ((h + (h >> 2) + (h >> 4)) & 3);
    const int kvoff = lane * 16;
    constexpr int STEP = (T_MAIN << 16) / 256 * PACE_G;
    int due = T_K0 * 65536 - STEP + (STEP / 256) * (PHASE);
#define PACE(T0, I) do { if ((I) % PACE_G == 0) { due += STEP; pace_wait(T0, due >> 16); } } while (0)
    __builtin_amdgcn_s_barrier();
    SB();
    {
      const rsrc_t rg = mkrsrc(mat ? wu : wg, D * F * 2);
      const int voff = j < 28 ? (lane >> 3) * (F * 2) + (lane & 7) * 16 : OOB, sb = (rb * 512 + wave * 64) * (F * 2) + cb * 128;
      constexpr int FSTEP = ((T_W2 - T_F1) << 16) / 8;
      int fdue = T_F1 * 65536 + (FSTEP / 256) * (wave * 32 + j);
#pragma unroll
      for (int i = 0; i < 8; ++i) { pace_wait(t0, fdue >> 16); fdue += FSTEP; bB[i] = ld16_nt(rg, voff, sb + i * 8 * (F * 2)); SB(); }
    }
#pragma unroll
    for (int i = 0; i < 16; ++i) { PACE(t0, i); bA[i] = ld16_nt(rk, kvoff, kvbase + (rot + i) * 8192); SB(); }
    {
      cnt_wait(lds, C_XN, 1);
      float ag[8] = {0.f, 0.f, 0.f, 0.f, 0.f, 0.f, 0.f, 0.f};
#pragma unroll
      for (int i = 0; i < 8; ++i) {
        const int k = rb * 512 + wave * 64 + 8 * i + (lane >> 3);
        fma8(ag, lds[L_XN + (k & 3) * 256 + (k >> 2)], bB[i]);
      }
#pragma unroll
      for (int e = 0; e < 8; ++e) ag[e] += dpp<ROR8>(ag[e]);
      const f32x2 z = fold_rows(ag);
      if (c < 8) *(LAS f32x2*)(lds + L_FP + wave * 64 + 8 * c + 2 * r) = z;
      cnt_arrive(lds, C_F1, lane);
      SB();
    }
#pragma unroll
    for (int i = 0; i < 16; ++i) { PACE(t0, i); bB[i] = ld16_nt(rk, kvoff, kvbase + (rot + 16 + i) * 8192); SB(); }
    cnt_wait(lds, C_QR, 1);
    const u32x4 qp = *(LAS u32x4*)(lds + L_QS + 4 * c);
    const unsigned t0m = __builtin_amdgcn_readfirstlane(*(LAS unsigned*)(lds + L_CNT + W_T0));
    constexpr float SCALE = 0.08838834764831845f, LOG2E = 1.4426950408889634f;
    LAS float* scp = lds + L_SC + wave * 512 + lane * 8;
    float mx = -INFINITY;
    auto kgroup = [&](u32x4 (&kb)[16], const bool more, const rsrc_t rn, const unsigned tb, const int g) __attribute__((always_inline)) {
      float sx[16];
#pragma unroll
      for (int i = 0; i < 16; ++i) {
        float d = 0.f;
#pragma unroll
        for (int t = 0; t < 4; ++t) d = dot2(kb[i][t], qp[t], d);
        asm volatile("" : "+v"(d));
        sx[i] = d;
        SB();
        if (more) { PACE(t0m, i); kb[i] = ld16_nt(rn, kvoff, tb + i * 8192); SB(); }
      }
#define TR_STEP(HALF, CTRL, BIT) _Pragma("unroll") for (int hh = 0; hh < (HALF); ++hh) { \
        const float lo_ = sx[hh], hi_ = sx[hh + (HALF)]; const float t1_ = lo_ + dpp<CTRL>(lo_), t2_ = hi_ + dpp<CTRL>(hi_); \
        sx[hh] = (c & (BIT)) ? t2_ : t1_; }
      TR_STEP(8, ROR8, 8) TR_STEP(4, XOR7, 4) TR_STEP(2, XOR2, 2) TR_STEP(1, XOR1, 1)
#undef TR_STEP
      const float sc = rbf(rbf(sx[0]) * SCALE) * LOG2E;
      mx = fmaxf(mx, sc);
      scp[g] = sc;
      SB();
    };
    kgroup(bA, false, rk, 0, 0);
#pragma unroll 1
    for (int g = 1; g < 8; ++g)
      kgroup(bB, true, mkrsrc(g == 7 ? Vc : Kc, (unsigned)H * SK * DH * 2), kvbase + (((g + 1) * 16 + rot) & 127u) * 8192, g);
    {
      const float m = wave_max(mx);
      f32x4 p0 = *(LAS f32x4*)scp, p1 = *(LAS f32x4*)(scp + 4);
      float l = 0.f;
#pragma unroll
      for (int e = 0; e < 4; ++e) { p0[e] = exp2_(p0[e] - m); p1[e] = exp2_(p1[e] - m); l += p0[e] + p1[e]; }
      *(LAS f32x4*)scp = p0; *(LAS f32x4*)(scp + 4) = p1;
      l = wave_sum(l);
      if (lane == 0) *(LAS f32x2*)(lds + L_ML + 2 * wave) = f32x2{m, l};
      cnt_arrive(lds, C_ML, lane);
    }
    float acc[8] = {0.f, 0.f, 0.f, 0.f, 0.f, 0.f, 0.f, 0.f};
    auto vgroup = [&](const bool more, const unsigned tb, const float p) __attribute__((always_inline)) {
#define PV_STEP(I) { fma8(acc, dpp<BCAST + (I)>(p), bB[I]); \
        asm volatile("" : "+v"(acc[0]), "+v"(acc[1]), "+v"(acc[2]), "+v"(acc[3]), "+v"(acc[4]), "+v"(acc[5]), "+v"(acc[6]), "+v"(acc[7])); SB(); \
        if (more) { PACE(t0m, I); bB[I] = ld16_nt(rv, kvoff, tb + (I) * 8192); SB(); } \
        else if ((I) == 11) {   \
          pace_wait(t0m, T_K0 + T_MAIN); \
          _Pragma("unroll") for (int u = 0; u < 4; ++u) { \
            bB[u] = ld16_nt(mkrsrc(wo, H * DH * D * 2), r * 2048 + c * 16, (h * 128 + wave * 16 + 4 * u) * 2048 + s * 256); SB(); } } }
      PV_STEP(0) PV_STEP(1) PV_STEP(2) PV_STEP(3) PV_STEP(4) PV_STEP(5) PV_STEP(6) PV_STEP(7)
      PV_STEP(8) PV_STEP(9) PV_STEP(10) PV_STEP(11) PV_STEP(12) PV_STEP(13) PV_STEP(14) PV_STEP(15)
#undef PV_STEP
    };
#pragma unroll 1
    for (int g = 0; g < 7; ++g) vgroup(true, kvbase + (((g + 1) * 16 + rot) & 127u) * 8192, scp[g]);
    __builtin_amdgcn_s_setprio(3);
    cnt_wait(lds, C_WG, 1);
    {
      const float wgt = lds[L_WG + wave];
#pragma unroll
      for (int e = 0; e < 8; ++e) acc[e] *= wgt;
      vgroup(false, 0, scp[7] * wgt);
    }
    *(LAS f32x2*)(lds + L_MRG + wave * 128 + 8 * c + 2 * r) = fold_rows(acc);
    cnt_arrive(lds, C_END, lane);
#pragma unroll
    for (int u = 0; u < 4; ++u) *(LAS u32x4*)(lds + L_WT + (wave * 16 + 4 * u + r) * WT_STR + 4 * c) = bB[u];
    cnt_arrive(lds, C_WT, lane);
    return;
#undef PACE
  }

  const int a = wave - NMAIN;
  const rsrc_t rx = mkrsrc(x, D * 2), rws = mkrsrc(ws, WS_END);
  unsigned tagA, tagB, tagC, tagD;
  unsigned long long e2;
  {
    u32x4 xa = ld16(rx, lane * 32, 0), xb = ld16(rx, lane * 32 + 16, 0);
    const u32x2 ep = __builtin_amdgcn_raw_buffer_load_b64(rws, 0, WS_EPOCH, SC1);
    const rsrc_t rwq = mkrsrc(wq, D * H * DH * 2);
    u32x4 w[8];
    const int voff = r * 8192 + c * 16, sb = (s * 128 + a * 32) * 8192 + h * 256;
#pragma unroll
    for (int i = 0; i < 8; ++i) { w[i] = ld16_nt(rwq, voff, sb + i * 32768); SB(); }
    if (a == 0 && lane < C_NUM) *(LAS unsigned*)(lds + L_CNT + lane) = 0u;
    __syncthreads();
    asm volatile("" : "+v"(xa), "+v"(xb));
    float v[16];
#pragma unroll
    for (int i = 0; i < 4; ++i) { v[2 * i] = lo2f(xa[i]); v[2 * i + 1] = hi2f(xa[i]); v[8 + 2 * i] = lo2f(xb[i]); v[9 + 2 * i] = hi2f(xb[i]); }
    float sm = 0.f;
#pragma unroll
    for (int i = 0; i < 16; ++i) sm += v[i];
    const float mean = wave_sum(sm) * (1.f / D);
    float ss = 0.f;
#pragma unroll
    for (int i = 0; i < 16; ++i) { const float d = v[i] - mean; ss = fmaf(d, d, ss); }
    const float var = wave_sum(ss) * (1.f / D);
    const float mb = rbf(mean), den = rbf(sqrtf(rbf(rbf(var) + 1e-5f)));
    const float rinv = 1.0f / den;
#pragma unroll
    for (int i = 0; i < 16; ++i) {
      const float nu = rbf(v[i] - mb), q0 = nu * rinv;
      v[i] = rbf(fmaf(fmaf(-q0, den, nu), rinv, q0));
    }
#pragma unroll
    for (int q = 0; q < 4; ++q) *(LAS f32x4*)(lds + L_XN + q * 256 + 4 * lane) = f32x4{v[q], v[4 + q], v[8 + q], v[12 + q]};
    cnt_arrive(lds, C_XN, lane);
    const unsigned long long e1 = (((unsigned long long)__builtin_amdgcn_readfirstlane(ep[1]) << 32) | (unsigned)__builtin_amdgcn_readfirstlane(ep[0])) + 1ull;
    e2 = e1 + 1ull;
    tagA = (unsigned)e1; tagB = (unsigned)(e1 >> 32) + (unsigned)e1 * 0x9E3779B1u;
    tagC = (unsigned)e2; tagD = (unsigned)(e2 >> 32) + (unsigned)e2 * 0x9E3779B1u;
    float acc[8] = {0.f, 0.f, 0.f, 0.f, 0.f, 0.f, 0.f, 0.f};
#pragma unroll
    for (int q = 0; q < 2; ++q) {
      const f32x4 xv = *(LAS f32x4*)(lds + L_XN + r * 256 + s * 32 + a * 8 + 4 * q);
#pragma unroll
      for (int e = 0; e < 4; ++e) fma8(acc, xv[e], w[4 * q + e]);
    }
    *(LAS f32x2*)(lds + L_QP + a * 128 + 8 * c + 2 * r) = fold_rows(acc);
  }
  cnt_arrive(lds, C_Q, lane);
  if (a == 0) {
    cnt_wait<false>(lds, C_Q, NAUX);
    f32x2 v = *(LAS f32x2*)(lds + L_QP + 2 * lane);
#pragma unroll
    for (int q = 1; q < 4; ++q) v += *(LAS f32x2*)(lds + L_QP + q * 128 + 2 * lane);
    st16_sc1(rws, WS_Q + (h * 8 + s) * 1024 + lane * 16, rec(v[0], v[1], tagA, tagB));
    st16_sc1(rws, (bid == 0 && lane == 0) ? WS_T0 : OOB, u32x4{t0, tagA, t0, tagB});
  }
  if (a == 1) {
    pace_wait(t0, T_QPOLL);
    float q0 = 0.f, q1 = 0.f;
    unsigned tg = t0;
#pragma unroll 1
    for (unsigned sp = 0; sp < SPIN_MEM; ++sp) {
      REREAD();
      u32x4 g[8];
#pragma unroll
      for (int i = 0; i < 8; ++i) g[i] = ld16_sc1(rws, lane * 16, WS_Q + (h * 8 + i) * 1024);
      const u32x4 gt = ld16_sc1(rws, 0, WS_T0);
      bool ok = rok(gt, tagA, tagB);
      tg = gt[0];
      q0 = 0.f; q1 = 0.f;
#pragma unroll
      for (int i = 0; i < 8; ++i) { ok = ok && rok(g[i], tagA, tagB); q0 += r0(g[i]); q1 += r1(g[i]); }
      if (__all(ok)) break;
      __builtin_amdgcn_s_sleep(2);
    }
    *(LAS unsigned*)(lds + L_QS + lane) = pack2(q0, q1);
    if (lane == 0) *(LAS unsigned*)(lds + L_CNT + W_T0) = ((int)(t0 - tg) < 0 || (int)(t0 - tg) > 300) ? t0 : tg;
    cnt_arrive(lds, C_QR, lane);
  }
  const int ks = xl * 2 + (j >> 4), cg = j & 15;
  u32x4 dw[7];
  {
    const rsrc_t rd = mkrsrc(wd, D * F * 2);
    const int voff = (lane >> 3) * 2048 + (lane & 7) * 16, sb = (ks * 224 + a * 56) * 2048 + cg * 128;
    constexpr int WSTEP = ((T_K0 - T_W2) << 16) / 7;
    int wdue = T_W2 * 65536 + (WSTEP / 128) * (a * 32 + j);
#pragma unroll
    for (int i = 0; i < 7; ++i) { pace_wait(t0, wdue >> 16); wdue += WSTEP; dw[i] = ld16_nt(rd, voff, sb + i * 16384); SB(); }
  }
  if (a == 3 && Y_TOUCH) {
    pace_wait(t0, T_TOUCH);
    const u32x4 p = ld16_sc1(rws, lane * 16, WS_Y + (h * 8 + s) * 1024);
    asm volatile("" :: "v"(p));
  }
  if (a == 0 && j < 28) {
    cnt_wait(lds, C_F1, NMAIN);
    f32x2 v = *(LAS f32x2*)(lds + L_FP + 2 * (lane & 31));
#pragma unroll
    for (int q = 1; q < 8; ++q) v += *(LAS f32x2*)(lds + L_FP + q * 64 + 2 * (lane & 31));
    st16_sc1(rws, lane < 32 ? WS_G1 + ((mat * 2 + rb) * F + cb * 64) * 8 + lane * 16 : OOB, rec(v[0], v[1], tagA, tagB));
  }
  {
    pace_wait(t0, T_K0 + 2000);
    u32x4 g[4];
    const int voff = (lane < 28 ? lane : 27) * 16;
#pragma unroll 1
    for (unsigned sp = 0; sp < SPIN_MEM; ++sp) {
      REREAD();
#pragma unroll
      for (int k = 0; k < 4; ++k) g[k] = ld16_sc1(rws, voff, WS_G1 + (k * F + ks * 224 + a * 56) * 8);
      if (__all(rok(g[0], tagA, tagB) && rok(g[1], tagA, tagB) && rok(g[2], tagA, tagB) && rok(g[3], tagA, tagB))) break;
      __builtin_amdgcn_s_sleep(32);
    }
    float hh[2];
#pragma unroll
    for (int e = 0; e < 2; ++e) {
      const float gs = rbf(__uint_as_float(g[0][2 * e]) + __uint_as_float(g[1][2 * e]));
      const float us = rbf(__uint_as_float(g[2][2 * e]) + __uint_as_float(g[3][2 * e]));
      const float ge = 0.5f * gs * (1.f + tanhf(0.7978845608028654f * (gs + 0.044715f * gs * gs * gs)));
      hh[e] = rbf(rbf(ge) * us);
    }
    if (lane < 28) *(LAS f32x2*)(lds + L_H + a * 64 + 2 * lane) = f32x2{hh[0], hh[1]};
    float y[8] = {0.f, 0.f, 0.f, 0.f, 0.f, 0.f, 0.f, 0.f};
#pragma unroll
    for (int i = 0; i < 7; ++i) fma8(y, lds[L_H + a * 64 + 8 * i + (lane >> 3)], dw[i]);
#pragma unroll
    for (int e = 0; e < 8; ++e) y[e] += dpp<ROR8>(y[e]);
    const f32x2 z = fold_rows(y);
    if (c < 8) *(LAS f32x2*)(lds + L_F2 + a * 64 + 8 * c + 2 * r) = z;
    cnt_arrive(lds, C_F2, lane);
  }
  const int col0 = s * 128 + 4 * h;
  unsigned xw = 0;
  if (a == 0) xw = __builtin_amdgcn_raw_buffer_load_b32(rx, (col0 + 2 * (lane & 1)) * 2, 0, 0);
  if (a == 1) {
    cnt_wait(lds, C_ML, NMAIN);
    const f32x2 ml = *(LAS f32x2*)(lds + L_ML + 2 * (lane & 7));
    const float Mw = max8(ml[0]), Lw = sum8(exp2_(ml[0] - Mw) * ml[1]);
    st16_sc1(rws, lane == 0 ? WS_ML + (h * 8 + s) * 128 : OOB, rec(Mw, Lw, tagA, tagB));
    pace_wait(t0, T_K0 + T_MAIN / 2 + 1500);
    u32x4 g;
#pragma unroll 1
    for (unsigned sp = 0; sp < SPIN_MEM; ++sp) {
      REREAD();
      g = ld16_sc1(rws, (lane & 7) * 128, WS_ML + h * 8 * 128);
      if (__all(rok(g, tagA, tagB))) break;
      __builtin_amdgcn_s_sleep(32);
    }
    const float M = max8(r0(g)), L = sum8(exp2_(r0(g) - M) * r1(g));
    if (lane < 8) lds[L_WG + lane] = exp2_(ml[0] - M) / L;
    cnt_arrive(lds, C_WG, lane);
  }
  pace_wait(t0, T_K0 + T_MAIN - 300);
  if (a == 0) {
    const unsigned po = __builtin_amdgcn_raw_buffer_load_b32(mkrsrc(out, D * 2), (col0 + 2 * (lane & 1)) * 2, 0, SC1);
    asm volatile("" :: "v"(po));
  }
  pace_wait(t0, T_K0 + T_MAIN - 100);
  __builtin_amdgcn_s_setprio(3);
  if (a == 2) {
    cnt_wait(lds, C_F2, NAUX);
    f32x2 v = *(LAS f32x2*)(lds + L_F2 + 2 * (lane & 31));
#pragma unroll
    for (int q = 1; q < 4; ++q) v += *(LAS f32x2*)(lds + L_F2 + q * 64 + 2 * (lane & 31));
    pace_wait(t0, T_K0 + T_MAIN + 60);
    st16_sc1(rws, lane < 32 ? WS_FFN + (ks * D + cg * 64) * 8 + lane * 16 : OOB, rec(v[0], v[1], tagA, tagB));
  }
  if (a == 0) {
    cnt_wait<false>(lds, C_END, NMAIN);
    f32x2 v = *(LAS f32x2*)(lds + L_MRG + 2 * lane);
#pragma unroll
    for (int w = 1; w < 8; ++w) v += *(LAS f32x2*)(lds + L_MRG + w * 128 + 2 * lane);
    st16_sc1(rws, WS_Q + (h * 8 + s) * 1024 + lane * 16, rec(v[0], v[1], tagC, tagD));
  } else {
    cnt_wait(lds, C_END, NMAIN);
  }
  u32x4 bfr[2][4];
  cnt_wait<false>(lds, C_WT, NMAIN);
#pragma unroll
  for (int q = 0; q < 4; ++q)
#pragma unroll
    for (int d = 0; d < 4; ++d) {
      const unsigned w0 = *(LAS unsigned*)(lds + L_WT + (32 * q + 8 * r + 2 * d) * WT_STR + 16 * a + c);
      const unsigned w1 = *(LAS unsigned*)(lds + L_WT + (32 * q + 8 * r + 2 * d + 1) * WT_STR + 16 * a + c);
      bfr[0][q][d] = __builtin_amdgcn_perm(w1, w0, 0x05040100u);
      bfr[1][q][d] = __builtin_amdgcn_perm(w1, w0, 0x07060302u);
    }
  {
    u32x4 g0, g1;
    const int vo = r * 1024 + (16 * a + c) * 16;
#pragma unroll 1
    for (unsigned sp = 0; sp < SPIN_MEM; ++sp) {
      REREAD();
      g0 = ld16_sc1(rws, vo, WS_Q + h * 8 * 1024);
      g1 = ld16_sc1(rws, vo, WS_Q + (h * 8 + 4) * 1024);
      if (__all(rok(g0, tagC, tagD) && rok(g1, tagC, tagD))) break;
    }
    float o = fold32(r0(g0) + r0(g1), r1(g0) + r1(g1));
    o = fold16(o, o);
    if ((r & 1) == 0) *((LAS u16*)(lds + L_OB) + 32 * a + 2 * c + (r >> 1)) = __builtin_bit_cast(u16, (__bf16)o);
  }
  cnt_arrive(lds, C_OB, lane);
  cnt_wait<false>(lds, C_OB, NAUX);
  {
    f32x4 y0 = {0.f, 0.f, 0.f, 0.f}, y1 = y0;
#pragma unroll
    for (int q = 0; q < 4; ++q) {
      const u32x4 af = *(LAS u32x4*)(lds + L_OB + 16 * q + 4 * r);
      y0 = __builtin_amdgcn_mfma_f32_16x16x32_bf16(__builtin_bit_cast(bf16x8, af), __builtin_bit_cast(bf16x8, bfr[0][q]), y0, 0, 0, 0);
      y1 = __builtin_amdgcn_mfma_f32_16x16x32_bf16(__builtin_bit_cast(bf16x8, af), __builtin_bit_cast(bf16x8, bfr[1][q]), y1, 0, 0, 0);
    }
    st16_sc1(rws, lane < 16 ? WS_Y + (h * 8 + s) * 1024 + (16 * a + lane) * 16 : OOB, rec(y0[0], y1[0], tagA, tagB));
  }
  if (a == 0) {
    u32x4 g, gf;
    pace_wait((unsigned)__builtin_amdgcn_s_memrealtime(), 30);
#pragma unroll 1
    for (unsigned sp = 0; sp < SPIN_MEM; ++sp) {
      REREAD();
      g = ld16_sc1(rws, (lane >> 1) * (8 * 1024) + (lane & 1) * 16, WS_Y + s * 1024 + h * 32);
      gf = ld16_sc1(rws, ((lane >> 1) & 15) * (D * 8) + (lane & 1) * 16, WS_FFN + col0 * 8);
      if (__all(rok(g, tagA, tagB) && rok(gf, tagA, tagB))) break;
    }
    const float f0 = rbf(parity_sum(r0(gf)) * 0.5f), f1v = rbf(parity_sum(r1(gf)) * 0.5f);
    const float a0 = rbf(parity_sum(r0(g))), a1 = rbf(parity_sum(r1(g)));
    const unsigned o = pack2(rbf(lo2f(xw) + a0) + f0, rbf(hi2f(xw) + a1) + f1v);
    __builtin_amdgcn_raw_buffer_store_b32(o, mkrsrc(out, D * 2), lane < 2 ? (col0 + 2 * lane) * 2 : OOB, 0, SC1);
    __builtin_amdgcn_raw_buffer_store_b64(u32x2{(unsigned)e2, (unsigned)(e2 >> 32)}, rws, (bid == 0 && lane == 0) ? WS_EPOCH : OOB, 0, SC1);
  }
}

extern "C" void kernel_launch(void* const* d_in, const int* in_sizes, int n_in, void* d_out, int out_size, void* d_ws, size_t ws_size,
                              hipStream_t stream) {
  fused_decode_kernel<<<256, NTHR, 0, stream>>>((const u16*)d_in[0], (const u16*)d_in[1], (const u16*)d_in[2], (const u16*)d_in[3],
                                                 (const u16*)d_in[4], (const u16*)d_in[6], (const u16*)d_in[7], (const u16*)d_in[8],
                                                 (u16*)d_out, d_ws);
}
```

```cpp
#define T_MAIN 7150
#define T_K0 460
#include <hip/hip_runtime.h>
#include <stdint.h>

typedef unsigned u32x4 __attribute__((ext_vector_type(4)));
typedef unsigned u32x2 __attribute__((ext_vector_type(2)));
typedef float f32x4 __attribute__((ext_vector_type(4)));
typedef float f32x2 __attribute__((ext_vector_type(2)));
typedef __bf16 bf16x2 __attribute__((ext_vector_type(2)));
typedef __bf16 bf16x8 __attribute__((ext_vector_type(8)));
typedef unsigned short u16;
#define LAS __attribute__((address_space(3)))
typedef __amdgpu_buffer_rsrc_t rsrc_t;

#define T_F1 150
#define T_W2 360
#define PACE_G 2
#define PHASE (wave * 32 + j)
constexpr int T_QPOLL = 390;

constexpr int D = 1024, H = 32, SK = 32768, DH = 128, F = 3584;
constexpr int NMAIN = 8, NAUX = 4, NTHR = (NMAIN + NAUX) * 64;
constexpr int NT = 2, SC1 = 16;
constexpr int OOB = 0x40000000;
constexpr unsigned WS_EPOCH = 0, WS_T0 = 1024, WS_ML = 4096, WS_Q = WS_ML + 256 * 128, WS_Y = WS_Q + 256 * 1024, WS_G1 = WS_Y + 256 * 1024,
                   WS_FFN = WS_G1 + 4 * F * 8, WS_END = WS_FFN + 16 * D * 8;
constexpr int L_XN = 0, L_QP = 1024, L_QS = L_QP + 4 * 128, L_MRG = L_QS + 64, L_ML = L_MRG + 8 * 128, L_WG = L_ML + 16, L_FP = L_WG + 8,
              L_H = L_FP + 8 * 64, L_F2 = L_H + 4 * 64, L_OB = L_F2 + 4 * 64, L_CNT = L_OB + 64, L_SC = L_CNT + 16, L_WT = L_SC + 8 * 512,
              WT_STR = 68, L_END = L_WT + 128 * WT_STR;
constexpr int C_Q = 0, C_XN = 1, C_F2 = 2, C_WT = 3, C_ML = 4, C_WG = 5, C_END = 6, C_QR = 7, C_F1 = 8, C_OB = 9, C_NUM = 10, W_T0 = 12;
constexpr unsigned SPIN_MEM = 1u << 20, SPIN_LDS = 1u << 23;

__device__ __forceinline__ float lo2f(unsigned w) { return __builtin_bit_cast(float, w << 16); }
__device__ __forceinline__ float hi2f(unsigned w) { return __builtin_bit_cast(float, w & 0xffff0000u); }
__device__ __forceinline__ float rbf(float x) { return (float)(__bf16)x; }
__device__ __forceinline__ unsigned pack2(float a, float b) { bf16x2 v = {(__bf16)a, (__bf16)b}; return __builtin_bit_cast(unsigned, v); }
__device__ __forceinline__ float dot2(unsigned k, unsigned q, float acc) {
  return __builtin_amdgcn_fdot2_f32_bf16(__builtin_bit_cast(bf16x2, k), __builtin_bit_cast(bf16x2, q), acc, false);
}
template <int CTRL> __device__ __forceinline__ float dpp(float x) {
  return __builtin_bit_cast(float, __builtin_amdgcn_mov_dpp(__builtin_bit_cast(int, x), CTRL, 0xf, 0xf, true));
}
constexpr int XOR1 = 0xB1, XOR2 = 0x4E, XOR7 = 0x141, ROR8 = 0x128, ROR4 = 0x124, BCAST = 0x150;
__device__ __forceinline__ float fold32(float a, float b) {
  auto t = __builtin_amdgcn_permlane32_swap(__float_as_uint(a), __float_as_uint(b), false, false);
  return __uint_as_float(t[0]) + __uint_as_float(t[1]);
}
__device__ __forceinline__ float fold16(float a, float b) {
  auto t = __builtin_amdgcn_permlane16_swap(__float_as_uint(a), __float_as_uint(b), false, false);
  return __uint_as_float(t[0]) + __uint_as_float(t[1]);
}
__device__ __forceinline__ f32x2 fold_rows(const float (&v)[8]) {
  const float u0 = fold32(v[0], v[4]), u1 = fold32(v[1], v[5]), u2 = fold32(v[2], v[6]), u3 = fold32(v[3], v[7]);
  return f32x2{fold16(u0, u2), fold16(u1, u3)};
}
__device__ __forceinline__ float xrow_sum(float x) { x = fold16(x, x); return fold32(x, x); }
__device__ __forceinline__ float xrow_max(float x) {
  auto s = __builtin_amdgcn_permlane16_swap(__float_as_uint(x), __float_as_uint(x), false, false);
  x = fmaxf(__uint_as_float(s[0]), __uint_as_float(s[1]));
  auto t = __builtin_amdgcn_permlane32_swap(__float_as_uint(x), __float_as_uint(x), false, false);
  return fmaxf(__uint_as_float(t[0]), __uint_as_float(t[1]));
}
__device__ __forceinline__ float parity_sum(float x) {
  x += dpp<XOR2>(x); x += dpp<ROR4>(x); x += dpp<ROR8>(x); return xrow_sum(x);
}
__device__ __forceinline__ float wave_sum(float x) { x += dpp<XOR1>(x); return parity_sum(x); }
__device__ __forceinline__ float max8(float x) { x = fmaxf(x, dpp<XOR1>(x)); x = fmaxf(x, dpp<XOR2>(x)); return fmaxf(x, dpp<XOR7>(x)); }
__device__ __forceinline__ float sum8(float x) { x += dpp<XOR1>(x); x += dpp<XOR2>(x); return x + dpp<XOR7>(x); }
__device__ __forceinline__ float wave_max(float x) { x = max8(x); x = fmaxf(x, dpp<ROR8>(x)); return xrow_max(x); }
__device__ __forceinline__ float exp2_(float x) { return __builtin_amdgcn_exp2f(x); }
__device__ __forceinline__ rsrc_t mkrsrc(const void* p, unsigned bytes) { return __builtin_amdgcn_make_buffer_rsrc((void*)p, 0, (int)bytes, 0x00020000); }
__device__ __forceinline__ u32x4 ld16(rsrc_t r, int voff, int soff) { return __builtin_amdgcn_raw_buffer_load_b128(r, voff, soff, 0); }
__device__ __forceinline__ u32x4 ld16_nt(rsrc_t r, int voff, int soff) { return __builtin_amdgcn_raw_buffer_load_b128(r, voff, soff, NT); }
__device__ __forceinline__ u32x4 ld16_sc1(rsrc_t r, int voff, int soff) { return __builtin_amdgcn_raw_buffer_load_b128(r, voff, soff, SC1); }
__device__ __forceinline__ void st16_sc1(rsrc_t r, int off, u32x4 v) { __builtin_amdgcn_raw_buffer_store_b128(v, r, off, 0, SC1); }
#define SB() __builtin_amdgcn_sched_barrier(0)
#define REREAD() asm volatile("" ::: "memory")
__device__ __forceinline__ void pace_wait(unsigned t0, int due) {
  asm volatile("1:\n\ts_memrealtime s[96:97]\n\ts_waitcnt lgkmcnt(0)\n\ts_sub_u32 s96, s96, %0\n\ts_cmp_lt_i32 s96, %1\n\t"
               "s_cbranch_scc0 2f\n\ts_sleep 1\n\ts_branch 1b\n2:"
               :: "s"(t0), "s"(due) : "s96", "s97", "scc", "memory");
}
__device__ __forceinline__ void fma8(float (&acc)[8], float s, u32x4 w) {
#pragma unroll
  for (int t = 0; t < 4; ++t) { acc[2 * t] = fmaf(s, lo2f(w[t]), acc[2 * t]); acc[2 * t + 1] = fmaf(s, hi2f(w[t]), acc[2 * t + 1]); }
}
__device__ __forceinline__ u32x4 rec(float a, float b, unsigned ta, unsigned tb) { return u32x4{__float_as_uint(a), ta, __float_as_uint(b), tb}; }
__device__ __forceinline__ bool rok(u32x4 g, unsigned ta, unsigned tb) { return g[1] == ta && g[3] == tb; }
__device__ __forceinline__ float r0(u32x4 g) { return __uint_as_float(g[0]); }
__device__ __forceinline__ float r1(u32x4 g) { return __uint_as_float(g[2]); }

__device__ __forceinline__ void cnt_arrive(LAS float* lds, int which, int lane) {
  asm volatile("s_waitcnt lgkmcnt(0)" ::: "memory");
  if (lane == 0) __hip_atomic_fetch_add((LAS unsigned*)(lds + L_CNT + which), 1u, __ATOMIC_RELAXED, __HIP_MEMORY_SCOPE_WORKGROUP);
}
template <bool SLEEP = true>
__device__ __forceinline__ void cnt_wait(LAS float* lds, int which, unsigned n) {
#pragma unroll 1
  for (unsigned sp = 0; sp < SPIN_LDS; ++sp) {
    if (__builtin_amdgcn_readfirstlane(*(volatile LAS unsigned*)(lds + L_CNT + which)) >= n) break;
    if (SLEEP) __builtin_amdgcn_s_sleep(1);
  }
  REREAD();
}

__global__ void __launch_bounds__(NTHR, 3)
fused_decode_kernel(const u16* __restrict__ x, const u16* __restrict__ Kc, const u16* __restrict__ Vc, const u16* __restrict__ wq,
                    const u16* __restrict__ wo, const u16* __restrict__ wg, const u16* __restrict__ wu, const u16* __restrict__ wd,
                    u16* __restrict__ out, void* ws) {
  __shared__ __attribute__((aligned(16))) float lds_[L_END];
  LAS float* lds = (LAS float*)lds_;
  const unsigned t0 = (unsigned)__builtin_amdgcn_s_memrealtime();
  const int tid = threadIdx.x, lane = tid & 63, wave = __builtin_amdgcn_readfirstlane(tid >> 6);
  const int r = lane >> 4, c = lane & 15;
  const int bid = blockIdx.x, xl = bid & 7, j = bid >> 3;
  const int h = xl * 4 + (j >> 3), s = j & 7;
  const int f1 = xl * 28 + j, mat = f1 / 112, rb = (f1 / 56) & 1, cb = f1 % 56;

  if (wave < NMAIN) {
    const rsrc_t rk = mkrsrc(Kc, (unsigned)H * SK * DH * 2), rv = mkrsrc(Vc, (unsigned)H * SK * DH * 2);
    u32x4 bA[16], bB[16];
    const unsigned kvbase = (unsigned)h * (8u << 20) + (unsigned)s * (1u << 20) + wave * 1024;
    const unsigned rot = 32u * ((h + (h >> 2) + (h >> 4)) & 3);
    const int kvoff = lane * 16;
    constexpr int STEP = (T_MAIN << 16) / 256 * PACE_G;
    int due = T_K0 * 65536 - STEP + (STEP / 256) * (PHASE);
#define PACE(T0, I) do { if ((I) % PACE_G == 0) { due += STEP; pace_wait(T0, due >> 16); } } while (0)
    __builtin_amdgcn_s_barrier();
    SB();
    {
      const rsrc_t rg = mkrsrc(mat ? wu : wg, D * F * 2);
      const int voff = j < 28 ? (lane >> 3) * (F * 2) + (lane & 7) * 16 : OOB, sb = (rb * 512 + wave * 64) * (F * 2) + cb * 128;
      constexpr int FSTEP = ((T_W2 - T_F1) << 16) / 8;
      int fdue = T_F1 * 65536 + (FSTEP / 256) * (wave * 32 + j);
#pragma unroll
      for (int i = 0; i < 8; ++i) { pace_wait(t0, fdue >> 16); fdue += FSTEP; bB[i] = ld16_nt(rg, voff, sb + i * 8 * (F * 2)); SB(); }
    }
#pragma unroll
    for (int i = 0; i < 16; ++i) { PACE(t0, i); bA[i] = ld16_nt(rk, kvoff, kvbase + (rot + i) * 8192); SB(); }
    {
      cnt_wait(lds, C_XN, 1);
      float ag[8] = {0.f, 0.f, 0.f, 0.f, 0.f, 0.f, 0.f, 0.f};
#pragma unroll
      for (int i = 0; i < 8; ++i) {
        const int k = rb * 512 + wave * 64 + 8 * i + (lane >> 3);
        fma8(ag, lds[L_XN + (k & 3) * 256 + (k >> 2)], bB[i]);
      }
#pragma unroll
      for (int e = 0; e < 8; ++e) ag[e] += dpp<ROR8>(ag[e]);
      const f32x2 z = fold_rows(ag);
      if (c < 8) *(LAS f32x2*)(lds + L_FP + wave * 64 + 8 * c + 2 * r) = z;
      cnt_arrive(lds, C_F1, lane);
      SB();
    }
#pragma unroll
    for (int i = 0; i < 16; ++i) { PACE(t0, i); bB[i] = ld16_nt(rk, kvoff, kvbase + (rot + 16 + i) * 8192); SB(); }
    cnt_wait(lds, C_QR, 1);
    const u32x4 qp = *(LAS u32x4*)(lds + L_QS + 4 * c);
    const unsigned t0m = __builtin_amdgcn_readfirstlane(*(LAS unsigned*)(lds + L_CNT + W_T0));
    constexpr float SCALE = 0.08838834764831845f, LOG2E = 1.4426950408889634f;
    LAS float* scp = lds + L_SC + wave * 512 + lane * 8;
    float mx = -INFINITY;
    auto kgroup = [&](u32x4 (&kb)[16], const bool more, const rsrc_t rn, const unsigned tb, const int g) __attribute__((always_inline)) {
      float sx[16];
#pragma unroll
      for (int i = 0; i < 16; ++i) {
        float d = 0.f;
#pragma unroll
        for (int t = 0; t < 4; ++t) d = dot2(kb[i][t], qp[t], d);
        asm volatile("" : "+v"(d));
        sx[i] = d;
        SB();
        if (more) { PACE(t0m, i); kb[i] = ld16_nt(rn, kvoff, tb + i * 8192); SB(); }
      }
#define TR_STEP(HALF, CTRL, BIT) _Pragma("unroll") for (int hh = 0; hh < (HALF); ++hh) { \
        const float lo_ = sx[hh], hi_ = sx[hh + (HALF)]; const float t1_ = lo_ + dpp<CTRL>(lo_), t2_ = hi_ + dpp<CTRL>(hi_); \
        sx[hh] = (c & (BIT)) ? t2_ : t1_; }
      TR_STEP(8, ROR8, 8) TR_STEP(4, XOR7, 4) TR_STEP(2, XOR2, 2) TR_STEP(1, XOR1, 1)
#undef TR_STEP
      const float sc = rbf(rbf(sx[0]) * SCALE) * LOG2E;
      mx = fmaxf(mx, sc);
      scp[g] = sc;
      SB();
    };
    kgroup(bA, false, rk, 0, 0);
#pragma unroll 1
    for (int g = 1; g < 8; ++g)
      kgroup(bB, true, mkrsrc(g == 7 ? Vc : Kc, (unsigned)H * SK * DH * 2), kvbase + (((g + 1) * 16 + rot) & 127u) * 8192, g);
    {
      const float m = wave_max(mx);
      f32x4 p0 = *(LAS f32x4*)scp, p1 = *(LAS f32x4*)(scp + 4);
      float l = 0.f;
#pragma unroll
      for (int e = 0; e < 4; ++e) { p0[e] = exp2_(p0[e] - m); p1[e] = exp2_(p1[e] - m); l += p0[e] + p1[e]; }
      *(LAS f32x4*)scp = p0; *(LAS f32x4*)(scp + 4) = p1;
      l = wave_sum(l);
      if (lane == 0) *(LAS f32x2*)(lds + L_ML + 2 * wave) = f32x2{m, l};
      cnt_arrive(lds, C_ML, lane);
    }
    float acc[8] = {0.f, 0.f, 0.f, 0.f, 0.f, 0.f, 0.f, 0.f};
    auto vgroup = [&](const bool more, const unsigned tb, const float p) __attribute__((always_inline)) {
#define PV_STEP(I) { fma8(acc, dpp<BCAST + (I)>(p), bB[I]); \
        asm volatile("" : "+v"(acc[0]), "+v"(acc[1]), "+v"(acc[2]), "+v"(acc[3]), "+v"(acc[4]), "+v"(acc[5]), "+v"(acc[6]), "+v"(acc[7])); SB(); \
        if (more) { PACE(t0m, I); bB[I] = ld16_nt(rv, kvoff, tb + (I) * 8192); SB(); } \
        else if ((I) == 11) {   \
          pace_wait(t0m, T_K0 + T_MAIN); \
          _Pragma("unroll") for (int u = 0; u < 4; ++u) { \
            bB[u] = ld16_nt(mkrsrc(wo, H * DH * D * 2), r * 2048 + c * 16, (h * 128 + wave * 16 + 4 * u) * 2048 + s * 256); SB(); } } }
      PV_STEP(0) PV_STEP(1) PV_STEP(2) PV_STEP(3) PV_STEP(4) PV_STEP(5) PV_STEP(6) PV_STEP(7)
      PV_STEP(8) PV_STEP(9) PV_STEP(10) PV_STEP(11) PV_STEP(12) PV_STEP(13) PV_STEP(14) PV_STEP(15)
#undef PV_STEP
    };
#pragma unroll 1
    for (int g = 0; g < 7; ++g) vgroup(true, kvbase + (((g + 1) * 16 + rot) & 127u) * 8192, scp[g]);
    __builtin_amdgcn_s_setprio(3);
    cnt_wait(lds, C_WG, 1);
    {
      const float wgt = lds[L_WG + wave];
#pragma unroll
      for (int e = 0; e < 8; ++e) acc[e] *= wgt;
      vgroup(false, 0, scp[7] * wgt);
    }
    *(LAS f32x2*)(lds + L_MRG + wave * 128 + 8 * c + 2 * r) = fold_rows(acc);
    cnt_arrive(lds, C_END, lane);
#pragma unroll
    for (int u = 0; u < 4; ++u) *(LAS u32x4*)(lds + L_WT + (wave * 16 + 4 * u + r) * WT_STR + 4 * c) = bB[u];
    cnt_arrive(lds, C_WT, lane);
    return;
#undef PACE
  }

  const int a = wave - NMAIN;
  const rsrc_t rx = mkrsrc(x, D * 2), rws = mkrsrc(ws, WS_END);
  unsigned tagA, tagB, tagC, tagD;
  unsigned long long e2;
  {
    u32x4 xa = ld16(rx, lane * 32, 0), xb = ld16(rx, lane * 32 + 16, 0);
    const u32x2 ep = __builtin_amdgcn_raw_buffer_load_b64(rws, 0, WS_EPOCH, SC1);
    const rsrc_t rwq = mkrsrc(wq, D * H * DH * 2);
    u32x4 w[8];
    const int voff = r * 8192 + c * 16, sb = (s * 128 + a * 32) * 8192 + h * 256;
#pragma unroll
    for (int i = 0; i < 8; ++i) { w[i] = ld16_nt(rwq, voff, sb + i * 32768); SB(); }
    if (a == 0 && lane < C_NUM) *(LAS unsigned*)(lds + L_CNT + lane) = 0u;
    __syncthreads();
    asm volatile("" : "+v"(xa), "+v"(xb));
    float v[16];
#pragma unroll
    for (int i = 0; i < 4; ++i) { v[2 * i] = lo2f(xa[i]); v[2 * i + 1] = hi2f(xa[i]); v[8 + 2 * i] = lo2f(xb[i]); v[9 + 2 * i] = hi2f(xb[i]); }
    float sm = 0.f;
#pragma unroll
    for (int i = 0; i < 16; ++i) sm += v[i];
    const float mean = wave_sum(sm) * (1.f / D);
    float ss = 0.f;
#pragma unroll
    for (int i = 0; i < 16; ++i) { const float d = v[i] - mean; ss = fmaf(d, d, ss); }
    const float var = wave_sum(ss) * (1.f / D);
    const float mb = rbf(mean), den = rbf(sqrtf(rbf(rbf(var) + 1e-5f)));
    const float rinv = 1.0f / den;
#pragma unroll
    for (int i = 0; i < 16; ++i) {
      const float nu = rbf(v[i] - mb), q0 = nu * rinv;
      v[i] = rbf(fmaf(fmaf(-q0, den, nu), rinv, q0));
    }
#pragma unroll
    for (int q = 0; q < 4; ++q) *(LAS f32x4*)(lds + L_XN + q * 256 + 4 * lane) = f32x4{v[q], v[4 + q], v[8 + q], v[12 + q]};
    cnt_arrive(lds, C_XN, lane);
    const unsigned long long e1 = (((unsigned long long)__builtin_amdgcn_readfirstlane(ep[1]) << 32) | (unsigned)__builtin_amdgcn_readfirstlane(ep[0])) + 1ull;
    e2 = e1 + 1ull;
    tagA = (unsigned)e1; tagB = (unsigned)(e1 >> 32) + (unsigned)e1 * 0x9E3779B1u;
    tagC = (unsigned)e2; tagD = (unsigned)(e2 >> 32) + (unsigned)e2 * 0x9E3779B1u;
    float acc[8] = {0.f, 0.f, 0.f, 0.f, 0.f, 0.f, 0.f, 0.f};
#pragma unroll
    for (int q = 0; q < 2; ++q) {
      const f32x4 xv = *(LAS f32x4*)(lds + L_XN + r * 256 + s * 32 + a * 8 + 4 * q);
#pragma unroll
      for (int e = 0; e < 4; ++e) fma8(acc, xv[e], w[4 * q + e]);
    }
    *(LAS f32x2*)(lds + L_QP + a * 128 + 8 * c + 2 * r) = fold_rows(acc);
  }
  cnt_arrive(lds, C_Q, lane);
  if (a == 0) {
    cnt_wait<false>(lds, C_Q, NAUX);
    f32x2 v = *(LAS f32x2*)(lds + L_QP + 2 * lane);
#pragma unroll
    for (int q = 1; q < 4; ++q) v += *(LAS f32x2*)(lds + L_QP + q * 128 + 2 * lane);
    st16_sc1(rws, WS_Q + (h * 8 + s) * 1024 + lane * 16, rec(v[0], v[1], tagA, tagB));
    st16_sc1(rws, (bid == 0 && lane == 0) ? WS_T0 : OOB, u32x4{t0, tagA, t0, tagB});
  }
  if (a == 1) {
    pace_wait(t0, T_QPOLL);
    float q0 = 0.f, q1 = 0.f;
    unsigned tg = t0;
#pragma unroll 1
    for (unsigned sp = 0; sp < SPIN_MEM; ++sp) {
      REREAD();
      u32x4 g[8];
#pragma unroll
      for (int i = 0; i < 8; ++i) g[i] = ld16_sc1(rws, lane * 16, WS_Q + (h * 8 + i) * 1024);
      const u32x4 gt = ld16_sc1(rws, 0, WS_T0);
      bool ok = rok(gt, tagA, tagB);
      tg = gt[0];
      q0 = 0.f; q1 = 0.f;
#pragma unroll
      for (int i = 0; i < 8; ++i) { ok = ok && rok(g[i], tagA, tagB); q0 += r0(g[i]); q1 += r1(g[i]); }
      if (__all(ok)) break;
      __builtin_amdgcn_s_sleep(2);
    }
    *(LAS unsigned*)(lds + L_QS + lane) = pack2(q0, q1);
    if (lane == 0) *(LAS unsigned*)(lds + L_CNT + W_T0) = ((int)(t0 - tg) < 0 || (int)(t0 - tg) > 300) ? t0 : tg;
    cnt_arrive(lds, C_QR, lane);
  }
  const int ks = xl * 2 + (j >> 4), cg = j & 15;
  u32x4 dw[7];
  {
    const rsrc_t rd = mkrsrc(wd, D * F * 2);
    const int voff = (lane >> 3) * 2048 + (lane & 7) * 16, sb = (ks * 224 + a * 56) * 2048 + cg * 128;
    constexpr int WSTEP = ((T_K0 - T_W2) << 16) / 7;
    int wdue = T_W2 * 65536 + (WSTEP / 128) * (a * 32 + j);
#pragma unroll
    for (int i = 0; i < 7; ++i) { pace_wait(t0, wdue >> 16); wdue += WSTEP; dw[i] = ld16_nt(rd, voff, sb + i * 16384); SB(); }
  }
  if (a == 0 && j < 28) {
    cnt_wait(lds, C_F1, NMAIN);
    f32x2 v = *(LAS f32x2*)(lds + L_FP + 2 * (lane & 31));
#pragma unroll
    for (int q = 1; q < 8; ++q) v += *(LAS f32x2*)(lds + L_FP + q * 64 + 2 * (lane & 31));
    st16_sc1(rws, lane < 32 ? WS_G1 + ((mat * 2 + rb) * F + cb * 64) * 8 + lane * 16 : OOB, rec(v[0], v[1], tagA, tagB));
  }
  {
    pace_wait(t0, T_K0 + 2000);
    u32x4 g[4];
    const int voff = (lane < 28 ? lane : 27) * 16;
#pragma unroll 1
    for (unsigned sp = 0; sp < SPIN_MEM; ++sp) {
      REREAD();
#pragma unroll
      for (int k = 0; k < 4; ++k) g[k] = ld16_sc1(rws, voff, WS_G1 + (k * F + ks * 224 + a * 56) * 8);
      if (__all(rok(g[0], tagA, tagB) && rok(g[1], tagA, tagB) && rok(g[2], tagA, tagB) && rok(g[3], tagA, tagB))) break;
      __builtin_amdgcn_s_sleep(32);
    }
    float hh[2];
#pragma unroll
    for (int e = 0; e < 2; ++e) {
      const float gs = rbf(__uint_as_float(g[0][2 * e]) + __uint_as_float(g[1][2 * e]));
      const float us = rbf(__uint_as_float(g[2][2 * e]) + __uint_as_float(g[3][2 * e]));
      const float ge = 0.5f * gs * (1.f + tanhf(0.7978845608028654f * (gs + 0.044715f * gs * gs * gs)));
      hh[e] = rbf(rbf(ge) * us);
    }
    if (lane < 28) *(LAS f32x2*)(lds + L_H + a * 64 + 2 * lane) = f32x2{hh[0], hh[1]};
    float y[8] = {0.f, 0.f, 0.f, 0.f, 0.f, 0.f, 0.f, 0.f};
#pragma unroll
    for (int i = 0; i < 7; ++i) fma8(y, lds[L_H + a * 64 + 8 * i + (lane >> 3)], dw[i]);
#pragma unroll
    for (int e = 0; e < 8; ++e) y[e] += dpp<ROR8>(y[e]);
    const f32x2 z = fold_rows(y);
    if (c < 8) *(LAS f32x2*)(lds + L_F2 + a * 64 + 8 * c + 2 * r) = z;
    cnt_arrive(lds, C_F2, lane);
  }
  const int col0 = s * 128 + 4 * h;
  unsigned xw = 0;
  if (a == 0) xw = __builtin_amdgcn_raw_buffer_load_b32(rx, (col0 + 2 * (lane & 1)) * 2, 0, 0);
  if (a == 1) {
    cnt_wait(lds, C_ML, NMAIN);
    const f32x2 ml = *(LAS f32x2*)(lds + L_ML + 2 * (lane & 7));
    const float Mw = max8(ml[0]), Lw = sum8(exp2_(ml[0] - Mw) * ml[1]);
    st16_sc1(rws, lane == 0 ? WS_ML + (h * 8 + s) * 128 : OOB, rec(Mw, Lw, tagA, tagB));
    pace_wait(t0, T_K0 + T_MAIN / 2 + 1500);
    u32x4 g;
#pragma unroll 1
    for (unsigned sp = 0; sp < SPIN_MEM; ++sp) {
      REREAD();
      g = ld16_sc1(rws, (lane & 7) * 128, WS_ML + h * 8 * 128);
      if (__all(rok(g, tagA, tagB))) break;
      __builtin_amdgcn_s_sleep(32);
    }
    const float M = max8(r0(g)), L = sum8(exp2_(r0(g) - M) * r1(g));
    if (lane < 8) lds[L_WG + lane] = exp2_(ml[0] - M) / L;
    cnt_arrive(lds, C_WG, lane);
  }
  pace_wait(t0, T_K0 + T_MAIN - 300);
  if (a == 0) {
    const unsigned po = __builtin_amdgcn_raw_buffer_load_b32(mkrsrc(out, D * 2), (col0 + 2 * (lane & 1)) * 2, 0, SC1);
    asm volatile("" :: "v"(po));
  }
  pace_wait(t0, T_K0 + T_MAIN - 100);
  __builtin_amdgcn_s_setprio(3);
  if (a == 2) {
    cnt_wait(lds, C_F2, NAUX);
    f32x2 v = *(LAS f32x2*)(lds + L_F2 + 2 * (lane & 31));
#pragma unroll
    for (int q = 1; q < 4; ++q) v += *(LAS f32x2*)(lds + L_F2 + q * 64 + 2 * (lane & 31));
    pace_wait(t0, T_K0 + T_MAIN + 60);
    st16_sc1(rws, lane < 32 ? WS_FFN + (ks * D + cg * 64) * 8 + lane * 16 : OOB, rec(v[0], v[1], tagA, tagB));
  }
  if (a == 0) {
    cnt_wait<false>(lds, C_END, NMAIN);
    f32x2 v = *(LAS f32x2*)(lds + L_MRG + 2 * lane);
#pragma unroll
    for (int w = 1; w < 8; ++w) v += *(LAS f32x2*)(lds + L_MRG + w * 128 + 2 * lane);
    st16_sc1(rws, WS_Q + (h * 8 + s) * 1024 + lane * 16, rec(v[0], v[1], tagC, tagD));
  } else {
    cnt_wait(lds, C_END, NMAIN);
  }
  u32x4 bfr[2][4];
  cnt_wait<false>(lds, C_WT, NMAIN);
#pragma unroll
  for (int q = 0; q < 4; ++q)
#pragma unroll
    for (int d = 0; d < 4; ++d) {
      const unsigned w0 = *(LAS unsigned*)(lds + L_WT + (32 * q + 8 * r + 2 * d) * WT_STR + 16 * a + c);
      const unsigned w1 = *(LAS unsigned*)(lds + L_WT + (32 * q + 8 * r + 2 * d + 1) * WT_STR + 16 * a + c);
      bfr[0][q][d] = __builtin_amdgcn_perm(w1, w0, 0x05040100u);
      bfr[1][q][d] = __builtin_amdgcn_perm(w1, w0, 0x07060302u);
    }
  {
    u32x4 g0, g1;
    const int vo = r * 1024 + (16 * a + c) * 16;
#pragma unroll 1
    for (unsigned sp = 0; sp < SPIN_MEM; ++sp) {
      REREAD();
      g0 = ld16_sc1(rws, vo, WS_Q + h * 8 * 1024);
      g1 = ld16_sc1(rws, vo, WS_Q + (h * 8 + 4) * 1024);
      if (__all(rok(g0, tagC, tagD) && rok(g1, tagC, tagD))) break;
    }
    float o = fold32(r0(g0) + r0(g1), r1(g0) + r1(g1));
    o = fold16(o, o);
    if ((r & 1) == 0) *((LAS u16*)(lds + L_OB) + 32 * a + 2 * c + (r >> 1)) = __builtin_bit_cast(u16, (__bf16)o);
  }
  cnt_arrive(lds, C_OB, lane);
  cnt_wait<false>(lds, C_OB, NAUX);
  {
    f32x4 y0 = {0.f, 0.f, 0.f, 0.f}, y1 = y0;
#pragma unroll
    for (int q = 0; q < 4; ++q) {
      const u32x4 af = *(LAS u32x4*)(lds + L_OB + 16 * q + 4 * r);
      y0 = __builtin_amdgcn_mfma_f32_16x16x32_bf16(__builtin_bit_cast(bf16x8, af), __builtin_bit_cast(bf16x8, bfr[0][q]), y0, 0, 0, 0);
      y1 = __builtin_amdgcn_mfma_f32_16x16x32_bf16(__builtin_bit_cast(bf16x8, af), __builtin_bit_cast(bf16x8, bfr[1][q]), y1, 0, 0, 0);
    }
    st16_sc1(rws, lane < 16 ? WS_Y + (h * 8 + s) * 1024 + (16 * a + lane) * 16 : OOB, rec(y0[0], y1[0], tagA, tagB));
  }
  if (a == 0) {
    u32x4 g, gf;
    pace_wait((unsigned)__builtin_amdgcn_s_memrealtime(), 30);
#pragma unroll 1
    for (unsigned sp = 0; sp < SPIN_MEM; ++sp) {
      REREAD();
      g = ld16_sc1(rws, (lane >> 1) * (8 * 1024) + (lane & 1) * 16, WS_Y + s * 1024 + h * 32);
      gf = ld16_sc1(rws, ((lane >> 1) & 15) * (D * 8) + (lane & 1) * 16, WS_FFN + col0 * 8);
      if (__all(rok(g, tagA, tagB) && rok(gf, tagA, tagB))) break;
    }
    const float f0 = rbf(parity_sum(r0(gf)) * 0.5f), f1v = rbf(parity_sum(r1(gf)) * 0.5f);
    const float a0 = rbf(parity_sum(r0(g))), a1 = rbf(parity_sum(r1(g)));
    const unsigned o = pack2(rbf(lo2f(xw) + a0) + f0, rbf(hi2f(xw) + a1) + f1v);
    __builtin_amdgcn_raw_buffer_store_b32(o, mkrsrc(out, D * 2), lane < 2 ? (col0 + 2 * lane) * 2 : OOB, 0, SC1);
    __builtin_amdgcn_raw_buffer_store_b64(u32x2{(unsigned)e2, (unsigned)(e2 >> 32)}, rws, (bid == 0 && lane == 0) ? WS_EPOCH : OOB, 0, SC1);
  }
}

extern "C" void kernel_launch(void* const* d_in, const int* in_sizes, int n_in, void* d_out, int out_size, void* d_ws, size_t ws_size,
                              hipStream_t stream) {
  fused_decode_kernel<<<256, NTHR, 0, stream>>>((const u16*)d_in[0], (const u16*)d_in[1], (const u16*)d_in[2], (const u16*)d_in[3],
                                                 (const u16*)d_in[4], (const u16*)d_in[6], (const u16*)d_in[7], (const u16*)d_in[8],
                                                 (u16*)d_out, d_ws);
}
```

```cpp
#include <hip/hip_runtime.h>
#include <stdint.h>

typedef unsigned u32x4 __attribute__((ext_vector_type(4)));
typedef unsigned u32x2 __attribute__((ext_vector_type(2)));
typedef float f32x4 __attribute__((ext_vector_type(4)));
typedef float f32x2 __attribute__((ext_vector_type(2)));
typedef __bf16 bf16x2 __attribute__((ext_vector_type(2)));
typedef __bf16 bf16x8 __attribute__((ext_vector_type(8)));
typedef unsigned short u16;
#define LAS __attribute__((address_space(3)))
typedef __amdgpu_buffer_rsrc_t rsrc_t;

constexpr int T_F1 = 150;
constexpr int T_W2 = 360;
constexpr int T_K0 = 460;
constexpr int T_MAIN = 7070;
constexpr int PACE_G = 2;
constexpr int T_QPOLL = 390;

constexpr int D = 1024, H = 32, SK = 32768, DH = 128, F = 3584;
constexpr int NMAIN = 8, NAUX = 4, NTHR = (NMAIN + NAUX) * 64;
constexpr int NT = 2, SC1 = 16;
constexpr int OOB = 0x40000000;
constexpr unsigned WS_EPOCH = 0, WS_T0 = 1024, WS_ML = 4096, WS_Q = WS_ML + 256 * 128, WS_Y = WS_Q + 256 * 1024, WS_G1 = WS_Y + 256 * 1024,
                   WS_FFN = WS_G1 + 4 * F * 8, WS_END = WS_FFN + 16 * D * 8;
constexpr int L_XN = 0, L_QP = 1024, L_QS = L_QP + 4 * 128, L_MRG = L_QS + 64, L_ML = L_MRG + 8 * 128, L_WG = L_ML + 16, L_FP = L_WG + 8,
              L_H = L_FP + 8 * 64, L_F2 = L_H + 4 * 64, L_OB = L_F2 + 4 * 64, L_CNT = L_OB + 64, L_SC = L_CNT + 16, L_WT = L_SC + 8 * 512,
              WT_STR = 68, L_END = L_WT + 128 * WT_STR;
constexpr int C_Q = 0, C_XN = 1, C_F2 = 2, C_WT = 3, C_ML = 4, C_WG = 5, C_END = 6, C_QR = 7, C_F1 = 8, C_OB = 9, C_NUM = 10, W_T0 = 12;
constexpr unsigned SPIN_MEM = 1u << 20, SPIN_LDS = 1u << 23;

__device__ __forceinline__ float lo2f(unsigned w) { return __builtin_bit_cast(float, w << 16); }
__device__ __forceinline__ float hi2f(unsigned w) { return __builtin_bit_cast(float, w & 0xffff0000u); }
__device__ __forceinline__ float rbf(float x) { return (float)(__bf16)x; }
__device__ __forceinline__ unsigned pack2(float a, float b) { bf16x2 v = {(__bf16)a, (__bf16)b}; return __builtin_bit_cast(unsigned, v); }
__device__ __forceinline__ float dot2(unsigned k, unsigned q, float acc) {
  return __builtin_amdgcn_fdot2_f32_bf16(__builtin_bit_cast(bf16x2, k), __builtin_bit_cast(bf16x2, q), acc, false);
}
template <int CTRL> __device__ __forceinline__ float dpp(float x) {
  return __builtin_bit_cast(float, __builtin_amdgcn_mov_dpp(__builtin_bit_cast(int, x), CTRL, 0xf, 0xf, true));
}
constexpr int XOR1 = 0xB1, XOR2 = 0x4E, XOR7 = 0x141, ROR8 = 0x128, ROR4 = 0x124, BCAST = 0x150;
__device__ __forceinline__ float fold32(float a, float b) {
  auto t = __builtin_amdgcn_permlane32_swap(__float_as_uint(a), __float_as_uint(b), false, false);
  return __uint_as_float(t[0]) + __uint_as_float(t[1]);
}
__device__ __forceinline__ float fold16(float a, float b) {
  auto t = __builtin_amdgcn_permlane16_swap(__float_as_uint(a), __float_as_uint(b), false, false);
  return __uint_as_float(t[0]) + __uint_as_float(t[1]);
}
__device__ __forceinline__ f32x2 fold_rows(const float (&v)[8]) {
  const float u0 = fold32(v[0], v[4]), u1 = fold32(v[1], v[5]), u2 = fold32(v[2], v[6]), u3 = fold32(v[3], v[7]);
  return f32x2{fold16(u0, u2), fold16(u1, u3)};
}
__device__ __forceinline__ float xrow_sum(float x) { x = fold16(x, x); return fold32(x, x); }
__device__ __forceinline__ float xrow_max(float x) {
  auto s = __builtin_amdgcn_permlane16_swap(__float_as_uint(x), __float_as_uint(x), false, false);
  x = fmaxf(__uint_as_float(s[0]), __uint_as_float(s[1]));
  auto t = __builtin_amdgcn_permlane32_swap(__float_as_uint(x), __float_as_uint(x), false, false);
  return fmaxf(__uint_as_float(t[0]), __uint_as_float(t[1]));
}
__device__ __forceinline__ float parity_sum(float x) {
  x += dpp<XOR2>(x); x += dpp<ROR4>(x); x += dpp<ROR8>(x); return xrow_sum(x);
}
__device__ __forceinline__ float wave_sum(float x) { x += dpp<XOR1>(x); return parity_sum(x); }
__device__ __forceinline__ float max8(float x) { x = fmaxf(x, dpp<XOR1>(x)); x = fmaxf(x, dpp<XOR2>(x)); return fmaxf(x, dpp<XOR7>(x)); }
__device__ __forceinline__ float sum8(float x) { x += dpp<XOR1>(x); x += dpp<XOR2>(x); return x + dpp<XOR7>(x); }
__device__ __forceinline__ float wave_max(float x) { x = max8(x); x = fmaxf(x, dpp<ROR8>(x)); return xrow_max(x); }
__device__ __forceinline__ float exp2_(float x) { return __builtin_amdgcn_exp2f(x); }
__device__ __forceinline__ rsrc_t mkrsrc(const void* p, unsigned bytes) { return __builtin_amdgcn_make_buffer_rsrc((void*)p, 0, (int)bytes, 0x00020000); }
__device__ __forceinline__ u32x4 ld16(rsrc_t r, int voff, int soff) { return __builtin_amdgcn_raw_buffer_load_b128(r, voff, soff, 0); }
__device__ __forceinline__ u32x4 ld16_nt(rsrc_t r, int voff, int soff) { return __builtin_amdgcn_raw_buffer_load_b128(r, voff, soff, NT); }
__device__ __forceinline__ u32x4 ld16_sc1(rsrc_t r, int voff, int soff) { return __builtin_amdgcn_raw_buffer_load_b128(r, voff, soff, SC1); }
__device__ __forceinline__ void st16_sc1(rsrc_t r, int off, u32x4 v) { __builtin_amdgcn_raw_buffer_store_b128(v, r, off, 0, SC1); }
#define SB() __builtin_amdgcn_sched_barrier(0)
#define REREAD() asm volatile("" ::: "memory")
__device__ __forceinline__ void pace_wait(unsigned t0, int due) {
  asm volatile("1:\n\ts_memrealtime s[96:97]\n\ts_waitcnt lgkmcnt(0)\n\ts_sub_u32 s96, s96, %0\n\ts_cmp_lt_i32 s96, %1\n\t"
               "s_cbranch_scc0 2f\n\ts_sleep 1\n\ts_branch 1b\n2:"
               :: "s"(t0), "s"(due) : "s96", "s97", "scc", "memory");
}
__device__ __forceinline__ void fma8(float (&acc)[8], float s, u32x4 w) {
#pragma unroll
  for (int t = 0; t < 4; ++t) { acc[2 * t] = fmaf(s, lo2f(w[t]), acc[2 * t]); acc[2 * t + 1] = fmaf(s, hi2f(w[t]), acc[2 * t + 1]); }
}
__device__ __forceinline__ u32x4 rec(float a, float b, unsigned ta, unsigned tb) { return u32x4{__float_as_uint(a), ta, __float_as_uint(b), tb}; }
__device__ __forceinline__ bool rok(u32x4 g, unsigned ta, unsigned tb) { return g[1] == ta && g[3] == tb; }
__device__ __forceinline__ float r0(u32x4 g) { return __uint_as_float(g[0]); }
__device__ __forceinline__ float r1(u32x4 g) { return __uint_as_float(g[2]); }

__device__ __forceinline__ void cnt_arrive(LAS float* lds, int which, int lane) {
  asm volatile("s_waitcnt lgkmcnt(0)" ::: "memory");
  if (lane == 0) __hip_atomic_fetch_add((LAS unsigned*)(lds + L_CNT + which), 1u, __ATOMIC_RELAXED, __HIP_MEMORY_SCOPE_WORKGROUP);
}
template <bool SLEEP = true>
__device__ __forceinline__ void cnt_wait(LAS float* lds, int which, unsigned n) {
#pragma unroll 1
  for (unsigned sp = 0; sp < SPIN_LDS; ++sp) {
    if (__builtin_amdgcn_readfirstlane(*(volatile LAS unsigned*)(lds + L_CNT + which)) >= n) break;
    if (SLEEP) __builtin_amdgcn_s_sleep(1);
  }
  REREAD();
}

__global__ void __launch_bounds__(NTHR, 3)
fused_decode_kernel(const u16* __restrict__ x, const u16* __restrict__ Kc, const u16* __restrict__ Vc, const u16* __restrict__ wq,
                    const u16* __restrict__ wo, const u16* __restrict__ wg, const u16* __restrict__ wu, const u16* __restrict__ wd,
                    u16* __restrict__ out, void* ws) {
  __shared__ __attribute__((aligned(16))) float lds_[L_END];
  LAS float* lds = (LAS float*)lds_;
  const unsigned t0 = (unsigned)__builtin_amdgcn_s_memrealtime();
  const int tid = threadIdx.x, lane = tid & 63, wave = __builtin_amdgcn_readfirstlane(tid >> 6);
  const int r = lane >> 4, c = lane & 15;
  const int bid = blockIdx.x, xl = bid & 7, j = bid >> 3;
  const int h = xl * 4 + (j >> 3), s = j & 7;
  const int f1 = xl * 28 + j, mat = f1 / 112, rb = (f1 / 56) & 1, cb = f1 % 56;

  if (wave < NMAIN) {
    const rsrc_t rk = mkrsrc(Kc, (unsigned)H * SK * DH * 2), rv = mkrsrc(Vc, (unsigned)H * SK * DH * 2);
    u32x4 bA[16], bB[16];
    const unsigned kvbase = (unsigned)h * (8u << 20) + (unsigned)s * (1u << 20) + wave * 1024;
    const unsigned rot = 32u * ((h + (h >> 2) + (h >> 4)) & 3);
    const int kvoff = lane * 16;
    constexpr int STEP = (T_MAIN << 16) / 256 * PACE_G;
    int due = T_K0 * 65536 - STEP + (STEP / 256) * (wave * 32 + j);
#define PACE(T0, I) do { if ((I) % PACE_G == 0) { due += STEP; pace_wait(T0, due >> 16); } } while (0)
    __builtin_amdgcn_s_barrier();
    SB();
    {
      const rsrc_t rg = mkrsrc(mat ? wu : wg, D * F * 2);
      const int voff = j < 28 ? (lane >> 3) * (F * 2) + (lane & 7) * 16 : OOB, sb = (rb * 512 + wave * 64) * (F * 2) + cb * 128;
      constexpr int FSTEP = ((T_W2 - T_F1) << 16) / 8;
      int fdue = T_F1 * 65536 + (FSTEP / 256) * (wave * 32 + j);
#pragma unroll
      for (int i = 0; i < 8; ++i) { pace_wait(t0, fdue >> 16); fdue += FSTEP; bB[i] = ld16_nt(rg, voff, sb + i * 8 * (F * 2)); SB(); }
    }
#pragma unroll
    for (int i = 0; i < 16; ++i) { PACE(t0, i); bA[i] = ld16_nt(rk, kvoff, kvbase + (rot + i) * 8192); SB(); }
    {
      cnt_wait(lds, C_XN, 1);
      float ag[8] = {0.f, 0.f, 0.f, 0.f, 0.f, 0.f, 0.f, 0.f};
#pragma unroll
      for (int i = 0; i < 8; ++i) {
        const int k = rb * 512 + wave * 64 + 8 * i + (lane >> 3);
        fma8(ag, lds[L_XN + (k & 3) * 256 + (k >> 2)], bB[i]);
      }
#pragma unroll
      for (int e = 0; e < 8; ++e) ag[e] += dpp<ROR8>(ag[e]);
      const f32x2 z = fold_rows(ag);
      if (c < 8) *(LAS f32x2*)(lds + L_FP + wave * 64 + 8 * c + 2 * r) = z;
      cnt_arrive(lds, C_F1, lane);
      SB();
    }
#pragma unroll
    for (int i = 0; i < 16; ++i) { PACE(t0, i); bB[i] = ld16_nt(rk, kvoff, kvbase + (rot + 16 + i) * 8192); SB(); }
    cnt_wait(lds, C_QR, 1);
    const u32x4 qp = *(LAS u32x4*)(lds + L_QS + 4 * c);
    const unsigned t0m = __builtin_amdgcn_readfirstlane(*(LAS unsigned*)(lds + L_CNT + W_T0));
    constexpr float SCALE = 0.08838834764831845f, LOG2E = 1.4426950408889634f;
    LAS float* scp = lds + L_SC + wave * 512 + lane * 8;
    float mx = -INFINITY;
    auto kgroup = [&](u32x4 (&kb)[16], const bool more, const rsrc_t rn, const unsigned tb, const int g) __attribute__((always_inline)) {
      float sx[16];
#pragma unroll
      for (int i = 0; i < 16; ++i) {
        float d = 0.f;
#pragma unroll
        for (int t = 0; t < 4; ++t) d = dot2(kb[i][t], qp[t], d);
        asm volatile("" : "+v"(d));
        sx[i] = d;
        SB();
        if (more) { PACE(t0m, i); kb[i] = ld16_nt(rn, kvoff, tb + i * 8192); SB(); }
      }
#define TR_STEP(HALF, CTRL, BIT) _Pragma("unroll") for (int hh = 0; hh < (HALF); ++hh) { \
        const float lo_ = sx[hh], hi_ = sx[hh + (HALF)]; const float t1_ = lo_ + dpp<CTRL>(lo_), t2_ = hi_ + dpp<CTRL>(hi_); \
        sx[hh] = (c & (BIT)) ? t2_ : t1_; }
      TR_STEP(8, ROR8, 8) TR_STEP(4, XOR7, 4) TR_STEP(2, XOR2, 2) TR_STEP(1, XOR1, 1)
#undef TR_STEP
      const float sc = rbf(rbf(sx[0]) * SCALE) * LOG2E;
      mx = fmaxf(mx, sc);
      scp[g] = sc;
      SB();
    };
    kgroup(bA, false, rk, 0, 0);
#pragma unroll 1
    for (int g = 1; g < 8; ++g)
      kgroup(bB, true, mkrsrc(g == 7 ? Vc : Kc, (unsigned)H * SK * DH * 2), kvbase + (((g + 1) * 16 + rot) & 127u) * 8192, g);
    {
      const float m = wave_max(mx);
      f32x4 p0 = *(LAS f32x4*)scp, p1 = *(LAS f32x4*)(scp + 4);
      float l = 0.f;
#pragma unroll
      for (int e = 0; e < 4; ++e) { p0[e] = exp2_(p0[e] - m); p1[e] = exp2_(p1[e] - m); l += p0[e] + p1[e]; }
      *(LAS f32x4*)scp = p0; *(LAS f32x4*)(scp + 4) = p1;
      l = wave_sum(l);
      if (lane == 0) *(LAS f32x2*)(lds + L_ML + 2 * wave) = f32x2{m, l};
      cnt_arrive(lds, C_ML, lane);
    }
    float acc[8] = {0.f, 0.f, 0.f, 0.f, 0.f, 0.f, 0.f, 0.f};
    auto vgroup = [&](const bool more, const unsigned tb, const float p) __attribute__((always_inline)) {
#define PV_STEP(I) { fma8(acc, dpp<BCAST + (I)>(p), bB[I]); \
        asm volatile("" : "+v"(acc[0]), "+v"(acc[1]), "+v"(acc[2]), "+v"(acc[3]), "+v"(acc[4]), "+v"(acc[5]), "+v"(acc[6]), "+v"(acc[7])); SB(); \
        if (more) { PACE(t0m, I); bB[I] = ld16_nt(rv, kvoff, tb + (I) * 8192); SB(); } \
        else if ((I) == 11) {   \
          pace_wait(t0m, T_K0 + T_MAIN); \
          _Pragma("unroll") for (int u = 0; u < 4; ++u) { \
            bB[u] = ld16_nt(mkrsrc(wo, H * DH * D * 2), r * 2048 + c * 16, (h * 128 + wave * 16 + 4 * u) * 2048 + s * 256); SB(); } } }
      PV_STEP(0) PV_STEP(1) PV_STEP(2) PV_STEP(3) PV_STEP(4) PV_STEP(5) PV_STEP(6) PV_STEP(7)
      PV_STEP(8) PV_STEP(9) PV_STEP(10) PV_STEP(11) PV_STEP(12) PV_STEP(13) PV_STEP(14) PV_STEP(15)
#undef PV_STEP
    };
#pragma unroll 1
    for (int g = 0; g < 7; ++g) vgroup(true, kvbase + (((g + 1) * 16 + rot) & 127u) * 8192, scp[g]);
    __builtin_amdgcn_s_setprio(3);
    cnt_wait(lds, C_WG, 1);
    {
      const float wgt = lds[L_WG + wave];
#pragma unroll
      for (int e = 0; e < 8; ++e) acc[e] *= wgt;
      vgroup(false, 0, scp[7] * wgt);
    }
    *(LAS f32x2*)(lds + L_MRG + wave * 128 + 8 * c + 2 * r) = fold_rows(acc);
    cnt_arrive(lds, C_END, lane);
#pragma unroll
    for (int u = 0; u < 4; ++u) *(LAS u32x4*)(lds + L_WT + (wave * 16 + 4 * u + r) * WT_STR + 4 * c) = bB[u];
    cnt_arrive(lds, C_WT, lane);
    return;
#undef PACE
  }

  const int a = wave - NMAIN;
  const rsrc_t rx = mkrsrc(x, D * 2), rws = mkrsrc(ws, WS_END);
  unsigned tagA, tagB, tagC, tagD;
  unsigned long long e2;
  {
    u32x4 xa = ld16(rx, lane * 32, 0), xb = ld16(rx, lane * 32 + 16, 0);
    const u32x2 ep = __builtin_amdgcn_raw_buffer_load_b64(rws, 0, WS_EPOCH, SC1);
    const rsrc_t rwq = mkrsrc(wq, D * H * DH * 2);
    u32x4 w[8];
    const int voff = r * 8192 + c * 16, sb = (s * 128 + a * 32) * 8192 + h * 256;
#pragma unroll
    for (int i = 0; i < 8; ++i) { w[i] = ld16_nt(rwq, voff, sb + i * 32768); SB(); }
    if (a == 0 && lane < C_NUM) *(LAS unsigned*)(lds + L_CNT + lane) = 0u;
    __syncthreads();
    asm volatile("" : "+v"(xa), "+v"(xb));
    float v[16];
#pragma unroll
    for (int i = 0; i < 4; ++i) { v[2 * i] = lo2f(xa[i]); v[2 * i + 1] = hi2f(xa[i]); v[8 + 2 * i] = lo2f(xb[i]); v[9 + 2 * i] = hi2f(xb[i]); }
    float sm = 0.f;
#pragma unroll
    for (int i = 0; i < 16; ++i) sm += v[i];
    const float mean = wave_sum(sm) * (1.f / D);
    float ss = 0.f;
#pragma unroll
    for (int i = 0; i < 16; ++i) { const float d = v[i] - mean; ss = fmaf(d, d, ss); }
    const float var = wave_sum(ss) * (1.f / D);
    const float mb = rbf(mean), den = rbf(sqrtf(rbf(rbf(var) + 1e-5f)));
    const float rinv = 1.0f / den;
#pragma unroll
    for (int i = 0; i < 16; ++i) {
      const float nu = rbf(v[i] - mb), q0 = nu * rinv;
      v[i] = rbf(fmaf(fmaf(-q0, den, nu), rinv, q0));
    }
#pragma unroll
    for (int q = 0; q < 4; ++q) *(LAS f32x4*)(lds + L_XN + q * 256 + 4 * lane) = f32x4{v[q], v[4 + q], v[8 + q], v[12 + q]};
    cnt_arrive(lds, C_XN, lane);
    const unsigned long long e1 = (((unsigned long long)__builtin_amdgcn_readfirstlane(ep[1]) << 32) | (unsigned)__builtin_amdgcn_readfirstlane(ep[0])) + 1ull;
    e2 = e1 + 1ull;
    tagA = (unsigned)e1; tagB = (unsigned)(e1 >> 32) + (unsigned)e1 * 0x9E3779B1u;
    tagC = (unsigned)e2; tagD = (unsigned)(e2 >> 32) + (unsigned)e2 * 0x9E3779B1u;
    float acc[8] = {0.f, 0.f, 0.f, 0.f, 0.f, 0.f, 0.f, 0.f};
#pragma unroll
    for (int q = 0; q < 2; ++q) {
      const f32x4 xv = *(LAS f32x4*)(lds + L_XN + r * 256 + s * 32 + a * 8 + 4 * q);
#pragma unroll
      for (int e = 0; e < 4; ++e) fma8(acc, xv[e], w[4 * q + e]);
    }
    *(LAS f32x2*)(lds + L_QP + a * 128 + 8 * c + 2 * r) = fold_rows(acc);
  }
  cnt_arrive(lds, C_Q, lane);
  if (a == 0) {
    cnt_wait<false>(lds, C_Q, NAUX);
    f32x2 v = *(LAS f32x2*)(lds + L_QP + 2 * lane);
#pragma unroll
    for (int q = 1; q < 4; ++q) v += *(LAS f32x2*)(lds + L_QP + q * 128 + 2 * lane);
    st16_sc1(rws, WS_Q + (h * 8 + s) * 1024 + lane * 16, rec(v[0], v[1], tagA, tagB));
    st16_sc1(rws, (bid == 0 && lane == 0) ? WS_T0 : OOB, u32x4{t0, tagA, t0, tagB});
  }
  if (a == 1) {
    pace_wait(t0, T_QPOLL);
    float q0 = 0.f, q1 = 0.f;
    unsigned tg = t0;
#pragma unroll 1
    for (unsigned sp = 0; sp < SPIN_MEM; ++sp) {
      REREAD();
      u32x4 g[8];
#pragma unroll
      for (int i = 0; i < 8; ++i) g[i] = ld16_sc1(rws, lane * 16, WS_Q + (h * 8 + i) * 1024);
      const u32x4 gt = ld16_sc1(rws, 0, WS_T0);
      bool ok = rok(gt, tagA, tagB);
      tg = gt[0];
      q0 = 0.f; q1 = 0.f;
#pragma unroll
      for (int i = 0; i < 8; ++i) { ok = ok && rok(g[i], tagA, tagB); q0 += r0(g[i]); q1 += r1(g[i]); }
      if (__all(ok)) break;
      __builtin_amdgcn_s_sleep(2);
    }
    *(LAS unsigned*)(lds + L_QS + lane) = pack2(q0, q1);
    if (lane == 0) *(LAS unsigned*)(lds + L_CNT + W_T0) = ((int)(t0 - tg) < 0 || (int)(t0 - tg) > 300) ? t0 : tg;
    cnt_arrive(lds, C_QR, lane);
  }
  const int ks = xl * 2 + (j >> 4), cg = j & 15;
  u32x4 dw[7];
  {
    const rsrc_t rd = mkrsrc(wd, D * F * 2);
    const int voff = (lane >> 3) * 2048 + (lane & 7) * 16, sb = (ks * 224 + a * 56) * 2048 + cg * 128;
    constexpr int WSTEP = ((T_K0 - T_W2) << 16) / 7;
    int wdue = T_W2 * 65536 + (WSTEP / 128) * (a * 32 + j);
#pragma unroll
    for (int i = 0; i < 7; ++i) { pace_wait(t0, wdue >> 16); wdue += WSTEP; dw[i] = ld16_nt(rd, voff, sb + i * 16384); SB(); }
  }
  if (a == 0 && j < 28) {
    cnt_wait(lds, C_F1, NMAIN);
    f32x2 v = *(LAS f32x2*)(lds + L_FP + 2 * (lane & 31));
#pragma unroll
    for (int q = 1; q < 8; ++q) v += *(LAS f32x2*)(lds + L_FP + q * 64 + 2 * (lane & 31));
    st16_sc1(rws, lane < 32 ? WS_G1 + ((mat * 2 + rb) * F + cb * 64) * 8 + lane * 16 : OOB, rec(v[0], v[1], tagA, tagB));
  }
  {
    pace_wait(t0, T_K0 + 2000);
    u32x4 g[4];
    const int voff = (lane < 28 ? lane : 27) * 16;
#pragma unroll 1
    for (unsigned sp = 0; sp < SPIN_MEM; ++sp) {
      REREAD();
#pragma unroll
      for (int k = 0; k < 4; ++k) g[k] = ld16_sc1(rws, voff, WS_G1 + (k * F + ks * 224 + a * 56) * 8);
      if (__all(rok(g[0], tagA, tagB) && rok(g[1], tagA, tagB) && rok(g[2], tagA, tagB) && rok(g[3], tagA, tagB))) break;
      __builtin_amdgcn_s_sleep(32);
    }
    float hh[2];
#pragma unroll
    for (int e = 0; e < 2; ++e) {
      const float gs = rbf(__uint_as_float(g[0][2 * e]) + __uint_as_float(g[1][2 * e]));
      const float us = rbf(__uint_as_float(g[2][2 * e]) + __uint_as_float(g[3][2 * e]));
      const float ge = 0.5f * gs * (1.f + tanhf(0.7978845608028654f * (gs + 0.044715f * gs * gs * gs)));
      hh[e] = rbf(rbf(ge) * us);
    }
    if (lane < 28) *(LAS f32x2*)(lds + L_H + a * 64 + 2 * lane) = f32x2{hh[0], hh[1]};
    float y[8] = {0.f, 0.f, 0.f, 0.f, 0.f, 0.f, 0.f, 0.f};
#pragma unroll
    for (int i = 0; i < 7; ++i) fma8(y, lds[L_H + a * 64 + 8 * i + (lane >> 3)], dw[i]);
#pragma unroll
    for (int e = 0; e < 8; ++e) y[e] += dpp<ROR8>(y[e]);
    const f32x2 z = fold_rows(y);
    if (c < 8) *(LAS f32x2*)(lds + L_F2 + a * 64 + 8 * c + 2 * r) = z;
    cnt_arrive(lds, C_F2, lane);
  }
  const int col0 = s * 128 + 4 * h;
  unsigned xw = 0;
  if (a == 0) xw = __builtin_amdgcn_raw_buffer_load_b32(rx, (col0 + 2 * (lane & 1)) * 2, 0, 0);
  if (a == 1) {
    cnt_wait(lds, C_ML, NMAIN);
    const f32x2 ml = *(LAS f32x2*)(lds + L_ML + 2 * (lane & 7));
    const float Mw = max8(ml[0]), Lw = sum8(exp2_(ml[0] - Mw) * ml[1]);
    st16_sc1(rws, lane == 0 ? WS_ML + (h * 8 + s) * 128 : OOB, rec(Mw, Lw, tagA, tagB));
    pace_wait(t0, T_K0 + T_MAIN / 2 + 1500);
    u32x4 g;
#pragma unroll 1
    for (unsigned sp = 0; sp < SPIN_MEM; ++sp) {
      REREAD();
      g = ld16_sc1(rws, (lane & 7) * 128, WS_ML + h * 8 * 128);
      if (__all(rok(g, tagA, tagB))) break;
      __builtin_amdgcn_s_sleep(32);
    }
    const float M = max8(r0(g)), L = sum8(exp2_(r0(g) - M) * r1(g));
    if (lane < 8) lds[L_WG + lane] = exp2_(ml[0] - M) / L;
    cnt_arrive(lds, C_WG, lane);
  }
  pace_wait(t0, T_K0 + T_MAIN - 300);
  if (a == 0) {
    const unsigned po = __builtin_amdgcn_raw_buffer_load_b32(mkrsrc(out, D * 2), (col0 + 2 * (lane & 1)) * 2, 0, SC1);
    asm volatile("" :: "v"(po));
  }
  pace_wait(t0, T_K0 + T_MAIN - 100);
  __builtin_amdgcn_s_setprio(3);
  if (a == 2) {
    cnt_wait(lds, C_F2, NAUX);
    f32x2 v = *(LAS f32x2*)(lds + L_F2 + 2 * (lane & 31));
#pragma unroll
    for (int q = 1; q < 4; ++q) v += *(LAS f32x2*)(lds + L_F2 + q * 64 + 2 * (lane & 31));
    pace_wait(t0, T_K0 + T_MAIN + 60);
    st16_sc1(rws, lane < 32 ? WS_FFN + (ks * D + cg * 64) * 8 + lane * 16 : OOB, rec(v[0], v[1], tagA, tagB));
  }
  if (a == 0) {
    cnt_wait<false>(lds, C_END, NMAIN);
    f32x2 v = *(LAS f32x2*)(lds + L_MRG + 2 * lane);
#pragma unroll
    for (int w = 1; w < 8; ++w) v += *(LAS f32x2*)(lds + L_MRG + w * 128 + 2 * lane);
    st16_sc1(rws, WS_Q + (h * 8 + s) * 1024 + lane * 16, rec(v[0], v[1], tagC, tagD));
  } else {
    cnt_wait(lds, C_END, NMAIN);
  }
  u32x4 bfr[2][4];
  cnt_wait<false>(lds, C_WT, NMAIN);
#pragma unroll
  for (int q = 0; q < 4; ++q)
#pragma unroll
    for (int d = 0; d < 4; ++d) {
      const unsigned w0 = *(LAS unsigned*)(lds + L_WT + (32 * q + 8 * r + 2 * d) * WT_STR + 16 * a + c);
      const unsigned w1 = *(LAS unsigned*)(lds + L_WT + (32 * q + 8 * r + 2 * d + 1) * WT_STR + 16 * a + c);
      bfr[0][q][d] = __builtin_amdgcn_perm(w1, w0, 0x05040100u);
      bfr[1][q][d] = __builtin_amdgcn_perm(w1, w0, 0x07060302u);
    }
  {
    u32x4 g0, g1;
    const int vo = r * 1024 + (16 * a + c) * 16;
#pragma unroll 1
    for (unsigned sp = 0; sp < SPIN_MEM; ++sp) {
      REREAD();
      g0 = ld16_sc1(rws, vo, WS_Q + h * 8 * 1024);
      g1 = ld16_sc1(rws, vo, WS_Q + (h * 8 + 4) * 1024);
      if (__all(rok(g0, tagC, tagD) && rok(g1, tagC, tagD))) break;
    }
    float o = fold32(r0(g0) + r0(g1), r1(g0) + r1(g1));
    o = fold16(o, o);
    if ((r & 1) == 0) *((LAS u16*)(lds + L_OB) + 32 * a + 2 * c + (r >> 1)) = __builtin_bit_cast(u16, (__bf16)o);
  }
  cnt_arrive(lds, C_OB, lane);
  cnt_wait<false>(lds, C_OB, NAUX);
  {
    f32x4 y0 = {0.f, 0.f, 0.f, 0.f}, y1 = y0;
#pragma unroll
    for (int q = 0; q < 4; ++q) {
      const u32x4 af = *(LAS u32x4*)(lds + L_OB + 16 * q + 4 * r);
      y0 = __builtin_amdgcn_mfma_f32_16x16x32_bf16(__builtin_bit_cast(bf16x8, af), __builtin_bit_cast(bf16x8, bfr[0][q]), y0, 0, 0, 0);
      y1 = __builtin_amdgcn_mfma_f32_16x16x32_bf16(__builtin_bit_cast(bf16x8, af), __builtin_bit_cast(bf16x8, bfr[1][q]), y1, 0, 0, 0);
    }
    st16_sc1(rws, lane < 16 ? WS_Y + (h * 8 + s) * 1024 + (16 * a + lane) * 16 : OOB, rec(y0[0], y1[0], tagA, tagB));
  }
  if (a == 0) {
    u32x4 g, gf;
    pace_wait((unsigned)__builtin_amdgcn_s_memrealtime(), 30);
#pragma unroll 1
    for (unsigned sp = 0; sp < SPIN_MEM; ++sp) {
      REREAD();
      g = ld16_sc1(rws, (lane >> 1) * (8 * 1024) + (lane & 1) * 16, WS_Y + s * 1024 + h * 32);
      gf = ld16_sc1(rws, ((lane >> 1) & 15) * (D * 8) + (lane & 1) * 16, WS_FFN + col0 * 8);
      if (__all(rok(g, tagA, tagB) && rok(gf, tagA, tagB))) break;
    }
    const float f0 = rbf(parity_sum(r0(gf)) * 0.5f), f1v = rbf(parity_sum(r1(gf)) * 0.5f);
    const float a0 = rbf(parity_sum(r0(g))), a1 = rbf(parity_sum(r1(g)));
    const unsigned o = pack2(rbf(lo2f(xw) + a0) + f0, rbf(hi2f(xw) + a1) + f1v);
    __builtin_amdgcn_raw_buffer_store_b32(o, mkrsrc(out, D * 2), lane < 2 ? (col0 + 2 * lane) * 2 : OOB, 0, SC1);
    __builtin_amdgcn_raw_buffer_store_b64(u32x2{(unsigned)e2, (unsigned)(e2 >> 32)}, rws, (bid == 0 && lane == 0) ? WS_EPOCH : OOB, 0, SC1);
  }
}

extern "C" void kernel_launch(void* const* d_in, const int* in_sizes, int n_in, void* d_out, int out_size, void* d_ws, size_t ws_size,
                              hipStream_t stream) {
  fused_decode_kernel<<<256, NTHR, 0, stream>>>((const u16*)d_in[0], (const u16*)d_in[1], (const u16*)d_in[2], (const u16*)d_in[3],
                                                 (const u16*)d_in[4], (const u16*)d_in[6], (const u16*)d_in[7], (const u16*)d_in[8],
                                                 (u16*)d_out, d_ws);
}
```

```cpp
#include <hip/hip_runtime.h>
#include <stdint.h>

typedef unsigned u32x4 __attribute__((ext_vector_type(4)));
typedef unsigned u32x2 __attribute__((ext_vector_type(2)));
typedef float f32x4 __attribute__((ext_vector_type(4)));
typedef float f32x2 __attribute__((ext_vector_type(2)));
typedef __bf16 bf16x2 __attribute__((ext_vector_type(2)));
typedef __bf16 bf16x8 __attribute__((ext_vector_type(8)));
typedef unsigned short u16;
#define LAS __attribute__((address_space(3)))
typedef __amdgpu_buffer_rsrc_t rsrc_t;

constexpr int T_F1 = 150;
constexpr int T_W2 = 360;
constexpr int T_K0 = 460;
constexpr int T_MAIN = 7150;
constexpr int PACE_G = 2;
constexpr int T_QPOLL = 390;
constexpr int T_HA = 1600, T_HB = 2800;

constexpr int D = 1024, H = 32, SK = 32768, DH = 128, F = 3584;
constexpr int NMAIN = 8, NAUX = 4, NTHR = (NMAIN + NAUX) * 64;
constexpr int NT = 2, SC1 = 16;
constexpr int OOB = 0x40000000;
constexpr unsigned WS_EPOCH = 0, WS_T0 = 1024, WS_ML = 4096, WS_Q = WS_ML + 256 * 128, WS_Y = WS_Q + 256 * 1024, WS_G1 = WS_Y + 256 * 1024,
                   WS_H = WS_G1 + 4 * F * 8, WS_FFN = WS_H + F * 4, WS_END = WS_FFN + 16 * D * 8;
constexpr int L_XN = 0, L_QP = 1024, L_QS = L_QP + 4 * 128, L_MRG = L_QS + 64, L_ML = L_MRG + 8 * 128, L_WG = L_ML + 16, L_FP = L_WG + 8,
              L_H = L_FP + 8 * 64, L_F2 = L_H + 4 * 64, L_OB = L_F2 + 4 * 64, L_CNT = L_OB + 64, L_SC = L_CNT + 16, L_WT = L_SC + 8 * 512,
              WT_STR = 68, L_END = L_WT + 128 * WT_STR;
constexpr int C_Q = 0, C_XN = 1, C_F2 = 2, C_WT = 3, C_ML = 4, C_WG = 5, C_END = 6, C_QR = 7, C_F1 = 8, C_OB = 9, C_NUM = 10, W_T0 = 12;
constexpr unsigned SPIN_MEM = 1u << 20, SPIN_LDS = 1u << 23;

__device__ __forceinline__ float lo2f(unsigned w) { return __builtin_bit_cast(float, w << 16); }
__device__ __forceinline__ float hi2f(unsigned w) { return __builtin_bit_cast(float, w & 0xffff0000u); }
__device__ __forceinline__ float rbf(float x) { return (float)(__bf16)x; }
__device__ __forceinline__ unsigned pack2(float a, float b) { bf16x2 v = {(__bf16)a, (__bf16)b}; return __builtin_bit_cast(unsigned, v); }
__device__ __forceinline__ float dot2(unsigned k, unsigned q, float acc) {
  return __builtin_amdgcn_fdot2_f32_bf16(__builtin_bit_cast(bf16x2, k), __builtin_bit_cast(bf16x2, q), acc, false);
}
template <int CTRL> __device__ __forceinline__ float dpp(float x) {
  return __builtin_bit_cast(float, __builtin_amdgcn_mov_dpp(__builtin_bit_cast(int, x), CTRL, 0xf, 0xf, true));
}
constexpr int XOR1 = 0xB1, XOR2 = 0x4E, XOR7 = 0x141, ROR8 = 0x128, ROR4 = 0x124, BCAST = 0x150;
__device__ __forceinline__ float fold32(float a, float b) {
  auto t = __builtin_amdgcn_permlane32_swap(__float_as_uint(a), __float_as_uint(b), false, false);
  return __uint_as_float(t[0]) + __uint_as_float(t[1]);
}
__device__ __forceinline__ float fold16(float a, float b) {
  auto t = __builtin_amdgcn_permlane16_swap(__float_as_uint(a), __float_as_uint(b), false, false);
  return __uint_as_float(t[0]) + __uint_as_float(t[1]);
}
__device__ __forceinline__ f32x2 fold_rows(const float (&v)[8]) {
  const float u0 = fold32(v[0], v[4]), u1 = fold32(v[1], v[5]), u2 = fold32(v[2], v[6]), u3 = fold32(v[3], v[7]);
  return f32x2{fold16(u0, u2), fold16(u1, u3)};
}
__device__ __forceinline__ float xrow_sum(float x) { x = fold16(x, x); return fold32(x, x); }
__device__ __forceinline__ float xrow_max(float x) {
  auto s = __builtin_amdgcn_permlane16_swap(__float_as_uint(x), __float_as_uint(x), false, false);
  x = fmaxf(__uint_as_float(s[0]), __uint_as_float(s[1]));
  auto t = __builtin_amdgcn_permlane32_swap(__float_as_uint(x), __float_as_uint(x), false, false);
  return fmaxf(__uint_as_float(t[0]), __uint_as_float(t[1]));
}
__device__ __forceinline__ float parity_sum(float x) {
  x += dpp<XOR2>(x); x += dpp<ROR4>(x); x += dpp<ROR8>(x); return xrow_sum(x);
}
__device__ __forceinline__ float wave_sum(float x) { x += dpp<XOR1>(x); return parity_sum(x); }
__device__ __forceinline__ float max8(float x) { x = fmaxf(x, dpp<XOR1>(x)); x = fmaxf(x, dpp<XOR2>(x)); return fmaxf(x, dpp<XOR7>(x)); }
__device__ __forceinline__ float sum8(float x) { x += dpp<XOR1>(x); x += dpp<XOR2>(x); return x + dpp<XOR7>(x); }
__device__ __forceinline__ float wave_max(float x) { x = max8(x); x = fmaxf(x, dpp<ROR8>(x)); return xrow_max(x); }
__device__ __forceinline__ float exp2_(float x) { return __builtin_amdgcn_exp2f(x); }
__device__ __forceinline__ rsrc_t mkrsrc(const void* p, unsigned bytes) { return __builtin_amdgcn_make_buffer_rsrc((void*)p, 0, (int)bytes, 0x00020000); }
__device__ __forceinline__ u32x4 ld16(rsrc_t r, int voff, int soff) { return __builtin_amdgcn_raw_buffer_load_b128(r, voff, soff, 0); }
__device__ __forceinline__ u32x4 ld16_nt(rsrc_t r, int voff, int soff) { return __builtin_amdgcn_raw_buffer_load_b128(r, voff, soff, NT); }
__device__ __forceinline__ u32x4 ld16_sc1(rsrc_t r, int voff, int soff) { return __builtin_amdgcn_raw_buffer_load_b128(r, voff, soff, SC1); }
__device__ __forceinline__ void st16_sc1(rsrc_t r, int off, u32x4 v) { __builtin_amdgcn_raw_buffer_store_b128(v, r, off, 0, SC1); }
#define SB() __builtin_amdgcn_sched_barrier(0)
#define REREAD() asm volatile("" ::: "memory")
__device__ __forceinline__ void pace_wait(unsigned t0, int due) {
  asm volatile("1:\n\ts_memrealtime s[96:97]\n\ts_waitcnt lgkmcnt(0)\n\ts_sub_u32 s96, s96, %0\n\ts_cmp_lt_i32 s96, %1\n\t"
               "s_cbranch_scc0 2f\n\ts_sleep 1\n\ts_branch 1b\n2:"
               :: "s"(t0), "s"(due) : "s96", "s97", "scc", "memory");
}
__device__ __forceinline__ void fma8(float (&acc)[8], float s, u32x4 w) {
#pragma unroll
  for (int t = 0; t < 4; ++t) { acc[2 * t] = fmaf(s, lo2f(w[t]), acc[2 * t]); acc[2 * t + 1] = fmaf(s, hi2f(w[t]), acc[2 * t + 1]); }
}
__device__ __forceinline__ u32x4 rec(float a, float b, unsigned ta, unsigned tb) { return u32x4{__float_as_uint(a), ta, __float_as_uint(b), tb}; }
__device__ __forceinline__ bool rok(u32x4 g, unsigned ta, unsigned tb) { return g[1] == ta && g[3] == tb; }
__device__ __forceinline__ float r0(u32x4 g) { return __uint_as_float(g[0]); }
__device__ __forceinline__ float r1(u32x4 g) { return __uint_as_float(g[2]); }

__device__ __forceinline__ void cnt_arrive(LAS float* lds, int which, int lane) {
  asm volatile("s_waitcnt lgkmcnt(0)" ::: "memory");
  if (lane == 0) __hip_atomic_fetch_add((LAS unsigned*)(lds + L_CNT + which), 1u, __ATOMIC_RELAXED, __HIP_MEMORY_SCOPE_WORKGROUP);
}
template <bool SLEEP = true>
__device__ __forceinline__ void cnt_wait(LAS float* lds, int which, unsigned n) {
#pragma unroll 1
  for (unsigned sp = 0; sp < SPIN_LDS; ++sp) {
    if (__builtin_amdgcn_readfirstlane(*(volatile LAS unsigned*)(lds + L_CNT + which)) >= n) break;
    if (SLEEP) __builtin_amdgcn_s_sleep(1);
  }
  REREAD();
}

__global__ void __launch_bounds__(NTHR, 3)
fused_decode_kernel(const u16* __restrict__ x, const u16* __restrict__ Kc, const u16* __restrict__ Vc, const u16* __restrict__ wq,
                    const u16* __restrict__ wo, const u16* __restrict__ wg, const u16* __restrict__ wu, const u16* __restrict__ wd,
                    u16* __restrict__ out, void* ws) {
  __shared__ __attribute__((aligned(16))) float lds_[L_END];
  LAS float* lds = (LAS float*)lds_;
  const unsigned t0 = (unsigned)__builtin_amdgcn_s_memrealtime();
  const int tid = threadIdx.x, lane = tid & 63, wave = __builtin_amdgcn_readfirstlane(tid >> 6);
  const int r = lane >> 4, c = lane & 15;
  const int bid = blockIdx.x, xl = bid & 7, j = bid >> 3;
  const int h = xl * 4 + (j >> 3), s = j & 7;
  const int f1 = xl * 28 + j, mat = f1 / 112, rb = (f1 / 56) & 1, cb = f1 % 56;

  if (wave < NMAIN) {
    const rsrc_t rk = mkrsrc(Kc, (unsigned)H * SK * DH * 2), rv = mkrsrc(Vc, (unsigned)H * SK * DH * 2);
    u32x4 bA[16], bB[16];
    const unsigned kvbase = (unsigned)h * (8u << 20) + (unsigned)s * (1u << 20) + wave * 1024;
    const unsigned rot = 32u * ((h + (h >> 2) + (h >> 4)) & 3);
    const int kvoff = lane * 16;
    constexpr int STEP = (T_MAIN << 16) / 256 * PACE_G;
    int due = T_K0 * 65536 - STEP + (STEP / 256) * (wave * 32 + j);
#define PACE(T0, I) do { if ((I) % PACE_G == 0) { due += STEP; pace_wait(T0, due >> 16); } } while (0)
    __builtin_amdgcn_s_barrier();
    SB();
    {
      const rsrc_t rg = mkrsrc(mat ? wu : wg, D * F * 2);
      const int voff = j < 28 ? (lane >> 3) * (F * 2) + (lane & 7) * 16 : OOB, sb = (rb * 512 + wave * 64) * (F * 2) + cb * 128;
      constexpr int FSTEP = ((T_W2 - T_F1) << 16) / 8;
      int fdue = T_F1 * 65536 + (FSTEP / 256) * (wave * 32 + j);
#pragma unroll
      for (int i = 0; i < 8; ++i) { pace_wait(t0, fdue >> 16); fdue += FSTEP; bB[i] = ld16_nt(rg, voff, sb + i * 8 * (F * 2)); SB(); }
    }
#pragma unroll
    for (int i = 0; i < 16; ++i) { PACE(t0, i); bA[i] = ld16_nt(rk, kvoff, kvbase + (rot + i) * 8192); SB(); }
    {
      cnt_wait(lds, C_XN, 1);
      float ag[8] = {0.f, 0.f, 0.f, 0.f, 0.f, 0.f, 0.f, 0.f};
#pragma unroll
      for (int i = 0; i < 8; ++i) {
        const int k = rb * 512 + wave * 64 + 8 * i + (lane >> 3);
        fma8(ag, lds[L_XN + (k & 3) * 256 + (k >> 2)], bB[i]);
      }
#pragma unroll
      for (int e = 0; e < 8; ++e) ag[e] += dpp<ROR8>(ag[e]);
      const f32x2 z = fold_rows(ag);
      if (c < 8) *(LAS f32x2*)(lds + L_FP + wave * 64 + 8 * c + 2 * r) = z;
      cnt_arrive(lds, C_F1, lane);
      SB();
    }
#pragma unroll
    for (int i = 0; i < 16; ++i) { PACE(t0, i); bB[i] = ld16_nt(rk, kvoff, kvbase + (rot + 16 + i) * 8192); SB(); }
    cnt_wait(lds, C_QR, 1);
    const u32x4 qp = *(LAS u32x4*)(lds + L_QS + 4 * c);
    const unsigned t0m = __builtin_amdgcn_readfirstlane(*(LAS unsigned*)(lds + L_CNT + W_T0));
    constexpr float SCALE = 0.08838834764831845f, LOG2E = 1.4426950408889634f;
    LAS float* scp = lds + L_SC + wave * 512 + lane * 8;
    float mx = -INFINITY;
    auto kgroup = [&](u32x4 (&kb)[16], const bool more, const rsrc_t rn, const unsigned tb, const int g) __attribute__((always_inline)) {
      float sx[16];
#pragma unroll
      for (int i = 0; i < 16; ++i) {
        float d = 0.f;
#pragma unroll
        for (int t = 0; t < 4; ++t) d = dot2(kb[i][t], qp[t], d);
        asm volatile("" : "+v"(d));
        sx[i] = d;
        SB();
        if (more) { PACE(t0m, i); kb[i] = ld16_nt(rn, kvoff, tb + i * 8192); SB(); }
      }
#define TR_STEP(HALF, CTRL, BIT) _Pragma("unroll") for (int hh = 0; hh < (HALF); ++hh) { \
        const float lo_ = sx[hh], hi_ = sx[hh + (HALF)]; const float t1_ = lo_ + dpp<CTRL>(lo_), t2_ = hi_ + dpp<CTRL>(hi_); \
        sx[hh] = (c & (BIT)) ? t2_ : t1_; }
      TR_STEP(8, ROR8, 8) TR_STEP(4, XOR7, 4) TR_STEP(2, XOR2, 2) TR_STEP(1, XOR1, 1)
#undef TR_STEP
      const float sc = rbf(rbf(sx[0]) * SCALE) * LOG2E;
      mx = fmaxf(mx, sc);
      scp[g] = sc;
      SB();
    };
    kgroup(bA, false, rk, 0, 0);
#pragma unroll 1
    for (int g = 1; g < 8; ++g)
      kgroup(bB, true, mkrsrc(g == 7 ? Vc : Kc, (unsigned)H * SK * DH * 2), kvbase + (((g + 1) * 16 + rot) & 127u) * 8192, g);
    {
      const float m = wave_max(mx);
      f32x4 p0 = *(LAS f32x4*)scp, p1 = *(LAS f32x4*)(scp + 4);
      float l = 0.f;
#pragma unroll
      for (int e = 0; e < 4; ++e) { p0[e] = exp2_(p0[e] - m); p1[e] = exp2_(p1[e] - m); l += p0[e] + p1[e]; }
      *(LAS f32x4*)scp = p0; *(LAS f32x4*)(scp + 4) = p1;
      l = wave_sum(l);
      if (lane == 0) *(LAS f32x2*)(lds + L_ML + 2 * wave) = f32x2{m, l};
      cnt_arrive(lds, C_ML, lane);
    }
    float acc[8] = {0.f, 0.f, 0.f, 0.f, 0.f, 0.f, 0.f, 0.f};
    auto vgroup = [&](const bool more, const unsigned tb, const float p) __attribute__((always_inline)) {
#define PV_STEP(I) { fma8(acc, dpp<BCAST + (I)>(p), bB[I]); \
        asm volatile("" : "+v"(acc[0]), "+v"(acc[1]), "+v"(acc[2]), "+v"(acc[3]), "+v"(acc[4]), "+v"(acc[5]), "+v"(acc[6]), "+v"(acc[7])); SB(); \
        if (more) { PACE(t0m, I); bB[I] = ld16_nt(rv, kvoff, tb + (I) * 8192); SB(); } \
        else if ((I) == 11) {   \
          pace_wait(t0m, T_K0 + T_MAIN); \
          _Pragma("unroll") for (int u = 0; u < 4; ++u) { \
            bB[u] = ld16_nt(mkrsrc(wo, H * DH * D * 2), r * 2048 + c * 16, (h * 128 + wave * 16 + 4 * u) * 2048 + s * 256); SB(); } } }
      PV_STEP(0) PV_STEP(1) PV_STEP(2) PV_STEP(3) PV_STEP(4) PV_STEP(5) PV_STEP(6) PV_STEP(7)
      PV_STEP(8) PV_STEP(9) PV_STEP(10) PV_STEP(11) PV_STEP(12) PV_STEP(13) PV_STEP(14) PV_STEP(15)
#undef PV_STEP
    };
#pragma unroll 1
    for (int g = 0; g < 7; ++g) vgroup(true, kvbase + (((g + 1) * 16 + rot) & 127u) * 8192, scp[g]);
    __builtin_amdgcn_s_setprio(3);
    cnt_wait(lds, C_WG, 1);
    {
      const float wgt = lds[L_WG + wave];
#pragma unroll
      for (int e = 0; e < 8; ++e) acc[e] *= wgt;
      vgroup(false, 0, scp[7] * wgt);
    }
    *(LAS f32x2*)(lds + L_MRG + wave * 128 + 8 * c + 2 * r) = fold_rows(acc);
    cnt_arrive(lds, C_END, lane);
#pragma unroll
    for (int u = 0; u < 4; ++u) *(LAS u32x4*)(lds + L_WT + (wave * 16 + 4 * u + r) * WT_STR + 4 * c) = bB[u];
    cnt_arrive(lds, C_WT, lane);
    return;
#undef PACE
  }

  const int a = wave - NMAIN;
  const rsrc_t rx = mkrsrc(x, D * 2), rws = mkrsrc(ws, WS_END);
  unsigned tagA, tagB, tagC, tagD;
  unsigned long long e2;
  {
    u32x4 xa = ld16(rx, lane * 32, 0), xb = ld16(rx, lane * 32 + 16, 0);
    const u32x2 ep = __builtin_amdgcn_raw_buffer_load_b64(rws, 0, WS_EPOCH, SC1);
    const rsrc_t rwq = mkrsrc(wq, D * H * DH * 2);
    u32x4 w[8];
    const int voff = r * 8192 + c * 16, sb = (s * 128 + a * 32) * 8192 + h * 256;
#pragma unroll
    for (int i = 0; i < 8; ++i) { w[i] = ld16_nt(rwq, voff, sb + i * 32768); SB(); }
    if (a == 0 && lane < C_NUM) *(LAS unsigned*)(lds + L_CNT + lane) = 0u;
    __syncthreads();
    asm volatile("" : "+v"(xa), "+v"(xb));
    float v[16];
#pragma unroll
    for (int i = 0; i < 4; ++i) { v[2 * i] = lo2f(xa[i]); v[2 * i + 1] = hi2f(xa[i]); v[8 + 2 * i] = lo2f(xb[i]); v[9 + 2 * i] = hi2f(xb[i]); }
    float sm = 0.f;
#pragma unroll
    for (int i = 0; i < 16; ++i) sm += v[i];
    const float mean = wave_sum(sm) * (1.f / D);
    float ss = 0.f;
#pragma unroll
    for (int i = 0; i < 16; ++i) { const float d = v[i] - mean; ss = fmaf(d, d, ss); }
    const float var = wave_sum(ss) * (1.f / D);
    const float mb = rbf(mean), den = rbf(sqrtf(rbf(rbf(var) + 1e-5f)));
    const float rinv = 1.0f / den;
#pragma unroll
    for (int i = 0; i < 16; ++i) {
      const float nu = rbf(v[i] - mb), q0 = nu * rinv;
      v[i] = rbf(fmaf(fmaf(-q0, den, nu), rinv, q0));
    }
#pragma unroll
    for (int q = 0; q < 4; ++q) *(LAS f32x4*)(lds + L_XN + q * 256 + 4 * lane) = f32x4{v[q], v[4 + q], v[8 + q], v[12 + q]};
    cnt_arrive(lds, C_XN, lane);
    const unsigned long long e1 = (((unsigned long long)__builtin_amdgcn_readfirstlane(ep[1]) << 32) | (unsigned)__builtin_amdgcn_readfirstlane(ep[0])) + 1ull;
    e2 = e1 + 1ull;
    tagA = (unsigned)e1; tagB = (unsigned)(e1 >> 32) + (unsigned)e1 * 0x9E3779B1u;
    tagC = (unsigned)e2; tagD = (unsigned)(e2 >> 32) + (unsigned)e2 * 0x9E3779B1u;
    float acc[8] = {0.f, 0.f, 0.f, 0.f, 0.f, 0.f, 0.f, 0.f};
#pragma unroll
    for (int q = 0; q < 2; ++q) {
      const f32x4 xv = *(LAS f32x4*)(lds + L_XN + r * 256 + s * 32 + a * 8 + 4 * q);
#pragma unroll
      for (int e = 0; e < 4; ++e) fma8(acc, xv[e], w[4 * q + e]);
    }
    *(LAS f32x2*)(lds + L_QP + a * 128 + 8 * c + 2 * r) = fold_rows(acc);
  }
  cnt_arrive(lds, C_Q, lane);
  if (a == 0) {
    cnt_wait<false>(lds, C_Q, NAUX);
    f32x2 v = *(LAS f32x2*)(lds + L_QP + 2 * lane);
#pragma unroll
    for (int q = 1; q < 4; ++q) v += *(LAS f32x2*)(lds + L_QP + q * 128 + 2 * lane);
    st16_sc1(rws, WS_Q + (h * 8 + s) * 1024 + lane * 16, rec(v[0], v[1], tagA, tagB));
    st16_sc1(rws, (bid == 0 && lane == 0) ? WS_T0 : OOB, u32x4{t0, tagA, t0, tagB});
  }
  if (a == 1) {
    pace_wait(t0, T_QPOLL);
    float q0 = 0.f, q1 = 0.f;
    unsigned tg = t0;
#pragma unroll 1
    for (unsigned sp = 0; sp < SPIN_MEM; ++sp) {
      REREAD();
      u32x4 g[8];
#pragma unroll
      for (int i = 0; i < 8; ++i) g[i] = ld16_sc1(rws, lane * 16, WS_Q + (h * 8 + i) * 1024);
      const u32x4 gt = ld16_sc1(rws, 0, WS_T0);
      bool ok = rok(gt, tagA, tagB);
      tg = gt[0];
      q0 = 0.f; q1 = 0.f;
#pragma unroll
      for (int i = 0; i < 8; ++i) { ok = ok && rok(g[i], tagA, tagB); q0 += r0(g[i]); q1 += r1(g[i]); }
      if (__all(ok)) break;
      __builtin_amdgcn_s_sleep(2);
    }
    *(LAS unsigned*)(lds + L_QS + lane) = pack2(q0, q1);
    if (lane == 0) *(LAS unsigned*)(lds + L_CNT + W_T0) = ((int)(t0 - tg) < 0 || (int)(t0 - tg) > 300) ? t0 : tg;
    cnt_arrive(lds, C_QR, lane);
  }
  const int ks = xl * 2 + (j >> 4), cg = j & 15;
  u32x4 dw[7];
  {
    const rsrc_t rd = mkrsrc(wd, D * F * 2);
    const int voff = (lane >> 3) * 2048 + (lane & 7) * 16, sb = (ks * 224 + a * 56) * 2048 + cg * 128;
    constexpr int WSTEP = ((T_K0 - T_W2) << 16) / 7;
    int wdue = T_W2 * 65536 + (WSTEP / 128) * (a * 32 + j);
#pragma unroll
    for (int i = 0; i < 7; ++i) { pace_wait(t0, wdue >> 16); wdue += WSTEP; dw[i] = ld16_nt(rd, voff, sb + i * 16384); SB(); }
  }
  if (a == 0 && j < 28) {
    cnt_wait(lds, C_F1, NMAIN);
    f32x2 v = *(LAS f32x2*)(lds + L_FP + 2 * (lane & 31));
#pragma unroll
    for (int q = 1; q < 8; ++q) v += *(LAS f32x2*)(lds + L_FP + q * 64 + 2 * (lane & 31));
    if (f1 >= 56) {
      st16_sc1(rws, lane < 32 ? WS_G1 + ((mat * 2 + rb) * F + cb * 64) * 8 + lane * 16 : OOB, rec(v[0], v[1], tagA, tagB));
    } else {
      pace_wait(t0, T_K0 + T_HA);
      u32x4 g[3];
#pragma unroll 1
      for (unsigned sp = 0; sp < SPIN_MEM; ++sp) {
        REREAD();
#pragma unroll
        for (int k = 0; k < 3; ++k) g[k] = ld16_sc1(rws, (lane & 31) * 16, WS_G1 + ((k + 1) * F + cb * 64) * 8);
        if (__all(rok(g[0], tagA, tagB) && rok(g[1], tagA, tagB) && rok(g[2], tagA, tagB))) break;
        __builtin_amdgcn_s_sleep(32);
      }
      float hh[2];
#pragma unroll
      for (int e = 0; e < 2; ++e) {
        const float gs = rbf(v[e] + __uint_as_float(g[0][2 * e]));
        const float us = rbf(__uint_as_float(g[1][2 * e]) + __uint_as_float(g[2][2 * e]));
        const float ge = 0.5f * gs * (1.f + tanhf(0.7978845608028654f * (gs + 0.044715f * gs * gs * gs)));
        hh[e] = rbf(ge) * us;
      }
      const float hp = __uint_as_float(pack2(hh[0], hh[1]));
      st16_sc1(rws, (lane < 32 && !(lane & 1)) ? WS_H + cb * 256 + lane * 8 : OOB, rec(hp, dpp<XOR1>(hp), tagA, tagB));
    }
  }
  {
    pace_wait(t0, T_K0 + T_HB);
    u32x4 g;
#pragma unroll 1
    for (unsigned sp = 0; sp < SPIN_MEM; ++sp) {
      REREAD();
      g = ld16_sc1(rws, (lane < 14 ? lane : 13) * 16, WS_H + (ks * 56 + a * 14) * 16);
      if (__all(rok(g, tagA, tagB))) break;
      __builtin_amdgcn_s_sleep(32);
    }
    if (lane < 14) *(LAS f32x4*)(lds + L_H + a * 64 + 4 * lane) = f32x4{lo2f(g[0]), hi2f(g[0]), lo2f(g[2]), hi2f(g[2])};
    float y[8] = {0.f, 0.f, 0.f, 0.f, 0.f, 0.f, 0.f, 0.f};
#pragma unroll
    for (int i = 0; i < 7; ++i) fma8(y, lds[L_H + a * 64 + 8 * i + (lane >> 3)], dw[i]);
#pragma unroll
    for (int e = 0; e < 8; ++e) y[e] += dpp<ROR8>(y[e]);
    const f32x2 z = fold_rows(y);
    if (c < 8) *(LAS f32x2*)(lds + L_F2 + a * 64 + 8 * c + 2 * r) = z;
    cnt_arrive(lds, C_F2, lane);
  }
  const int col0 = s * 128 + 4 * h;
  unsigned xw = 0;
  if (a == 0) xw = __builtin_amdgcn_raw_buffer_load_b32(rx, (col0 + 2 * (lane & 1)) * 2, 0, 0);
  if (a == 3) {
    const unsigned po = __builtin_amdgcn_raw_buffer_load_b32(mkrsrc(out, D * 2), (col0 + 2 * (lane & 1)) * 2, 0, SC1);
    asm volatile("" :: "v"(po));
  }
  if (a == 1) {
    cnt_wait(lds, C_ML, NMAIN);
    const f32x2 ml = *(LAS f32x2*)(lds + L_ML + 2 * (lane & 7));
    const float Mw = max8(ml[0]), Lw = sum8(exp2_(ml[0] - Mw) * ml[1]);
    st16_sc1(rws, lane == 0 ? WS_ML + (h * 8 + s) * 128 : OOB, rec(Mw, Lw, tagA, tagB));
    pace_wait(t0, T_K0 + T_MAIN / 2 + 1500);
    u32x4 g;
#pragma unroll 1
    for (unsigned sp = 0; sp < SPIN_MEM; ++sp) {
      REREAD();
      g = ld16_sc1(rws, (lane & 7) * 128, WS_ML + h * 8 * 128);
      if (__all(rok(g, tagA, tagB))) break;
      __builtin_amdgcn_s_sleep(32);
    }
    const float M = max8(r0(g)), L = sum8(exp2_(r0(g) - M) * r1(g));
    if (lane < 8) lds[L_WG + lane] = exp2_(ml[0] - M) / L;
    cnt_arrive(lds, C_WG, lane);
  }
  pace_wait(t0, T_K0 + T_MAIN - 100);
  __builtin_amdgcn_s_setprio(3);
  if (a == 2) {
    cnt_wait(lds, C_F2, NAUX);
    f32x2 v = *(LAS f32x2*)(lds + L_F2 + 2 * (lane & 31));
#pragma unroll
    for (int q = 1; q < 4; ++q) v += *(LAS f32x2*)(lds + L_F2 + q * 64 + 2 * (lane & 31));
    pace_wait(t0, T_K0 + T_MAIN + 60);
    st16_sc1(rws, lane < 32 ? WS_FFN + (ks * D + cg * 64) * 8 + lane * 16 : OOB, rec(v[0], v[1], tagA, tagB));
  }
  if (a == 0) {
    cnt_wait<false>(lds, C_END, NMAIN);
    f32x2 v = *(LAS f32x2*)(lds + L_MRG + 2 * lane);
#pragma unroll
    for (int w = 1; w < 8; ++w) v += *(LAS f32x2*)(lds + L_MRG + w * 128 + 2 * lane);
    st16_sc1(rws, WS_Q + (h * 8 + s) * 1024 + lane * 16, rec(v[0], v[1], tagC, tagD));
  } else {
    cnt_wait(lds, C_END, NMAIN);
  }
  u32x4 bfr[2][4];
  cnt_wait<false>(lds, C_WT, NMAIN);
#pragma unroll
  for (int q = 0; q < 4; ++q)
#pragma unroll
    for (int d = 0; d < 4; ++d) {
      const unsigned w0 = *(LAS unsigned*)(lds + L_WT + (32 * q + 8 * r + 2 * d) * WT_STR + 16 * a + c);
      const unsigned w1 = *(LAS unsigned*)(lds + L_WT + (32 * q + 8 * r + 2 * d + 1) * WT_STR + 16 * a + c);
      bfr[0][q][d] = __builtin_amdgcn_perm(w1, w0, 0x05040100u);
      bfr[1][q][d] = __builtin_amdgcn_perm(w1, w0, 0x07060302u);
    }
  {
    u32x4 g0, g1;
    const int vo = r * 1024 + (16 * a + c) * 16;
#pragma unroll 1
    for (unsigned sp = 0; sp < SPIN_MEM; ++sp) {
      REREAD();
      g0 = ld16_sc1(rws, vo, WS_Q + h * 8 * 1024);
      g1 = ld16_sc1(rws, vo, WS_Q + (h * 8 + 4) * 1024);
      if (__all(rok(g0, tagC, tagD) && rok(g1, tagC, tagD))) break;
    }
    float o = fold32(r0(g0) + r0(g1), r1(g0) + r1(g1));
    o = fold16(o, o);
    if ((r & 1) == 0) *((LAS u16*)(lds + L_OB) + 32 * a + 2 * c + (r >> 1)) = __builtin_bit_cast(u16, (__bf16)o);
  }
  cnt_arrive(lds, C_OB, lane);
  cnt_wait<false>(lds, C_OB, NAUX);
  {
    f32x4 y0 = {0.f, 0.f, 0.f, 0.f}, y1 = y0;
#pragma unroll
    for (int q = 0; q < 4; ++q) {
      const u32x4 af = *(LAS u32x4*)(lds + L_OB + 16 * q + 4 * r);
      y0 = __builtin_amdgcn_mfma_f32_16x16x32_bf16(__builtin_bit_cast(bf16x8, af), __builtin_bit_cast(bf16x8, bfr[0][q]), y0, 0, 0, 0);
      y1 = __builtin_amdgcn_mfma_f32_16x16x32_bf16(__builtin_bit_cast(bf16x8, af), __builtin_bit_cast(bf16x8, bfr[1][q]), y1, 0, 0, 0);
    }
    st16_sc1(rws, lane < 16 ? WS_Y + (h * 8 + s) * 1024 + (16 * a + lane) * 16 : OOB, rec(y0[0], y1[0], tagA, tagB));
  }
  if (a == 0) {
    u32x4 g, gf;
    pace_wait((unsigned)__builtin_amdgcn_s_memrealtime(), 25);
#pragma unroll 1
    for (unsigned sp = 0; sp < SPIN_MEM; ++sp) {
      REREAD();
      g = ld16_sc1(rws, (lane >> 1) * (8 * 1024) + (lane & 1) * 16, WS_Y + s * 1024 + h * 32);
      gf = ld16_sc1(rws, ((lane >> 1) & 15) * (D * 8) + (lane & 1) * 16, WS_FFN + col0 * 8);
      if (__all(rok(g, tagA, tagB) && rok(gf, tagA, tagB))) break;
    }
    const float f0 = rbf(parity_sum(r0(gf)) * 0.5f), f1v = rbf(parity_sum(r1(gf)) * 0.5f);
    const float a0 = rbf(parity_sum(r0(g))), a1 = rbf(parity_sum(r1(g)));
    const unsigned o = pack2(rbf(lo2f(xw) + a0) + f0, rbf(hi2f(xw) + a1) + f1v);
    __builtin_amdgcn_raw_buffer_store_b32(o, mkrsrc(out, D * 2), lane < 2 ? (col0 + 2 * lane) * 2 : OOB, 0, SC1);
    __builtin_amdgcn_raw_buffer_store_b64(u32x2{(unsigned)e2, (unsigned)(e2 >> 32)}, rws, (bid == 0 && lane == 0) ? WS_EPOCH : OOB, 0, SC1);
  }
}

extern "C" void kernel_launch(void* const* d_in, const int* in_sizes, int n_in, void* d_out, int out_size, void* d_ws, size_t ws_size,
                              hipStream_t stream) {
  fused_decode_kernel<<<256, NTHR, 0, stream>>>((const u16*)d_in[0], (const u16*)d_in[1], (const u16*)d_in[2], (const u16*)d_in[3],
                                                 (const u16*)d_in[4], (const u16*)d_in[6], (const u16*)d_in[7], (const u16*)d_in[8],
                                                 (u16*)d_out, d_ws);
}
```

```cpp
#define DG 11
#include <hip/hip_runtime.h>
#include <stdint.h>

typedef unsigned u32x4 __attribute__((ext_vector_type(4)));
typedef unsigned u32x2 __attribute__((ext_vector_type(2)));
typedef float f32x4 __attribute__((ext_vector_type(4)));
typedef float f32x2 __attribute__((ext_vector_type(2)));
typedef __bf16 bf16x2 __attribute__((ext_vector_type(2)));
typedef __bf16 bf16x8 __attribute__((ext_vector_type(8)));
typedef unsigned short u16;
#define LAS __attribute__((address_space(3)))
typedef __amdgpu_buffer_rsrc_t rsrc_t;

constexpr int T_F1 = 150;
constexpr int T_W2 = 360;
constexpr int T_K0 = 460;
constexpr int T_MAIN = 7090;
constexpr int PACE_G = 2;
constexpr int T_QPOLL = 390;
constexpr int T_HA = 1600, T_HB = 2800;

constexpr int D = 1024, H = 32, SK = 32768, DH = 128, F = 3584;
constexpr int NMAIN = 8, NAUX = 4, NTHR = (NMAIN + NAUX) * 64;
constexpr int NT = 2, SC1 = 16;
constexpr int OOB = 0x40000000;
constexpr unsigned WS_EPOCH = 0, WS_T0 = 1024, WS_ML = 4096, WS_Q = WS_ML + 256 * 128, WS_Y = WS_Q + 256 * 1024, WS_G1 = WS_Y + 256 * 1024,
                   WS_H = WS_G1 + 4 * F * 8, WS_FFN = WS_H + F * 4, WS_END = WS_FFN + 16 * D * 8;
constexpr int L_XN = 0, L_QP = 1024, L_QS = L_QP + 4 * 128, L_MRG = L_QS + 64, L_ML = L_MRG + 8 * 128, L_WG = L_ML + 16, L_FP = L_WG + 8,
              L_H = L_FP + 8 * 64, L_F2 = L_H + 4 * 64, L_OB = L_F2 + 4 * 64, L_CNT = L_OB + 64, L_SC = L_CNT + 16, L_WT = L_SC + 8 * 512,
              WT_STR = 68, L_END = L_WT + 128 * WT_STR;
constexpr int C_Q = 0, C_XN = 1, C_F2 = 2, C_WT = 3, C_ML = 4, C_WG = 5, C_END = 6, C_QR = 7, C_F1 = 8, C_OB = 9, C_LAG = 10, C_NUM = 11, W_T0 = 12;
constexpr unsigned SPIN_MEM = 1u << 20, SPIN_LDS = 1u << 23;

__device__ __forceinline__ float lo2f(unsigned w) { return __builtin_bit_cast(float, w << 16); }
__device__ __forceinline__ float hi2f(unsigned w) { return __builtin_bit_cast(float, w & 0xffff0000u); }
__device__ __forceinline__ float rbf(float x) { return (float)(__bf16)x; }
__device__ __forceinline__ unsigned pack2(float a, float b) { bf16x2 v = {(__bf16)a, (__bf16)b}; return __builtin_bit_cast(unsigned, v); }
__device__ __forceinline__ float dot2(unsigned k, unsigned q, float acc) {
  return __builtin_amdgcn_fdot2_f32_bf16(__builtin_bit_cast(bf16x2, k), __builtin_bit_cast(bf16x2, q), acc, false);
}
template <int CTRL> __device__ __forceinline__ float dpp(float x) {
  return __builtin_bit_cast(float, __builtin_amdgcn_mov_dpp(__builtin_bit_cast(int, x), CTRL, 0xf, 0xf, true));
}
constexpr int XOR1 = 0xB1, XOR2 = 0x4E, XOR7 = 0x141, ROR8 = 0x128, ROR4 = 0x124, BCAST = 0x150;
__device__ __forceinline__ float fold32(float a, float b) {
  auto t = __builtin_amdgcn_permlane32_swap(__float_as_uint(a), __float_as_uint(b), false, false);
  return __uint_as_float(t[0]) + __uint_as_float(t[1]);
}
__device__ __forceinline__ float fold16(float a, float b) {
  auto t = __builtin_amdgcn_permlane16_swap(__float_as_uint(a), __float_as_uint(b), false, false);
  return __uint_as_float(t[0]) + __uint_as_float(t[1]);
}
__device__ __forceinline__ f32x2 fold_rows(const float (&v)[8]) {
  const float u0 = fold32(v[0], v[4]), u1 = fold32(v[1], v[5]), u2 = fold32(v[2], v[6]), u3 = fold32(v[3], v[7]);
  return f32x2{fold16(u0, u2), fold16(u1, u3)};
}
__device__ __forceinline__ float xrow_sum(float x) { x = fold16(x, x); return fold32(x, x); }
__device__ __forceinline__ float xrow_max(float x) {
  auto s = __builtin_amdgcn_permlane16_swap(__float_as_uint(x), __float_as_uint(x), false, false);
  x = fmaxf(__uint_as_float(s[0]), __uint_as_float(s[1]));
  auto t = __builtin_amdgcn_permlane32_swap(__float_as_uint(x), __float_as_uint(x), false, false);
  return fmaxf(__uint_as_float(t[0]), __uint_as_float(t[1]));
}
__device__ __forceinline__ float parity_sum(float x) {
  x += dpp<XOR2>(x); x += dpp<ROR4>(x); x += dpp<ROR8>(x); return xrow_sum(x);
}
__device__ __forceinline__ float wave_sum(float x) { x += dpp<XOR1>(x); return parity_sum(x); }
__device__ __forceinline__ float max8(float x) { x = fmaxf(x, dpp<XOR1>(x)); x = fmaxf(x, dpp<XOR2>(x)); return fmaxf(x, dpp<XOR7>(x)); }
__device__ __forceinline__ float sum8(float x) { x += dpp<XOR1>(x); x += dpp<XOR2>(x); return x + dpp<XOR7>(x); }
__device__ __forceinline__ float wave_max(float x) { x = max8(x); x = fmaxf(x, dpp<ROR8>(x)); return xrow_max(x); }
__device__ __forceinline__ float exp2_(float x) { return __builtin_amdgcn_exp2f(x); }
__device__ __forceinline__ rsrc_t mkrsrc(const void* p, unsigned bytes) { return __builtin_amdgcn_make_buffer_rsrc((void*)p, 0, (int)bytes, 0x00020000); }
__device__ __forceinline__ u32x4 ld16(rsrc_t r, int voff, int soff) { return __builtin_amdgcn_raw_buffer_load_b128(r, voff, soff, 0); }
__device__ __forceinline__ u32x4 ld16_nt(rsrc_t r, int voff, int soff) { return __builtin_amdgcn_raw_buffer_load_b128(r, voff, soff, NT); }
__device__ __forceinline__ u32x4 ld16_sc1(rsrc_t r, int voff, int soff) { return __builtin_amdgcn_raw_buffer_load_b128(r, voff, soff, SC1); }
__device__ __forceinline__ void st16_sc1(rsrc_t r, int off, u32x4 v) { __builtin_amdgcn_raw_buffer_store_b128(v, r, off, 0, SC1); }
#define SB() __builtin_amdgcn_sched_barrier(0)
#define REREAD() asm volatile("" ::: "memory")
__device__ __forceinline__ void pace_wait(unsigned t0, int due) {
  asm volatile("1:\n\ts_memrealtime s[96:97]\n\ts_waitcnt lgkmcnt(0)\n\ts_sub_u32 s96, s96, %0\n\ts_cmp_lt_i32 s96, %1\n\t"
               "s_cbranch_scc0 2f\n\ts_sleep 1\n\ts_branch 1b\n2:"
               :: "s"(t0), "s"(due) : "s96", "s97", "scc", "memory");
}
__device__ __forceinline__ void fma8(float (&acc)[8], float s, u32x4 w) {
#pragma unroll
  for (int t = 0; t < 4; ++t) { acc[2 * t] = fmaf(s, lo2f(w[t]), acc[2 * t]); acc[2 * t + 1] = fmaf(s, hi2f(w[t]), acc[2 * t + 1]); }
}
__device__ __forceinline__ u32x4 rec(float a, float b, unsigned ta, unsigned tb) { return u32x4{__float_as_uint(a), ta, __float_as_uint(b), tb}; }
__device__ __forceinline__ bool rok(u32x4 g, unsigned ta, unsigned tb) { return g[1] == ta && g[3] == tb; }
__device__ __forceinline__ float r0(u32x4 g) { return __uint_as_float(g[0]); }
__device__ __forceinline__ float r1(u32x4 g) { return __uint_as_float(g[2]); }

__device__ __forceinline__ void cnt_arrive(LAS float* lds, int which, int lane) {
  asm volatile("s_waitcnt lgkmcnt(0)" ::: "memory");
  if (lane == 0) __hip_atomic_fetch_add((LAS unsigned*)(lds + L_CNT + which), 1u, __ATOMIC_RELAXED, __HIP_MEMORY_SCOPE_WORKGROUP);
}
template <bool SLEEP = true>
__device__ __forceinline__ void cnt_wait(LAS float* lds, int which, unsigned n) {
#pragma unroll 1
  for (unsigned sp = 0; sp < SPIN_LDS; ++sp) {
    if (__builtin_amdgcn_readfirstlane(*(volatile LAS unsigned*)(lds + L_CNT + which)) >= n) break;
    if (SLEEP) __builtin_amdgcn_s_sleep(1);
  }
  REREAD();
}

__global__ void __launch_bounds__(NTHR, 3)
fused_decode_kernel(const u16* __restrict__ x, const u16* __restrict__ Kc, const u16* __restrict__ Vc, const u16* __restrict__ wq,
                    const u16* __restrict__ wo, const u16* __restrict__ wg, const u16* __restrict__ wu, const u16* __restrict__ wd,
                    u16* __restrict__ out, void* ws) {
  __shared__ __attribute__((aligned(16))) float lds_[L_END];
  LAS float* lds = (LAS float*)lds_;
  const unsigned t0 = (unsigned)__builtin_amdgcn_s_memrealtime();
  const int tid = threadIdx.x, lane = tid & 63, wave = __builtin_amdgcn_readfirstlane(tid >> 6);
  const int r = lane >> 4, c = lane & 15;
  const int bid = blockIdx.x, xl = bid & 7, j = bid >> 3;
  const int h = xl * 4 + (j >> 3), s = j & 7;
  const int f1 = xl * 28 + j, mat = f1 / 112, rb = (f1 / 56) & 1, cb = f1 % 56;

  if (wave < NMAIN) {
    const rsrc_t rk = mkrsrc(Kc, (unsigned)H * SK * DH * 2), rv = mkrsrc(Vc, (unsigned)H * SK * DH * 2);
    u32x4 bA[16], bB[16];
    const unsigned kvbase = (unsigned)h * (8u << 20) + (unsigned)s * (1u << 20) + wave * 1024;
    const unsigned rot = 32u * ((h + (h >> 2) + (h >> 4)) & 3);
    const int kvoff = lane * 16;
    constexpr int STEP = (T_MAIN << 16) / 256 * PACE_G;
    int due = T_K0 * 65536 - STEP + (STEP / 256) * (wave * 32 + j);
#define PACE(T0, I) do { if ((I) % PACE_G == 0) { due += STEP; pace_wait(T0, due >> 16); } } while (0)
#define LAGAT(G, T0) do { if ((G) == DG) { const int l_ = (int)((unsigned)__builtin_amdgcn_s_memrealtime() - (T0)) - (due >> 16); \
      if (lane == 0) __hip_atomic_fetch_max((LAS int*)(lds + L_CNT + C_LAG), l_, __ATOMIC_RELAXED, __HIP_MEMORY_SCOPE_WORKGROUP); } } while (0)
    __builtin_amdgcn_s_barrier();
    SB();
    {
      const rsrc_t rg = mkrsrc(mat ? wu : wg, D * F * 2);
      const int voff = j < 28 ? (lane >> 3) * (F * 2) + (lane & 7) * 16 : OOB, sb = (rb * 512 + wave * 64) * (F * 2) + cb * 128;
      constexpr int FSTEP = ((T_W2 - T_F1) << 16) / 8;
      int fdue = T_F1 * 65536 + (FSTEP / 256) * (wave * 32 + j);
#pragma unroll
      for (int i = 0; i < 8; ++i) { pace_wait(t0, fdue >> 16); fdue += FSTEP; bB[i] = ld16_nt(rg, voff, sb + i * 8 * (F * 2)); SB(); }
    }
#pragma unroll
    for (int i = 0; i < 16; ++i) { PACE(t0, i); bA[i] = ld16_nt(rk, kvoff, kvbase + (rot + i) * 8192); SB(); }
    {
      cnt_wait(lds, C_XN, 1);
      float ag[8] = {0.f, 0.f, 0.f, 0.f, 0.f, 0.f, 0.f, 0.f};
#pragma unroll
      for (int i = 0; i < 8; ++i) {
        const int k = rb * 512 + wave * 64 + 8 * i + (lane >> 3);
        fma8(ag, lds[L_XN + (k & 3) * 256 + (k >> 2)], bB[i]);
      }
#pragma unroll
      for (int e = 0; e < 8; ++e) ag[e] += dpp<ROR8>(ag[e]);
      const f32x2 z = fold_rows(ag);
      if (c < 8) *(LAS f32x2*)(lds + L_FP + wave * 64 + 8 * c + 2 * r) = z;
      cnt_arrive(lds, C_F1, lane);
      SB();
    }
#pragma unroll
    for (int i = 0; i < 16; ++i) { PACE(t0, i); bB[i] = ld16_nt(rk, kvoff, kvbase + (rot + 16 + i) * 8192); SB(); }
    LAGAT(1, t0);
    cnt_wait(lds, C_QR, 1);
    const u32x4 qp = *(LAS u32x4*)(lds + L_QS + 4 * c);
    const unsigned t0m = __builtin_amdgcn_readfirstlane(*(LAS unsigned*)(lds + L_CNT + W_T0));
    constexpr float SCALE = 0.08838834764831845f, LOG2E = 1.4426950408889634f;
    LAS float* scp = lds + L_SC + wave * 512 + lane * 8;
    float mx = -INFINITY;
    auto kgroup = [&](u32x4 (&kb)[16], const bool more, const rsrc_t rn, const unsigned tb, const int g) __attribute__((always_inline)) {
      float sx[16];
#pragma unroll
      for (int i = 0; i < 16; ++i) {
        float d = 0.f;
#pragma unroll
        for (int t = 0; t < 4; ++t) d = dot2(kb[i][t], qp[t], d);
        asm volatile("" : "+v"(d));
        sx[i] = d;
        SB();
        if (more) { PACE(t0m, i); kb[i] = ld16_nt(rn, kvoff, tb + i * 8192); SB(); }
      }
#define TR_STEP(HALF, CTRL, BIT) _Pragma("unroll") for (int hh = 0; hh < (HALF); ++hh) { \
        const float lo_ = sx[hh], hi_ = sx[hh + (HALF)]; const float t1_ = lo_ + dpp<CTRL>(lo_), t2_ = hi_ + dpp<CTRL>(hi_); \
        sx[hh] = (c & (BIT)) ? t2_ : t1_; }
      TR_STEP(8, ROR8, 8) TR_STEP(4, XOR7, 4) TR_STEP(2, XOR2, 2) TR_STEP(1, XOR1, 1)
#undef TR_STEP
      const float sc = rbf(rbf(sx[0]) * SCALE) * LOG2E;
      mx = fmaxf(mx, sc);
      scp[g] = sc;
      SB();
    };
    kgroup(bA, false, rk, 0, 0);
#pragma unroll 1
    for (int g = 1; g < 8; ++g) {
      kgroup(bB, true, mkrsrc(g == 7 ? Vc : Kc, (unsigned)H * SK * DH * 2), kvbase + (((g + 1) * 16 + rot) & 127u) * 8192, g);
      LAGAT(g + 1, t0m); }
    {
      const float m = wave_max(mx);
      f32x4 p0 = *(LAS f32x4*)scp, p1 = *(LAS f32x4*)(scp + 4);
      float l = 0.f;
#pragma unroll
      for (int e = 0; e < 4; ++e) { p0[e] = exp2_(p0[e] - m); p1[e] = exp2_(p1[e] - m); l += p0[e] + p1[e]; }
      *(LAS f32x4*)scp = p0; *(LAS f32x4*)(scp + 4) = p1;
      l = wave_sum(l);
      if (lane == 0) *(LAS f32x2*)(lds + L_ML + 2 * wave) = f32x2{m, l};
      cnt_arrive(lds, C_ML, lane);
    }
    float acc[8] = {0.f, 0.f, 0.f, 0.f, 0.f, 0.f, 0.f, 0.f};
    auto vgroup = [&](const bool more, const unsigned tb, const float p) __attribute__((always_inline)) {
#define PV_STEP(I) { fma8(acc, dpp<BCAST + (I)>(p), bB[I]); \
        asm volatile("" : "+v"(acc[0]), "+v"(acc[1]), "+v"(acc[2]), "+v"(acc[3]), "+v"(acc[4]), "+v"(acc[5]), "+v"(acc[6]), "+v"(acc[7])); SB(); \
        if (more) { PACE(t0m, I); bB[I] = ld16_nt(rv, kvoff, tb + (I) * 8192); SB(); } \
        else if ((I) == 11) {   \
          pace_wait(t0m, T_K0 + T_MAIN); \
          _Pragma("unroll") for (int u = 0; u < 4; ++u) { \
            bB[u] = ld16_nt(mkrsrc(wo, H * DH * D * 2), r * 2048 + c * 16, (h * 128 + wave * 16 + 4 * u) * 2048 + s * 256); SB(); } } }
      PV_STEP(0) PV_STEP(1) PV_STEP(2) PV_STEP(3) PV_STEP(4) PV_STEP(5) PV_STEP(6) PV_STEP(7)
      PV_STEP(8) PV_STEP(9) PV_STEP(10) PV_STEP(11) PV_STEP(12) PV_STEP(13) PV_STEP(14) PV_STEP(15)
#undef PV_STEP
    };
#pragma unroll 1
    for (int g = 0; g < 7; ++g) { vgroup(true, kvbase + (((g + 1) * 16 + rot) & 127u) * 8192, scp[g]); LAGAT(g + 9, t0m); }
    __builtin_amdgcn_s_setprio(3);
    cnt_wait(lds, C_WG, 1);
    {
      const float wgt = lds[L_WG + wave];
#pragma unroll
      for (int e = 0; e < 8; ++e) acc[e] *= wgt;
      vgroup(false, 0, scp[7] * wgt);
    }
    *(LAS f32x2*)(lds + L_MRG + wave * 128 + 8 * c + 2 * r) = fold_rows(acc);
    cnt_arrive(lds, C_END, lane);
#pragma unroll
    for (int u = 0; u < 4; ++u) *(LAS u32x4*)(lds + L_WT + (wave * 16 + 4 * u + r) * WT_STR + 4 * c) = bB[u];
    cnt_arrive(lds, C_WT, lane);
    return;
#undef PACE
  }

  const int a = wave - NMAIN;
  const rsrc_t rx = mkrsrc(x, D * 2), rws = mkrsrc(ws, WS_END);
  unsigned tagA, tagB, tagC, tagD;
  unsigned long long e2;
  {
    u32x4 xa = ld16(rx, lane * 32, 0), xb = ld16(rx, lane * 32 + 16, 0);
    const u32x2 ep = __builtin_amdgcn_raw_buffer_load_b64(rws, 0, WS_EPOCH, SC1);
    const rsrc_t rwq = mkrsrc(wq, D * H * DH * 2);
    u32x4 w[8];
    const int voff = r * 8192 + c * 16, sb = (s * 128 + a * 32) * 8192 + h * 256;
#pragma unroll
    for (int i = 0; i < 8; ++i) { w[i] = ld16_nt(rwq, voff, sb + i * 32768); SB(); }
    if (a == 0 && lane < C_NUM) *(LAS unsigned*)(lds + L_CNT + lane) = 0u;
    __syncthreads();
    asm volatile("" : "+v"(xa), "+v"(xb));
    float v[16];
#pragma unroll
    for (int i = 0; i < 4; ++i) { v[2 * i] = lo2f(xa[i]); v[2 * i + 1] = hi2f(xa[i]); v[8 + 2 * i] = lo2f(xb[i]); v[9 + 2 * i] = hi2f(xb[i]); }
    float sm = 0.f;
#pragma unroll
    for (int i = 0; i < 16; ++i) sm += v[i];
    const float mean = wave_sum(sm) * (1.f / D);
    float ss = 0.f;
#pragma unroll
    for (int i = 0; i < 16; ++i) { const float d = v[i] - mean; ss = fmaf(d, d, ss); }
    const float var = wave_sum(ss) * (1.f / D);
    const float mb = rbf(mean), den = rbf(sqrtf(rbf(rbf(var) + 1e-5f)));
    const float rinv = 1.0f / den;
#pragma unroll
    for (int i = 0; i < 16; ++i) {
      const float nu = rbf(v[i] - mb), q0 = nu * rinv;
      v[i] = rbf(fmaf(fmaf(-q0, den, nu), rinv, q0));
    }
#pragma unroll
    for (int q = 0; q < 4; ++q) *(LAS f32x4*)(lds + L_XN + q * 256 + 4 * lane) = f32x4{v[q], v[4 + q], v[8 + q], v[12 + q]};
    cnt_arrive(lds, C_XN, lane);
    const unsigned long long e1 = (((unsigned long long)__builtin_amdgcn_readfirstlane(ep[1]) << 32) | (unsigned)__builtin_amdgcn_readfirstlane(ep[0])) + 1ull;
    e2 = e1 + 1ull;
    tagA = (unsigned)e1; tagB = (unsigned)(e1 >> 32) + (unsigned)e1 * 0x9E3779B1u;
    tagC = (unsigned)e2; tagD = (unsigned)(e2 >> 32) + (unsigned)e2 * 0x9E3779B1u;
    float acc[8] = {0.f, 0.f, 0.f, 0.f, 0.f, 0.f, 0.f, 0.f};
#pragma unroll
    for (int q = 0; q < 2; ++q) {
      const f32x4 xv = *(LAS f32x4*)(lds + L_XN + r * 256 + s * 32 + a * 8 + 4 * q);
#pragma unroll
      for (int e = 0; e < 4; ++e) fma8(acc, xv[e], w[4 * q + e]);
    }
    *(LAS f32x2*)(lds + L_QP + a * 128 + 8 * c + 2 * r) = fold_rows(acc);
  }
  cnt_arrive(lds, C_Q, lane);
  if (a == 0) {
    cnt_wait<false>(lds, C_Q, NAUX);
    f32x2 v = *(LAS f32x2*)(lds + L_QP + 2 * lane);
#pragma unroll
    for (int q = 1; q < 4; ++q) v += *(LAS f32x2*)(lds + L_QP + q * 128 + 2 * lane);
    st16_sc1(rws, WS_Q + (h * 8 + s) * 1024 + lane * 16, rec(v[0], v[1], tagA, tagB));
    st16_sc1(rws, (bid == 0 && lane == 0) ? WS_T0 : OOB, u32x4{t0, tagA, t0, tagB});
  }
  if (a == 1) {
    pace_wait(t0, T_QPOLL);
    float q0 = 0.f, q1 = 0.f;
    unsigned tg = t0;
#pragma unroll 1
    for (unsigned sp = 0; sp < SPIN_MEM; ++sp) {
      REREAD();
      u32x4 g[8];
#pragma unroll
      for (int i = 0; i < 8; ++i) g[i] = ld16_sc1(rws, lane * 16, WS_Q + (h * 8 + i) * 1024);
      const u32x4 gt = ld16_sc1(rws, 0, WS_T0);
      bool ok = rok(gt, tagA, tagB);
      tg = gt[0];
      q0 = 0.f; q1 = 0.f;
#pragma unroll
      for (int i = 0; i < 8; ++i) { ok = ok && rok(g[i], tagA, tagB); q0 += r0(g[i]); q1 += r1(g[i]); }
      if (__all(ok)) break;
      __builtin_amdgcn_s_sleep(2);
    }
    *(LAS unsigned*)(lds + L_QS + lane) = pack2(q0, q1);
    if (lane == 0) *(LAS unsigned*)(lds + L_CNT + W_T0) = ((int)(t0 - tg) < 0 || (int)(t0 - tg) > 300) ? t0 : tg;
    cnt_arrive(lds, C_QR, lane);
  }
  const int ks = xl * 2 + (j >> 4), cg = j & 15;
  u32x4 dw[7];
  {
    const rsrc_t rd = mkrsrc(wd, D * F * 2);
    const int voff = (lane >> 3) * 2048 + (lane & 7) * 16, sb = (ks * 224 + a * 56) * 2048 + cg * 128;
    constexpr int WSTEP = ((T_K0 - T_W2) << 16) / 7;
    int wdue = T_W2 * 65536 + (WSTEP / 128) * (a * 32 + j);
#pragma unroll
    for (int i = 0; i < 7; ++i) { pace_wait(t0, wdue >> 16); wdue += WSTEP; dw[i] = ld16_nt(rd, voff, sb + i * 16384); SB(); }
  }
  if (a == 0 && j < 28) {
    cnt_wait(lds, C_F1, NMAIN);
    f32x2 v = *(LAS f32x2*)(lds + L_FP + 2 * (lane & 31));
#pragma unroll
    for (int q = 1; q < 8; ++q) v += *(LAS f32x2*)(lds + L_FP + q * 64 + 2 * (lane & 31));
    if (f1 >= 56) {
      st16_sc1(rws, lane < 32 ? WS_G1 + ((mat * 2 + rb) * F + cb * 64) * 8 + lane * 16 : OOB, rec(v[0], v[1], tagA, tagB));
    } else {
      pace_wait(t0, T_K0 + T_HA);
      u32x4 g[3];
#pragma unroll 1
      for (unsigned sp = 0; sp < SPIN_MEM; ++sp) {
        REREAD();
#pragma unroll
        for (int k = 0; k < 3; ++k) g[k] = ld16_sc1(rws, (lane & 31) * 16, WS_G1 + ((k + 1) * F + cb * 64) * 8);
        if (__all(rok(g[0], tagA, tagB) && rok(g[1], tagA, tagB) && rok(g[2], tagA, tagB))) break;
        __builtin_amdgcn_s_sleep(32);
      }
      float hh[2];
#pragma unroll
      for (int e = 0; e < 2; ++e) {
        const float gs = rbf(v[e] + __uint_as_float(g[0][2 * e]));
        const float us = rbf(__uint_as_float(g[1][2 * e]) + __uint_as_float(g[2][2 * e]));
        const float ge = 0.5f * gs * (1.f + tanhf(0.7978845608028654f * (gs + 0.044715f * gs * gs * gs)));
        hh[e] = rbf(ge) * us;
      }
      const float hp = __uint_as_float(pack2(hh[0], hh[1]));
      st16_sc1(rws, (lane < 32 && !(lane & 1)) ? WS_H + cb * 256 + lane * 8 : OOB, rec(hp, dpp<XOR1>(hp), tagA, tagB));
    }
  }
  {
    pace_wait(t0, T_K0 + T_HB);
    u32x4 g;
#pragma unroll 1
    for (unsigned sp = 0; sp < SPIN_MEM; ++sp) {
      REREAD();
      g = ld16_sc1(rws, (lane < 14 ? lane : 13) * 16, WS_H + (ks * 56 + a * 14) * 16);
      if (__all(rok(g, tagA, tagB))) break;
      __builtin_amdgcn_s_sleep(32);
    }
    if (lane < 14) *(LAS f32x4*)(lds + L_H + a * 64 + 4 * lane) = f32x4{lo2f(g[0]), hi2f(g[0]), lo2f(g[2]), hi2f(g[2])};
    float y[8] = {0.f, 0.f, 0.f, 0.f, 0.f, 0.f, 0.f, 0.f};
#pragma unroll
    for (int i = 0; i < 7; ++i) fma8(y, lds[L_H + a * 64 + 8 * i + (lane >> 3)], dw[i]);
#pragma unroll
    for (int e = 0; e < 8; ++e) y[e] += dpp<ROR8>(y[e]);
    const f32x2 z = fold_rows(y);
    if (c < 8) *(LAS f32x2*)(lds + L_F2 + a * 64 + 8 * c + 2 * r) = z;
    cnt_arrive(lds, C_F2, lane);
  }
  const int col0 = s * 128 + 4 * h;
  unsigned xw = 0;
  if (a == 0) xw = __builtin_amdgcn_raw_buffer_load_b32(rx, (col0 + 2 * (lane & 1)) * 2, 0, 0);
  if (a == 3) {
    const unsigned po = __builtin_amdgcn_raw_buffer_load_b32(mkrsrc(out, D * 2), (col0 + 2 * (lane & 1)) * 2, 0, SC1);
    asm volatile("" :: "v"(po));
  }
  if (a == 1) {
    cnt_wait(lds, C_ML, NMAIN);
    const f32x2 ml = *(LAS f32x2*)(lds + L_ML + 2 * (lane & 7));
    const float Mw = max8(ml[0]), Lw = sum8(exp2_(ml[0] - Mw) * ml[1]);
    st16_sc1(rws, lane == 0 ? WS_ML + (h * 8 + s) * 128 : OOB, rec(Mw, Lw, tagA, tagB));
    pace_wait(t0, T_K0 + T_MAIN / 2 + 1500);
    u32x4 g;
#pragma unroll 1
    for (unsigned sp = 0; sp < SPIN_MEM; ++sp) {
      REREAD();
      g = ld16_sc1(rws, (lane & 7) * 128, WS_ML + h * 8 * 128);
      if (__all(rok(g, tagA, tagB))) break;
      __builtin_amdgcn_s_sleep(32);
    }
    const float M = max8(r0(g)), L = sum8(exp2_(r0(g) - M) * r1(g));
    if (lane < 8) lds[L_WG + lane] = exp2_(ml[0] - M) / L;
    cnt_arrive(lds, C_WG, lane);
  }
  pace_wait(t0, T_K0 + T_MAIN - 100);
  __builtin_amdgcn_s_setprio(3);
  if (a == 2) {
    cnt_wait(lds, C_F2, NAUX);
    f32x2 v = *(LAS f32x2*)(lds + L_F2 + 2 * (lane & 31));
#pragma unroll
    for (int q = 1; q < 4; ++q) v += *(LAS f32x2*)(lds + L_F2 + q * 64 + 2 * (lane & 31));
    pace_wait(t0, T_K0 + T_MAIN + 60);
    st16_sc1(rws, lane < 32 ? WS_FFN + (ks * D + cg * 64) * 8 + lane * 16 : OOB, rec(v[0], v[1], tagA, tagB));
  }
  if (a == 0) {
    cnt_wait<false>(lds, C_END, NMAIN);
    f32x2 v = *(LAS f32x2*)(lds + L_MRG + 2 * lane);
#pragma unroll
    for (int w = 1; w < 8; ++w) v += *(LAS f32x2*)(lds + L_MRG + w * 128 + 2 * lane);
    st16_sc1(rws, WS_Q + (h * 8 + s) * 1024 + lane * 16, rec(v[0], v[1], tagC, tagD));
  } else {
    cnt_wait(lds, C_END, NMAIN);
  }
  u32x4 bfr[2][4];
  cnt_wait<false>(lds, C_WT, NMAIN);
#pragma unroll
  for (int q = 0; q < 4; ++q)
#pragma unroll
    for (int d = 0; d < 4; ++d) {
      const unsigned w0 = *(LAS unsigned*)(lds + L_WT + (32 * q + 8 * r + 2 * d) * WT_STR + 16 * a + c);
      const unsigned w1 = *(LAS unsigned*)(lds + L_WT + (32 * q + 8 * r + 2 * d + 1) * WT_STR + 16 * a + c);
      bfr[0][q][d] = __builtin_amdgcn_perm(w1, w0, 0x05040100u);
      bfr[1][q][d] = __builtin_amdgcn_perm(w1, w0, 0x07060302u);
    }
  {
    u32x4 g0, g1;
    const int vo = r * 1024 + (16 * a + c) * 16;
#pragma unroll 1
    for (unsigned sp = 0; sp < SPIN_MEM; ++sp) {
      REREAD();
      g0 = ld16_sc1(rws, vo, WS_Q + h * 8 * 1024);
      g1 = ld16_sc1(rws, vo, WS_Q + (h * 8 + 4) * 1024);
      if (__all(rok(g0, tagC, tagD) && rok(g1, tagC, tagD))) break;
    }
    float o = fold32(r0(g0) + r0(g1), r1(g0) + r1(g1));
    o = fold16(o, o);
    if ((r & 1) == 0) *((LAS u16*)(lds + L_OB) + 32 * a + 2 * c + (r >> 1)) = __builtin_bit_cast(u16, (__bf16)o);
  }
  cnt_arrive(lds, C_OB, lane);
  cnt_wait<false>(lds, C_OB, NAUX);
  {
    f32x4 y0 = {0.f, 0.f, 0.f, 0.f}, y1 = y0;
#pragma unroll
    for (int q = 0; q < 4; ++q) {
      const u32x4 af = *(LAS u32x4*)(lds + L_OB + 16 * q + 4 * r);
      y0 = __builtin_amdgcn_mfma_f32_16x16x32_bf16(__builtin_bit_cast(bf16x8, af), __builtin_bit_cast(bf16x8, bfr[0][q]), y0, 0, 0, 0);
      y1 = __builtin_amdgcn_mfma_f32_16x16x32_bf16(__builtin_bit_cast(bf16x8, af), __builtin_bit_cast(bf16x8, bfr[1][q]), y1, 0, 0, 0);
    }
    st16_sc1(rws, lane < 16 ? WS_Y + (h * 8 + s) * 1024 + (16 * a + lane) * 16 : OOB, rec(y0[0], y1[0], tagA, tagB));
  }
  const rsrc_t rdg = mkrsrc((char*)ws + (6u << 20), 8192);
  if (a == 2) st16_sc1(rdg, lane == 0 ? bid * 16 : OOB, u32x4{t0, tagA, *(LAS unsigned*)(lds + L_CNT + C_LAG), tagB});
  if (a == 0) {
    u32x4 g, gf;
    pace_wait((unsigned)__builtin_amdgcn_s_memrealtime(), 20);
#pragma unroll 1
    for (unsigned sp = 0; sp < SPIN_MEM; ++sp) {
      REREAD();
      g = ld16_sc1(rws, (lane >> 1) * (8 * 1024) + (lane & 1) * 16, WS_Y + s * 1024 + h * 32);
      gf = ld16_sc1(rws, ((lane >> 1) & 15) * (D * 8) + (lane & 1) * 16, WS_FFN + col0 * 8);
      if (__all(rok(g, tagA, tagB) && rok(gf, tagA, tagB))) break;
    }
    const float f0 = rbf(parity_sum(r0(gf)) * 0.5f), f1v = rbf(parity_sum(r1(gf)) * 0.5f);
    const float a0 = rbf(parity_sum(r0(g))), a1 = rbf(parity_sum(r1(g)));
    const unsigned o = pack2(rbf(lo2f(xw) + a0) + f0, rbf(hi2f(xw) + a1) + f1v);
    __builtin_amdgcn_raw_buffer_store_b32(o, mkrsrc(out, D * 2), lane < 2 ? (col0 + 2 * lane) * 2 : OOB, 0, SC1);
    __builtin_amdgcn_raw_buffer_store_b64(u32x2{(unsigned)e2, (unsigned)(e2 >> 32)}, rws, (bid == 0 && lane == 0) ? WS_EPOCH : OOB, 0, SC1);
    if (bid == 0) {
      u32x4 gg[4];
      for (unsigned sp = 0; sp < SPIN_MEM; ++sp) { REREAD();
        for (int k = 0; k < 4; ++k) gg[k] = ld16_sc1(rdg, lane * 16 + k * 1024, 0);
        if (__all(rok(gg[0], tagA, tagB) && rok(gg[1], tagA, tagB) && rok(gg[2], tagA, tagB) && rok(gg[3], tagA, tagB))) break; }
      int m2 = 0;
      for (int k = 0; k < 4; ++k) m2 += (int)gg[k][2];
      for (int o = 1; o < 64; o <<= 1) m2 += __shfl_xor(m2, o);
      int v = m2 / 256;
      v = v < 0 ? 0 : v > 1000 ? 1000 : v;
      pace_wait(t0, __builtin_amdgcn_readfirstlane(9500 + v * 20));
    }
  }
}

extern "C" void kernel_launch(void* const* d_in, const int* in_sizes, int n_in, void* d_out, int out_size, void* d_ws, size_t ws_size,
                              hipStream_t stream) {
  fused_decode_kernel<<<256, NTHR, 0, stream>>>((const u16*)d_in[0], (const u16*)d_in[1], (const u16*)d_in[2], (const u16*)d_in[3],
                                                 (const u16*)d_in[4], (const u16*)d_in[6], (const u16*)d_in[7], (const u16*)d_in[8],
                                                 (u16*)d_out, d_ws);
}
```

```cpp
#define DG 5
#include <hip/hip_runtime.h>
#include <stdint.h>

typedef unsigned u32x4 __attribute__((ext_vector_type(4)));
typedef unsigned u32x2 __attribute__((ext_vector_type(2)));
typedef float f32x4 __attribute__((ext_vector_type(4)));
typedef float f32x2 __attribute__((ext_vector_type(2)));
typedef __bf16 bf16x2 __attribute__((ext_vector_type(2)));
typedef __bf16 bf16x8 __attribute__((ext_vector_type(8)));
typedef unsigned short u16;
#define LAS __attribute__((address_space(3)))
typedef __amdgpu_buffer_rsrc_t rsrc_t;

constexpr int T_F1 = 150;
constexpr int T_W2 = 360;
constexpr int T_K0 = 460;
constexpr int T_MAIN = 7090;
constexpr int PACE_G = 2;
constexpr int T_QPOLL = 390;
constexpr int T_HA = 1600, T_HB = 2800;

constexpr int D = 1024, H = 32, SK = 32768, DH = 128, F = 3584;
constexpr int NMAIN = 8, NAUX = 4, NTHR = (NMAIN + NAUX) * 64;
constexpr int NT = 2, SC1 = 16;
constexpr int OOB = 0x40000000;
constexpr unsigned WS_EPOCH = 0, WS_T0 = 1024, WS_ML = 4096, WS_Q = WS_ML + 256 * 128, WS_Y = WS_Q + 256 * 1024, WS_G1 = WS_Y + 256 * 1024,
                   WS_H = WS_G1 + 4 * F * 8, WS_FFN = WS_H + F * 4, WS_END = WS_FFN + 16 * D * 8;
constexpr int L_XN = 0, L_QP = 1024, L_QS = L_QP + 4 * 128, L_MRG = L_QS + 64, L_ML = L_MRG + 8 * 128, L_WG = L_ML + 16, L_FP = L_WG + 8,
              L_H = L_FP + 8 * 64, L_F2 = L_H + 4 * 64, L_OB = L_F2 + 4 * 64, L_CNT = L_OB + 64, L_SC = L_CNT + 16, L_WT = L_SC + 8 * 512,
              WT_STR = 68, L_END = L_WT + 128 * WT_STR;
constexpr int C_Q = 0, C_XN = 1, C_F2 = 2, C_WT = 3, C_ML = 4, C_WG = 5, C_END = 6, C_QR = 7, C_F1 = 8, C_OB = 9, C_LAG = 10, C_NUM = 11, W_T0 = 12;
constexpr unsigned SPIN_MEM = 1u << 20, SPIN_LDS = 1u << 23;

__device__ __forceinline__ float lo2f(unsigned w) { return __builtin_bit_cast(float, w << 16); }
__device__ __forceinline__ float hi2f(unsigned w) { return __builtin_bit_cast(float, w & 0xffff0000u); }
__device__ __forceinline__ float rbf(float x) { return (float)(__bf16)x; }
__device__ __forceinline__ unsigned pack2(float a, float b) { bf16x2 v = {(__bf16)a, (__bf16)b}; return __builtin_bit_cast(unsigned, v); }
__device__ __forceinline__ float dot2(unsigned k, unsigned q, float acc) {
  return __builtin_amdgcn_fdot2_f32_bf16(__builtin_bit_cast(bf16x2, k), __builtin_bit_cast(bf16x2, q), acc, false);
}
template <int CTRL> __device__ __forceinline__ float dpp(float x) {
  return __builtin_bit_cast(float, __builtin_amdgcn_mov_dpp(__builtin_bit_cast(int, x), CTRL, 0xf, 0xf, true));
}
constexpr int XOR1 = 0xB1, XOR2 = 0x4E, XOR7 = 0x141, ROR8 = 0x128, ROR4 = 0x124, BCAST = 0x150;
__device__ __forceinline__ float fold32(float a, float b) {
  auto t = __builtin_amdgcn_permlane32_swap(__float_as_uint(a), __float_as_uint(b), false, false);
  return __uint_as_float(t[0]) + __uint_as_float(t[1]);
}
__device__ __forceinline__ float fold16(float a, float b) {
  auto t = __builtin_amdgcn_permlane16_swap(__float_as_uint(a), __float_as_uint(b), false, false);
  return __uint_as_float(t[0]) + __uint_as_float(t[1]);
}
__device__ __forceinline__ f32x2 fold_rows(const float (&v)[8]) {
  const float u0 = fold32(v[0], v[4]), u1 = fold32(v[1], v[5]), u2 = fold32(v[2], v[6]), u3 = fold32(v[3], v[7]);
  return f32x2{fold16(u0, u2), fold16(u1, u3)};
}
__device__ __forceinline__ float xrow_sum(float x) { x = fold16(x, x); return fold32(x, x); }
__device__ __forceinline__ float xrow_max(float x) {
  auto s = __builtin_amdgcn_permlane16_swap(__float_as_uint(x), __float_as_uint(x), false, false);
  x = fmaxf(__uint_as_float(s[0]), __uint_as_float(s[1]));
  auto t = __builtin_amdgcn_permlane32_swap(__float_as_uint(x), __float_as_uint(x), false, false);
  return fmaxf(__uint_as_float(t[0]), __uint_as_float(t[1]));
}
__device__ __forceinline__ float parity_sum(float x) {
  x += dpp<XOR2>(x); x += dpp<ROR4>(x); x += dpp<ROR8>(x); return xrow_sum(x);
}
__device__ __forceinline__ float wave_sum(float x) { x += dpp<XOR1>(x); return parity_sum(x); }
__device__ __forceinline__ float max8(float x) { x = fmaxf(x, dpp<XOR1>(x)); x = fmaxf(x, dpp<XOR2>(x)); return fmaxf(x, dpp<XOR7>(x)); }
__device__ __forceinline__ float sum8(float x) { x += dpp<XOR1>(x); x += dpp<XOR2>(x); return x + dpp<XOR7>(x); }
__device__ __forceinline__ float wave_max(float x) { x = max8(x); x = fmaxf(x, dpp<ROR8>(x)); return xrow_max(x); }
__device__ __forceinline__ float exp2_(float x) { return __builtin_amdgcn_exp2f(x); }
__device__ __forceinline__ rsrc_t mkrsrc(const void* p, unsigned bytes) { return __builtin_amdgcn_make_buffer_rsrc((void*)p, 0, (int)bytes, 0x00020000); }
__device__ __forceinline__ u32x4 ld16(rsrc_t r, int voff, int soff) { return __builtin_amdgcn_raw_buffer_load_b128(r, voff, soff, 0); }
__device__ __forceinline__ u32x4 ld16_nt(rsrc_t r, int voff, int soff) { return __builtin_amdgcn_raw_buffer_load_b128(r, voff, soff, NT); }
__device__ __forceinline__ u32x4 ld16_sc1(rsrc_t r, int voff, int soff) { return __builtin_amdgcn_raw_buffer_load_b128(r, voff, soff, SC1); }
__device__ __forceinline__ void st16_sc1(rsrc_t r, int off, u32x4 v) { __builtin_amdgcn_raw_buffer_store_b128(v, r, off, 0, SC1); }
#define SB() __builtin_amdgcn_sched_barrier(0)
#define REREAD() asm volatile("" ::: "memory")
__device__ __forceinline__ void pace_wait(unsigned t0, int due) {
  asm volatile("1:\n\ts_memrealtime s[96:97]\n\ts_waitcnt lgkmcnt(0)\n\ts_sub_u32 s96, s96, %0\n\ts_cmp_lt_i32 s96, %1\n\t"
               "s_cbranch_scc0 2f\n\ts_sleep 1\n\ts_branch 1b\n2:"
               :: "s"(t0), "s"(due) : "s96", "s97", "scc", "memory");
}
__device__ __forceinline__ void fma8(float (&acc)[8], float s, u32x4 w) {
#pragma unroll
  for (int t = 0; t < 4; ++t) { acc[2 * t] = fmaf(s, lo2f(w[t]), acc[2 * t]); acc[2 * t + 1] = fmaf(s, hi2f(w[t]), acc[2 * t + 1]); }
}
__device__ __forceinline__ u32x4 rec(float a, float b, unsigned ta, unsigned tb) { return u32x4{__float_as_uint(a), ta, __float_as_uint(b), tb}; }
__device__ __forceinline__ bool rok(u32x4 g, unsigned ta, unsigned tb) { return g[1] == ta && g[3] == tb; }
__device__ __forceinline__ float r0(u32x4 g) { return __uint_as_float(g[0]); }
__device__ __forceinline__ float r1(u32x4 g) { return __uint_as_float(g[2]); }

__device__ __forceinline__ void cnt_arrive(LAS float* lds, int which, int lane) {
  asm volatile("s_waitcnt lgkmcnt(0)" ::: "memory");
  if (lane == 0) __hip_atomic_fetch_add((LAS unsigned*)(lds + L_CNT + which), 1u, __ATOMIC_RELAXED, __HIP_MEMORY_SCOPE_WORKGROUP);
}
template <bool SLEEP = true>
__device__ __forceinline__ void cnt_wait(LAS float* lds, int which, unsigned n) {
#pragma unroll 1
  for (unsigned sp = 0; sp < SPIN_LDS; ++sp) {
    if (__builtin_amdgcn_readfirstlane(*(volatile LAS unsigned*)(lds + L_CNT + which)) >= n) break;
    if (SLEEP) __builtin_amdgcn_s_sleep(1);
  }
  REREAD();
}

__global__ void __launch_bounds__(NTHR, 3)
fused_decode_kernel(const u16* __restrict__ x, const u16* __restrict__ Kc, const u16* __restrict__ Vc, const u16* __restrict__ wq,
                    const u16* __restrict__ wo, const u16* __restrict__ wg, const u16* __restrict__ wu, const u16* __restrict__ wd,
                    u16* __restrict__ out, void* ws) {
  __shared__ __attribute__((aligned(16))) float lds_[L_END];
  LAS float* lds = (LAS float*)lds_;
  const unsigned t0 = (unsigned)__builtin_amdgcn_s_memrealtime();
  const int tid = threadIdx.x, lane = tid & 63, wave = __builtin_amdgcn_readfirstlane(tid >> 6);
  const int r = lane >> 4, c = lane & 15;
  const int bid = blockIdx.x, xl = bid & 7, j = bid >> 3;
  const int h = xl * 4 + (j >> 3), s = j & 7;
  const int f1 = xl * 28 + j, mat = f1 / 112, rb = (f1 / 56) & 1, cb = f1 % 56;

  if (wave < NMAIN) {
    const rsrc_t rk = mkrsrc(Kc, (unsigned)H * SK * DH * 2), rv = mkrsrc(Vc, (unsigned)H * SK * DH * 2);
    u32x4 bA[16], bB[16];
    const unsigned kvbase = (unsigned)h * (8u << 20) + (unsigned)s * (1u << 20) + wave * 1024;
    const unsigned rot = 32u * ((h + (h >> 2) + (h >> 4)) & 3);
    const int kvoff = lane * 16;
    constexpr int STEP = (T_MAIN << 16) / 256 * PACE_G;
    int due = T_K0 * 65536 - STEP + (STEP / 256) * (wave * 32 + j);
#define PACE(T0, I) do { if ((I) % PACE_G == 0) { due += STEP; pace_wait(T0, due >> 16); } } while (0)
    int lagv = 0;
#define LAGAT(G, T0) do { const int l_ = (int)((unsigned)__builtin_amdgcn_s_memrealtime() - (T0)) - (due >> 16); lagv = (G) == DG ? l_ : lagv; SB(); } while (0)
    __builtin_amdgcn_s_barrier();
    SB();
    {
      const rsrc_t rg = mkrsrc(mat ? wu : wg, D * F * 2);
      const int voff = j < 28 ? (lane >> 3) * (F * 2) + (lane & 7) * 16 : OOB, sb = (rb * 512 + wave * 64) * (F * 2) + cb * 128;
      constexpr int FSTEP = ((T_W2 - T_F1) << 16) / 8;
      int fdue = T_F1 * 65536 + (FSTEP / 256) * (wave * 32 + j);
#pragma unroll
      for (int i = 0; i < 8; ++i) { pace_wait(t0, fdue >> 16); fdue += FSTEP; bB[i] = ld16_nt(rg, voff, sb + i * 8 * (F * 2)); SB(); }
    }
#pragma unroll
    for (int i = 0; i < 16; ++i) { PACE(t0, i); bA[i] = ld16_nt(rk, kvoff, kvbase + (rot + i) * 8192); SB(); }
    {
      cnt_wait(lds, C_XN, 1);
      float ag[8] = {0.f, 0.f, 0.f, 0.f, 0.f, 0.f, 0.f, 0.f};
#pragma unroll
      for (int i = 0; i < 8; ++i) {
        const int k = rb * 512 + wave * 64 + 8 * i + (lane >> 3);
        fma8(ag, lds[L_XN + (k & 3) * 256 + (k >> 2)], bB[i]);
      }
#pragma unroll
      for (int e = 0; e < 8; ++e) ag[e] += dpp<ROR8>(ag[e]);
      const f32x2 z = fold_rows(ag);
      if (c < 8) *(LAS f32x2*)(lds + L_FP + wave * 64 + 8 * c + 2 * r) = z;
      cnt_arrive(lds, C_F1, lane);
      SB();
    }
#pragma unroll
    for (int i = 0; i < 16; ++i) { PACE(t0, i); bB[i] = ld16_nt(rk, kvoff, kvbase + (rot + 16 + i) * 8192); SB(); }
    LAGAT(1, t0);
    cnt_wait(lds, C_QR, 1);
    const u32x4 qp = *(LAS u32x4*)(lds + L_QS + 4 * c);
    const unsigned t0m = __builtin_amdgcn_readfirstlane(*(LAS unsigned*)(lds + L_CNT + W_T0));
    constexpr float SCALE = 0.08838834764831845f, LOG2E = 1.4426950408889634f;
    LAS float* scp = lds + L_SC + wave * 512 + lane * 8;
    float mx = -INFINITY;
    auto kgroup = [&](u32x4 (&kb)[16], const bool more, const rsrc_t rn, const unsigned tb, const int g) __attribute__((always_inline)) {
      float sx[16];
#pragma unroll
      for (int i = 0; i < 16; ++i) {
        float d = 0.f;
#pragma unroll
        for (int t = 0; t < 4; ++t) d = dot2(kb[i][t], qp[t], d);
        asm volatile("" : "+v"(d));
        sx[i] = d;
        SB();
        if (more) { PACE(t0m, i); kb[i] = ld16_nt(rn, kvoff, tb + i * 8192); SB(); }
      }
#define TR_STEP(HALF, CTRL, BIT) _Pragma("unroll") for (int hh = 0; hh < (HALF); ++hh) { \
        const float lo_ = sx[hh], hi_ = sx[hh + (HALF)]; const float t1_ = lo_ + dpp<CTRL>(lo_), t2_ = hi_ + dpp<CTRL>(hi_); \
        sx[hh] = (c & (BIT)) ? t2_ : t1_; }
      TR_STEP(8, ROR8, 8) TR_STEP(4, XOR7, 4) TR_STEP(2, XOR2, 2) TR_STEP(1, XOR1, 1)
#undef TR_STEP
      const float sc = rbf(rbf(sx[0]) * SCALE) * LOG2E;
      mx = fmaxf(mx, sc);
      scp[g] = sc;
      SB();
    };
    kgroup(bA, false, rk, 0, 0);
#pragma unroll 1
    for (int g = 1; g < 8; ++g) {
      kgroup(bB, true, mkrsrc(g == 7 ? Vc : Kc, (unsigned)H * SK * DH * 2), kvbase + (((g + 1) * 16 + rot) & 127u) * 8192, g);
      LAGAT(g + 1, t0m); }
    {
      const float m = wave_max(mx);
      f32x4 p0 = *(LAS f32x4*)scp, p1 = *(LAS f32x4*)(scp + 4);
      float l = 0.f;
#pragma unroll
      for (int e = 0; e < 4; ++e) { p0[e] = exp2_(p0[e] - m); p1[e] = exp2_(p1[e] - m); l += p0[e] + p1[e]; }
      *(LAS f32x4*)scp = p0; *(LAS f32x4*)(scp + 4) = p1;
      l = wave_sum(l);
      if (lane == 0) *(LAS f32x2*)(lds + L_ML + 2 * wave) = f32x2{m, l};
      cnt_arrive(lds, C_ML, lane);
    }
    float acc[8] = {0.f, 0.f, 0.f, 0.f, 0.f, 0.f, 0.f, 0.f};
    auto vgroup = [&](const bool more, const unsigned tb, const float p) __attribute__((always_inline)) {
#define PV_STEP(I) { fma8(acc, dpp<BCAST + (I)>(p), bB[I]); \
        asm volatile("" : "+v"(acc[0]), "+v"(acc[1]), "+v"(acc[2]), "+v"(acc[3]), "+v"(acc[4]), "+v"(acc[5]), "+v"(acc[6]), "+v"(acc[7])); SB(); \
        if (more) { PACE(t0m, I); bB[I] = ld16_nt(rv, kvoff, tb + (I) * 8192); SB(); } \
        else if ((I) == 11) {   \
          pace_wait(t0m, T_K0 + T_MAIN); \
          _Pragma("unroll") for (int u = 0; u < 4; ++u) { \
            bB[u] = ld16_nt(mkrsrc(wo, H * DH * D * 2), r * 2048 + c * 16, (h * 128 + wave * 16 + 4 * u) * 2048 + s * 256); SB(); } } }
      PV_STEP(0) PV_STEP(1) PV_STEP(2) PV_STEP(3) PV_STEP(4) PV_STEP(5) PV_STEP(6) PV_STEP(7)
      PV_STEP(8) PV_STEP(9) PV_STEP(10) PV_STEP(11) PV_STEP(12) PV_STEP(13) PV_STEP(14) PV_STEP(15)
#undef PV_STEP
    };
#pragma unroll 1
    for (int g = 0; g < 7; ++g) { vgroup(true, kvbase + (((g + 1) * 16 + rot) & 127u) * 8192, scp[g]); LAGAT(g + 9, t0m); }
    if (lane == 0) __hip_atomic_fetch_max((LAS int*)(lds + L_CNT + C_LAG), lagv, __ATOMIC_RELAXED, __HIP_MEMORY_SCOPE_WORKGROUP);
    __builtin_amdgcn_s_setprio(3);
    cnt_wait(lds, C_WG, 1);
    {
      const float wgt = lds[L_WG + wave];
#pragma unroll
      for (int e = 0; e < 8; ++e) acc[e] *= wgt;
      vgroup(false, 0, scp[7] * wgt);
    }
    *(LAS f32x2*)(lds + L_MRG + wave * 128 + 8 * c + 2 * r) = fold_rows(acc);
    cnt_arrive(lds, C_END, lane);
#pragma unroll
    for (int u = 0; u < 4; ++u) *(LAS u32x4*)(lds + L_WT + (wave * 16 + 4 * u + r) * WT_STR + 4 * c) = bB[u];
    cnt_arrive(lds, C_WT, lane);
    return;
#undef PACE
  }

  const int a = wave - NMAIN;
  const rsrc_t rx = mkrsrc(x, D * 2), rws = mkrsrc(ws, WS_END);
  unsigned tagA, tagB, tagC, tagD;
  unsigned long long e2;
  {
    u32x4 xa = ld16(rx, lane * 32, 0), xb = ld16(rx, lane * 32 + 16, 0);
    const u32x2 ep = __builtin_amdgcn_raw_buffer_load_b64(rws, 0, WS_EPOCH, SC1);
    const rsrc_t rwq = mkrsrc(wq, D * H * DH * 2);
    u32x4 w[8];
    const int voff = r * 8192 + c * 16, sb = (s * 128 + a * 32) * 8192 + h * 256;
#pragma unroll
    for (int i = 0; i < 8; ++i) { w[i] = ld16_nt(rwq, voff, sb + i * 32768); SB(); }
    if (a == 0 && lane < C_NUM) *(LAS unsigned*)(lds + L_CNT + lane) = 0u;
    __syncthreads();
    asm volatile("" : "+v"(xa), "+v"(xb));
    float v[16];
#pragma unroll
    for (int i = 0; i < 4; ++i) { v[2 * i] = lo2f(xa[i]); v[2 * i + 1] = hi2f(xa[i]); v[8 + 2 * i] = lo2f(xb[i]); v[9 + 2 * i] = hi2f(xb[i]); }
    float sm = 0.f;
#pragma unroll
    for (int i = 0; i < 16; ++i) sm += v[i];
    const float mean = wave_sum(sm) * (1.f / D);
    float ss = 0.f;
#pragma unroll
    for (int i = 0; i < 16; ++i) { const float d = v[i] - mean; ss = fmaf(d, d, ss); }
    const float var = wave_sum(ss) * (1.f / D);
    const float mb = rbf(mean), den = rbf(sqrtf(rbf(rbf(var) + 1e-5f)));
    const float rinv = 1.0f / den;
#pragma unroll
    for (int i = 0; i < 16; ++i) {
      const float nu = rbf(v[i] - mb), q0 = nu * rinv;
      v[i] = rbf(fmaf(fmaf(-q0, den, nu), rinv, q0));
    }
#pragma unroll
    for (int q = 0; q < 4; ++q) *(LAS f32x4*)(lds + L_XN + q * 256 + 4 * lane) = f32x4{v[q], v[4 + q], v[8 + q], v[12 + q]};
    cnt_arrive(lds, C_XN, lane);
    const unsigned long long e1 = (((unsigned long long)__builtin_amdgcn_readfirstlane(ep[1]) << 32) | (unsigned)__builtin_amdgcn_readfirstlane(ep[0])) + 1ull;
    e2 = e1 + 1ull;
    tagA = (unsigned)e1; tagB = (unsigned)(e1 >> 32) + (unsigned)e1 * 0x9E3779B1u;
    tagC = (unsigned)e2; tagD = (unsigned)(e2 >> 32) + (unsigned)e2 * 0x9E3779B1u;
    float acc[8] = {0.f, 0.f, 0.f, 0.f, 0.f, 0.f, 0.f, 0.f};
#pragma unroll
    for (int q = 0; q < 2; ++q) {
      const f32x4 xv = *(LAS f32x4*)(lds + L_XN + r * 256 + s * 32 + a * 8 + 4 * q);
#pragma unroll
      for (int e = 0; e < 4; ++e) fma8(acc, xv[e], w[4 * q + e]);
    }
    *(LAS f32x2*)(lds + L_QP + a * 128 + 8 * c + 2 * r) = fold_rows(acc);
  }
  cnt_arrive(lds, C_Q, lane);
  if (a == 0) {
    cnt_wait<false>(lds, C_Q, NAUX);
    f32x2 v = *(LAS f32x2*)(lds + L_QP + 2 * lane);
#pragma unroll
    for (int q = 1; q < 4; ++q) v += *(LAS f32x2*)(lds + L_QP + q * 128 + 2 * lane);
    st16_sc1(rws, WS_Q + (h * 8 + s) * 1024 + lane * 16, rec(v[0], v[1], tagA, tagB));
    st16_sc1(rws, (bid == 0 && lane == 0) ? WS_T0 : OOB, u32x4{t0, tagA, t0, tagB});
  }
  if (a == 1) {
    pace_wait(t0, T_QPOLL);
    float q0 = 0.f, q1 = 0.f;
    unsigned tg = t0;
#pragma unroll 1
    for (unsigned sp = 0; sp < SPIN_MEM; ++sp) {
      REREAD();
      u32x4 g[8];
#pragma unroll
      for (int i = 0; i < 8; ++i) g[i] = ld16_sc1(rws, lane * 16, WS_Q + (h * 8 + i) * 1024);
      const u32x4 gt = ld16_sc1(rws, 0, WS_T0);
      bool ok = rok(gt, tagA, tagB);
      tg = gt[0];
      q0 = 0.f; q1 = 0.f;
#pragma unroll
      for (int i = 0; i < 8; ++i) { ok = ok && rok(g[i], tagA, tagB); q0 += r0(g[i]); q1 += r1(g[i]); }
      if (__all(ok)) break;
      __builtin_amdgcn_s_sleep(2);
    }
    *(LAS unsigned*)(lds + L_QS + lane) = pack2(q0, q1);
    if (lane == 0) *(LAS unsigned*)(lds + L_CNT + W_T0) = ((int)(t0 - tg) < 0 || (int)(t0 - tg) > 300) ? t0 : tg;
    cnt_arrive(lds, C_QR, lane);
  }
  const int ks = xl * 2 + (j >> 4), cg = j & 15;
  u32x4 dw[7];
  {
    const rsrc_t rd = mkrsrc(wd, D * F * 2);
    const int voff = (lane >> 3) * 2048 + (lane & 7) * 16, sb = (ks * 224 + a * 56) * 2048 + cg * 128;
    constexpr int WSTEP = ((T_K0 - T_W2) << 16) / 7;
    int wdue = T_W2 * 65536 + (WSTEP / 128) * (a * 32 + j);
#pragma unroll
    for (int i = 0; i < 7; ++i) { pace_wait(t0, wdue >> 16); wdue += WSTEP; dw[i] = ld16_nt(rd, voff, sb + i * 16384); SB(); }
  }
  if (a == 0 && j < 28) {
    cnt_wait(lds, C_F1, NMAIN);
    f32x2 v = *(LAS f32x2*)(lds + L_FP + 2 * (lane & 31));
#pragma unroll
    for (int q = 1; q < 8; ++q) v += *(LAS f32x2*)(lds + L_FP + q * 64 + 2 * (lane & 31));
    if (f1 >= 56) {
      st16_sc1(rws, lane < 32 ? WS_G1 + ((mat * 2 + rb) * F + cb * 64) * 8 + lane * 16 : OOB, rec(v[0], v[1], tagA, tagB));
    } else {
      pace_wait(t0, T_K0 + T_HA);
      u32x4 g[3];
#pragma unroll 1
      for (unsigned sp = 0; sp < SPIN_MEM; ++sp) {
        REREAD();
#pragma unroll
        for (int k = 0; k < 3; ++k) g[k] = ld16_sc1(rws, (lane & 31) * 16, WS_G1 + ((k + 1) * F + cb * 64) * 8);
        if (__all(rok(g[0], tagA, tagB) && rok(g[1], tagA, tagB) && rok(g[2], tagA, tagB))) break;
        __builtin_amdgcn_s_sleep(32);
      }
      float hh[2];
#pragma unroll
      for (int e = 0; e < 2; ++e) {
        const float gs = rbf(v[e] + __uint_as_float(g[0][2 * e]));
        const float us = rbf(__uint_as_float(g[1][2 * e]) + __uint_as_float(g[2][2 * e]));
        const float ge = 0.5f * gs * (1.f + tanhf(0.7978845608028654f * (gs + 0.044715f * gs * gs * gs)));
        hh[e] = rbf(ge) * us;
      }
      const float hp = __uint_as_float(pack2(hh[0], hh[1]));
      st16_sc1(rws, (lane < 32 && !(lane & 1)) ? WS_H + cb * 256 + lane * 8 : OOB, rec(hp, dpp<XOR1>(hp), tagA, tagB));
    }
  }
  {
    pace_wait(t0, T_K0 + T_HB);
    u32x4 g;
#pragma unroll 1
    for (unsigned sp = 0; sp < SPIN_MEM; ++sp) {
      REREAD();
      g = ld16_sc1(rws, (lane < 14 ? lane : 13) * 16, WS_H + (ks * 56 + a * 14) * 16);
      if (__all(rok(g, tagA, tagB))) break;
      __builtin_amdgcn_s_sleep(32);
    }
    if (lane < 14) *(LAS f32x4*)(lds + L_H + a * 64 + 4 * lane) = f32x4{lo2f(g[0]), hi2f(g[0]), lo2f(g[2]), hi2f(g[2])};
    float y[8] = {0.f, 0.f, 0.f, 0.f, 0.f, 0.f, 0.f, 0.f};
#pragma unroll
    for (int i = 0; i < 7; ++i) fma8(y, lds[L_H + a * 64 + 8 * i + (lane >> 3)], dw[i]);
#pragma unroll
    for (int e = 0; e < 8; ++e) y[e] += dpp<ROR8>(y[e]);
    const f32x2 z = fold_rows(y);
    if (c < 8) *(LAS f32x2*)(lds + L_F2 + a * 64 + 8 * c + 2 * r) = z;
    cnt_arrive(lds, C_F2, lane);
  }
  const int col0 = s * 128 + 4 * h;
  unsigned xw = 0;
  if (a == 0) xw = __builtin_amdgcn_raw_buffer_load_b32(rx, (col0 + 2 * (lane & 1)) * 2, 0, 0);
  if (a == 3) {
    const unsigned po = __builtin_amdgcn_raw_buffer_load_b32(mkrsrc(out, D * 2), (col0 + 2 * (lane & 1)) * 2, 0, SC1);
    asm volatile("" :: "v"(po));
  }
  if (a == 1) {
    cnt_wait(lds, C_ML, NMAIN);
    const f32x2 ml = *(LAS f32x2*)(lds + L_ML + 2 * (lane & 7));
    const float Mw = max8(ml[0]), Lw = sum8(exp2_(ml[0] - Mw) * ml[1]);
    st16_sc1(rws, lane == 0 ? WS_ML + (h * 8 + s) * 128 : OOB, rec(Mw, Lw, tagA, tagB));
    pace_wait(t0, T_K0 + T_MAIN / 2 + 1500);
    u32x4 g;
#pragma unroll 1
    for (unsigned sp = 0; sp < SPIN_MEM; ++sp) {
      REREAD();
      g = ld16_sc1(rws, (lane & 7) * 128, WS_ML + h * 8 * 128);
      if (__all(rok(g, tagA, tagB))) break;
      __builtin_amdgcn_s_sleep(32);
    }
    const float M = max8(r0(g)), L = sum8(exp2_(r0(g) - M) * r1(g));
    if (lane < 8) lds[L_WG + lane] = exp2_(ml[0] - M) / L;
    cnt_arrive(lds, C_WG, lane);
  }
  pace_wait(t0, T_K0 + T_MAIN - 100);
  __builtin_amdgcn_s_setprio(3);
  if (a == 2) {
    cnt_wait(lds, C_F2, NAUX);
    f32x2 v = *(LAS f32x2*)(lds + L_F2 + 2 * (lane & 31));
#pragma unroll
    for (int q = 1; q < 4; ++q) v += *(LAS f32x2*)(lds + L_F2 + q * 64 + 2 * (lane & 31));
    pace_wait(t0, T_K0 + T_MAIN + 60);
    st16_sc1(rws, lane < 32 ? WS_FFN + (ks * D + cg * 64) * 8 + lane * 16 : OOB, rec(v[0], v[1], tagA, tagB));
  }
  if (a == 0) {
    cnt_wait<false>(lds, C_END, NMAIN);
    f32x2 v = *(LAS f32x2*)(lds + L_MRG + 2 * lane);
#pragma unroll
    for (int w = 1; w < 8; ++w) v += *(LAS f32x2*)(lds + L_MRG + w * 128 + 2 * lane);
    st16_sc1(rws, WS_Q + (h * 8 + s) * 1024 + lane * 16, rec(v[0], v[1], tagC, tagD));
  } else {
    cnt_wait(lds, C_END, NMAIN);
  }
  u32x4 bfr[2][4];
  cnt_wait<false>(lds, C_WT, NMAIN);
#pragma unroll
  for (int q = 0; q < 4; ++q)
#pragma unroll
    for (int d = 0; d < 4; ++d) {
      const unsigned w0 = *(LAS unsigned*)(lds + L_WT + (32 * q + 8 * r + 2 * d) * WT_STR + 16 * a + c);
      const unsigned w1 = *(LAS unsigned*)(lds + L_WT + (32 * q + 8 * r + 2 * d + 1) * WT_STR + 16 * a + c);
      bfr[0][q][d] = __builtin_amdgcn_perm(w1, w0, 0x05040100u);
      bfr[1][q][d] = __builtin_amdgcn_perm(w1, w0, 0x07060302u);
    }
  {
    u32x4 g0, g1;
    const int vo = r * 1024 + (16 * a + c) * 16;
#pragma unroll 1
    for (unsigned sp = 0; sp < SPIN_MEM; ++sp) {
      REREAD();
      g0 = ld16_sc1(rws, vo, WS_Q + h * 8 * 1024);
      g1 = ld16_sc1(rws, vo, WS_Q + (h * 8 + 4) * 1024);
      if (__all(rok(g0, tagC, tagD) && rok(g1, tagC, tagD))) break;
    }
    float o = fold32(r0(g0) + r0(g1), r1(g0) + r1(g1));
    o = fold16(o, o);
    if ((r & 1) == 0) *((LAS u16*)(lds + L_OB) + 32 * a + 2 * c + (r >> 1)) = __builtin_bit_cast(u16, (__bf16)o);
  }
  cnt_arrive(lds, C_OB, lane);
  cnt_wait<false>(lds, C_OB, NAUX);
  {
    f32x4 y0 = {0.f, 0.f, 0.f, 0.f}, y1 = y0;
#pragma unroll
    for (int q = 0; q < 4; ++q) {
      const u32x4 af = *(LAS u32x4*)(lds + L_OB + 16 * q + 4 * r);
      y0 = __builtin_amdgcn_mfma_f32_16x16x32_bf16(__builtin_bit_cast(bf16x8, af), __builtin_bit_cast(bf16x8, bfr[0][q]), y0, 0, 0, 0);
      y1 = __builtin_amdgcn_mfma_f32_16x16x32_bf16(__builtin_bit_cast(bf16x8, af), __builtin_bit_cast(bf16x8, bfr[1][q]), y1, 0, 0, 0);
    }
    st16_sc1(rws, lane < 16 ? WS_Y + (h * 8 + s) * 1024 + (16 * a + lane) * 16 : OOB, rec(y0[0], y1[0], tagA, tagB));
  }
  const rsrc_t rdg = mkrsrc((char*)ws + (6u << 20), 8192);
  if (a == 2) st16_sc1(rdg, lane == 0 ? bid * 16 : OOB, u32x4{t0, tagA, *(LAS unsigned*)(lds + L_CNT + C_LAG), tagB});
  if (a == 0) {
    u32x4 g, gf;
    pace_wait((unsigned)__builtin_amdgcn_s_memrealtime(), 20);
#pragma unroll 1
    for (unsigned sp = 0; sp < SPIN_MEM; ++sp) {
      REREAD();
      g = ld16_sc1(rws, (lane >> 1) * (8 * 1024) + (lane & 1) * 16, WS_Y + s * 1024 + h * 32);
      gf = ld16_sc1(rws, ((lane >> 1) & 15) * (D * 8) + (lane & 1) * 16, WS_FFN + col0 * 8);
      if (__all(rok(g, tagA, tagB) && rok(gf, tagA, tagB))) break;
    }
    const float f0 = rbf(parity_sum(r0(gf)) * 0.5f), f1v = rbf(parity_sum(r1(gf)) * 0.5f);
    const float a0 = rbf(parity_sum(r0(g))), a1 = rbf(parity_sum(r1(g)));
    const unsigned o = pack2(rbf(lo2f(xw) + a0) + f0, rbf(hi2f(xw) + a1) + f1v);
    __builtin_amdgcn_raw_buffer_store_b32(o, mkrsrc(out, D * 2), lane < 2 ? (col0 + 2 * lane) * 2 : OOB, 0, SC1);
    __builtin_amdgcn_raw_buffer_store_b64(u32x2{(unsigned)e2, (unsigned)(e2 >> 32)}, rws, (bid == 0 && lane == 0) ? WS_EPOCH : OOB, 0, SC1);
    if (bid == 0) {
      u32x4 gg[4];
      for (unsigned sp = 0; sp < SPIN_MEM; ++sp) { REREAD();
        for (int k = 0; k < 4; ++k) gg[k] = ld16_sc1(rdg, lane * 16 + k * 1024, 0);
        if (__all(rok(gg[0], tagA, tagB) && rok(gg[1], tagA, tagB) && rok(gg[2], tagA, tagB) && rok(gg[3], tagA, tagB))) break; }
      int m2 = 0;
      for (int k = 0; k < 4; ++k) m2 += (int)gg[k][2];
      for (int o = 1; o < 64; o <<= 1) m2 += __shfl_xor(m2, o);
      int v = m2 / 256;
      v = v < 0 ? 0 : v > 1000 ? 1000 : v;
      pace_wait(t0, __builtin_amdgcn_readfirstlane(9500 + v * 20));
    }
  }
}

extern "C" void kernel_launch(void* const* d_in, const int* in_sizes, int n_in, void* d_out, int out_size, void* d_ws, size_t ws_size,
                              hipStream_t stream) {
  fused_decode_kernel<<<256, NTHR, 0, stream>>>((const u16*)d_in[0], (const u16*)d_in[1], (const u16*)d_in[2], (const u16*)d_in[3],
                                                 (const u16*)d_in[4], (const u16*)d_in[6], (const u16*)d_in[7], (const u16*)d_in[8],
                                                 (u16*)d_out, d_ws);
}
```

```cpp
#include <hip/hip_runtime.h>
#include <stdint.h>

typedef unsigned u32x4 __attribute__((ext_vector_type(4)));
typedef unsigned u32x2 __attribute__((ext_vector_type(2)));
typedef float f32x4 __attribute__((ext_vector_type(4)));
typedef float f32x2 __attribute__((ext_vector_type(2)));
typedef __bf16 bf16x2 __attribute__((ext_vector_type(2)));
typedef __bf16 bf16x8 __attribute__((ext_vector_type(8)));
typedef unsigned short u16;
#define LAS __attribute__((address_space(3)))
typedef __amdgpu_buffer_rsrc_t rsrc_t;

constexpr int T_F1 = 150;
constexpr int T_W2 = 360;
constexpr int T_K0 = 460;
constexpr int T_MAIN = 7090;
constexpr int PACE_G = 2;
constexpr int T_QPOLL = 390;
constexpr int T_HA = 1600, T_HB = 2800;

constexpr int D = 1024, H = 32, SK = 32768, DH = 128, F = 3584;
constexpr int NMAIN = 8, NAUX = 4, NTHR = (NMAIN + NAUX) * 64;
constexpr int NT = 2, SC1 = 16;
constexpr int OOB = 0x40000000;
constexpr unsigned WS_EPOCH = 0, WS_T0 = 1024, WS_ML = 4096, WS_Q = WS_ML + 256 * 128, WS_Y = WS_Q + 256 * 1024, WS_G1 = WS_Y + 256 * 1024,
                   WS_H = WS_G1 + 4 * F * 8, WS_FFN = WS_H + F * 4, WS_END = WS_FFN + 16 * D * 8;
constexpr int L_XN = 0, L_QP = 1024, L_QS = L_QP + 4 * 128, L_MRG = L_QS + 64, L_ML = L_MRG + 8 * 128, L_WG = L_ML + 16, L_FP = L_WG + 8,
              L_H = L_FP + 8 * 64, L_F2 = L_H + 4 * 64, L_OB = L_F2 + 4 * 64, L_CNT = L_OB + 64, L_SC = L_CNT + 16, L_WT = L_SC + 8 * 512,
              WT_STR = 68, L_END = L_WT + 128 * WT_STR;
constexpr int C_Q = 0, C_XN = 1, C_F2 = 2, C_WT = 3, C_ML = 4, C_WG = 5, C_END = 6, C_QR = 7, C_F1 = 8, C_OB = 9, C_NUM = 10, W_T0 = 12;
constexpr unsigned SPIN_MEM = 1u << 20, SPIN_LDS = 1u << 23;

__device__ __forceinline__ float lo2f(unsigned w) { return __builtin_bit_cast(float, w << 16); }
__device__ __forceinline__ float hi2f(unsigned w) { return __builtin_bit_cast(float, w & 0xffff0000u); }
__device__ __forceinline__ float rbf(float x) { return (float)(__bf16)x; }
__device__ __forceinline__ unsigned pack2(float a, float b) { bf16x2 v = {(__bf16)a, (__bf16)b}; return __builtin_bit_cast(unsigned, v); }
__device__ __forceinline__ float dot2(unsigned k, unsigned q, float acc) {
  return __builtin_amdgcn_fdot2_f32_bf16(__builtin_bit_cast(bf16x2, k), __builtin_bit_cast(bf16x2, q), acc, false);
}
template <int CTRL> __device__ __forceinline__ float dpp(float x) {
  return __builtin_bit_cast(float, __builtin_amdgcn_mov_dpp(__builtin_bit_cast(int, x), CTRL, 0xf, 0xf, true));
}
constexpr int XOR1 = 0xB1, XOR2 = 0x4E, XOR7 = 0x141, ROR8 = 0x128, ROR4 = 0x124, BCAST = 0x150;
__device__ __forceinline__ float fold32(float a, float b) {
  auto t = __builtin_amdgcn_permlane32_swap(__float_as_uint(a), __float_as_uint(b), false, false);
  return __uint_as_float(t[0]) + __uint_as_float(t[1]);
}
__device__ __forceinline__ float fold16(float a, float b) {
  auto t = __builtin_amdgcn_permlane16_swap(__float_as_uint(a), __float_as_uint(b), false, false);
  return __uint_as_float(t[0]) + __uint_as_float(t[1]);
}
__device__ __forceinline__ f32x2 fold_rows(const float (&v)[8]) {
  const float u0 = fold32(v[0], v[4]), u1 = fold32(v[1], v[5]), u2 = fold32(v[2], v[6]), u3 = fold32(v[3], v[7]);
  return f32x2{fold16(u0, u2), fold16(u1, u3)};
}
__device__ __forceinline__ float xrow_sum(float x) { x = fold16(x, x); return fold32(x, x); }
__device__ __forceinline__ float xrow_max(float x) {
  auto s = __builtin_amdgcn_permlane16_swap(__float_as_uint(x), __float_as_uint(x), false, false);
  x = fmaxf(__uint_as_float(s[0]), __uint_as_float(s[1]));
  auto t = __builtin_amdgcn_permlane32_swap(__float_as_uint(x), __float_as_uint(x), false, false);
  return fmaxf(__uint_as_float(t[0]), __uint_as_float(t[1]));
}
__device__ __forceinline__ float parity_sum(float x) {
  x += dpp<XOR2>(x); x += dpp<ROR4>(x); x += dpp<ROR8>(x); return xrow_sum(x);
}
__device__ __forceinline__ float wave_sum(float x) { x += dpp<XOR1>(x); return parity_sum(x); }
__device__ __forceinline__ float max8(float x) { x = fmaxf(x, dpp<XOR1>(x)); x = fmaxf(x, dpp<XOR2>(x)); return fmaxf(x, dpp<XOR7>(x)); }
__device__ __forceinline__ float sum8(float x) { x += dpp<XOR1>(x); x += dpp<XOR2>(x); return x + dpp<XOR7>(x); }
__device__ __forceinline__ float wave_max(float x) { x = max8(x); x = fmaxf(x, dpp<ROR8>(x)); return xrow_max(x); }
__device__ __forceinline__ float exp2_(float x) { return __builtin_amdgcn_exp2f(x); }
__device__ __forceinline__ rsrc_t mkrsrc(const void* p, unsigned bytes) { return __builtin_amdgcn_make_buffer_rsrc((void*)p, 0, (int)bytes, 0x00020000); }
__device__ __forceinline__ u32x4 ld16(rsrc_t r, int voff, int soff) { return __builtin_amdgcn_raw_buffer_load_b128(r, voff, soff, 0); }
__device__ __forceinline__ u32x4 ld16_nt(rsrc_t r, int voff, int soff) { return __builtin_amdgcn_raw_buffer_load_b128(r, voff, soff, NT); }
__device__ __forceinline__ u32x4 ld16_sc1(rsrc_t r, int voff, int soff) { return __builtin_amdgcn_raw_buffer_load_b128(r, voff, soff, SC1); }
__device__ __forceinline__ void st16_sc1(rsrc_t r, int off, u32x4 v) { __builtin_amdgcn_raw_buffer_store_b128(v, r, off, 0, SC1); }
#define SB() __builtin_amdgcn_sched_barrier(0)
#define REREAD() asm volatile("" ::: "memory")
__device__ __forceinline__ void pace_wait(unsigned t0, int due) {
  asm volatile("1:\n\ts_memrealtime s[96:97]\n\ts_waitcnt lgkmcnt(0)\n\ts_sub_u32 s96, s96, %0\n\ts_cmp_lt_i32 s96, %1\n\t"
               "s_cbranch_scc0 2f\n\ts_sleep 1\n\ts_branch 1b\n2:"
               :: "s"(t0), "s"(due) : "s96", "s97", "scc", "memory");
}
__device__ __forceinline__ void fma8(float (&acc)[8], float s, u32x4 w) {
#pragma unroll
  for (int t = 0; t < 4; ++t) { acc[2 * t] = fmaf(s, lo2f(w[t]), acc[2 * t]); acc[2 * t + 1] = fmaf(s, hi2f(w[t]), acc[2 * t + 1]); }
}
__device__ __forceinline__ u32x4 rec(float a, float b, unsigned ta, unsigned tb) { return u32x4{__float_as_uint(a), ta, __float_as_uint(b), tb}; }
__device__ __forceinline__ bool rok(u32x4 g, unsigned ta, unsigned tb) { return g[1] == ta && g[3] == tb; }
__device__ __forceinline__ float r0(u32x4 g) { return __uint_as_float(g[0]); }
__device__ __forceinline__ float r1(u32x4 g) { return __uint_as_float(g[2]); }

__device__ __forceinline__ void cnt_arrive(LAS float* lds, int which, int lane) {
  asm volatile("s_waitcnt lgkmcnt(0)" ::: "memory");
  if (lane == 0) __hip_atomic_fetch_add((LAS unsigned*)(lds + L_CNT + which), 1u, __ATOMIC_RELAXED, __HIP_MEMORY_SCOPE_WORKGROUP);
}
template <bool SLEEP = true>
__device__ __forceinline__ void cnt_wait(LAS float* lds, int which, unsigned n) {
#pragma unroll 1
  for (unsigned sp = 0; sp < SPIN_LDS; ++sp) {
    if (__builtin_amdgcn_readfirstlane(*(volatile LAS unsigned*)(lds + L_CNT + which)) >= n) break;
    if (SLEEP) __builtin_amdgcn_s_sleep(1);
  }
  REREAD();
}

__global__ void __launch_bounds__(NTHR, 3)
fused_decode_kernel(const u16* __restrict__ x, const u16* __restrict__ Kc, const u16* __restrict__ Vc, const u16* __restrict__ wq,
                    const u16* __restrict__ wo, const u16* __restrict__ wg, const u16* __restrict__ wu, const u16* __restrict__ wd,
                    u16* __restrict__ out, void* ws) {
  __shared__ __attribute__((aligned(16))) float lds_[L_END];
  LAS float* lds = (LAS float*)lds_;
  const unsigned t0 = (unsigned)__builtin_amdgcn_s_memrealtime();
  const int tid = threadIdx.x, lane = tid & 63, wave = __builtin_amdgcn_readfirstlane(tid >> 6);
  const int r = lane >> 4, c = lane & 15;
  const int bid = blockIdx.x, xl = bid & 7, j = bid >> 3;
  const int h = xl * 4 + (j >> 3), s = j & 7;
  const int f1 = xl * 28 + j, mat = f1 / 112, rb = (f1 / 56) & 1, cb = f1 % 56;

  if (wave < NMAIN) {
    const rsrc_t rk = mkrsrc(Kc, (unsigned)H * SK * DH * 2), rv = mkrsrc(Vc, (unsigned)H * SK * DH * 2);
    u32x4 bA[16], bB[16];
    const unsigned kvbase = (unsigned)h * (8u << 20) + (unsigned)s * (1u << 20) + wave * 1024;
    const unsigned rot = 32u * ((h + (h >> 2) + (h >> 4)) & 3);
    const int kvoff = lane * 16;
    constexpr int STEP = (T_MAIN << 16) / 256 * PACE_G;
    int due = T_K0 * 65536 - STEP + (STEP / 256) * (wave * 32 + j);
#define PACE(T0, I) do { if ((I) % PACE_G == 0) { due += STEP; pace_wait(T0, due >> 16); } } while (0)
    __builtin_amdgcn_s_barrier();
    SB();
    {
      const rsrc_t rg = mkrsrc(mat ? wu : wg, D * F * 2);
      const int voff = j < 28 ? (lane >> 3) * (F * 2) + (lane & 7) * 16 : OOB, sb = (rb * 512 + wave * 64) * (F * 2) + cb * 128;
      constexpr int FSTEP = ((T_W2 - T_F1) << 16) / 8;
      int fdue = T_F1 * 65536 + (FSTEP / 256) * (wave * 32 + j);
#pragma unroll
      for (int i = 0; i < 8; ++i) { pace_wait(t0, fdue >> 16); fdue += FSTEP; bB[i] = ld16_nt(rg, voff, sb + i * 8 * (F * 2)); SB(); }
    }
#pragma unroll
    for (int i = 0; i < 16; ++i) { PACE(t0, i); bA[i] = ld16_nt(rk, kvoff, kvbase + (rot + i) * 8192); SB(); }
    {
      cnt_wait(lds, C_XN, 1);
      float ag[8] = {0.f, 0.f, 0.f, 0.f, 0.f, 0.f, 0.f, 0.f};
#pragma unroll
      for (int i = 0; i < 8; ++i) {
        const int k = rb * 512 + wave * 64 + 8 * i + (lane >> 3);
        fma8(ag, lds[L_XN + (k & 3) * 256 + (k >> 2)], bB[i]);
      }
#pragma unroll
      for (int e = 0; e < 8; ++e) ag[e] += dpp<ROR8>(ag[e]);
      const f32x2 z = fold_rows(ag);
      if (c < 8) *(LAS f32x2*)(lds + L_FP + wave * 64 + 8 * c + 2 * r) = z;
      cnt_arrive(lds, C_F1, lane);
      SB();
    }
#pragma unroll
    for (int i = 0; i < 16; ++i) { PACE(t0, i); bB[i] = ld16_nt(rk, kvoff, kvbase + (rot + 16 + i) * 8192); SB(); }
    cnt_wait(lds, C_QR, 1);
    const u32x4 qp = *(LAS u32x4*)(lds + L_QS + 4 * c);
    const unsigned t0m = __builtin_amdgcn_readfirstlane(*(LAS unsigned*)(lds + L_CNT + W_T0));
    constexpr float SCALE = 0.08838834764831845f, LOG2E = 1.4426950408889634f;
    LAS float* scp = lds + L_SC + wave * 512 + lane * 8;
    float mx = -INFINITY;
    auto kgroup = [&](u32x4 (&kb)[16], const bool more, const rsrc_t rn, const unsigned tb, const int g) __attribute__((always_inline)) {
      float sx[16];
#pragma unroll
      for (int i = 0; i < 16; ++i) {
        float d = 0.f;
#pragma unroll
        for (int t = 0; t < 4; ++t) d = dot2(kb[i][t], qp[t], d);
        asm volatile("" : "+v"(d));
        sx[i] = d;
        SB();
        if (more) { PACE(t0m, i); kb[i] = ld16_nt(rn, kvoff, tb + i * 8192); SB(); }
      }
#define TR_STEP(HALF, CTRL, BIT) _Pragma("unroll") for (int hh = 0; hh < (HALF); ++hh) { \
        const float lo_ = sx[hh], hi_ = sx[hh + (HALF)]; const float t1_ = lo_ + dpp<CTRL>(lo_), t2_ = hi_ + dpp<CTRL>(hi_); \
        sx[hh] = (c & (BIT)) ? t2_ : t1_; }
      TR_STEP(8, ROR8, 8) TR_STEP(4, XOR7, 4) TR_STEP(2, XOR2, 2) TR_STEP(1, XOR1, 1)
#undef TR_STEP
      const float sc = rbf(rbf(sx[0]) * SCALE) * LOG2E;
      mx = fmaxf(mx, sc);
      scp[g] = sc;
      SB();
    };
    kgroup(bA, false, rk, 0, 0);
#pragma unroll 1
    for (int g = 1; g < 8; ++g)
      kgroup(bB, true, mkrsrc(g == 7 ? Vc : Kc, (unsigned)H * SK * DH * 2), kvbase + (((g + 1) * 16 + rot) & 127u) * 8192, g);
    {
      const float m = wave_max(mx);
      f32x4 p0 = *(LAS f32x4*)scp, p1 = *(LAS f32x4*)(scp + 4);
      float l = 0.f;
#pragma unroll
      for (int e = 0; e < 4; ++e) { p0[e] = exp2_(p0[e] - m); p1[e] = exp2_(p1[e] - m); l += p0[e] + p1[e]; }
      *(LAS f32x4*)scp = p0; *(LAS f32x4*)(scp + 4) = p1;
      l = wave_sum(l);
      if (lane == 0) *(LAS f32x2*)(lds + L_ML + 2 * wave) = f32x2{m, l};
      cnt_arrive(lds, C_ML, lane);
    }
    float acc[8] = {0.f, 0.f, 0.f, 0.f, 0.f, 0.f, 0.f, 0.f};
    auto vgroup = [&](const bool more, const unsigned tb, const float p) __attribute__((always_inline)) {
#define PV_STEP(I) { fma8(acc, dpp<BCAST + (I)>(p), bB[I]); \
        asm volatile("" : "+v"(acc[0]), "+v"(acc[1]), "+v"(acc[2]), "+v"(acc[3]), "+v"(acc[4]), "+v"(acc[5]), "+v"(acc[6]), "+v"(acc[7])); SB(); \
        if (more) { PACE(t0m, I); bB[I] = ld16_nt(rv, kvoff, tb + (I) * 8192); SB(); } \
        else if ((I) == 11) {   \
          pace_wait(t0m, T_K0 + T_MAIN); \
          _Pragma("unroll") for (int u = 0; u < 4; ++u) { \
            bB[u] = ld16_nt(mkrsrc(wo, H * DH * D * 2), r * 2048 + c * 16, (h * 128 + wave * 16 + 4 * u) * 2048 + s * 256); SB(); } } }
      PV_STEP(0) PV_STEP(1) PV_STEP(2) PV_STEP(3) PV_STEP(4) PV_STEP(5) PV_STEP(6) PV_STEP(7)
      PV_STEP(8) PV_STEP(9) PV_STEP(10) PV_STEP(11) PV_STEP(12) PV_STEP(13) PV_STEP(14) PV_STEP(15)
#undef PV_STEP
    };
#pragma unroll 1
    for (int g = 0; g < 7; ++g) vgroup(true, kvbase + (((g + 1) * 16 + rot) & 127u) * 8192, scp[g]);
    __builtin_amdgcn_s_setprio(3);
    cnt_wait(lds, C_WG, 1);
    {
      const float wgt = lds[L_WG + wave];
#pragma unroll
      for (int e = 0; e < 8; ++e) acc[e] *= wgt;
      vgroup(false, 0, scp[7] * wgt);
    }
    *(LAS f32x2*)(lds + L_MRG + wave * 128 + 8 * c + 2 * r) = fold_rows(acc);
    cnt_arrive(lds, C_END, lane);
#pragma unroll
    for (int u = 0; u < 4; ++u) *(LAS u32x4*)(lds + L_WT + (wave * 16 + 4 * u + r) * WT_STR + 4 * c) = bB[u];
    cnt_arrive(lds, C_WT, lane);
    return;
#undef PACE
  }

  const int a = wave - NMAIN;
  const rsrc_t rx = mkrsrc(x, D * 2), rws = mkrsrc(ws, WS_END);
  unsigned tagA, tagB, tagC, tagD;
  unsigned long long e2;
  {
    u32x4 xa = ld16(rx, lane * 32, 0), xb = ld16(rx, lane * 32 + 16, 0);
    const u32x2 ep = __builtin_amdgcn_raw_buffer_load_b64(rws, 0, WS_EPOCH, SC1);
    const rsrc_t rwq = mkrsrc(wq, D * H * DH * 2);
    u32x4 w[8];
    const int voff = r * 8192 + c * 16, sb = (s * 128 + a * 32) * 8192 + h * 256;
#pragma unroll
    for (int i = 0; i < 8; ++i) { w[i] = ld16_nt(rwq, voff, sb + i * 32768); SB(); }
    if (a == 0 && lane < C_NUM) *(LAS unsigned*)(lds + L_CNT + lane) = 0u;
    __syncthreads();
    asm volatile("" : "+v"(xa), "+v"(xb));
    float v[16];
#pragma unroll
    for (int i = 0; i < 4; ++i) { v[2 * i] = lo2f(xa[i]); v[2 * i + 1] = hi2f(xa[i]); v[8 + 2 * i] = lo2f(xb[i]); v[9 + 2 * i] = hi2f(xb[i]); }
    float sm = 0.f;
#pragma unroll
    for (int i = 0; i < 16; ++i) sm += v[i];
    const float mean = wave_sum(sm) * (1.f / D);
    float ss = 0.f;
#pragma unroll
    for (int i = 0; i < 16; ++i) { const float d = v[i] - mean; ss = fmaf(d, d, ss); }
    const float var = wave_sum(ss) * (1.f / D);
    const float mb = rbf(mean), den = rbf(sqrtf(rbf(rbf(var) + 1e-5f)));
    const float rinv = 1.0f / den;
#pragma unroll
    for (int i = 0; i < 16; ++i) {
      const float nu = rbf(v[i] - mb), q0 = nu * rinv;
      v[i] = rbf(fmaf(fmaf(-q0, den, nu), rinv, q0));
    }
#pragma unroll
    for (int q = 0; q < 4; ++q) *(LAS f32x4*)(lds + L_XN + q * 256 + 4 * lane) = f32x4{v[q], v[4 + q], v[8 + q], v[12 + q]};
    cnt_arrive(lds, C_XN, lane);
    const unsigned long long e1 = (((unsigned long long)__builtin_amdgcn_readfirstlane(ep[1]) << 32) | (unsigned)__builtin_amdgcn_readfirstlane(ep[0])) + 1ull;
    e2 = e1 + 1ull;
    tagA = (unsigned)e1; tagB = (unsigned)(e1 >> 32) + (unsigned)e1 * 0x9E3779B1u;
    tagC = (unsigned)e2; tagD = (unsigned)(e2 >> 32) + (unsigned)e2 * 0x9E3779B1u;
    float acc[8] = {0.f, 0.f, 0.f, 0.f, 0.f, 0.f, 0.f, 0.f};
#pragma unroll
    for (int q = 0; q < 2; ++q) {
      const f32x4 xv = *(LAS f32x4*)(lds + L_XN + r * 256 + s * 32 + a * 8 + 4 * q);
#pragma unroll
      for (int e = 0; e < 4; ++e) fma8(acc, xv[e], w[4 * q + e]);
    }
    *(LAS f32x2*)(lds + L_QP + a * 128 + 8 * c + 2 * r) = fold_rows(acc);
  }
  cnt_arrive(lds, C_Q, lane);
  if (a == 0) {
    cnt_wait<false>(lds, C_Q, NAUX);
    f32x2 v = *(LAS f32x2*)(lds + L_QP + 2 * lane);
#pragma unroll
    for (int q = 1; q < 4; ++q) v += *(LAS f32x2*)(lds + L_QP + q * 128 + 2 * lane);
    st16_sc1(rws, WS_Q + (h * 8 + s) * 1024 + lane * 16, rec(v[0], v[1], tagA, tagB));
    st16_sc1(rws, (bid == 0 && lane == 0) ? WS_T0 : OOB, u32x4{t0, tagA, t0, tagB});
  }
  if (a == 1) {
    pace_wait(t0, T_QPOLL);
    float q0 = 0.f, q1 = 0.f;
    unsigned tg = t0;
#pragma unroll 1
    for (unsigned sp = 0; sp < SPIN_MEM; ++sp) {
      REREAD();
      u32x4 g[8];
#pragma unroll
      for (int i = 0; i < 8; ++i) g[i] = ld16_sc1(rws, lane * 16, WS_Q + (h * 8 + i) * 1024);
      const u32x4 gt = ld16_sc1(rws, 0, WS_T0);
      bool ok = true;
      tg = rok(gt, tagA, tagB) ? gt[0] : t0;
      q0 = 0.f; q1 = 0.f;
#pragma unroll
      for (int i = 0; i < 8; ++i) { ok = ok && rok(g[i], tagA, tagB); q0 += r0(g[i]); q1 += r1(g[i]); }
      if (__all(ok)) break;
      __builtin_amdgcn_s_sleep(2);
    }
    *(LAS unsigned*)(lds + L_QS + lane) = pack2(q0, q1);
    if (lane == 0) *(LAS unsigned*)(lds + L_CNT + W_T0) = ((int)(t0 - tg) < 0 || (int)(t0 - tg) > 300) ? t0 : tg;
    cnt_arrive(lds, C_QR, lane);
  }
  const int ks = xl * 2 + (j >> 4), cg = j & 15;
  u32x4 dw[7];
  {
    const rsrc_t rd = mkrsrc(wd, D * F * 2);
    const int voff = (lane >> 3) * 2048 + (lane & 7) * 16, sb = (ks * 224 + a * 56) * 2048 + cg * 128;
    constexpr int WSTEP = ((T_K0 - T_W2) << 16) / 7;
    int wdue = T_W2 * 65536 + (WSTEP / 128) * (a * 32 + j);
#pragma unroll
    for (int i = 0; i < 7; ++i) { pace_wait(t0, wdue >> 16); wdue += WSTEP; dw[i] = ld16_nt(rd, voff, sb + i * 16384); SB(); }
  }
  if (a == 0 && j < 28) {
    cnt_wait(lds, C_F1, NMAIN);
    f32x2 v = *(LAS f32x2*)(lds + L_FP + 2 * (lane & 31));
#pragma unroll
    for (int q = 1; q < 8; ++q) v += *(LAS f32x2*)(lds + L_FP + q * 64 + 2 * (lane & 31));
    if (f1 >= 56) {
      st16_sc1(rws, lane < 32 ? WS_G1 + ((mat * 2 + rb) * F + cb * 64) * 8 + lane * 16 : OOB, rec(v[0], v[1], tagA, tagB));
    } else {
      pace_wait(t0, T_K0 + T_HA);
      u32x4 g[3];
#pragma unroll 1
      for (unsigned sp = 0; sp < SPIN_MEM; ++sp) {
        REREAD();
#pragma unroll
        for (int k = 0; k < 3; ++k) g[k] = ld16_sc1(rws, (lane & 31) * 16, WS_G1 + ((k + 1) * F + cb * 64) * 8);
        if (__all(rok(g[0], tagA, tagB) && rok(g[1], tagA, tagB) && rok(g[2], tagA, tagB))) break;
        __builtin_amdgcn_s_sleep(32);
      }
      float hh[2];
#pragma unroll
      for (int e = 0; e < 2; ++e) {
        const float gs = rbf(v[e] + __uint_as_float(g[0][2 * e]));
        const float us = rbf(__uint_as_float(g[1][2 * e]) + __uint_as_float(g[2][2 * e]));
        const float ge = 0.5f * gs * (1.f + tanhf(0.7978845608028654f * (gs + 0.044715f * gs * gs * gs)));
        hh[e] = rbf(ge) * us;
      }
      const float hp = __uint_as_float(pack2(hh[0], hh[1]));
      st16_sc1(rws, (lane < 32 && !(lane & 1)) ? WS_H + cb * 256 + lane * 8 : OOB, rec(hp, dpp<XOR1>(hp), tagA, tagB));
    }
  }
  {
    pace_wait(t0, T_K0 + T_HB);
    u32x4 g;
#pragma unroll 1
    for (unsigned sp = 0; sp < SPIN_MEM; ++sp) {
      REREAD();
      g = ld16_sc1(rws, (lane < 14 ? lane : 13) * 16, WS_H + (ks * 56 + a * 14) * 16);
      if (__all(rok(g, tagA, tagB))) break;
      __builtin_amdgcn_s_sleep(32);
    }
    if (lane < 14) *(LAS f32x4*)(lds + L_H + a * 64 + 4 * lane) = f32x4{lo2f(g[0]), hi2f(g[0]), lo2f(g[2]), hi2f(g[2])};
    float y[8] = {0.f, 0.f, 0.f, 0.f, 0.f, 0.f, 0.f, 0.f};
#pragma unroll
    for (int i = 0; i < 7; ++i) fma8(y, lds[L_H + a * 64 + 8 * i + (lane >> 3)], dw[i]);
#pragma unroll
    for (int e = 0; e < 8; ++e) y[e] += dpp<ROR8>(y[e]);
    const f32x2 z = fold_rows(y);
    if (c < 8) *(LAS f32x2*)(lds + L_F2 + a * 64 + 8 * c + 2 * r) = z;
    cnt_arrive(lds, C_F2, lane);
  }
  const int col0 = s * 128 + 4 * h;
  unsigned xw = 0;
  if (a == 0) xw = __builtin_amdgcn_raw_buffer_load_b32(rx, (col0 + 2 * (lane & 1)) * 2, 0, 0);
  if (a == 3) {
    const unsigned po = __builtin_amdgcn_raw_buffer_load_b32(mkrsrc(out, D * 2), (col0 + 2 * (lane & 1)) * 2, 0, SC1);
    asm volatile("" :: "v"(po));
  }
  if (a == 1) {
    cnt_wait(lds, C_ML, NMAIN);
    const f32x2 ml = *(LAS f32x2*)(lds + L_ML + 2 * (lane & 7));
    const float Mw = max8(ml[0]), Lw = sum8(exp2_(ml[0] - Mw) * ml[1]);
    st16_sc1(rws, lane == 0 ? WS_ML + (h * 8 + s) * 128 : OOB, rec(Mw, Lw, tagA, tagB));
    pace_wait(t0, T_K0 + T_MAIN / 2 + 1500);
    u32x4 g;
#pragma unroll 1
    for (unsigned sp = 0; sp < SPIN_MEM; ++sp) {
      REREAD();
      g = ld16_sc1(rws, (lane & 7) * 128, WS_ML + h * 8 * 128);
      if (__all(rok(g, tagA, tagB))) break;
      __builtin_amdgcn_s_sleep(32);
    }
    const float M = max8(r0(g)), L = sum8(exp2_(r0(g) - M) * r1(g));
    if (lane < 8) lds[L_WG + lane] = exp2_(ml[0] - M) / L;
    cnt_arrive(lds, C_WG, lane);
  }
  pace_wait(t0, T_K0 + T_MAIN - 100);
  __builtin_amdgcn_s_setprio(3);
  if (a == 2) {
    cnt_wait(lds, C_F2, NAUX);
    f32x2 v = *(LAS f32x2*)(lds + L_F2 + 2 * (lane & 31));
#pragma unroll
    for (int q = 1; q < 4; ++q) v += *(LAS f32x2*)(lds + L_F2 + q * 64 + 2 * (lane & 31));
    pace_wait(t0, T_K0 + T_MAIN + 60);
    st16_sc1(rws, lane < 32 ? WS_FFN + (ks * D + cg * 64) * 8 + lane * 16 : OOB, rec(v[0], v[1], tagA, tagB));
  }
  if (a == 0) {
    cnt_wait<false>(lds, C_END, NMAIN);
    f32x2 v = *(LAS f32x2*)(lds + L_MRG + 2 * lane);
#pragma unroll
    for (int w = 1; w < 8; ++w) v += *(LAS f32x2*)(lds + L_MRG + w * 128 + 2 * lane);
    st16_sc1(rws, WS_Q + (h * 8 + s) * 1024 + lane * 16, rec(v[0], v[1], tagC, tagD));
  } else {
    cnt_wait(lds, C_END, NMAIN);
  }
  u32x4 bfr[2][4];
  cnt_wait<false>(lds, C_WT, NMAIN);
#pragma unroll
  for (int q = 0; q < 4; ++q)
#pragma unroll
    for (int d = 0; d < 4; ++d) {
      const unsigned w0 = *(LAS unsigned*)(lds + L_WT + (32 * q + 8 * r + 2 * d) * WT_STR + 16 * a + c);
      const unsigned w1 = *(LAS unsigned*)(lds + L_WT + (32 * q + 8 * r + 2 * d + 1) * WT_STR + 16 * a + c);
      bfr[0][q][d] = __builtin_amdgcn_perm(w1, w0, 0x05040100u);
      bfr[1][q][d] = __builtin_amdgcn_perm(w1, w0, 0x07060302u);
    }
  {
    u32x4 g0, g1;
    const int vo = r * 1024 + (16 * a + c) * 16;
#pragma unroll 1
    for (unsigned sp = 0; sp < SPIN_MEM; ++sp) {
      REREAD();
      g0 = ld16_sc1(rws, vo, WS_Q + h * 8 * 1024);
      g1 = ld16_sc1(rws, vo, WS_Q + (h * 8 + 4) * 1024);
      if (__all(rok(g0, tagC, tagD) && rok(g1, tagC, tagD))) break;
    }
    float o = fold32(r0(g0) + r0(g1), r1(g0) + r1(g1));
    o = fold16(o, o);
    if ((r & 1) == 0) *((LAS u16*)(lds + L_OB) + 32 * a + 2 * c + (r >> 1)) = __builtin_bit_cast(u16, (__bf16)o);
  }
  cnt_arrive(lds, C_OB, lane);
  cnt_wait<false>(lds, C_OB, NAUX);
  {
    f32x4 y0 = {0.f, 0.f, 0.f, 0.f}, y1 = y0;
#pragma unroll
    for (int q = 0; q < 4; ++q) {
      const u32x4 af = *(LAS u32x4*)(lds + L_OB + 16 * q + 4 * r);
      y0 = __builtin_amdgcn_mfma_f32_16x16x32_bf16(__builtin_bit_cast(bf16x8, af), __builtin_bit_cast(bf16x8, bfr[0][q]), y0, 0, 0, 0);
      y1 = __builtin_amdgcn_mfma_f32_16x16x32_bf16(__builtin_bit_cast(bf16x8, af), __builtin_bit_cast(bf16x8, bfr[1][q]), y1, 0, 0, 0);
    }
    st16_sc1(rws, lane < 16 ? WS_Y + (h * 8 + s) * 1024 + (16 * a + lane) * 16 : OOB, rec(y0[0], y1[0], tagA, tagB));
  }
  if (a == 0) {
    u32x4 g, gf;
    pace_wait((unsigned)__builtin_amdgcn_s_memrealtime(), 20);
#pragma unroll 1
    for (unsigned sp = 0; sp < SPIN_MEM; ++sp) {
      REREAD();
      g = ld16_sc1(rws, (lane >> 1) * (8 * 1024) + (lane & 1) * 16, WS_Y + s * 1024 + h * 32);
      gf = ld16_sc1(rws, ((lane >> 1) & 15) * (D * 8) + (lane & 1) * 16, WS_FFN + col0 * 8);
      if (__all(rok(g, tagA, tagB) && rok(gf, tagA, tagB))) break;
    }
    const float f0 = rbf(parity_sum(r0(gf)) * 0.5f), f1v = rbf(parity_sum(r1(gf)) * 0.5f);
    const float a0 = rbf(parity_sum(r0(g))), a1 = rbf(parity_sum(r1(g)));
    const unsigned o = pack2(rbf(lo2f(xw) + a0) + f0, rbf(hi2f(xw) + a1) + f1v);
    __builtin_amdgcn_raw_buffer_store_b32(o, mkrsrc(out, D * 2), lane < 2 ? (col0 + 2 * lane) * 2 : OOB, 0, SC1);
    __builtin_amdgcn_raw_buffer_store_b64(u32x2{(unsigned)e2, (unsigned)(e2 >> 32)}, rws, (bid == 0 && lane == 0) ? WS_EPOCH : OOB, 0, SC1);
  }
}

extern "C" void kernel_launch(void* const* d_in, const int* in_sizes, int n_in, void* d_out, int out_size, void* d_ws, size_t ws_size,
                              hipStream_t stream) {
  fused_decode_kernel<<<256, NTHR, 0, stream>>>((const u16*)d_in[0], (const u16*)d_in[1], (const u16*)d_in[2], (const u16*)d_in[3],
                                                 (const u16*)d_in[4], (const u16*)d_in[6], (const u16*)d_in[7], (const u16*)d_in[8],
                                                 (u16*)d_out, d_ws);
}
```

```cpp
#include <hip/hip_runtime.h>
#include <stdint.h>

typedef unsigned u32x4 __attribute__((ext_vector_type(4)));
typedef unsigned u32x2 __attribute__((ext_vector_type(2)));
typedef float f32x4 __attribute__((ext_vector_type(4)));
typedef float f32x2 __attribute__((ext_vector_type(2)));
typedef __bf16 bf16x2 __attribute__((ext_vector_type(2)));
typedef __bf16 bf16x8 __attribute__((ext_vector_type(8)));
typedef unsigned short u16;
#define LAS __attribute__((address_space(3)))
typedef __amdgpu_buffer_rsrc_t rsrc_t;

constexpr int T_F1 = 150;
constexpr int T_W2 = 360;
constexpr int T_K0 = 460;
constexpr int T_MAIN = 7190;
constexpr int PACE_G = 2;
constexpr int T_QPOLL = 390;
constexpr int T_HA = 1600, T_HB = 2800;

constexpr int D = 1024, H = 32, SK = 32768, DH = 128, F = 3584;
constexpr int NMAIN = 8, NAUX = 4, NTHR = (NMAIN + NAUX) * 64;
constexpr int NT = 2, SC1 = 16;
constexpr int OOB = 0x40000000;
constexpr unsigned WS_EPOCH = 0, WS_T0 = 1024, WS_ML = 4096, WS_Q = WS_ML + 256 * 128, WS_Y = WS_Q + 256 * 1024, WS_G1 = WS_Y + 256 * 1024,
                   WS_H = WS_G1 + 4 * F * 8, WS_FFN = WS_H + F * 4, WS_END = WS_FFN + 16 * D * 8;
constexpr int L_XN = 0, L_QP = 1024, L_QS = L_QP + 4 * 128, L_MRG = L_QS + 64, L_ML = L_MRG + 8 * 128, L_WG = L_ML + 16, L_FP = L_WG + 8,
              L_H = L_FP + 8 * 64, L_F2 = L_H + 4 * 64, L_OB = L_F2 + 4 * 64, L_CNT = L_OB + 64, L_SC = L_CNT + 16, L_WT = L_SC + 8 * 512,
              WT_STR = 68, L_END = L_WT + 128 * WT_STR;
constexpr int C_Q = 0, C_XN = 1, C_F2 = 2, C_WT = 3, C_ML = 4, C_WG = 5, C_END = 6, C_QR = 7, C_F1 = 8, C_OB = 9, C_NUM = 10, W_T0 = 12;
constexpr unsigned SPIN_MEM = 1u << 20, SPIN_LDS = 1u << 23;

__device__ __forceinline__ float lo2f(unsigned w) { return __builtin_bit_cast(float, w << 16); }
__device__ __forceinline__ float hi2f(unsigned w) { return __builtin_bit_cast(float, w & 0xffff0000u); }
__device__ __forceinline__ float rbf(float x) { return (float)(__bf16)x; }
__device__ __forceinline__ unsigned pack2(float a, float b) { bf16x2 v = {(__bf16)a, (__bf16)b}; return __builtin_bit_cast(unsigned, v); }
__device__ __forceinline__ float dot2(unsigned k, unsigned q, float acc) {
  return __builtin_amdgcn_fdot2_f32_bf16(__builtin_bit_cast(bf16x2, k), __builtin_bit_cast(bf16x2, q), acc, false);
}
template <int CTRL> __device__ __forceinline__ float dpp(float x) {
  return __builtin_bit_cast(float, __builtin_amdgcn_mov_dpp(__builtin_bit_cast(int, x), CTRL, 0xf, 0xf, true));
}
constexpr int XOR1 = 0xB1, XOR2 = 0x4E, XOR7 = 0x141, ROR8 = 0x128, ROR4 = 0x124, BCAST = 0x150;
__device__ __forceinline__ float fold32(float a, float b) {
  auto t = __builtin_amdgcn_permlane32_swap(__float_as_uint(a), __float_as_uint(b), false, false);
  return __uint_as_float(t[0]) + __uint_as_float(t[1]);
}
__device__ __forceinline__ float fold16(float a, float b) {
  auto t = __builtin_amdgcn_permlane16_swap(__float_as_uint(a), __float_as_uint(b), false, false);
  return __uint_as_float(t[0]) + __uint_as_float(t[1]);
}
__device__ __forceinline__ f32x2 fold_rows(const float (&v)[8]) {
  const float u0 = fold32(v[0], v[4]), u1 = fold32(v[1], v[5]), u2 = fold32(v[2], v[6]), u3 = fold32(v[3], v[7]);
  return f32x2{fold16(u0, u2), fold16(u1, u3)};
}
__device__ __forceinline__ float xrow_sum(float x) { x = fold16(x, x); return fold32(x, x); }
__device__ __forceinline__ float xrow_max(float x) {
  auto s = __builtin_amdgcn_permlane16_swap(__float_as_uint(x), __float_as_uint(x), false, false);
  x = fmaxf(__uint_as_float(s[0]), __uint_as_float(s[1]));
  auto t = __builtin_amdgcn_permlane32_swap(__float_as_uint(x), __float_as_uint(x), false, false);
  return fmaxf(__uint_as_float(t[0]), __uint_as_float(t[1]));
}
__device__ __forceinline__ float parity_sum(float x) {
  x += dpp<XOR2>(x); x += dpp<ROR4>(x); x += dpp<ROR8>(x); return xrow_sum(x);
}
__device__ __forceinline__ float wave_sum(float x) { x += dpp<XOR1>(x); return parity_sum(x); }
__device__ __forceinline__ float max8(float x) { x = fmaxf(x, dpp<XOR1>(x)); x = fmaxf(x, dpp<XOR2>(x)); return fmaxf(x, dpp<XOR7>(x)); }
__device__ __forceinline__ float sum8(float x) { x += dpp<XOR1>(x); x += dpp<XOR2>(x); return x + dpp<XOR7>(x); }
__device__ __forceinline__ float wave_max(float x) { x = max8(x); x = fmaxf(x, dpp<ROR8>(x)); return xrow_max(x); }
__device__ __forceinline__ float exp2_(float x) { return __builtin_amdgcn_exp2f(x); }
__device__ __forceinline__ rsrc_t mkrsrc(const void* p, unsigned bytes) { return __builtin_amdgcn_make_buffer_rsrc((void*)p, 0, (int)bytes, 0x00020000); }
__device__ __forceinline__ u32x4 ld16(rsrc_t r, int voff, int soff) { return __builtin_amdgcn_raw_buffer_load_b128(r, voff, soff, 0); }
__device__ __forceinline__ u32x4 ld16_nt(rsrc_t r, int voff, int soff) { return __builtin_amdgcn_raw_buffer_load_b128(r, voff, soff, NT); }
__device__ __forceinline__ u32x4 ld16_sc1(rsrc_t r, int voff, int soff) { return __builtin_amdgcn_raw_buffer_load_b128(r, voff, soff, SC1); }
__device__ __forceinline__ void st16_sc1(rsrc_t r, int off, u32x4 v) { __builtin_amdgcn_raw_buffer_store_b128(v, r, off, 0, SC1); }
#define SB() __builtin_amdgcn_sched_barrier(0)
#define REREAD() asm volatile("" ::: "memory")
__device__ __forceinline__ void pace_wait(unsigned t0, int due) {
  asm volatile("1:\n\ts_memrealtime s[96:97]\n\ts_waitcnt lgkmcnt(0)\n\ts_sub_u32 s96, s96, %0\n\ts_cmp_lt_i32 s96, %1\n\t"
               "s_cbranch_scc0 2f\n\ts_sleep 1\n\ts_branch 1b\n2:"
               :: "s"(t0), "s"(due) : "s96", "s97", "scc", "memory");
}
__device__ __forceinline__ void fma8(float (&acc)[8], float s, u32x4 w) {
#pragma unroll
  for (int t = 0; t < 4; ++t) { acc[2 * t] = fmaf(s, lo2f(w[t]), acc[2 * t]); acc[2 * t + 1] = fmaf(s, hi2f(w[t]), acc[2 * t + 1]); }
}
__device__ __forceinline__ u32x4 rec(float a, float b, unsigned ta, unsigned tb) { return u32x4{__float_as_uint(a), ta, __float_as_uint(b), tb}; }
__device__ __forceinline__ bool rok(u32x4 g, unsigned ta, unsigned tb) { return g[1] == ta && g[3] == tb; }
__device__ __forceinline__ float r0(u32x4 g) { return __uint_as_float(g[0]); }
__device__ __forceinline__ float r1(u32x4 g) { return __uint_as_float(g[2]); }

__device__ __forceinline__ void cnt_arrive(LAS float* lds, int which, int lane) {
  asm volatile("s_waitcnt lgkmcnt(0)" ::: "memory");
  if (lane == 0) __hip_atomic_fetch_add((LAS unsigned*)(lds + L_CNT + which), 1u, __ATOMIC_RELAXED, __HIP_MEMORY_SCOPE_WORKGROUP);
}
template <bool SLEEP = true>
__device__ __forceinline__ void cnt_wait(LAS float* lds, int which, unsigned n) {
#pragma unroll 1
  for (unsigned sp = 0; sp < SPIN_LDS; ++sp) {
    if (__builtin_amdgcn_readfirstlane(*(volatile LAS unsigned*)(lds + L_CNT + which)) >= n) break;
    if (SLEEP) __builtin_amdgcn_s_sleep(1);
  }
  REREAD();
}

__global__ void __launch_bounds__(NTHR, 3)
fused_decode_kernel(const u16* __restrict__ x, const u16* __restrict__ Kc, const u16* __restrict__ Vc, const u16* __restrict__ wq,
                    const u16* __restrict__ wo, const u16* __restrict__ wg, const u16* __restrict__ wu, const u16* __restrict__ wd,
                    u16* __restrict__ out, void* ws) {
  __shared__ __attribute__((aligned(16))) float lds_[L_END];
  LAS float* lds = (LAS float*)lds_;
  const unsigned t0 = (unsigned)__builtin_amdgcn_s_memrealtime();
  const int tid = threadIdx.x, lane = tid & 63, wave = __builtin_amdgcn_readfirstlane(tid >> 6);
  const int r = lane >> 4, c = lane & 15;
  const int bid = blockIdx.x, xl = bid & 7, j = bid >> 3;
  const int h = xl * 4 + (j >> 3), s = j & 7;
  const int f1 = xl * 28 + j, mat = f1 / 112, rb = (f1 / 56) & 1, cb = f1 % 56;

  if (wave < NMAIN) {
    const rsrc_t rk = mkrsrc(Kc, (unsigned)H * SK * DH * 2), rv = mkrsrc(Vc, (unsigned)H * SK * DH * 2);
    u32x4 bA[16], bB[16];
    const unsigned kvbase = (unsigned)h * (8u << 20) + (unsigned)s * (1u << 20) + wave * 1024;
    const unsigned rot = 32u * ((h + (h >> 2) + (h >> 4)) & 3);
    const int kvoff = lane * 16;
    constexpr int STEP = (T_MAIN << 16) / 256 * PACE_G;
    int due = T_K0 * 65536 - STEP + (STEP / 256) * (wave * 32 + j);
#define PACE(T0, I) do { if ((I) % PACE_G == 0) { due += STEP; pace_wait(T0, due >> 16); } } while (0)
    __builtin_amdgcn_s_barrier();
    SB();
    {
      const rsrc_t rg = mkrsrc(mat ? wu : wg, D * F * 2);
      const int voff = j < 28 ? (lane >> 3) * (F * 2) + (lane & 7) * 16 : OOB, sb = (rb * 512 + wave * 64) * (F * 2) + cb * 128;
      constexpr int FSTEP = ((T_W2 - T_F1) << 16) / 8;
      int fdue = T_F1 * 65536 + (FSTEP / 256) * (wave * 32 + j);
#pragma unroll
      for (int i = 0; i < 8; ++i) { pace_wait(t0, fdue >> 16); fdue += FSTEP; bB[i] = ld16_nt(rg, voff, sb + i * 8 * (F * 2)); SB(); }
    }
#pragma unroll
    for (int i = 0; i < 16; ++i) { PACE(t0, i); bA[i] = ld16_nt(rk, kvoff, kvbase + (rot + i) * 8192); SB(); }
    {
      cnt_wait(lds, C_XN, 1);
      float ag[8] = {0.f, 0.f, 0.f, 0.f, 0.f, 0.f, 0.f, 0.f};
#pragma unroll
      for (int i = 0; i < 8; ++i) {
        const int k = rb * 512 + wave * 64 + 8 * i + (lane >> 3);
        fma8(ag, lds[L_XN + (k & 3) * 256 + (k >> 2)], bB[i]);
      }
#pragma unroll
      for (int e = 0; e < 8; ++e) ag[e] += dpp<ROR8>(ag[e]);
      const f32x2 z = fold_rows(ag);
      if (c < 8) *(LAS f32x2*)(lds + L_FP + wave * 64 + 8 * c + 2 * r) = z;
      cnt_arrive(lds, C_F1, lane);
      SB();
    }
#pragma unroll
    for (int i = 0; i < 16; ++i) { PACE(t0, i); bB[i] = ld16_nt(rk, kvoff, kvbase + (rot + 16 + i) * 8192); SB(); }
    cnt_wait(lds, C_QR, 1);
    const u32x4 qp = *(LAS u32x4*)(lds + L_QS + 4 * c);
    const unsigned t0m = __builtin_amdgcn_readfirstlane(*(LAS unsigned*)(lds + L_CNT + W_T0));
    constexpr float SCALE = 0.08838834764831845f, LOG2E = 1.4426950408889634f;
    LAS float* scp = lds + L_SC + wave * 512 + lane * 8;
    float mx = -INFINITY;
    auto kgroup = [&](u32x4 (&kb)[16], const bool more, const rsrc_t rn, const unsigned tb, const int g) __attribute__((always_inline)) {
      float sx[16];
#pragma unroll
      for (int i = 0; i < 16; ++i) {
        float d = 0.f;
#pragma unroll
        for (int t = 0; t < 4; ++t) d = dot2(kb[i][t], qp[t], d);
        asm volatile("" : "+v"(d));
        sx[i] = d;
        SB();
        if (more) { PACE(t0m, i); kb[i] = ld16_nt(rn, kvoff, tb + i * 8192); SB(); }
      }
#define TR_STEP(HALF, CTRL, BIT) _Pragma("unroll") for (int hh = 0; hh < (HALF); ++hh) { \
        const float lo_ = sx[hh], hi_ = sx[hh + (HALF)]; const float t1_ = lo_ + dpp<CTRL>(lo_), t2_ = hi_ + dpp<CTRL>(hi_); \
        sx[hh] = (c & (BIT)) ? t2_ : t1_; }
      TR_STEP(8, ROR8, 8) TR_STEP(4, XOR7, 4) TR_STEP(2, XOR2, 2) TR_STEP(1, XOR1, 1)
#undef TR_STEP
      const float sc = rbf(rbf(sx[0]) * SCALE) * LOG2E;
      mx = fmaxf(mx, sc);
      scp[g] = sc;
      SB();
    };
    kgroup(bA, false, rk, 0, 0);
#pragma unroll 1
    for (int g = 1; g < 8; ++g)
      kgroup(bB, true, mkrsrc(g == 7 ? Vc : Kc, (unsigned)H * SK * DH * 2), kvbase + (((g + 1) * 16 + rot) & 127u) * 8192, g);
    {
      const float m = wave_max(mx);
      f32x4 p0 = *(LAS f32x4*)scp, p1 = *(LAS f32x4*)(scp + 4);
      float l = 0.f;
#pragma unroll
      for (int e = 0; e < 4; ++e) { p0[e] = exp2_(p0[e] - m); p1[e] = exp2_(p1[e] - m); l += p0[e] + p1[e]; }
      *(LAS f32x4*)scp = p0; *(LAS f32x4*)(scp + 4) = p1;
      l = wave_sum(l);
      if (lane == 0) *(LAS f32x2*)(lds + L_ML + 2 * wave) = f32x2{m, l};
      cnt_arrive(lds, C_ML, lane);
    }
    float acc[8] = {0.f, 0.f, 0.f, 0.f, 0.f, 0.f, 0.f, 0.f};
    auto vgroup = [&](const bool more, const unsigned tb, const float p) __attribute__((always_inline)) {
#define PV_STEP(I) { fma8(acc, dpp<BCAST + (I)>(p), bB[I]); \
        asm volatile("" : "+v"(acc[0]), "+v"(acc[1]), "+v"(acc[2]), "+v"(acc[3]), "+v"(acc[4]), "+v"(acc[5]), "+v"(acc[6]), "+v"(acc[7])); SB(); \
        if (more) { PACE(t0m, I); bB[I] = ld16_nt(rv, kvoff, tb + (I) * 8192); SB(); } \
        else if ((I) == 11) {   \
          pace_wait(t0m, T_K0 + T_MAIN); \
          _Pragma("unroll") for (int u = 0; u < 4; ++u) { \
            bB[u] = ld16_nt(mkrsrc(wo, H * DH * D * 2), r * 2048 + c * 16, (h * 128 + wave * 16 + 4 * u) * 2048 + s * 256); SB(); } } }
      PV_STEP(0) PV_STEP(1) PV_STEP(2) PV_STEP(3) PV_STEP(4) PV_STEP(5) PV_STEP(6) PV_STEP(7)
      PV_STEP(8) PV_STEP(9) PV_STEP(10) PV_STEP(11) PV_STEP(12) PV_STEP(13) PV_STEP(14) PV_STEP(15)
#undef PV_STEP
    };
#pragma unroll 1
    for (int g = 0; g < 7; ++g) vgroup(true, kvbase + (((g + 1) * 16 + rot) & 127u) * 8192, scp[g]);
    __builtin_amdgcn_s_setprio(3);
    cnt_wait(lds, C_WG, 1);
    {
      const float wgt = lds[L_WG + wave];
#pragma unroll
      for (int e = 0; e < 8; ++e) acc[e] *= wgt;
      vgroup(false, 0, scp[7] * wgt);
    }
    *(LAS f32x2*)(lds + L_MRG + wave * 128 + 8 * c + 2 * r) = fold_rows(acc);
    cnt_arrive(lds, C_END, lane);
#pragma unroll
    for (int u = 0; u < 4; ++u) *(LAS u32x4*)(lds + L_WT + (wave * 16 + 4 * u + r) * WT_STR + 4 * c) = bB[u];
    cnt_arrive(lds, C_WT, lane);
    return;
#undef PACE
  }

  const int a = wave - NMAIN;
  const rsrc_t rx = mkrsrc(x, D * 2), rws = mkrsrc(ws, WS_END);
  unsigned tagA, tagB, tagC, tagD, tm0, tm1, tm2, tm3, tm4;
  unsigned long long e2;
  {
    asm volatile("" :: "s"(x), "s"(ws), "s"(wq));
    tm0 = (unsigned)__builtin_amdgcn_s_memrealtime() - t0; SB();
    u32x4 xa = ld16(rx, lane * 32, 0), xb = ld16(rx, lane * 32 + 16, 0);
    const u32x2 ep = __builtin_amdgcn_raw_buffer_load_b64(rws, 0, WS_EPOCH, SC1);
    const rsrc_t rwq = mkrsrc(wq, D * H * DH * 2);
    u32x4 w[8];
    const int voff = r * 8192 + c * 16, sb = (s * 128 + a * 32) * 8192 + h * 256;
#pragma unroll
    for (int i = 0; i < 8; ++i) { w[i] = ld16_nt(rwq, voff, sb + i * 32768); SB(); }
    if (a == 0 && lane < C_NUM) *(LAS unsigned*)(lds + L_CNT + lane) = 0u;
    __syncthreads();
    tm1 = (unsigned)__builtin_amdgcn_s_memrealtime() - t0; SB();
    asm volatile("" : "+v"(xa), "+v"(xb));
    SB(); tm2 = (unsigned)__builtin_amdgcn_s_memrealtime() - t0; SB();
    float v[16];
#pragma unroll
    for (int i = 0; i < 4; ++i) { v[2 * i] = lo2f(xa[i]); v[2 * i + 1] = hi2f(xa[i]); v[8 + 2 * i] = lo2f(xb[i]); v[9 + 2 * i] = hi2f(xb[i]); }
    float sm = 0.f;
#pragma unroll
    for (int i = 0; i < 16; ++i) sm += v[i];
    const float mean = wave_sum(sm) * (1.f / D);
    float ss = 0.f;
#pragma unroll
    for (int i = 0; i < 16; ++i) { const float d = v[i] - mean; ss = fmaf(d, d, ss); }
    const float var = wave_sum(ss) * (1.f / D);
    const float mb = rbf(mean), den = rbf(sqrtf(rbf(rbf(var) + 1e-5f)));
    const float rinv = 1.0f / den;
#pragma unroll
    for (int i = 0; i < 16; ++i) {
      const float nu = rbf(v[i] - mb), q0 = nu * rinv;
      v[i] = rbf(fmaf(fmaf(-q0, den, nu), rinv, q0));
    }
#pragma unroll
    for (int q = 0; q < 4; ++q) *(LAS f32x4*)(lds + L_XN + q * 256 + 4 * lane) = f32x4{v[q], v[4 + q], v[8 + q], v[12 + q]};
    cnt_arrive(lds, C_XN, lane);
    const unsigned long long e1 = (((unsigned long long)__builtin_amdgcn_readfirstlane(ep[1]) << 32) | (unsigned)__builtin_amdgcn_readfirstlane(ep[0])) + 1ull;
    e2 = e1 + 1ull;
    tagA = (unsigned)e1; tagB = (unsigned)(e1 >> 32) + (unsigned)e1 * 0x9E3779B1u;
    tagC = (unsigned)e2; tagD = (unsigned)(e2 >> 32) + (unsigned)e2 * 0x9E3779B1u;
    SB(); tm3 = (unsigned)__builtin_amdgcn_s_memrealtime() - t0; SB();
    asm volatile("" : "+v"(w[0]), "+v"(w[7]));
    SB(); tm4 = (unsigned)__builtin_amdgcn_s_memrealtime() - t0; SB();
    float acc[8] = {0.f, 0.f, 0.f, 0.f, 0.f, 0.f, 0.f, 0.f};
#pragma unroll
    for (int q = 0; q < 2; ++q) {
      const f32x4 xv = *(LAS f32x4*)(lds + L_XN + r * 256 + s * 32 + a * 8 + 4 * q);
#pragma unroll
      for (int e = 0; e < 4; ++e) fma8(acc, xv[e], w[4 * q + e]);
    }
    *(LAS f32x2*)(lds + L_QP + a * 128 + 8 * c + 2 * r) = fold_rows(acc);
  }
  cnt_arrive(lds, C_Q, lane);
  if (a == 0) {
    cnt_wait<false>(lds, C_Q, NAUX);
    f32x2 v = *(LAS f32x2*)(lds + L_QP + 2 * lane);
#pragma unroll
    for (int q = 1; q < 4; ++q) v += *(LAS f32x2*)(lds + L_QP + q * 128 + 2 * lane);
    st16_sc1(rws, WS_Q + (h * 8 + s) * 1024 + lane * 16, rec(v[0], v[1], tagA, tagB));
    st16_sc1(rws, (bid == 0 && lane == 0) ? WS_T0 : OOB, u32x4{t0, tagA, t0, tagB});
  }
  if (a == 1) {
    pace_wait(t0, T_QPOLL);
    float q0 = 0.f, q1 = 0.f;
    unsigned tg = t0;
#pragma unroll 1
    for (unsigned sp = 0; sp < SPIN_MEM; ++sp) {
      REREAD();
      u32x4 g[8];
#pragma unroll
      for (int i = 0; i < 8; ++i) g[i] = ld16_sc1(rws, lane * 16, WS_Q + (h * 8 + i) * 1024);
      const u32x4 gt = ld16_sc1(rws, 0, WS_T0);
      bool ok = true;
      tg = rok(gt, tagA, tagB) ? gt[0] : t0;
      q0 = 0.f; q1 = 0.f;
#pragma unroll
      for (int i = 0; i < 8; ++i) { ok = ok && rok(g[i], tagA, tagB); q0 += r0(g[i]); q1 += r1(g[i]); }
      if (__all(ok)) break;
      __builtin_amdgcn_s_sleep(2);
    }
    *(LAS unsigned*)(lds + L_QS + lane) = pack2(q0, q1);
    if (lane == 0) *(LAS unsigned*)(lds + L_CNT + W_T0) = ((int)(t0 - tg) < 0 || (int)(t0 - tg) > 300) ? t0 : tg;
    cnt_arrive(lds, C_QR, lane);
  }
  const int ks = xl * 2 + (j >> 4), cg = j & 15;
  u32x4 dw[7];
  {
    const rsrc_t rd = mkrsrc(wd, D * F * 2);
    const int voff = (lane >> 3) * 2048 + (lane & 7) * 16, sb = (ks * 224 + a * 56) * 2048 + cg * 128;
    constexpr int WSTEP = ((T_K0 - T_W2) << 16) / 7;
    int wdue = T_W2 * 65536 + (WSTEP / 128) * (a * 32 + j);
#pragma unroll
    for (int i = 0; i < 7; ++i) { pace_wait(t0, wdue >> 16); wdue += WSTEP; dw[i] = ld16_nt(rd, voff, sb + i * 16384); SB(); }
  }
  if (a == 0 && j < 28) {
    cnt_wait(lds, C_F1, NMAIN);
    f32x2 v = *(LAS f32x2*)(lds + L_FP + 2 * (lane & 31));
#pragma unroll
    for (int q = 1; q < 8; ++q) v += *(LAS f32x2*)(lds + L_FP + q * 64 + 2 * (lane & 31));
    if (f1 >= 56) {
      st16_sc1(rws, lane < 32 ? WS_G1 + ((mat * 2 + rb) * F + cb * 64) * 8 + lane * 16 : OOB, rec(v[0], v[1], tagA, tagB));
    } else {
      pace_wait(t0, T_K0 + T_HA);
      u32x4 g[3];
#pragma unroll 1
      for (unsigned sp = 0; sp < SPIN_MEM; ++sp) {
        REREAD();
#pragma unroll
        for (int k = 0; k < 3; ++k) g[k] = ld16_sc1(rws, (lane & 31) * 16, WS_G1 + ((k + 1) * F + cb * 64) * 8);
        if (__all(rok(g[0], tagA, tagB) && rok(g[1], tagA, tagB) && rok(g[2], tagA, tagB))) break;
        __builtin_amdgcn_s_sleep(32);
      }
      float hh[2];
#pragma unroll
      for (int e = 0; e < 2; ++e) {
        const float gs = rbf(v[e] + __uint_as_float(g[0][2 * e]));
        const float us = rbf(__uint_as_float(g[1][2 * e]) + __uint_as_float(g[2][2 * e]));
        const float ge = 0.5f * gs * (1.f + tanhf(0.7978845608028654f * (gs + 0.044715f * gs * gs * gs)));
        hh[e] = rbf(ge) * us;
      }
      const float hp = __uint_as_float(pack2(hh[0], hh[1]));
      st16_sc1(rws, (lane < 32 && !(lane & 1)) ? WS_H + cb * 256 + lane * 8 : OOB, rec(hp, dpp<XOR1>(hp), tagA, tagB));
    }
  }
  {
    pace_wait(t0, T_K0 + T_HB);
    u32x4 g;
#pragma unroll 1
    for (unsigned sp = 0; sp < SPIN_MEM; ++sp) {
      REREAD();
      g = ld16_sc1(rws, (lane < 14 ? lane : 13) * 16, WS_H + (ks * 56 + a * 14) * 16);
      if (__all(rok(g, tagA, tagB))) break;
      __builtin_amdgcn_s_sleep(32);
    }
    if (lane < 14) *(LAS f32x4*)(lds + L_H + a * 64 + 4 * lane) = f32x4{lo2f(g[0]), hi2f(g[0]), lo2f(g[2]), hi2f(g[2])};
    float y[8] = {0.f, 0.f, 0.f, 0.f, 0.f, 0.f, 0.f, 0.f};
#pragma unroll
    for (int i = 0; i < 7; ++i) fma8(y, lds[L_H + a * 64 + 8 * i + (lane >> 3)], dw[i]);
#pragma unroll
    for (int e = 0; e < 8; ++e) y[e] += dpp<ROR8>(y[e]);
    const f32x2 z = fold_rows(y);
    if (c < 8) *(LAS f32x2*)(lds + L_F2 + a * 64 + 8 * c + 2 * r) = z;
    cnt_arrive(lds, C_F2, lane);
  }
  const int col0 = s * 128 + 4 * h;
  unsigned xw = 0;
  if (a == 0) xw = __builtin_amdgcn_raw_buffer_load_b32(rx, (col0 + 2 * (lane & 1)) * 2, 0, 0);
  if (a == 3) {
    const unsigned po = __builtin_amdgcn_raw_buffer_load_b32(mkrsrc(out, D * 2), (col0 + 2 * (lane & 1)) * 2, 0, SC1);
    asm volatile("" :: "v"(po));
  }
  if (a == 1) {
    cnt_wait(lds, C_ML, NMAIN);
    const f32x2 ml = *(LAS f32x2*)(lds + L_ML + 2 * (lane & 7));
    const float Mw = max8(ml[0]), Lw = sum8(exp2_(ml[0] - Mw) * ml[1]);
    st16_sc1(rws, lane == 0 ? WS_ML + (h * 8 + s) * 128 : OOB, rec(Mw, Lw, tagA, tagB));
    pace_wait(t0, T_K0 + T_MAIN / 2 + 1500);
    u32x4 g;
#pragma unroll 1
    for (unsigned sp = 0; sp < SPIN_MEM; ++sp) {
      REREAD();
      g = ld16_sc1(rws, (lane & 7) * 128, WS_ML + h * 8 * 128);
      if (__all(rok(g, tagA, tagB))) break;
      __builtin_amdgcn_s_sleep(32);
    }
    const float M = max8(r0(g)), L = sum8(exp2_(r0(g) - M) * r1(g));
    if (lane < 8) lds[L_WG + lane] = exp2_(ml[0] - M) / L;
    cnt_arrive(lds, C_WG, lane);
  }
  pace_wait(t0, T_K0 + T_MAIN - 100);
  __builtin_amdgcn_s_setprio(3);
  if (a == 2) {
    cnt_wait(lds, C_F2, NAUX);
    f32x2 v = *(LAS f32x2*)(lds + L_F2 + 2 * (lane & 31));
#pragma unroll
    for (int q = 1; q < 4; ++q) v += *(LAS f32x2*)(lds + L_F2 + q * 64 + 2 * (lane & 31));
    pace_wait(t0, T_K0 + T_MAIN + 60);
    st16_sc1(rws, lane < 32 ? WS_FFN + (ks * D + cg * 64) * 8 + lane * 16 : OOB, rec(v[0], v[1], tagA, tagB));
  }
  if (a == 0) {
    cnt_wait<false>(lds, C_END, NMAIN);
    f32x2 v = *(LAS f32x2*)(lds + L_MRG + 2 * lane);
#pragma unroll
    for (int w = 1; w < 8; ++w) v += *(LAS f32x2*)(lds + L_MRG + w * 128 + 2 * lane);
    st16_sc1(rws, WS_Q + (h * 8 + s) * 1024 + lane * 16, rec(v[0], v[1], tagC, tagD));
  } else {
    cnt_wait(lds, C_END, NMAIN);
  }
  u32x4 bfr[2][4];
  cnt_wait<false>(lds, C_WT, NMAIN);
#pragma unroll
  for (int q = 0; q < 4; ++q)
#pragma unroll
    for (int d = 0; d < 4; ++d) {
      const unsigned w0 = *(LAS unsigned*)(lds + L_WT + (32 * q + 8 * r + 2 * d) * WT_STR + 16 * a + c);
      const unsigned w1 = *(LAS unsigned*)(lds + L_WT + (32 * q + 8 * r + 2 * d + 1) * WT_STR + 16 * a + c);
      bfr[0][q][d] = __builtin_amdgcn_perm(w1, w0, 0x05040100u);
      bfr[1][q][d] = __builtin_amdgcn_perm(w1, w0, 0x07060302u);
    }
  {
    u32x4 g0, g1;
    const int vo = r * 1024 + (16 * a + c) * 16;
#pragma unroll 1
    for (unsigned sp = 0; sp < SPIN_MEM; ++sp) {
      REREAD();
      g0 = ld16_sc1(rws, vo, WS_Q + h * 8 * 1024);
      g1 = ld16_sc1(rws, vo, WS_Q + (h * 8 + 4) * 1024);
      if (__all(rok(g0, tagC, tagD) && rok(g1, tagC, tagD))) break;
    }
    float o = fold32(r0(g0) + r0(g1), r1(g0) + r1(g1));
    o = fold16(o, o);
    if ((r & 1) == 0) *((LAS u16*)(lds + L_OB) + 32 * a + 2 * c + (r >> 1)) = __builtin_bit_cast(u16, (__bf16)o);
  }
  cnt_arrive(lds, C_OB, lane);
  cnt_wait<false>(lds, C_OB, NAUX);
  {
    f32x4 y0 = {0.f, 0.f, 0.f, 0.f}, y1 = y0;
#pragma unroll
    for (int q = 0; q < 4; ++q) {
      const u32x4 af = *(LAS u32x4*)(lds + L_OB + 16 * q + 4 * r);
      y0 = __builtin_amdgcn_mfma_f32_16x16x32_bf16(__builtin_bit_cast(bf16x8, af), __builtin_bit_cast(bf16x8, bfr[0][q]), y0, 0, 0, 0);
      y1 = __builtin_amdgcn_mfma_f32_16x16x32_bf16(__builtin_bit_cast(bf16x8, af), __builtin_bit_cast(bf16x8, bfr[1][q]), y1, 0, 0, 0);
    }
    st16_sc1(rws, lane < 16 ? WS_Y + (h * 8 + s) * 1024 + (16 * a + lane) * 16 : OOB, rec(y0[0], y1[0], tagA, tagB));
  }
  const rsrc_t rdg = mkrsrc((char*)ws + (6u << 20), 8192);
  if (a == 2) st16_sc1(rdg, lane == 0 ? bid * 16 : OOB, u32x4{t0, tagA, tm0, tagB});
  if (a == 0) {
    u32x4 g, gf;
    pace_wait((unsigned)__builtin_amdgcn_s_memrealtime(), 20);
#pragma unroll 1
    for (unsigned sp = 0; sp < SPIN_MEM; ++sp) {
      REREAD();
      g = ld16_sc1(rws, (lane >> 1) * (8 * 1024) + (lane & 1) * 16, WS_Y + s * 1024 + h * 32);
      gf = ld16_sc1(rws, ((lane >> 1) & 15) * (D * 8) + (lane & 1) * 16, WS_FFN + col0 * 8);
      if (__all(rok(g, tagA, tagB) && rok(gf, tagA, tagB))) break;
    }
    const float f0 = rbf(parity_sum(r0(gf)) * 0.5f), f1v = rbf(parity_sum(r1(gf)) * 0.5f);
    const float a0 = rbf(parity_sum(r0(g))), a1 = rbf(parity_sum(r1(g)));
    const unsigned o = pack2(rbf(lo2f(xw) + a0) + f0, rbf(hi2f(xw) + a1) + f1v);
    __builtin_amdgcn_raw_buffer_store_b32(o, mkrsrc(out, D * 2), lane < 2 ? (col0 + 2 * lane) * 2 : OOB, 0, SC1);
    __builtin_amdgcn_raw_buffer_store_b64(u32x2{(unsigned)e2, (unsigned)(e2 >> 32)}, rws, (bid == 0 && lane == 0) ? WS_EPOCH : OOB, 0, SC1);
    if (bid == 0) {
      u32x4 gg[4];
      for (unsigned sp = 0; sp < SPIN_MEM; ++sp) { REREAD();
        for (int k = 0; k < 4; ++k) gg[k] = ld16_sc1(rdg, lane * 16 + k * 1024, 0);
        if (__all(rok(gg[0], tagA, tagB) && rok(gg[1], tagA, tagB) && rok(gg[2], tagA, tagB) && rok(gg[3], tagA, tagB))) break; }
      int m2 = 0;
      for (int k = 0; k < 4; ++k) m2 += (int)gg[k][2];
      for (int o = 1; o < 64; o <<= 1) m2 += __shfl_xor(m2, o);
      int v = m2 / 256;
      v = v < 0 ? 0 : v > 4000 ? 4000 : v;
      pace_wait(t0, __builtin_amdgcn_readfirstlane(9500 + v * 10));
    }
  }
}

extern "C" void kernel_launch(void* const* d_in, const int* in_sizes, int n_in, void* d_out, int out_size, void* d_ws, size_t ws_size,
                              hipStream_t stream) {
  fused_decode_kernel<<<256, NTHR, 0, stream>>>((const u16*)d_in[0], (const u16*)d_in[1], (const u16*)d_in[2], (const u16*)d_in[3],
                                                 (const u16*)d_in[4], (const u16*)d_in[6], (const u16*)d_in[7], (const u16*)d_in[8],
                                                 (u16*)d_out, d_ws);
}
```

```cpp
#include <hip/hip_runtime.h>
#include <stdint.h>

typedef unsigned u32x4 __attribute__((ext_vector_type(4)));
typedef unsigned u32x2 __attribute__((ext_vector_type(2)));
typedef float f32x4 __attribute__((ext_vector_type(4)));
typedef float f32x2 __attribute__((ext_vector_type(2)));
typedef __bf16 bf16x2 __attribute__((ext_vector_type(2)));
typedef __bf16 bf16x8 __attribute__((ext_vector_type(8)));
typedef unsigned short u16;
#define LAS __attribute__((address_space(3)))
typedef __amdgpu_buffer_rsrc_t rsrc_t;

constexpr int T_F1 = 150;
constexpr int T_W2 = 360;
constexpr int T_K0 = 460;
constexpr int T_MAIN = 7090;
constexpr int PACE_G = 2;
constexpr int T_QLAG = 130;
constexpr int T_QPOLL = 390;
constexpr int T_HA = 1600, T_HB = 2800;

constexpr int D = 1024, H = 32, SK = 32768, DH = 128, F = 3584;
constexpr int NMAIN = 8, NAUX = 4, NTHR = (NMAIN + NAUX) * 64;
constexpr int NT = 2, SC1 = 16;
constexpr int OOB = 0x40000000;
constexpr unsigned WS_EPOCH = 0, WS_T0 = 1024, WS_ML = 4096, WS_Q = WS_ML + 256 * 128, WS_Y = WS_Q + 256 * 1024, WS_G1 = WS_Y + 256 * 1024,
                   WS_H = WS_G1 + 4 * F * 8, WS_FFN = WS_H + F * 4, WS_END = WS_FFN + 16 * D * 8;
constexpr int L_XN = 0, L_QP = 1024, L_QS = L_QP + 4 * 128, L_MRG = L_QS + 64, L_ML = L_MRG + 8 * 128, L_WG = L_ML + 16, L_FP = L_WG + 8,
              L_H = L_FP + 8 * 64, L_F2 = L_H + 4 * 64, L_OB = L_F2 + 4 * 64, L_CNT = L_OB + 64, L_SC = L_CNT + 16, L_WT = L_SC + 8 * 512,
              WT_STR = 68, L_END = L_WT + 128 * WT_STR;
constexpr int C_Q = 0, C_XN = 1, C_F2 = 2, C_WT = 3, C_ML = 4, C_WG = 5, C_END = 6, C_QR = 7, C_F1 = 8, C_OB = 9, C_NUM = 10, W_T0 = 12;
constexpr unsigned SPIN_MEM = 1u << 20, SPIN_LDS = 1u << 23;

__device__ __forceinline__ float lo2f(unsigned w) { return __builtin_bit_cast(float, w << 16); }
__device__ __forceinline__ float hi2f(unsigned w) { return __builtin_bit_cast(float, w & 0xffff0000u); }
__device__ __forceinline__ float rbf(float x) { return (float)(__bf16)x; }
__device__ __forceinline__ unsigned pack2(float a, float b) { bf16x2 v = {(__bf16)a, (__bf16)b}; return __builtin_bit_cast(unsigned, v); }
__device__ __forceinline__ float dot2(unsigned k, unsigned q, float acc) {
  return __builtin_amdgcn_fdot2_f32_bf16(__builtin_bit_cast(bf16x2, k), __builtin_bit_cast(bf16x2, q), acc, false);
}
template <int CTRL> __device__ __forceinline__ float dpp(float x) {
  return __builtin_bit_cast(float, __builtin_amdgcn_mov_dpp(__builtin_bit_cast(int, x), CTRL, 0xf, 0xf, true));
}
constexpr int XOR1 = 0xB1, XOR2 = 0x4E, XOR7 = 0x141, ROR8 = 0x128, ROR4 = 0x124, BCAST = 0x150;
__device__ __forceinline__ float fold32(float a, float b) {
  auto t = __builtin_amdgcn_permlane32_swap(__float_as_uint(a), __float_as_uint(b), false, false);
  return __uint_as_float(t[0]) + __uint_as_float(t[1]);
}
__device__ __forceinline__ float fold16(float a, float b) {
  auto t = __builtin_amdgcn_permlane16_swap(__float_as_uint(a), __float_as_uint(b), false, false);
  return __uint_as_float(t[0]) + __uint_as_float(t[1]);
}
__device__ __forceinline__ f32x2 fold_rows(const float (&v)[8]) {
  const float u0 = fold32(v[0], v[4]), u1 = fold32(v[1], v[5]), u2 = fold32(v[2], v[6]), u3 = fold32(v[3], v[7]);
  return f32x2{fold16(u0, u2), fold16(u1, u3)};
}
__device__ __forceinline__ float xrow_sum(float x) { x = fold16(x, x); return fold32(x, x); }
__device__ __forceinline__ float xrow_max(float x) {
  auto s = __builtin_amdgcn_permlane16_swap(__float_as_uint(x), __float_as_uint(x), false, false);
  x = fmaxf(__uint_as_float(s[0]), __uint_as_float(s[1]));
  auto t = __builtin_amdgcn_permlane32_swap(__float_as_uint(x), __float_as_uint(x), false, false);
  return fmaxf(__uint_as_float(t[0]), __uint_as_float(t[1]));
}
__device__ __forceinline__ float parity_sum(float x) {
  x += dpp<XOR2>(x); x += dpp<ROR4>(x); x += dpp<ROR8>(x); return xrow_sum(x);
}
__device__ __forceinline__ float wave_sum(float x) { x += dpp<XOR1>(x); return parity_sum(x); }
__device__ __forceinline__ float max8(float x) { x = fmaxf(x, dpp<XOR1>(x)); x = fmaxf(x, dpp<XOR2>(x)); return fmaxf(x, dpp<XOR7>(x)); }
__device__ __forceinline__ float sum8(float x) { x += dpp<XOR1>(x); x += dpp<XOR2>(x); return x + dpp<XOR7>(x); }
__device__ __forceinline__ float wave_max(float x) { x = max8(x); x = fmaxf(x, dpp<ROR8>(x)); return xrow_max(x); }
__device__ __forceinline__ float exp2_(float x) { return __builtin_amdgcn_exp2f(x); }
__device__ __forceinline__ rsrc_t mkrsrc(const void* p, unsigned bytes) { return __builtin_amdgcn_make_buffer_rsrc((void*)p, 0, (int)bytes, 0x00020000); }
__device__ __forceinline__ u32x4 ld16(rsrc_t r, int voff, int soff) { return __builtin_amdgcn_raw_buffer_load_b128(r, voff, soff, 0); }
__device__ __forceinline__ u32x4 ld16_nt(rsrc_t r, int voff, int soff) { return __builtin_amdgcn_raw_buffer_load_b128(r, voff, soff, NT); }
__device__ __forceinline__ u32x4 ld16_sc1(rsrc_t r, int voff, int soff) { return __builtin_amdgcn_raw_buffer_load_b128(r, voff, soff, SC1); }
__device__ __forceinline__ void st16_sc1(rsrc_t r, int off, u32x4 v) { __builtin_amdgcn_raw_buffer_store_b128(v, r, off, 0, SC1); }
#define SB() __builtin_amdgcn_sched_barrier(0)
#define REREAD() asm volatile("" ::: "memory")
__device__ __forceinline__ void pace_wait(unsigned t0, int due) {
  asm volatile("1:\n\ts_memrealtime s[96:97]\n\ts_waitcnt lgkmcnt(0)\n\ts_sub_u32 s96, s96, %0\n\ts_cmp_lt_i32 s96, %1\n\t"
               "s_cbranch_scc0 2f\n\ts_sleep 1\n\ts_branch 1b\n2:"
               :: "s"(t0), "s"(due) : "s96", "s97", "scc", "memory");
}
__device__ __forceinline__ void fma8(float (&acc)[8], float s, u32x4 w) {
#pragma unroll
  for (int t = 0; t < 4; ++t) { acc[2 * t] = fmaf(s, lo2f(w[t]), acc[2 * t]); acc[2 * t + 1] = fmaf(s, hi2f(w[t]), acc[2 * t + 1]); }
}
__device__ __forceinline__ u32x4 rec(float a, float b, unsigned ta, unsigned tb) { return u32x4{__float_as_uint(a), ta, __float_as_uint(b), tb}; }
__device__ __forceinline__ bool rok(u32x4 g, unsigned ta, unsigned tb) { return g[1] == ta && g[3] == tb; }
__device__ __forceinline__ float r0(u32x4 g) { return __uint_as_float(g[0]); }
__device__ __forceinline__ float r1(u32x4 g) { return __uint_as_float(g[2]); }

__device__ __forceinline__ void cnt_arrive(LAS float* lds, int which, int lane) {
  asm volatile("s_waitcnt lgkmcnt(0)" ::: "memory");
  if (lane == 0) __hip_atomic_fetch_add((LAS unsigned*)(lds + L_CNT + which), 1u, __ATOMIC_RELAXED, __HIP_MEMORY_SCOPE_WORKGROUP);
}
template <bool SLEEP = true>
__device__ __forceinline__ void cnt_wait(LAS float* lds, int which, unsigned n) {
#pragma unroll 1
  for (unsigned sp = 0; sp < SPIN_LDS; ++sp) {
    if (__builtin_amdgcn_readfirstlane(*(volatile LAS unsigned*)(lds + L_CNT + which)) >= n) break;
    if (SLEEP) __builtin_amdgcn_s_sleep(1);
  }
  REREAD();
}

__global__ void __launch_bounds__(NTHR, 3)
fused_decode_kernel(const u16* __restrict__ x, const u16* __restrict__ Kc, const u16* __restrict__ Vc, const u16* __restrict__ wq,
                    const u16* __restrict__ wo, const u16* __restrict__ wg, const u16* __restrict__ wu, const u16* __restrict__ wd,
                    u16* __restrict__ out, void* ws) {
  __shared__ __attribute__((aligned(16))) float lds_[L_END];
  LAS float* lds = (LAS float*)lds_;
  const unsigned t0 = (unsigned)__builtin_amdgcn_s_memrealtime();
  const int tid = threadIdx.x, lane = tid & 63, wave = __builtin_amdgcn_readfirstlane(tid >> 6);
  const int r = lane >> 4, c = lane & 15;
  const int bid = blockIdx.x, xl = bid & 7, j = bid >> 3;
  const int h = xl * 4 + (j >> 3), s = j & 7;
  const int f1 = xl * 28 + j, mat = f1 / 112, rb = (f1 / 56) & 1, cb = f1 % 56;

  if (wave < NMAIN) {
    const rsrc_t rk = mkrsrc(Kc, (unsigned)H * SK * DH * 2), rv = mkrsrc(Vc, (unsigned)H * SK * DH * 2);
    u32x4 bA[16], bB[16];
    const unsigned kvbase = (unsigned)h * (8u << 20) + (unsigned)s * (1u << 20) + wave * 1024;
    const unsigned rot = 32u * ((h + (h >> 2) + (h >> 4)) & 3);
    const int kvoff = lane * 16;
    constexpr int STEP = (T_MAIN << 16) / 256 * PACE_G;
    int due = T_K0 * 65536 - STEP + (STEP / 256) * (wave * 32 + j);
#define PACE(T0, I) do { if ((I) % PACE_G == 0) { due += STEP; pace_wait(T0, due >> 16); } } while (0)
    __builtin_amdgcn_s_barrier();
    SB();
    {
      const rsrc_t rg = mkrsrc(mat ? wu : wg, D * F * 2);
      const int voff = j < 28 ? (lane >> 3) * (F * 2) + (lane & 7) * 16 : OOB, sb = (rb * 512 + wave * 64) * (F * 2) + cb * 128;
      constexpr int FSTEP = ((T_W2 - T_F1) << 16) / 8;
      int fdue = T_F1 * 65536 + (FSTEP / 256) * (wave * 32 + j);
#pragma unroll
      for (int i = 0; i < 8; ++i) { pace_wait(t0, fdue >> 16); fdue += FSTEP; bB[i] = ld16_nt(rg, voff, sb + i * 8 * (F * 2)); SB(); }
    }
#pragma unroll
    for (int i = 0; i < 16; ++i) { PACE(t0, i); bA[i] = ld16_nt(rk, kvoff, kvbase + (rot + i) * 8192); SB(); }
    {
      cnt_wait(lds, C_XN, 1);
      float ag[8] = {0.f, 0.f, 0.f, 0.f, 0.f, 0.f, 0.f, 0.f};
#pragma unroll
      for (int i = 0; i < 8; ++i) {
        const int k = rb * 512 + wave * 64 + 8 * i + (lane >> 3);
        fma8(ag, lds[L_XN + (k & 3) * 256 + (k >> 2)], bB[i]);
      }
#pragma unroll
      for (int e = 0; e < 8; ++e) ag[e] += dpp<ROR8>(ag[e]);
      const f32x2 z = fold_rows(ag);
      if (c < 8) *(LAS f32x2*)(lds + L_FP + wave * 64 + 8 * c + 2 * r) = z;
      cnt_arrive(lds, C_F1, lane);
      SB();
    }
#pragma unroll
    for (int i = 0; i < 16; ++i) { PACE(t0, i); bB[i] = ld16_nt(rk, kvoff, kvbase + (rot + 16 + i) * 8192); SB(); }
    cnt_wait(lds, C_QR, 1);
    const u32x4 qp = *(LAS u32x4*)(lds + L_QS + 4 * c);
    const unsigned t0m = __builtin_amdgcn_readfirstlane(*(LAS unsigned*)(lds + L_CNT + W_T0));
    constexpr float SCALE = 0.08838834764831845f, LOG2E = 1.4426950408889634f;
    LAS float* scp = lds + L_SC + wave * 512 + lane * 8;
    float mx = -INFINITY;
    auto kgroup = [&](u32x4 (&kb)[16], const bool more, const rsrc_t rn, const unsigned tb, const int g) __attribute__((always_inline)) {
      float sx[16];
#pragma unroll
      for (int i = 0; i < 16; ++i) {
        float d = 0.f;
#pragma unroll
        for (int t = 0; t < 4; ++t) d = dot2(kb[i][t], qp[t], d);
        asm volatile("" : "+v"(d));
        sx[i] = d;
        SB();
        if (more) { PACE(t0m, i); kb[i] = ld16_nt(rn, kvoff, tb + i * 8192); SB(); }
      }
#define TR_STEP(HALF, CTRL, BIT) _Pragma("unroll") for (int hh = 0; hh < (HALF); ++hh) { \
        const float lo_ = sx[hh], hi_ = sx[hh + (HALF)]; const float t1_ = lo_ + dpp<CTRL>(lo_), t2_ = hi_ + dpp<CTRL>(hi_); \
        sx[hh] = (c & (BIT)) ? t2_ : t1_; }
      TR_STEP(8, ROR8, 8) TR_STEP(4, XOR7, 4) TR_STEP(2, XOR2, 2) TR_STEP(1, XOR1, 1)
#undef TR_STEP
      const float sc = rbf(rbf(sx[0]) * SCALE) * LOG2E;
      mx = fmaxf(mx, sc);
      scp[g] = sc;
      SB();
    };
    kgroup(bA, false, rk, 0, 0);
#pragma unroll 1
    for (int g = 1; g < 8; ++g)
      kgroup(bB, true, mkrsrc(g == 7 ? Vc : Kc, (unsigned)H * SK * DH * 2), kvbase + (((g + 1) * 16 + rot) & 127u) * 8192, g);
    {
      const float m = wave_max(mx);
      f32x4 p0 = *(LAS f32x4*)scp, p1 = *(LAS f32x4*)(scp + 4);
      float l = 0.f;
#pragma unroll
      for (int e = 0; e < 4; ++e) { p0[e] = exp2_(p0[e] - m); p1[e] = exp2_(p1[e] - m); l += p0[e] + p1[e]; }
      *(LAS f32x4*)scp = p0; *(LAS f32x4*)(scp + 4) = p1;
      l = wave_sum(l);
      if (lane == 0) *(LAS f32x2*)(lds + L_ML + 2 * wave) = f32x2{m, l};
      cnt_arrive(lds, C_ML, lane);
    }
    float acc[8] = {0.f, 0.f, 0.f, 0.f, 0.f, 0.f, 0.f, 0.f};
    auto vgroup = [&](const bool more, const unsigned tb, const float p) __attribute__((always_inline)) {
#define PV_STEP(I) { fma8(acc, dpp<BCAST + (I)>(p), bB[I]); \
        asm volatile("" : "+v"(acc[0]), "+v"(acc[1]), "+v"(acc[2]), "+v"(acc[3]), "+v"(acc[4]), "+v"(acc[5]), "+v"(acc[6]), "+v"(acc[7])); SB(); \
        if (more) { PACE(t0m, I); bB[I] = ld16_nt(rv, kvoff, tb + (I) * 8192); SB(); } \
        else if ((I) == 11) {   \
          pace_wait(t0m, T_K0 + T_MAIN); \
          _Pragma("unroll") for (int u = 0; u < 4; ++u) { \
            bB[u] = ld16_nt(mkrsrc(wo, H * DH * D * 2), r * 2048 + c * 16, (h * 128 + wave * 16 + 4 * u) * 2048 + s * 256); SB(); } } }
      PV_STEP(0) PV_STEP(1) PV_STEP(2) PV_STEP(3) PV_STEP(4) PV_STEP(5) PV_STEP(6) PV_STEP(7)
      PV_STEP(8) PV_STEP(9) PV_STEP(10) PV_STEP(11) PV_STEP(12) PV_STEP(13) PV_STEP(14) PV_STEP(15)
#undef PV_STEP
    };
#pragma unroll 1
    for (int g = 0; g < 7; ++g) vgroup(true, kvbase + (((g + 1) * 16 + rot) & 127u) * 8192, scp[g]);
    __builtin_amdgcn_s_setprio(3);
    cnt_wait(lds, C_WG, 1);
    {
      const float wgt = lds[L_WG + wave];
#pragma unroll
      for (int e = 0; e < 8; ++e) acc[e] *= wgt;
      vgroup(false, 0, scp[7] * wgt);
    }
    *(LAS f32x2*)(lds + L_MRG + wave * 128 + 8 * c + 2 * r) = fold_rows(acc);
    cnt_arrive(lds, C_END, lane);
#pragma unroll
    for (int u = 0; u < 4; ++u) *(LAS u32x4*)(lds + L_WT + (wave * 16 + 4 * u + r) * WT_STR + 4 * c) = bB[u];
    cnt_arrive(lds, C_WT, lane);
    return;
#undef PACE
  }

  const int a = wave - NMAIN;
  const rsrc_t rx = mkrsrc(x, D * 2), rws = mkrsrc(ws, WS_END);
  unsigned tagA, tagB, tagC, tagD;
  unsigned long long e2;
  {
    u32x4 xa = ld16(rx, lane * 32, 0), xb = ld16(rx, lane * 32 + 16, 0);
    const u32x2 ep = __builtin_amdgcn_raw_buffer_load_b64(rws, 0, WS_EPOCH, SC1);
    const rsrc_t rwq = mkrsrc(wq, D * H * DH * 2);
    u32x4 w[8];
    const int voff = r * 8192 + c * 16, sb = (s * 128 + a * 32) * 8192 + h * 256;
#pragma unroll
    for (int i = 0; i < 8; ++i) { w[i] = ld16_nt(rwq, voff, sb + i * 32768); SB(); }
    if (a == 0 && lane < C_NUM) *(LAS unsigned*)(lds + L_CNT + lane) = 0u;
    __syncthreads();
    asm volatile("" : "+v"(xa), "+v"(xb));
    float v[16];
#pragma unroll
    for (int i = 0; i < 4; ++i) { v[2 * i] = lo2f(xa[i]); v[2 * i + 1] = hi2f(xa[i]); v[8 + 2 * i] = lo2f(xb[i]); v[9 + 2 * i] = hi2f(xb[i]); }
    float sm = 0.f;
#pragma unroll
    for (int i = 0; i < 16; ++i) sm += v[i];
    const float mean = wave_sum(sm) * (1.f / D);
    float ss = 0.f;
#pragma unroll
    for (int i = 0; i < 16; ++i) { const float d = v[i] - mean; ss = fmaf(d, d, ss); }
    const float var = wave_sum(ss) * (1.f / D);
    const float mb = rbf(mean), den = rbf(sqrtf(rbf(rbf(var) + 1e-5f)));
    const float rinv = 1.0f / den;
#pragma unroll
    for (int i = 0; i < 16; ++i) {
      const float nu = rbf(v[i] - mb), q0 = nu * rinv;
      v[i] = rbf(fmaf(fmaf(-q0, den, nu), rinv, q0));
    }
#pragma unroll
    for (int q = 0; q < 4; ++q) *(LAS f32x4*)(lds + L_XN + q * 256 + 4 * lane) = f32x4{v[q], v[4 + q], v[8 + q], v[12 + q]};
    cnt_arrive(lds, C_XN, lane);
    const unsigned long long e1 = (((unsigned long long)__builtin_amdgcn_readfirstlane(ep[1]) << 32) | (unsigned)__builtin_amdgcn_readfirstlane(ep[0])) + 1ull;
    e2 = e1 + 1ull;
    tagA = (unsigned)e1; tagB = (unsigned)(e1 >> 32) + (unsigned)e1 * 0x9E3779B1u;
    tagC = (unsigned)e2; tagD = (unsigned)(e2 >> 32) + (unsigned)e2 * 0x9E3779B1u;
    float acc[8] = {0.f, 0.f, 0.f, 0.f, 0.f, 0.f, 0.f, 0.f};
#pragma unroll
    for (int q = 0; q < 2; ++q) {
      const f32x4 xv = *(LAS f32x4*)(lds + L_XN + r * 256 + s * 32 + a * 8 + 4 * q);
#pragma unroll
      for (int e = 0; e < 4; ++e) fma8(acc, xv[e], w[4 * q + e]);
    }
    *(LAS f32x2*)(lds + L_QP + a * 128 + 8 * c + 2 * r) = fold_rows(acc);
  }
  cnt_arrive(lds, C_Q, lane);
  if (a == 0) {
    cnt_wait<false>(lds, C_Q, NAUX);
    f32x2 v = *(LAS f32x2*)(lds + L_QP + 2 * lane);
#pragma unroll
    for (int q = 1; q < 4; ++q) v += *(LAS f32x2*)(lds + L_QP + q * 128 + 2 * lane);
    st16_sc1(rws, WS_Q + (h * 8 + s) * 1024 + lane * 16, rec(v[0], v[1], tagA, tagB));
    st16_sc1(rws, (bid == 0 && lane == 0) ? WS_T0 : OOB, u32x4{t0, tagA, t0, tagB});
  }
  if (a == 1) {
    cnt_wait<false>(lds, C_Q, NAUX);
    pace_wait((unsigned)__builtin_amdgcn_s_memrealtime(), T_QLAG);
    pace_wait(t0, T_QPOLL);
    float q0 = 0.f, q1 = 0.f;
    unsigned tg = t0;
#pragma unroll 1
    for (unsigned sp = 0; sp < SPIN_MEM; ++sp) {
      REREAD();
      u32x4 g[8];
#pragma unroll
      for (int i = 0; i < 8; ++i) g[i] = ld16_sc1(rws, lane * 16, WS_Q + (h * 8 + i) * 1024);
      const u32x4 gt = ld16_sc1(rws, 0, WS_T0);
      bool ok = true;
      tg = rok(gt, tagA, tagB) ? gt[0] : t0;
      q0 = 0.f; q1 = 0.f;
#pragma unroll
      for (int i = 0; i < 8; ++i) { ok = ok && rok(g[i], tagA, tagB); q0 += r0(g[i]); q1 += r1(g[i]); }
      if (__all(ok)) break;
      __builtin_amdgcn_s_sleep(2);
    }
    *(LAS unsigned*)(lds + L_QS + lane) = pack2(q0, q1);
    if (lane == 0) *(LAS unsigned*)(lds + L_CNT + W_T0) = ((int)(t0 - tg) < 0 || (int)(t0 - tg) > 300) ? t0 : tg;
    cnt_arrive(lds, C_QR, lane);
  }
  const int ks = xl * 2 + (j >> 4), cg = j & 15;
  u32x4 dw[7];
  {
    const rsrc_t rd = mkrsrc(wd, D * F * 2);
    const int voff = (lane >> 3) * 2048 + (lane & 7) * 16, sb = (ks * 224 + a * 56) * 2048 + cg * 128;
    constexpr int WSTEP = ((T_K0 - T_W2) << 16) / 7;
    int wdue = T_W2 * 65536 + (WSTEP / 128) * (a * 32 + j);
#pragma unroll
    for (int i = 0; i < 7; ++i) { pace_wait(t0, wdue >> 16); wdue += WSTEP; dw[i] = ld16_nt(rd, voff, sb + i * 16384); SB(); }
  }
  if (a == 0 && j < 28) {
    cnt_wait(lds, C_F1, NMAIN);
    f32x2 v = *(LAS f32x2*)(lds + L_FP + 2 * (lane & 31));
#pragma unroll
    for (int q = 1; q < 8; ++q) v += *(LAS f32x2*)(lds + L_FP + q * 64 + 2 * (lane & 31));
    if (f1 >= 56) {
      st16_sc1(rws, lane < 32 ? WS_G1 + ((mat * 2 + rb) * F + cb * 64) * 8 + lane * 16 : OOB, rec(v[0], v[1], tagA, tagB));
    } else {
      pace_wait(t0, T_K0 + T_HA);
      u32x4 g[3];
#pragma unroll 1
      for (unsigned sp = 0; sp < SPIN_MEM; ++sp) {
        REREAD();
#pragma unroll
        for (int k = 0; k < 3; ++k) g[k] = ld16_sc1(rws, (lane & 31) * 16, WS_G1 + ((k + 1) * F + cb * 64) * 8);
        if (__all(rok(g[0], tagA, tagB) && rok(g[1], tagA, tagB) && rok(g[2], tagA, tagB))) break;
        __builtin_amdgcn_s_sleep(32);
      }
      float hh[2];
#pragma unroll
      for (int e = 0; e < 2; ++e) {
        const float gs = rbf(v[e] + __uint_as_float(g[0][2 * e]));
        const float us = rbf(__uint_as_float(g[1][2 * e]) + __uint_as_float(g[2][2 * e]));
        const float ge = 0.5f * gs * (1.f + tanhf(0.7978845608028654f * (gs + 0.044715f * gs * gs * gs)));
        hh[e] = rbf(ge) * us;
      }
      const float hp = __uint_as_float(pack2(hh[0], hh[1]));
      st16_sc1(rws, (lane < 32 && !(lane & 1)) ? WS_H + cb * 256 + lane * 8 : OOB, rec(hp, dpp<XOR1>(hp), tagA, tagB));
    }
  }
  {
    pace_wait(t0, T_K0 + T_HB);
    u32x4 g;
#pragma unroll 1
    for (unsigned sp = 0; sp < SPIN_MEM; ++sp) {
      REREAD();
      g = ld16_sc1(rws, (lane < 14 ? lane : 13) * 16, WS_H + (ks * 56 + a * 14) * 16);
      if (__all(rok(g, tagA, tagB))) break;
      __builtin_amdgcn_s_sleep(32);
    }
    if (lane < 14) *(LAS f32x4*)(lds + L_H + a * 64 + 4 * lane) = f32x4{lo2f(g[0]), hi2f(g[0]), lo2f(g[2]), hi2f(g[2])};
    float y[8] = {0.f, 0.f, 0.f, 0.f, 0.f, 0.f, 0.f, 0.f};
#pragma unroll
    for (int i = 0; i < 7; ++i) fma8(y, lds[L_H + a * 64 + 8 * i + (lane >> 3)], dw[i]);
#pragma unroll
    for (int e = 0; e < 8; ++e) y[e] += dpp<ROR8>(y[e]);
    const f32x2 z = fold_rows(y);
    if (c < 8) *(LAS f32x2*)(lds + L_F2 + a * 64 + 8 * c + 2 * r) = z;
    cnt_arrive(lds, C_F2, lane);
  }
  const int col0 = s * 128 + 4 * h;
  unsigned xw = 0;
  if (a == 0) xw = __builtin_amdgcn_raw_buffer_load_b32(rx, (col0 + 2 * (lane & 1)) * 2, 0, 0);
  if (a == 3) {
    const unsigned po = __builtin_amdgcn_raw_buffer_load_b32(mkrsrc(out, D * 2), (col0 + 2 * (lane & 1)) * 2, 0, SC1);
    asm volatile("" :: "v"(po));
  }
  if (a == 1) {
    cnt_wait(lds, C_ML, NMAIN);
    const f32x2 ml = *(LAS f32x2*)(lds + L_ML + 2 * (lane & 7));
    const float Mw = max8(ml[0]), Lw = sum8(exp2_(ml[0] - Mw) * ml[1]);
    st16_sc1(rws, lane == 0 ? WS_ML + (h * 8 + s) * 128 : OOB, rec(Mw, Lw, tagA, tagB));
    pace_wait(t0, T_K0 + T_MAIN / 2 + 1500);
    u32x4 g;
#pragma unroll 1
    for (unsigned sp = 0; sp < SPIN_MEM; ++sp) {
      REREAD();
      g = ld16_sc1(rws, (lane & 7) * 128, WS_ML + h * 8 * 128);
      if (__all(rok(g, tagA, tagB))) break;
      __builtin_amdgcn_s_sleep(32);
    }
    const float M = max8(r0(g)), L = sum8(exp2_(r0(g) - M) * r1(g));
    if (lane < 8) lds[L_WG + lane] = exp2_(ml[0] - M) / L;
    cnt_arrive(lds, C_WG, lane);
  }
  pace_wait(t0, T_K0 + T_MAIN - 100);
  __builtin_amdgcn_s_setprio(3);
  if (a == 2) {
    cnt_wait(lds, C_F2, NAUX);
    f32x2 v = *(LAS f32x2*)(lds + L_F2 + 2 * (lane & 31));
#pragma unroll
    for (int q = 1; q < 4; ++q) v += *(LAS f32x2*)(lds + L_F2 + q * 64 + 2 * (lane & 31));
    pace_wait(t0, T_K0 + T_MAIN + 60);
    st16_sc1(rws, lane < 32 ? WS_FFN + (ks * D + cg * 64) * 8 + lane * 16 : OOB, rec(v[0], v[1], tagA, tagB));
  }
  if (a == 0) {
    cnt_wait<false>(lds, C_END, NMAIN);
    f32x2 v = *(LAS f32x2*)(lds + L_MRG + 2 * lane);
#pragma unroll
    for (int w = 1; w < 8; ++w) v += *(LAS f32x2*)(lds + L_MRG + w * 128 + 2 * lane);
    st16_sc1(rws, WS_Q + (h * 8 + s) * 1024 + lane * 16, rec(v[0], v[1], tagC, tagD));
  } else {
    cnt_wait(lds, C_END, NMAIN);
  }
  u32x4 bfr[2][4];
  cnt_wait<false>(lds, C_WT, NMAIN);
#pragma unroll
  for (int q = 0; q < 4; ++q)
#pragma unroll
    for (int d = 0; d < 4; ++d) {
      const unsigned w0 = *(LAS unsigned*)(lds + L_WT + (32 * q + 8 * r + 2 * d) * WT_STR + 16 * a + c);
      const unsigned w1 = *(LAS unsigned*)(lds + L_WT + (32 * q + 8 * r + 2 * d + 1) * WT_STR + 16 * a + c);
      bfr[0][q][d] = __builtin_amdgcn_perm(w1, w0, 0x05040100u);
      bfr[1][q][d] = __builtin_amdgcn_perm(w1, w0, 0x07060302u);
    }
  {
    u32x4 g0, g1;
    const int vo = r * 1024 + (16 * a + c) * 16;
#pragma unroll 1
    for (unsigned sp = 0; sp < SPIN_MEM; ++sp) {
      REREAD();
      g0 = ld16_sc1(rws, vo, WS_Q + h * 8 * 1024);
      g1 = ld16_sc1(rws, vo, WS_Q + (h * 8 + 4) * 1024);
      if (__all(rok(g0, tagC, tagD) && rok(g1, tagC, tagD))) break;
    }
    float o = fold32(r0(g0) + r0(g1), r1(g0) + r1(g1));
    o = fold16(o, o);
    if ((r & 1) == 0) *((LAS u16*)(lds + L_OB) + 32 * a + 2 * c + (r >> 1)) = __builtin_bit_cast(u16, (__bf16)o);
  }
  cnt_arrive(lds, C_OB, lane);
  cnt_wait<false>(lds, C_OB, NAUX);
  {
    f32x4 y0 = {0.f, 0.f, 0.f, 0.f}, y1 = y0;
#pragma unroll
    for (int q = 0; q < 4; ++q) {
      const u32x4 af = *(LAS u32x4*)(lds + L_OB + 16 * q + 4 * r);
      y0 = __builtin_amdgcn_mfma_f32_16x16x32_bf16(__builtin_bit_cast(bf16x8, af), __builtin_bit_cast(bf16x8, bfr[0][q]), y0, 0, 0, 0);
      y1 = __builtin_amdgcn_mfma_f32_16x16x32_bf16(__builtin_bit_cast(bf16x8, af), __builtin_bit_cast(bf16x8, bfr[1][q]), y1, 0, 0, 0);
    }
    st16_sc1(rws, lane < 16 ? WS_Y + (h * 8 + s) * 1024 + (16 * a + lane) * 16 : OOB, rec(y0[0], y1[0], tagA, tagB));
  }
  if (a == 0) {
    u32x4 g, gf;
    pace_wait((unsigned)__builtin_amdgcn_s_memrealtime(), 20);
#pragma unroll 1
    for (unsigned sp = 0; sp < SPIN_MEM; ++sp) {
      REREAD();
      g = ld16_sc1(rws, (lane >> 1) * (8 * 1024) + (lane & 1) * 16, WS_Y + s * 1024 + h * 32);
      gf = ld16_sc1(rws, ((lane >> 1) & 15) * (D * 8) + (lane & 1) * 16, WS_FFN + col0 * 8);
      if (__all(rok(g, tagA, tagB) && rok(gf, tagA, tagB))) break;
    }
    const float f0 = rbf(parity_sum(r0(gf)) * 0.5f), f1v = rbf(parity_sum(r1(gf)) * 0.5f);
    const float a0 = rbf(parity_sum(r0(g))), a1 = rbf(parity_sum(r1(g)));
    const unsigned o = pack2(rbf(lo2f(xw) + a0) + f0, rbf(hi2f(xw) + a1) + f1v);
    __builtin_amdgcn_raw_buffer_store_b32(o, mkrsrc(out, D * 2), lane < 2 ? (col0 + 2 * lane) * 2 : OOB, 0, SC1);
    __builtin_amdgcn_raw_buffer_store_b64(u32x2{(unsigned)e2, (unsigned)(e2 >> 32)}, rws, (bid == 0 && lane == 0) ? WS_EPOCH : OOB, 0, SC1);
  }
}

extern "C" void kernel_launch(void* const* d_in, const int* in_sizes, int n_in, void* d_out, int out_size, void* d_ws, size_t ws_size,
                              hipStream_t stream) {
  fused_decode_kernel<<<256, NTHR, 0, stream>>>((const u16*)d_in[0], (const u16*)d_in[1], (const u16*)d_in[2], (const u16*)d_in[3],
                                                 (const u16*)d_in[4], (const u16*)d_in[6], (const u16*)d_in[7], (const u16*)d_in[8],
                                                 (u16*)d_out, d_ws);
}
```

```cpp
#include <hip/hip_runtime.h>
#include <stdint.h>

typedef unsigned u32x4 __attribute__((ext_vector_type(4)));
typedef unsigned u32x2 __attribute__((ext_vector_type(2)));
typedef float f32x4 __attribute__((ext_vector_type(4)));
typedef float f32x2 __attribute__((ext_vector_type(2)));
typedef __bf16 bf16x2 __attribute__((ext_vector_type(2)));
typedef __bf16 bf16x8 __attribute__((ext_vector_type(8)));
typedef unsigned short u16;
#define LAS __attribute__((address_space(3)))
typedef __amdgpu_buffer_rsrc_t rsrc_t;

constexpr int T_F1 = 70;
constexpr int T_W2 = 280;
constexpr int T_K0 = 380;
constexpr int T_MAIN = 7000;
constexpr int PACE_G = 2;
constexpr int T_QPOLL = 315;
constexpr int T_HA = 1600, T_HB = 2800;

constexpr int D = 1024, H = 32, SK = 32768, DH = 128, F = 3584;
constexpr int NMAIN = 8, NAUX = 4, NTHR = (NMAIN + NAUX) * 64;
constexpr int NT = 2, SC1 = 16;
constexpr int OOB = 0x40000000;
constexpr unsigned WS_EPOCH = 0, WS_T0 = 1024, WS_ML = 4096, WS_Q = WS_ML + 256 * 128, WS_Y = WS_Q + 256 * 1024, WS_G1 = WS_Y + 256 * 1024,
                   WS_H = WS_G1 + 4 * F * 8, WS_FFN = WS_H + F * 4, WS_END = WS_FFN + 16 * D * 8;
constexpr int L_XN = 0, L_QP = 1024, L_QS = L_QP + 4 * 128, L_MRG = L_QS + 64, L_ML = L_MRG + 8 * 128, L_WG = L_ML + 16, L_FP = L_WG + 8,
              L_H = L_FP + 8 * 64, L_F2 = L_H + 4 * 64, L_OB = L_F2 + 4 * 64, L_CNT = L_OB + 64, L_SC = L_CNT + 16, L_WT = L_SC + 8 * 512,
              WT_STR = 68, L_END = L_WT + 128 * WT_STR;
constexpr int C_Q = 0, C_XN = 1, C_F2 = 2, C_WT = 3, C_ML = 4, C_WG = 5, C_END = 6, C_QR = 7, C_F1 = 8, C_OB = 9, C_NUM = 10, W_T0 = 12;
constexpr unsigned SPIN_MEM = 1u << 20, SPIN_LDS = 1u << 23;

__device__ __forceinline__ float lo2f(unsigned w) { return __builtin_bit_cast(float, w << 16); }
__device__ __forceinline__ float hi2f(unsigned w) { return __builtin_bit_cast(float, w & 0xffff0000u); }
__device__ __forceinline__ float rbf(float x) { return (float)(__bf16)x; }
__device__ __forceinline__ unsigned pack2(float a, float b) { bf16x2 v = {(__bf16)a, (__bf16)b}; return __builtin_bit_cast(unsigned, v); }
__device__ __forceinline__ float dot2(unsigned k, unsigned q, float acc) {
  return __builtin_amdgcn_fdot2_f32_bf16(__builtin_bit_cast(bf16x2, k), __builtin_bit_cast(bf16x2, q), acc, false);
}
template <int CTRL> __device__ __forceinline__ float dpp(float x) {
  return __builtin_bit_cast(float, __builtin_amdgcn_mov_dpp(__builtin_bit_cast(int, x), CTRL, 0xf, 0xf, true));
}
constexpr int XOR1 = 0xB1, XOR2 = 0x4E, XOR7 = 0x141, ROR8 = 0x128, ROR4 = 0x124, BCAST = 0x150;
__device__ __forceinline__ float fold32(float a, float b) {
  auto t = __builtin_amdgcn_permlane32_swap(__float_as_uint(a), __float_as_uint(b), false, false);
  return __uint_as_float(t[0]) + __uint_as_float(t[1]);
}
__device__ __forceinline__ float fold16(float a, float b) {
  auto t = __builtin_amdgcn_permlane16_swap(__float_as_uint(a), __float_as_uint(b), false, false);
  return __uint_as_float(t[0]) + __uint_as_float(t[1]);
}
__device__ __forceinline__ f32x2 fold_rows(const float (&v)[8]) {
  const float u0 = fold32(v[0], v[4]), u1 = fold32(v[1], v[5]), u2 = fold32(v[2], v[6]), u3 = fold32(v[3], v[7]);
  return f32x2{fold16(u0, u2), fold16(u1, u3)};
}
__device__ __forceinline__ float xrow_sum(float x) { x = fold16(x, x); return fold32(x, x); }
__device__ __forceinline__ float xrow_max(float x) {
  auto s = __builtin_amdgcn_permlane16_swap(__float_as_uint(x), __float_as_uint(x), false, false);
  x = fmaxf(__uint_as_float(s[0]), __uint_as_float(s[1]));
  auto t = __builtin_amdgcn_permlane32_swap(__float_as_uint(x), __float_as_uint(x), false, false);
  return fmaxf(__uint_as_float(t[0]), __uint_as_float(t[1]));
}
__device__ __forceinline__ float parity_sum(float x) {
  x += dpp<XOR2>(x); x += dpp<ROR4>(x); x += dpp<ROR8>(x); return xrow_sum(x);
}
__device__ __forceinline__ float wave_sum(float x) { x += dpp<XOR1>(x); return parity_sum(x); }
__device__ __forceinline__ float max8(float x) { x = fmaxf(x, dpp<XOR1>(x)); x = fmaxf(x, dpp<XOR2>(x)); return fmaxf(x, dpp<XOR7>(x)); }
__device__ __forceinline__ float sum8(float x) { x += dpp<XOR1>(x); x += dpp<XOR2>(x); return x + dpp<XOR7>(x); }
__device__ __forceinline__ float wave_max(float x) { x = max8(x); x = fmaxf(x, dpp<ROR8>(x)); return xrow_max(x); }
__device__ __forceinline__ float exp2_(float x) { return __builtin_amdgcn_exp2f(x); }
__device__ __forceinline__ rsrc_t mkrsrc(const void* p, unsigned bytes) { return __builtin_amdgcn_make_buffer_rsrc((void*)p, 0, (int)bytes, 0x00020000); }
__device__ __forceinline__ u32x4 ld16(rsrc_t r, int voff, int soff) { return __builtin_amdgcn_raw_buffer_load_b128(r, voff, soff, 0); }
__device__ __forceinline__ u32x4 ld16_nt(rsrc_t r, int voff, int soff) { return __builtin_amdgcn_raw_buffer_load_b128(r, voff, soff, NT); }
__device__ __forceinline__ u32x4 ld16_sc1(rsrc_t r, int voff, int soff) { return __builtin_amdgcn_raw_buffer_load_b128(r, voff, soff, SC1); }
__device__ __forceinline__ void st16_sc1(rsrc_t r, int off, u32x4 v) { __builtin_amdgcn_raw_buffer_store_b128(v, r, off, 0, SC1); }
#define SB() __builtin_amdgcn_sched_barrier(0)
#define REREAD() asm volatile("" ::: "memory")
__device__ __forceinline__ void pace_wait(unsigned t0, int due) {
  asm volatile("1:\n\ts_memrealtime s[96:97]\n\ts_waitcnt lgkmcnt(0)\n\ts_sub_u32 s96, s96, %0\n\ts_cmp_lt_i32 s96, %1\n\t"
               "s_cbranch_scc0 2f\n\ts_sleep 1\n\ts_branch 1b\n2:"
               :: "s"(t0), "s"(due) : "s96", "s97", "scc", "memory");
}
__device__ __forceinline__ void fma8(float (&acc)[8], float s, u32x4 w) {
#pragma unroll
  for (int t = 0; t < 4; ++t) { acc[2 * t] = fmaf(s, lo2f(w[t]), acc[2 * t]); acc[2 * t + 1] = fmaf(s, hi2f(w[t]), acc[2 * t + 1]); }
}
__device__ __forceinline__ u32x4 rec(float a, float b, unsigned ta, unsigned tb) { return u32x4{__float_as_uint(a), ta, __float_as_uint(b), tb}; }
__device__ __forceinline__ bool rok(u32x4 g, unsigned ta, unsigned tb) { return g[1] == ta && g[3] == tb; }
__device__ __forceinline__ float r0(u32x4 g) { return __uint_as_float(g[0]); }
__device__ __forceinline__ float r1(u32x4 g) { return __uint_as_float(g[2]); }

__device__ __forceinline__ void cnt_arrive(LAS float* lds, int which, int lane) {
  asm volatile("s_waitcnt lgkmcnt(0)" ::: "memory");
  if (lane == 0) __hip_atomic_fetch_add((LAS unsigned*)(lds + L_CNT + which), 1u, __ATOMIC_RELAXED, __HIP_MEMORY_SCOPE_WORKGROUP);
}
template <bool SLEEP = true>
__device__ __forceinline__ void cnt_wait(LAS float* lds, int which, unsigned n) {
#pragma unroll 1
  for (unsigned sp = 0; sp < SPIN_LDS; ++sp) {
    if (__builtin_amdgcn_readfirstlane(*(volatile LAS unsigned*)(lds + L_CNT + which)) >= n) break;
    if (SLEEP) __builtin_amdgcn_s_sleep(1);
  }
  REREAD();
}

__global__ void __launch_bounds__(NTHR, 3)
fused_decode_kernel(const u16* __restrict__ x, const u16* __restrict__ Kc, const u16* __restrict__ Vc, const u16* __restrict__ wq,
                    const u16* __restrict__ wo, const u16* __restrict__ wg, const u16* __restrict__ wu, const u16* __restrict__ wd,
                    u16* __restrict__ out, void* ws) {
  __shared__ __attribute__((aligned(16))) float lds_[L_END];
  LAS float* lds = (LAS float*)lds_;
  const int tid = threadIdx.x, lane = tid & 63, wave = __builtin_amdgcn_readfirstlane(tid >> 6);
  const int r = lane >> 4, c = lane & 15;
  const int bid = blockIdx.x, xl = bid & 7, j = bid >> 3;
  const int h = xl * 4 + (j >> 3), s = j & 7;
  const int f1 = xl * 28 + j, mat = f1 / 112, rb = (f1 / 56) & 1, cb = f1 % 56;
  asm volatile("" :: "s"(x), "s"(wq), "s"(ws), "s"(f1), "s"(wave));

  if (wave < NMAIN) {
    const rsrc_t rk = mkrsrc(Kc, (unsigned)H * SK * DH * 2), rv = mkrsrc(Vc, (unsigned)H * SK * DH * 2);
    u32x4 bA[16], bB[16];
    const unsigned kvbase = (unsigned)h * (8u << 20) + (unsigned)s * (1u << 20) + wave * 1024;
    const unsigned rot = 32u * ((h + (h >> 2) + (h >> 4)) & 3);
    const int kvoff = lane * 16;
    constexpr int STEP = (T_MAIN << 16) / 256 * PACE_G;
    int due = T_K0 * 65536 - STEP + (STEP / 256) * (wave * 32 + j);
#define PACE(T0, I) do { if ((I) % PACE_G == 0) { due += STEP; pace_wait(T0, due >> 16); } } while (0)
    __builtin_amdgcn_s_barrier();
    const unsigned t0 = (unsigned)__builtin_amdgcn_s_memrealtime();
    SB();
    {
      const rsrc_t rg = mkrsrc(mat ? wu : wg, D * F * 2);
      const int voff = j < 28 ? (lane >> 3) * (F * 2) + (lane & 7) * 16 : OOB, sb = (rb * 512 + wave * 64) * (F * 2) + cb * 128;
      constexpr int FSTEP = ((T_W2 - T_F1) << 16) / 8;
      int fdue = T_F1 * 65536 + (FSTEP / 256) * (wave * 32 + j);
#pragma unroll
      for (int i = 0; i < 8; ++i) { pace_wait(t0, fdue >> 16); fdue += FSTEP; bB[i] = ld16_nt(rg, voff, sb + i * 8 * (F * 2)); SB(); }
    }
#pragma unroll
    for (int i = 0; i < 16; ++i) { PACE(t0, i); bA[i] = ld16_nt(rk, kvoff, kvbase + (rot + i) * 8192); SB(); }
    {
      cnt_wait(lds, C_XN, 1);
      float ag[8] = {0.f, 0.f, 0.f, 0.f, 0.f, 0.f, 0.f, 0.f};
#pragma unroll
      for (int i = 0; i < 8; ++i) {
        const int k = rb * 512 + wave * 64 + 8 * i + (lane >> 3);
        fma8(ag, lds[L_XN + (k & 3) * 256 + (k >> 2)], bB[i]);
      }
#pragma unroll
      for (int e = 0; e < 8; ++e) ag[e] += dpp<ROR8>(ag[e]);
      const f32x2 z = fold_rows(ag);
      if (c < 8) *(LAS f32x2*)(lds + L_FP + wave * 64 + 8 * c + 2 * r) = z;
      cnt_arrive(lds, C_F1, lane);
      SB();
    }
#pragma unroll
    for (int i = 0; i < 16; ++i) { PACE(t0, i); bB[i] = ld16_nt(rk, kvoff, kvbase + (rot + 16 + i) * 8192); SB(); }
    cnt_wait(lds, C_QR, 1);
    const u32x4 qp = *(LAS u32x4*)(lds + L_QS + 4 * c);
    const unsigned t0m = __builtin_amdgcn_readfirstlane(*(LAS unsigned*)(lds + L_CNT + W_T0));
    constexpr float SCALE = 0.08838834764831845f, LOG2E = 1.4426950408889634f;
    LAS float* scp = lds + L_SC + wave * 512 + lane * 8;
    float mx = -INFINITY;
    auto kgroup = [&](u32x4 (&kb)[16], const bool more, const rsrc_t rn, const unsigned tb, const int g) __attribute__((always_inline)) {
      float sx[16];
#pragma unroll
      for (int i = 0; i < 16; ++i) {
        float d = 0.f;
#pragma unroll
        for (int t = 0; t < 4; ++t) d = dot2(kb[i][t], qp[t], d);
        asm volatile("" : "+v"(d));
        sx[i] = d;
        SB();
        if (more) { PACE(t0m, i); kb[i] = ld16_nt(rn, kvoff, tb + i * 8192); SB(); }
      }
#define TR_STEP(HALF, CTRL, BIT) _Pragma("unroll") for (int hh = 0; hh < (HALF); ++hh) { \
        const float lo_ = sx[hh], hi_ = sx[hh + (HALF)]; const float t1_ = lo_ + dpp<CTRL>(lo_), t2_ = hi_ + dpp<CTRL>(hi_); \
        sx[hh] = (c & (BIT)) ? t2_ : t1_; }
      TR_STEP(8, ROR8, 8) TR_STEP(4, XOR7, 4) TR_STEP(2, XOR2, 2) TR_STEP(1, XOR1, 1)
#undef TR_STEP
      const float sc = rbf(rbf(sx[0]) * SCALE) * LOG2E;
      mx = fmaxf(mx, sc);
      scp[g] = sc;
      SB();
    };
    kgroup(bA, false, rk, 0, 0);
#pragma unroll 1
    for (int g = 1; g < 8; ++g)
      kgroup(bB, true, mkrsrc(g == 7 ? Vc : Kc, (unsigned)H * SK * DH * 2), kvbase + (((g + 1) * 16 + rot) & 127u) * 8192, g);
    {
      const float m = wave_max(mx);
      f32x4 p0 = *(LAS f32x4*)scp, p1 = *(LAS f32x4*)(scp + 4);
      float l = 0.f;
#pragma unroll
      for (int e = 0; e < 4; ++e) { p0[e] = exp2_(p0[e] - m); p1[e] = exp2_(p1[e] - m); l += p0[e] + p1[e]; }
      *(LAS f32x4*)scp = p0; *(LAS f32x4*)(scp + 4) = p1;
      l = wave_sum(l);
      if (lane == 0) *(LAS f32x2*)(lds + L_ML + 2 * wave) = f32x2{m, l};
      cnt_arrive(lds, C_ML, lane);
    }
    float acc[8] = {0.f, 0.f, 0.f, 0.f, 0.f, 0.f, 0.f, 0.f};
    auto vgroup = [&](const bool more, const unsigned tb, const float p) __attribute__((always_inline)) {
#define PV_STEP(I) { fma8(acc, dpp<BCAST + (I)>(p), bB[I]); \
        asm volatile("" : "+v"(acc[0]), "+v"(acc[1]), "+v"(acc[2]), "+v"(acc[3]), "+v"(acc[4]), "+v"(acc[5]), "+v"(acc[6]), "+v"(acc[7])); SB(); \
        if (more) { PACE(t0m, I); bB[I] = ld16_nt(rv, kvoff, tb + (I) * 8192); SB(); } \
        else if ((I) == 11) {   \
          pace_wait(t0m, T_K0 + T_MAIN); \
          _Pragma("unroll") for (int u = 0; u < 4; ++u) { \
            bB[u] = ld16_nt(mkrsrc(wo, H * DH * D * 2), r * 2048 + c * 16, (h * 128 + wave * 16 + 4 * u) * 2048 + s * 256); SB(); } } }
      PV_STEP(0) PV_STEP(1) PV_STEP(2) PV_STEP(3) PV_STEP(4) PV_STEP(5) PV_STEP(6) PV_STEP(7)
      PV_STEP(8) PV_STEP(9) PV_STEP(10) PV_STEP(11) PV_STEP(12) PV_STEP(13) PV_STEP(14) PV_STEP(15)
#undef PV_STEP
    };
#pragma unroll 1
    for (int g = 0; g < 7; ++g) vgroup(true, kvbase + (((g + 1) * 16 + rot) & 127u) * 8192, scp[g]);
    __builtin_amdgcn_s_setprio(3);
    cnt_wait(lds, C_WG, 1);
    {
      const float wgt = lds[L_WG + wave];
#pragma unroll
      for (int e = 0; e < 8; ++e) acc[e] *= wgt;
      vgroup(false, 0, scp[7] * wgt);
    }
    *(LAS f32x2*)(lds + L_MRG + wave * 128 + 8 * c + 2 * r) = fold_rows(acc);
    cnt_arrive(lds, C_END, lane);
#pragma unroll
    for (int u = 0; u < 4; ++u) *(LAS u32x4*)(lds + L_WT + (wave * 16 + 4 * u + r) * WT_STR + 4 * c) = bB[u];
    cnt_arrive(lds, C_WT, lane);
    return;
#undef PACE
  }

  const int a = wave - NMAIN;
  const rsrc_t rx = mkrsrc(x, D * 2), rws = mkrsrc(ws, WS_END);
  unsigned t0, tagA, tagB, tagC, tagD;
  unsigned long long e2;
  {
    u32x4 xa = ld16(rx, lane * 32, 0), xb = ld16(rx, lane * 32 + 16, 0);
    const u32x2 ep = __builtin_amdgcn_raw_buffer_load_b64(rws, 0, WS_EPOCH, SC1);
    const rsrc_t rwq = mkrsrc(wq, D * H * DH * 2);
    u32x4 w[8];
    const int voff = r * 8192 + c * 16, sb = (s * 128 + a * 32) * 8192 + h * 256;
#pragma unroll
    for (int i = 0; i < 8; ++i) { w[i] = ld16_nt(rwq, voff, sb + i * 32768); SB(); }
    if (a == 0 && lane < C_NUM) *(LAS unsigned*)(lds + L_CNT + lane) = 0u;
    __syncthreads();
    t0 = (unsigned)__builtin_amdgcn_s_memrealtime();
    asm volatile("" : "+v"(xa), "+v"(xb));
    float v[16];
#pragma unroll
    for (int i = 0; i < 4; ++i) { v[2 * i] = lo2f(xa[i]); v[2 * i + 1] = hi2f(xa[i]); v[8 + 2 * i] = lo2f(xb[i]); v[9 + 2 * i] = hi2f(xb[i]); }
    float sm = 0.f;
#pragma unroll
    for (int i = 0; i < 16; ++i) sm += v[i];
    const float mean = wave_sum(sm) * (1.f / D);
    float ss = 0.f;
#pragma unroll
    for (int i = 0; i < 16; ++i) { const float d = v[i] - mean; ss = fmaf(d, d, ss); }
    const float var = wave_sum(ss) * (1.f / D);
    const float mb = rbf(mean), den = rbf(sqrtf(rbf(rbf(var) + 1e-5f)));
    const float rinv = 1.0f / den;
#pragma unroll
    for (int i = 0; i < 16; ++i) {
      const float nu = rbf(v[i] - mb), q0 = nu * rinv;
      v[i] = rbf(fmaf(fmaf(-q0, den, nu), rinv, q0));
    }
#pragma unroll
    for (int q = 0; q < 4; ++q) *(LAS f32x4*)(lds + L_XN + q * 256 + 4 * lane) = f32x4{v[q], v[4 + q], v[8 + q], v[12 + q]};
    cnt_arrive(lds, C_XN, lane);
    const unsigned long long e1 = (((unsigned long long)__builtin_amdgcn_readfirstlane(ep[1]) << 32) | (unsigned)__builtin_amdgcn_readfirstlane(ep[0])) + 1ull;
    e2 = e1 + 1ull;
    tagA = (unsigned)e1; tagB = (unsigned)(e1 >> 32) + (unsigned)e1 * 0x9E3779B1u;
    tagC = (unsigned)e2; tagD = (unsigned)(e2 >> 32) + (unsigned)e2 * 0x9E3779B1u;
    float acc[8] = {0.f, 0.f, 0.f, 0.f, 0.f, 0.f, 0.f, 0.f};
#pragma unroll
    for (int q = 0; q < 2; ++q) {
      const f32x4 xv = *(LAS f32x4*)(lds + L_XN + r * 256 + s * 32 + a * 8 + 4 * q);
#pragma unroll
      for (int e = 0; e < 4; ++e) fma8(acc, xv[e], w[4 * q + e]);
    }
    *(LAS f32x2*)(lds + L_QP + a * 128 + 8 * c + 2 * r) = fold_rows(acc);
  }
  cnt_arrive(lds, C_Q, lane);
  if (a == 0) {
    cnt_wait<false>(lds, C_Q, NAUX);
    f32x2 v = *(LAS f32x2*)(lds + L_QP + 2 * lane);
#pragma unroll
    for (int q = 1; q < 4; ++q) v += *(LAS f32x2*)(lds + L_QP + q * 128 + 2 * lane);
    st16_sc1(rws, WS_Q + (h * 8 + s) * 1024 + lane * 16, rec(v[0], v[1], tagA, tagB));
    st16_sc1(rws, (bid == 0 && lane == 0) ? WS_T0 : OOB, u32x4{t0, tagA, t0, tagB});
  }
  if (a == 1) {
    pace_wait(t0, T_QPOLL);
    float q0 = 0.f, q1 = 0.f;
    unsigned tg = t0;
#pragma unroll 1
    for (unsigned sp = 0; sp < SPIN_MEM; ++sp) {
      REREAD();
      u32x4 g[8];
#pragma unroll
      for (int i = 0; i < 8; ++i) g[i] = ld16_sc1(rws, lane * 16, WS_Q + (h * 8 + i) * 1024);
      const u32x4 gt = ld16_sc1(rws, 0, WS_T0);
      bool ok = true;
      tg = rok(gt, tagA, tagB) ? gt[0] : t0;
      q0 = 0.f; q1 = 0.f;
#pragma unroll
      for (int i = 0; i < 8; ++i) { ok = ok && rok(g[i], tagA, tagB); q0 += r0(g[i]); q1 += r1(g[i]); }
      if (__all(ok)) break;
      __builtin_amdgcn_s_sleep(2);
    }
    *(LAS unsigned*)(lds + L_QS + lane) = pack2(q0, q1);
    if (lane == 0) *(LAS unsigned*)(lds + L_CNT + W_T0) = ((int)(t0 - tg) < 0 || (int)(t0 - tg) > 300) ? t0 : tg;
    cnt_arrive(lds, C_QR, lane);
  }
  const int ks = xl * 2 + (j >> 4), cg = j & 15;
  u32x4 dw[7];
  {
    const rsrc_t rd = mkrsrc(wd, D * F * 2);
    const int voff = (lane >> 3) * 2048 + (lane & 7) * 16, sb = (ks * 224 + a * 56) * 2048 + cg * 128;
    constexpr int WSTEP = ((T_K0 - T_W2) << 16) / 7;
    int wdue = T_W2 * 65536 + (WSTEP / 128) * (a * 32 + j);
#pragma unroll
    for (int i = 0; i < 7; ++i) { pace_wait(t0, wdue >> 16); wdue += WSTEP; dw[i] = ld16_nt(rd, voff, sb + i * 16384); SB(); }
  }
  if (a == 0 && j < 28) {
    cnt_wait(lds, C_F1, NMAIN);
    f32x2 v = *(LAS f32x2*)(lds + L_FP + 2 * (lane & 31));
#pragma unroll
    for (int q = 1; q < 8; ++q) v += *(LAS f32x2*)(lds + L_FP + q * 64 + 2 * (lane & 31));
    if (f1 >= 56) {
      st16_sc1(rws, lane < 32 ? WS_G1 + ((mat * 2 + rb) * F + cb * 64) * 8 + lane * 16 : OOB, rec(v[0], v[1], tagA, tagB));
    } else {
      pace_wait(t0, T_K0 + T_HA);
      u32x4 g[3];
#pragma unroll 1
      for (unsigned sp = 0; sp < SPIN_MEM; ++sp) {
        REREAD();
#pragma unroll
        for (int k = 0; k < 3; ++k) g[k] = ld16_sc1(rws, (lane & 31) * 16, WS_G1 + ((k + 1) * F + cb * 64) * 8);
        if (__all(rok(g[0], tagA, tagB) && rok(g[1], tagA, tagB) && rok(g[2], tagA, tagB))) break;
        __builtin_amdgcn_s_sleep(32);
      }
      float hh[2];
#pragma unroll
      for (int e = 0; e < 2; ++e) {
        const float gs = rbf(v[e] + __uint_as_float(g[0][2 * e]));
        const float us = rbf(__uint_as_float(g[1][2 * e]) + __uint_as_float(g[2][2 * e]));
        const float ge = 0.5f * gs * (1.f + tanhf(0.7978845608028654f * (gs + 0.044715f * gs * gs * gs)));
        hh[e] = rbf(ge) * us;
      }
      const float hp = __uint_as_float(pack2(hh[0], hh[1]));
      st16_sc1(rws, (lane < 32 && !(lane & 1)) ? WS_H + cb * 256 + lane * 8 : OOB, rec(hp, dpp<XOR1>(hp), tagA, tagB));
    }
  }
  {
    pace_wait(t0, T_K0 + T_HB);
    u32x4 g;
#pragma unroll 1
    for (unsigned sp = 0; sp < SPIN_MEM; ++sp) {
      REREAD();
      g = ld16_sc1(rws, (lane < 14 ? lane : 13) * 16, WS_H + (ks * 56 + a * 14) * 16);
      if (__all(rok(g, tagA, tagB))) break;
      __builtin_amdgcn_s_sleep(32);
    }
    if (lane < 14) *(LAS f32x4*)(lds + L_H + a * 64 + 4 * lane) = f32x4{lo2f(g[0]), hi2f(g[0]), lo2f(g[2]), hi2f(g[2])};
    float y[8] = {0.f, 0.f, 0.f, 0.f, 0.f, 0.f, 0.f, 0.f};
#pragma unroll
    for (int i = 0; i < 7; ++i) fma8(y, lds[L_H + a * 64 + 8 * i + (lane >> 3)], dw[i]);
#pragma unroll
    for (int e = 0; e < 8; ++e) y[e] += dpp<ROR8>(y[e]);
    const f32x2 z = fold_rows(y);
    if (c < 8) *(LAS f32x2*)(lds + L_F2 + a * 64 + 8 * c + 2 * r) = z;
    cnt_arrive(lds, C_F2, lane);
  }
  const int col0 = s * 128 + 4 * h;
  unsigned xw = 0;
  if (a == 0) xw = __builtin_amdgcn_raw_buffer_load_b32(rx, (col0 + 2 * (lane & 1)) * 2, 0, 0);
  if (a == 3) {
    const unsigned po = __builtin_amdgcn_raw_buffer_load_b32(mkrsrc(out, D * 2), (col0 + 2 * (lane & 1)) * 2, 0, SC1);
    asm volatile("" :: "v"(po));
  }
  if (a == 1) {
    cnt_wait(lds, C_ML, NMAIN);
    const f32x2 ml = *(LAS f32x2*)(lds + L_ML + 2 * (lane & 7));
    const float Mw = max8(ml[0]), Lw = sum8(exp2_(ml[0] - Mw) * ml[1]);
    st16_sc1(rws, lane == 0 ? WS_ML + (h * 8 + s) * 128 : OOB, rec(Mw, Lw, tagA, tagB));
    pace_wait(t0, T_K0 + T_MAIN / 2 + 1500);
    u32x4 g;
#pragma unroll 1
    for (unsigned sp = 0; sp < SPIN_MEM; ++sp) {
      REREAD();
      g = ld16_sc1(rws, (lane & 7) * 128, WS_ML + h * 8 * 128);
      if (__all(rok(g, tagA, tagB))) break;
      __builtin_amdgcn_s_sleep(32);
    }
    const float M = max8(r0(g)), L = sum8(exp2_(r0(g) - M) * r1(g));
    if (lane < 8) lds[L_WG + lane] = exp2_(ml[0] - M) / L;
    cnt_arrive(lds, C_WG, lane);
  }
  pace_wait(t0, T_K0 + T_MAIN - 100);
  __builtin_amdgcn_s_setprio(3);
  if (a == 2) {
    cnt_wait(lds, C_F2, NAUX);
    f32x2 v = *(LAS f32x2*)(lds + L_F2 + 2 * (lane & 31));
#pragma unroll
    for (int q = 1; q < 4; ++q) v += *(LAS f32x2*)(lds + L_F2 + q * 64 + 2 * (lane & 31));
    pace_wait(t0, T_K0 + T_MAIN + 60);
    st16_sc1(rws, lane < 32 ? WS_FFN + (ks * D + cg * 64) * 8 + lane * 16 : OOB, rec(v[0], v[1], tagA, tagB));
  }
  if (a == 0) {
    cnt_wait<false>(lds, C_END, NMAIN);
    f32x2 v = *(LAS f32x2*)(lds + L_MRG + 2 * lane);
#pragma unroll
    for (int w = 1; w < 8; ++w) v += *(LAS f32x2*)(lds + L_MRG + w * 128 + 2 * lane);
    st16_sc1(rws, WS_Q + (h * 8 + s) * 1024 + lane * 16, rec(v[0], v[1], tagC, tagD));
  } else {
    cnt_wait(lds, C_END, NMAIN);
  }
  u32x4 bfr[2][4];
  cnt_wait<false>(lds, C_WT, NMAIN);
#pragma unroll
  for (int q = 0; q < 4; ++q)
#pragma unroll
    for (int d = 0; d < 4; ++d) {
      const unsigned w0 = *(LAS unsigned*)(lds + L_WT + (32 * q + 8 * r + 2 * d) * WT_STR + 16 * a + c);
      const unsigned w1 = *(LAS unsigned*)(lds + L_WT + (32 * q + 8 * r + 2 * d + 1) * WT_STR + 16 * a + c);
      bfr[0][q][d] = __builtin_amdgcn_perm(w1, w0, 0x05040100u);
      bfr[1][q][d] = __builtin_amdgcn_perm(w1, w0, 0x07060302u);
    }
  {
    u32x4 g0, g1;
    const int vo = r * 1024 + (16 * a + c) * 16;
#pragma unroll 1
    for (unsigned sp = 0; sp < SPIN_MEM; ++sp) {
      REREAD();
      g0 = ld16_sc1(rws, vo, WS_Q + h * 8 * 1024);
      g1 = ld16_sc1(rws, vo, WS_Q + (h * 8 + 4) * 1024);
      if (__all(rok(g0, tagC, tagD) && rok(g1, tagC, tagD))) break;
    }
    float o = fold32(r0(g0) + r0(g1), r1(g0) + r1(g1));
    o = fold16(o, o);
    if ((r & 1) == 0) *((LAS u16*)(lds + L_OB) + 32 * a + 2 * c + (r >> 1)) = __builtin_bit_cast(u16, (__bf16)o);
  }
  cnt_arrive(lds, C_OB, lane);
  cnt_wait<false>(lds, C_OB, NAUX);
  {
    f32x4 y0 = {0.f, 0.f, 0.f, 0.f}, y1 = y0;
#pragma unroll
    for (int q = 0; q < 4; ++q) {
      const u32x4 af = *(LAS u32x4*)(lds + L_OB + 16 * q + 4 * r);
      y0 = __builtin_amdgcn_mfma_f32_16x16x32_bf16(__builtin_bit_cast(bf16x8, af), __builtin_bit_cast(bf16x8, bfr[0][q]), y0, 0, 0, 0);
      y1 = __builtin_amdgcn_mfma_f32_16x16x32_bf16(__builtin_bit_cast(bf16x8, af), __builtin_bit_cast(bf16x8, bfr[1][q]), y1, 0, 0, 0);
    }
    st16_sc1(rws, lane < 16 ? WS_Y + (h * 8 + s) * 1024 + (16 * a + lane) * 16 : OOB, rec(y0[0], y1[0], tagA, tagB));
  }
  if (a == 0) {
    u32x4 g, gf;
    pace_wait((unsigned)__builtin_amdgcn_s_memrealtime(), 20);
#pragma unroll 1
    for (unsigned sp = 0; sp < SPIN_MEM; ++sp) {
      REREAD();
      g = ld16_sc1(rws, (lane >> 1) * (8 * 1024) + (lane & 1) * 16, WS_Y + s * 1024 + h * 32);
      gf = ld16_sc1(rws, ((lane >> 1) & 15) * (D * 8) + (lane & 1) * 16, WS_FFN + col0 * 8);
      if (__all(rok(g, tagA, tagB) && rok(gf, tagA, tagB))) break;
    }
    const float f0 = rbf(parity_sum(r0(gf)) * 0.5f), f1v = rbf(parity_sum(r1(gf)) * 0.5f);
    const float a0 = rbf(parity_sum(r0(g))), a1 = rbf(parity_sum(r1(g)));
    const unsigned o = pack2(rbf(lo2f(xw) + a0) + f0, rbf(hi2f(xw) + a1) + f1v);
    __builtin_amdgcn_raw_buffer_store_b32(o, mkrsrc(out, D * 2), lane < 2 ? (col0 + 2 * lane) * 2 : OOB, 0, SC1);
    __builtin_amdgcn_raw_buffer_store_b64(u32x2{(unsigned)e2, (unsigned)(e2 >> 32)}, rws, (bid == 0 && lane == 0) ? WS_EPOCH : OOB, 0, SC1);
  }
}

extern "C" void kernel_launch(void* const* d_in, const int* in_sizes, int n_in, void* d_out, int out_size, void* d_ws, size_t ws_size,
                              hipStream_t stream) {
  fused_decode_kernel<<<256, NTHR, 0, stream>>>((const u16*)d_in[0], (const u16*)d_in[1], (const u16*)d_in[2], (const u16*)d_in[3],
                                                 (const u16*)d_in[4], (const u16*)d_in[6], (const u16*)d_in[7], (const u16*)d_in[8],
                                                 (u16*)d_out, d_ws);
}
```

```cpp
#include <hip/hip_runtime.h>
#include <stdint.h>

typedef unsigned u32x4 __attribute__((ext_vector_type(4)));
typedef unsigned u32x2 __attribute__((ext_vector_type(2)));
typedef float f32x4 __attribute__((ext_vector_type(4)));
typedef float f32x2 __attribute__((ext_vector_type(2)));
typedef __bf16 bf16x2 __attribute__((ext_vector_type(2)));
typedef __bf16 bf16x8 __attribute__((ext_vector_type(8)));
typedef unsigned short u16;
#define LAS __attribute__((address_space(3)))
typedef __amdgpu_buffer_rsrc_t rsrc_t;

constexpr int T_F1 = 70;
constexpr int T_W2 = 262;
constexpr int T_K0 = 345;
constexpr int T_MAIN = 7040;
constexpr int PACE_G = 2;
constexpr int T_QPOLL = 315;
constexpr int T_HA = 1600, T_HB = 2800;

constexpr int D = 1024, H = 32, SK = 32768, DH = 128, F = 3584;
constexpr int NMAIN = 8, NAUX = 4, NTHR = (NMAIN + NAUX) * 64;
constexpr int NT = 2, SC1 = 16;
constexpr int OOB = 0x40000000;
constexpr unsigned WS_EPOCH = 0, WS_T0 = 1024, WS_ML = 4096, WS_Q = WS_ML + 256 * 128, WS_Y = WS_Q + 256 * 1024, WS_G1 = WS_Y + 256 * 1024,
                   WS_H = WS_G1 + 4 * F * 8, WS_FFN = WS_H + F * 4, WS_END = WS_FFN + 16 * D * 8;
constexpr int L_XN = 0, L_QP = 1024, L_QS = L_QP + 4 * 128, L_MRG = L_QS + 64, L_ML = L_MRG + 8 * 128, L_WG = L_ML + 16, L_FP = L_WG + 8,
              L_H = L_FP + 8 * 64, L_F2 = L_H + 4 * 64, L_OB = L_F2 + 4 * 64, L_CNT = L_OB + 64, L_SC = L_CNT + 16, L_WT = L_SC + 8 * 512,
              WT_STR = 68, L_END = L_WT + 128 * WT_STR;
constexpr int C_Q = 0, C_XN = 1, C_F2 = 2, C_WT = 3, C_ML = 4, C_WG = 5, C_END = 6, C_QR = 7, C_F1 = 8, C_OB = 9, C_NUM = 10, W_T0 = 12;
constexpr unsigned SPIN_MEM = 1u << 20, SPIN_LDS = 1u << 23;

__device__ __forceinline__ float lo2f(unsigned w) { return __builtin_bit_cast(float, w << 16); }
__device__ __forceinline__ float hi2f(unsigned w) { return __builtin_bit_cast(float, w & 0xffff0000u); }
__device__ __forceinline__ float rbf(float x) { return (float)(__bf16)x; }
__device__ __forceinline__ unsigned pack2(float a, float b) { bf16x2 v = {(__bf16)a, (__bf16)b}; return __builtin_bit_cast(unsigned, v); }
__device__ __forceinline__ float dot2(unsigned k, unsigned q, float acc) {
  return __builtin_amdgcn_fdot2_f32_bf16(__builtin_bit_cast(bf16x2, k), __builtin_bit_cast(bf16x2, q), acc, false);
}
template <int CTRL> __device__ __forceinline__ float dpp(float x) {
  return __builtin_bit_cast(float, __builtin_amdgcn_mov_dpp(__builtin_bit_cast(int, x), CTRL, 0xf, 0xf, true));
}
constexpr int XOR1 = 0xB1, XOR2 = 0x4E, XOR7 = 0x141, ROR8 = 0x128, ROR4 = 0x124, BCAST = 0x150;
__device__ __forceinline__ float fold32(float a, float b) {
  auto t = __builtin_amdgcn_permlane32_swap(__float_as_uint(a), __float_as_uint(b), false, false);
  return __uint_as_float(t[0]) + __uint_as_float(t[1]);
}
__device__ __forceinline__ float fold16(float a, float b) {
  auto t = __builtin_amdgcn_permlane16_swap(__float_as_uint(a), __float_as_uint(b), false, false);
  return __uint_as_float(t[0]) + __uint_as_float(t[1]);
}
__device__ __forceinline__ f32x2 fold_rows(const float (&v)[8]) {
  const float u0 = fold32(v[0], v[4]), u1 = fold32(v[1], v[5]), u2 = fold32(v[2], v[6]), u3 = fold32(v[3], v[7]);
  return f32x2{fold16(u0, u2), fold16(u1, u3)};
}
__device__ __forceinline__ float xrow_sum(float x) { x = fold16(x, x); return fold32(x, x); }
__device__ __forceinline__ float xrow_max(float x) {
  auto s = __builtin_amdgcn_permlane16_swap(__float_as_uint(x), __float_as_uint(x), false, false);
  x = fmaxf(__uint_as_float(s[0]), __uint_as_float(s[1]));
  auto t = __builtin_amdgcn_permlane32_swap(__float_as_uint(x), __float_as_uint(x), false, false);
  return fmaxf(__uint_as_float(t[0]), __uint_as_float(t[1]));
}
__device__ __forceinline__ float parity_sum(float x) {
  x += dpp<XOR2>(x); x += dpp<ROR4>(x); x += dpp<ROR8>(x); return xrow_sum(x);
}
__device__ __forceinline__ float wave_sum(float x) { x += dpp<XOR1>(x); return parity_sum(x); }
__device__ __forceinline__ float max8(float x) { x = fmaxf(x, dpp<XOR1>(x)); x = fmaxf(x, dpp<XOR2>(x)); return fmaxf(x, dpp<XOR7>(x)); }
__device__ __forceinline__ float sum8(float x) { x += dpp<XOR1>(x); x += dpp<XOR2>(x); return x + dpp<XOR7>(x); }
__device__ __forceinline__ float wave_max(float x) { x = max8(x); x = fmaxf(x, dpp<ROR8>(x)); return xrow_max(x); }
__device__ __forceinline__ float exp2_(float x) { return __builtin_amdgcn_exp2f(x); }
__device__ __forceinline__ rsrc_t mkrsrc(const void* p, unsigned bytes) { return __builtin_amdgcn_make_buffer_rsrc((void*)p, 0, (int)bytes, 0x00020000); }
__device__ __forceinline__ u32x4 ld16(rsrc_t r, int voff, int soff) { return __builtin_amdgcn_raw_buffer_load_b128(r, voff, soff, 0); }
__device__ __forceinline__ u32x4 ld16_nt(rsrc_t r, int voff, int soff) { return __builtin_amdgcn_raw_buffer_load_b128(r, voff, soff, NT); }
__device__ __forceinline__ u32x4 ld16_sc1(rsrc_t r, int voff, int soff) { return __builtin_amdgcn_raw_buffer_load_b128(r, voff, soff, SC1); }
__device__ __forceinline__ void st16_sc1(rsrc_t r, int off, u32x4 v) { __builtin_amdgcn_raw_buffer_store_b128(v, r, off, 0, SC1); }
#define SB() __builtin_amdgcn_sched_barrier(0)
#define REREAD() asm volatile("" ::: "memory")
__device__ __forceinline__ void pace_wait(unsigned t0, int due) {
  asm volatile("1:\n\ts_memrealtime s[96:97]\n\ts_waitcnt lgkmcnt(0)\n\ts_sub_u32 s96, s96, %0\n\ts_cmp_lt_i32 s96, %1\n\t"
               "s_cbranch_scc0 2f\n\ts_sleep 1\n\ts_branch 1b\n2:"
               :: "s"(t0), "s"(due) : "s96", "s97", "scc", "memory");
}
__device__ __forceinline__ void fma8(float (&acc)[8], float s, u32x4 w) {
#pragma unroll
  for (int t = 0; t < 4; ++t) { acc[2 * t] = fmaf(s, lo2f(w[t]), acc[2 * t]); acc[2 * t + 1] = fmaf(s, hi2f(w[t]), acc[2 * t + 1]); }
}
__device__ __forceinline__ u32x4 rec(float a, float b, unsigned ta, unsigned tb) { return u32x4{__float_as_uint(a), ta, __float_as_uint(b), tb}; }
__device__ __forceinline__ bool rok(u32x4 g, unsigned ta, unsigned tb) { return g[1] == ta && g[3] == tb; }
__device__ __forceinline__ float r0(u32x4 g) { return __uint_as_float(g[0]); }
__device__ __forceinline__ float r1(u32x4 g) { return __uint_as_float(g[2]); }

__device__ __forceinline__ void cnt_arrive(LAS float* lds, int which, int lane) {
  asm volatile("s_waitcnt lgkmcnt(0)" ::: "memory");
  if (lane == 0) __hip_atomic_fetch_add((LAS unsigned*)(lds + L_CNT + which), 1u, __ATOMIC_RELAXED, __HIP_MEMORY_SCOPE_WORKGROUP);
}
template <bool SLEEP = true>
__device__ __forceinline__ void cnt_wait(LAS float* lds, int which, unsigned n) {
#pragma unroll 1
  for (unsigned sp = 0; sp < SPIN_LDS; ++sp) {
    if (__builtin_amdgcn_readfirstlane(*(volatile LAS unsigned*)(lds + L_CNT + which)) >= n) break;
    if (SLEEP) __builtin_amdgcn_s_sleep(1);
  }
  REREAD();
}

__global__ void __launch_bounds__(NTHR, 3)
fused_decode_kernel(const u16* __restrict__ x, const u16* __restrict__ Kc, const u16* __restrict__ Vc, const u16* __restrict__ wq,
                    const u16* __restrict__ wo, const u16* __restrict__ wg, const u16* __restrict__ wu, const u16* __restrict__ wd,
                    u16* __restrict__ out, void* ws) {
  __shared__ __attribute__((aligned(16))) float lds_[L_END];
  LAS float* lds = (LAS float*)lds_;
  const int tid = threadIdx.x, lane = tid & 63, wave = __builtin_amdgcn_readfirstlane(tid >> 6);
  const int r = lane >> 4, c = lane & 15;
  const int bid = blockIdx.x, xl = bid & 7, j = bid >> 3;
  const int h = xl * 4 + (j >> 3), s = j & 7;
  const int f1 = xl * 28 + j, mat = f1 / 112, rb = (f1 / 56) & 1, cb = f1 % 56;
  asm volatile("" :: "s"(x), "s"(wq), "s"(ws), "s"(f1), "s"(wave));

  if (wave < NMAIN) {
    const rsrc_t rk = mkrsrc(Kc, (unsigned)H * SK * DH * 2), rv = mkrsrc(Vc, (unsigned)H * SK * DH * 2);
    u32x4 bA[16], bB[16];
    const unsigned kvbase = (unsigned)h * (8u << 20) + (unsigned)s * (1u << 20) + wave * 1024;
    const unsigned rot = 32u * ((h + (h >> 2) + (h >> 4)) & 3);
    const int kvoff = lane * 16;
    constexpr int STEP = (T_MAIN << 16) / 256 * PACE_G;
    int due = T_K0 * 65536 - STEP + (STEP / 256) * (wave * 32 + j);
#define PACE(T0, I) do { if ((I) % PACE_G == 0) { due += STEP; pace_wait(T0, due >> 16); } } while (0)
    __builtin_amdgcn_s_barrier();
    const unsigned t0 = (unsigned)__builtin_amdgcn_s_memrealtime();
    SB();
    {
      const rsrc_t rg = mkrsrc(mat ? wu : wg, D * F * 2);
      const int voff = j < 28 ? (lane >> 3) * (F * 2) + (lane & 7) * 16 : OOB, sb = (rb * 512 + wave * 64) * (F * 2) + cb * 128;
      constexpr int FSTEP = ((T_W2 - T_F1) << 16) / 8;
      int fdue = T_F1 * 65536 + (FSTEP / 256) * (wave * 32 + j);
#pragma unroll
      for (int i = 0; i < 8; ++i) { pace_wait(t0, fdue >> 16); fdue += FSTEP; bB[i] = ld16_nt(rg, voff, sb + i * 8 * (F * 2)); SB(); }
    }
#pragma unroll
    for (int i = 0; i < 16; ++i) { PACE(t0, i); bA[i] = ld16_nt(rk, kvoff, kvbase + (rot + i) * 8192); SB(); }
    {
      cnt_wait(lds, C_XN, 1);
      float ag[8] = {0.f, 0.f, 0.f, 0.f, 0.f, 0.f, 0.f, 0.f};
#pragma unroll
      for (int i = 0; i < 8; ++i) {
        const int k = rb * 512 + wave * 64 + 8 * i + (lane >> 3);
        fma8(ag, lds[L_XN + (k & 3) * 256 + (k >> 2)], bB[i]);
      }
#pragma unroll
      for (int e = 0; e < 8; ++e) ag[e] += dpp<ROR8>(ag[e]);
      const f32x2 z = fold_rows(ag);
      if (c < 8) *(LAS f32x2*)(lds + L_FP + wave * 64 + 8 * c + 2 * r) = z;
      cnt_arrive(lds, C_F1, lane);
      SB();
    }
#pragma unroll
    for (int i = 0; i < 16; ++i) { PACE(t0, i); bB[i] = ld16_nt(rk, kvoff, kvbase + (rot + 16 + i) * 8192); SB(); }
    cnt_wait(lds, C_QR, 1);
    const u32x4 qp = *(LAS u32x4*)(lds + L_QS + 4 * c);
    const unsigned t0m = __builtin_amdgcn_readfirstlane(*(LAS unsigned*)(lds + L_CNT + W_T0));
    constexpr float SCALE = 0.08838834764831845f, LOG2E = 1.4426950408889634f;
    LAS float* scp = lds + L_SC + wave * 512 + lane * 8;
    float mx = -INFINITY;
    auto kgroup = [&](u32x4 (&kb)[16], const bool more, const rsrc_t rn, const unsigned tb, const int g) __attribute__((always_inline)) {
      float sx[16];
#pragma unroll
      for (int i = 0; i < 16; ++i) {
        float d = 0.f;
#pragma unroll
        for (int t = 0; t < 4; ++t) d = dot2(kb[i][t], qp[t], d);
        asm volatile("" : "+v"(d));
        sx[i] = d;
        SB();
        if (more) { PACE(t0m, i); kb[i] = ld16_nt(rn, kvoff, tb + i * 8192); SB(); }
      }
#define TR_STEP(HALF, CTRL, BIT) _Pragma("unroll") for (int hh = 0; hh < (HALF); ++hh) { \
        const float lo_ = sx[hh], hi_ = sx[hh + (HALF)]; const float t1_ = lo_ + dpp<CTRL>(lo_), t2_ = hi_ + dpp<CTRL>(hi_); \
        sx[hh] = (c & (BIT)) ? t2_ : t1_; }
      TR_STEP(8, ROR8, 8) TR_STEP(4, XOR7, 4) TR_STEP(2, XOR2, 2) TR_STEP(1, XOR1, 1)
#undef TR_STEP
      const float sc = rbf(rbf(sx[0]) * SCALE) * LOG2E;
      mx = fmaxf(mx, sc);
      scp[g] = sc;
      SB();
    };
    kgroup(bA, false, rk, 0, 0);
#pragma unroll 1
    for (int g = 1; g < 8; ++g)
      kgroup(bB, true, mkrsrc(g == 7 ? Vc : Kc, (unsigned)H * SK * DH * 2), kvbase + (((g + 1) * 16 + rot) & 127u) * 8192, g);
    {
      const float m = wave_max(mx);
      f32x4 p0 = *(LAS f32x4*)scp, p1 = *(LAS f32x4*)(scp + 4);
      float l = 0.f;
#pragma unroll
      for (int e = 0; e < 4; ++e) { p0[e] = exp2_(p0[e] - m); p1[e] = exp2_(p1[e] - m); l += p0[e] + p1[e]; }
      *(LAS f32x4*)scp = p0; *(LAS f32x4*)(scp + 4) = p1;
      l = wave_sum(l);
      if (lane == 0) *(LAS f32x2*)(lds + L_ML + 2 * wave) = f32x2{m, l};
      cnt_arrive(lds, C_ML, lane);
    }
    float acc[8] = {0.f, 0.f, 0.f, 0.f, 0.f, 0.f, 0.f, 0.f};
    auto vgroup = [&](const bool more, const unsigned tb, const float p) __attribute__((always_inline)) {
#define PV_STEP(I) { fma8(acc, dpp<BCAST + (I)>(p), bB[I]); \
        asm volatile("" : "+v"(acc[0]), "+v"(acc[1]), "+v"(acc[2]), "+v"(acc[3]), "+v"(acc[4]), "+v"(acc[5]), "+v"(acc[6]), "+v"(acc[7])); SB(); \
        if (more) { PACE(t0m, I); bB[I] = ld16_nt(rv, kvoff, tb + (I) * 8192); SB(); } \
        else if ((I) == 11) {   \
          pace_wait(t0m, T_K0 + T_MAIN); \
          _Pragma("unroll") for (int u = 0; u < 4; ++u) { \
            bB[u] = ld16_nt(mkrsrc(wo, H * DH * D * 2), r * 2048 + c * 16, (h * 128 + wave * 16 + 4 * u) * 2048 + s * 256); SB(); } } }
      PV_STEP(0) PV_STEP(1) PV_STEP(2) PV_STEP(3) PV_STEP(4) PV_STEP(5) PV_STEP(6) PV_STEP(7)
      PV_STEP(8) PV_STEP(9) PV_STEP(10) PV_STEP(11) PV_STEP(12) PV_STEP(13) PV_STEP(14) PV_STEP(15)
#undef PV_STEP
    };
#pragma unroll 1
    for (int g = 0; g < 7; ++g) vgroup(true, kvbase + (((g + 1) * 16 + rot) & 127u) * 8192, scp[g]);
    __builtin_amdgcn_s_setprio(3);
    cnt_wait(lds, C_WG, 1);
    {
      const float wgt = lds[L_WG + wave];
#pragma unroll
      for (int e = 0; e < 8; ++e) acc[e] *= wgt;
      vgroup(false, 0, scp[7] * wgt);
    }
    *(LAS f32x2*)(lds + L_MRG + wave * 128 + 8 * c + 2 * r) = fold_rows(acc);
    cnt_arrive(lds, C_END, lane);
#pragma unroll
    for (int u = 0; u < 4; ++u) *(LAS u32x4*)(lds + L_WT + (wave * 16 + 4 * u + r) * WT_STR + 4 * c) = bB[u];
    cnt_arrive(lds, C_WT, lane);
    return;
#undef PACE
  }

  const int a = wave - NMAIN;
  const rsrc_t rx = mkrsrc(x, D * 2), rws = mkrsrc(ws, WS_END);
  unsigned t0, tagA, tagB, tagC, tagD;
  unsigned long long e2;
  {
    u32x4 xa = ld16(rx, lane * 32, 0), xb = ld16(rx, lane * 32 + 16, 0);
    const u32x2 ep = __builtin_amdgcn_raw_buffer_load_b64(rws, 0, WS_EPOCH, SC1);
    const rsrc_t rwq = mkrsrc(wq, D * H * DH * 2);
    u32x4 w[8];
    const int voff = r * 8192 + c * 16, sb = (s * 128 + a * 32) * 8192 + h * 256;
#pragma unroll
    for (int i = 0; i < 8; ++i) { w[i] = ld16_nt(rwq, voff, sb + i * 32768); SB(); }
    if (a == 0 && lane < C_NUM) *(LAS unsigned*)(lds + L_CNT + lane) = 0u;
    __syncthreads();
    t0 = (unsigned)__builtin_amdgcn_s_memrealtime();
    asm volatile("" : "+v"(xa), "+v"(xb));
    float v[16];
#pragma unroll
    for (int i = 0; i < 4; ++i) { v[2 * i] = lo2f(xa[i]); v[2 * i + 1] = hi2f(xa[i]); v[8 + 2 * i] = lo2f(xb[i]); v[9 + 2 * i] = hi2f(xb[i]); }
    float sm = 0.f;
#pragma unroll
    for (int i = 0; i < 16; ++i) sm += v[i];
    const float mean = wave_sum(sm) * (1.f / D);
    float ss = 0.f;
#pragma unroll
    for (int i = 0; i < 16; ++i) { const float d = v[i] - mean; ss = fmaf(d, d, ss); }
    const float var = wave_sum(ss) * (1.f / D);
    const float mb = rbf(mean), den = rbf(sqrtf(rbf(rbf(var) + 1e-5f)));
    const float rinv = 1.0f / den;
#pragma unroll
    for (int i = 0; i < 16; ++i) {
      const float nu = rbf(v[i] - mb), q0 = nu * rinv;
      v[i] = rbf(fmaf(fmaf(-q0, den, nu), rinv, q0));
    }
#pragma unroll
    for (int q = 0; q < 4; ++q) *(LAS f32x4*)(lds + L_XN + q * 256 + 4 * lane) = f32x4{v[q], v[4 + q], v[8 + q], v[12 + q]};
    cnt_arrive(lds, C_XN, lane);
    const unsigned long long e1 = (((unsigned long long)__builtin_amdgcn_readfirstlane(ep[1]) << 32) | (unsigned)__builtin_amdgcn_readfirstlane(ep[0])) + 1ull;
    e2 = e1 + 1ull;
    tagA = (unsigned)e1; tagB = (unsigned)(e1 >> 32) + (unsigned)e1 * 0x9E3779B1u;
    tagC = (unsigned)e2; tagD = (unsigned)(e2 >> 32) + (unsigned)e2 * 0x9E3779B1u;
    float acc[8] = {0.f, 0.f, 0.f, 0.f, 0.f, 0.f, 0.f, 0.f};
#pragma unroll
    for (int q = 0; q < 2; ++q) {
      const f32x4 xv = *(LAS f32x4*)(lds + L_XN + r * 256 + s * 32 + a * 8 + 4 * q);
#pragma unroll
      for (int e = 0; e < 4; ++e) fma8(acc, xv[e], w[4 * q + e]);
    }
    *(LAS f32x2*)(lds + L_QP + a * 128 + 8 * c + 2 * r) = fold_rows(acc);
  }
  cnt_arrive(lds, C_Q, lane);
  if (a == 0) {
    cnt_wait<false>(lds, C_Q, NAUX);
    f32x2 v = *(LAS f32x2*)(lds + L_QP + 2 * lane);
#pragma unroll
    for (int q = 1; q < 4; ++q) v += *(LAS f32x2*)(lds + L_QP + q * 128 + 2 * lane);
    st16_sc1(rws, WS_Q + (h * 8 + s) * 1024 + lane * 16, rec(v[0], v[1], tagA, tagB));
    st16_sc1(rws, (bid == 0 && lane == 0) ? WS_T0 : OOB, u32x4{t0, tagA, t0, tagB});
  }
  if (a == 1) {
    pace_wait(t0, T_QPOLL);
    float q0 = 0.f, q1 = 0.f;
    unsigned tg = t0;
#pragma unroll 1
    for (unsigned sp = 0; sp < SPIN_MEM; ++sp) {
      REREAD();
      u32x4 g[8];
#pragma unroll
      for (int i = 0; i < 8; ++i) g[i] = ld16_sc1(rws, lane * 16, WS_Q + (h * 8 + i) * 1024);
      const u32x4 gt = ld16_sc1(rws, 0, WS_T0);
      bool ok = true;
      tg = rok(gt, tagA, tagB) ? gt[0] : t0;
      q0 = 0.f; q1 = 0.f;
#pragma unroll
      for (int i = 0; i < 8; ++i) { ok = ok && rok(g[i], tagA, tagB); q0 += r0(g[i]); q1 += r1(g[i]); }
      if (__all(ok)) break;
      __builtin_amdgcn_s_sleep(2);
    }
    *(LAS unsigned*)(lds + L_QS + lane) = pack2(q0, q1);
    if (lane == 0) *(LAS unsigned*)(lds + L_CNT + W_T0) = t0 - (unsigned)min(max((int)(t0 - tg), 0), 100);
    cnt_arrive(lds, C_QR, lane);
  }
  const int ks = xl * 2 + (j >> 4), cg = j & 15;
  u32x4 dw[7];
  {
    const rsrc_t rd = mkrsrc(wd, D * F * 2);
    const int voff = (lane >> 3) * 2048 + (lane & 7) * 16, sb = (ks * 224 + a * 56) * 2048 + cg * 128;
    constexpr int WSTEP = ((T_K0 - T_W2) << 16) / 7;
    int wdue = T_W2 * 65536 + (WSTEP / 128) * (a * 32 + j);
#pragma unroll
    for (int i = 0; i < 7; ++i) { pace_wait(t0, wdue >> 16); wdue += WSTEP; dw[i] = ld16_nt(rd, voff, sb + i * 16384); SB(); }
  }
  if (a == 0 && j < 28) {
    cnt_wait(lds, C_F1, NMAIN);
    f32x2 v = *(LAS f32x2*)(lds + L_FP + 2 * (lane & 31));
#pragma unroll
    for (int q = 1; q < 8; ++q) v += *(LAS f32x2*)(lds + L_FP + q * 64 + 2 * (lane & 31));
    if (f1 >= 56) {
      st16_sc1(rws, lane < 32 ? WS_G1 + ((mat * 2 + rb) * F + cb * 64) * 8 + lane * 16 : OOB, rec(v[0], v[1], tagA, tagB));
    } else {
      pace_wait(t0, T_K0 + T_HA);
      u32x4 g[3];
#pragma unroll 1
      for (unsigned sp = 0; sp < SPIN_MEM; ++sp) {
        REREAD();
#pragma unroll
        for (int k = 0; k < 3; ++k) g[k] = ld16_sc1(rws, (lane & 31) * 16, WS_G1 + ((k + 1) * F + cb * 64) * 8);
        if (__all(rok(g[0], tagA, tagB) && rok(g[1], tagA, tagB) && rok(g[2], tagA, tagB))) break;
        __builtin_amdgcn_s_sleep(32);
      }
      float hh[2];
#pragma unroll
      for (int e = 0; e < 2; ++e) {
        const float gs = rbf(v[e] + __uint_as_float(g[0][2 * e]));
        const float us = rbf(__uint_as_float(g[1][2 * e]) + __uint_as_float(g[2][2 * e]));
        const float ge = 0.5f * gs * (1.f + tanhf(0.7978845608028654f * (gs + 0.044715f * gs * gs * gs)));
        hh[e] = rbf(ge) * us;
      }
      const float hp = __uint_as_float(pack2(hh[0], hh[1]));
      st16_sc1(rws, (lane < 32 && !(lane & 1)) ? WS_H + cb * 256 + lane * 8 : OOB, rec(hp, dpp<XOR1>(hp), tagA, tagB));
    }
  }
  {
    pace_wait(t0, T_K0 + T_HB);
    u32x4 g;
#pragma unroll 1
    for (unsigned sp = 0; sp < SPIN_MEM; ++sp) {
      REREAD();
      g = ld16_sc1(rws, (lane < 14 ? lane : 13) * 16, WS_H + (ks * 56 + a * 14) * 16);
      if (__all(rok(g, tagA, tagB))) break;
      __builtin_amdgcn_s_sleep(32);
    }
    if (lane < 14) *(LAS f32x4*)(lds + L_H + a * 64 + 4 * lane) = f32x4{lo2f(g[0]), hi2f(g[0]), lo2f(g[2]), hi2f(g[2])};
    float y[8] = {0.f, 0.f, 0.f, 0.f, 0.f, 0.f, 0.f, 0.f};
#pragma unroll
    for (int i = 0; i < 7; ++i) fma8(y, lds[L_H + a * 64 + 8 * i + (lane >> 3)], dw[i]);
#pragma unroll
    for (int e = 0; e < 8; ++e) y[e] += dpp<ROR8>(y[e]);
    const f32x2 z = fold_rows(y);
    if (c < 8) *(LAS f32x2*)(lds + L_F2 + a * 64 + 8 * c + 2 * r) = z;
    cnt_arrive(lds, C_F2, lane);
  }
  const int col0 = s * 128 + 4 * h;
  unsigned xw = 0;
  if (a == 0) xw = __builtin_amdgcn_raw_buffer_load_b32(rx, (col0 + 2 * (lane & 1)) * 2, 0, 0);
  if (a == 3) {
    const unsigned po = __builtin_amdgcn_raw_buffer_load_b32(mkrsrc(out, D * 2), (col0 + 2 * (lane & 1)) * 2, 0, SC1);
    asm volatile("" :: "v"(po));
  }
  if (a == 1) {
    cnt_wait(lds, C_ML, NMAIN);
    const f32x2 ml = *(LAS f32x2*)(lds + L_ML + 2 * (lane & 7));
    const float Mw = max8(ml[0]), Lw = sum8(exp2_(ml[0] - Mw) * ml[1]);
    st16_sc1(rws, lane == 0 ? WS_ML + (h * 8 + s) * 128 : OOB, rec(Mw, Lw, tagA, tagB));
    pace_wait(t0, T_K0 + T_MAIN / 2 + 1500);
    u32x4 g;
#pragma unroll 1
    for (unsigned sp = 0; sp < SPIN_MEM; ++sp) {
      REREAD();
      g = ld16_sc1(rws, (lane & 7) * 128, WS_ML + h * 8 * 128);
      if (__all(rok(g, tagA, tagB))) break;
      __builtin_amdgcn_s_sleep(32);
    }
    const float M = max8(r0(g)), L = sum8(exp2_(r0(g) - M) * r1(g));
    if (lane < 8) lds[L_WG + lane] = exp2_(ml[0] - M) / L;
    cnt_arrive(lds, C_WG, lane);
  }
  pace_wait(t0, T_K0 + T_MAIN - 100);
  __builtin_amdgcn_s_setprio(3);
  if (a == 2) {
    cnt_wait(lds, C_F2, NAUX);
    f32x2 v = *(LAS f32x2*)(lds + L_F2 + 2 * (lane & 31));
#pragma unroll
    for (int q = 1; q < 4; ++q) v += *(LAS f32x2*)(lds + L_F2 + q * 64 + 2 * (lane & 31));
    pace_wait(t0, T_K0 + T_MAIN + 60);
    st16_sc1(rws, lane < 32 ? WS_FFN + (ks * D + cg * 64) * 8 + lane * 16 : OOB, rec(v[0], v[1], tagA, tagB));
  }
  if (a == 0) {
    cnt_wait<false>(lds, C_END, NMAIN);
    f32x2 v = *(LAS f32x2*)(lds + L_MRG + 2 * lane);
#pragma unroll
    for (int w = 1; w < 8; ++w) v += *(LAS f32x2*)(lds + L_MRG + w * 128 + 2 * lane);
    st16_sc1(rws, WS_Q + (h * 8 + s) * 1024 + lane * 16, rec(v[0], v[1], tagC, tagD));
  } else {
    cnt_wait(lds, C_END, NMAIN);
  }
  u32x4 bfr[2][4];
  cnt_wait<false>(lds, C_WT, NMAIN);
#pragma unroll
  for (int q = 0; q < 4; ++q)
#pragma unroll
    for (int d = 0; d < 4; ++d) {
      const unsigned w0 = *(LAS unsigned*)(lds + L_WT + (32 * q + 8 * r + 2 * d) * WT_STR + 16 * a + c);
      const unsigned w1 = *(LAS unsigned*)(lds + L_WT + (32 * q + 8 * r + 2 * d + 1) * WT_STR + 16 * a + c);
      bfr[0][q][d] = __builtin_amdgcn_perm(w1, w0, 0x05040100u);
      bfr[1][q][d] = __builtin_amdgcn_perm(w1, w0, 0x07060302u);
    }
  {
    u32x4 g0, g1;
    const int vo = r * 1024 + (16 * a + c) * 16;
#pragma unroll 1
    for (unsigned sp = 0; sp < SPIN_MEM; ++sp) {
      REREAD();
      g0 = ld16_sc1(rws, vo, WS_Q + h * 8 * 1024);
      g1 = ld16_sc1(rws, vo, WS_Q + (h * 8 + 4) * 1024);
      if (__all(rok(g0, tagC, tagD) && rok(g1, tagC, tagD))) break;
    }
    float o = fold32(r0(g0) + r0(g1), r1(g0) + r1(g1));
    o = fold16(o, o);
    if ((r & 1) == 0) *((LAS u16*)(lds + L_OB) + 32 * a + 2 * c + (r >> 1)) = __builtin_bit_cast(u16, (__bf16)o);
  }
  cnt_arrive(lds, C_OB, lane);
  cnt_wait<false>(lds, C_OB, NAUX);
  {
    f32x4 y0 = {0.f, 0.f, 0.f, 0.f}, y1 = y0;
#pragma unroll
    for (int q = 0; q < 4; ++q) {
      const u32x4 af = *(LAS u32x4*)(lds + L_OB + 16 * q + 4 * r);
      y0 = __builtin_amdgcn_mfma_f32_16x16x32_bf16(__builtin_bit_cast(bf16x8, af), __builtin_bit_cast(bf16x8, bfr[0][q]), y0, 0, 0, 0);
      y1 = __builtin_amdgcn_mfma_f32_16x16x32_bf16(__builtin_bit_cast(bf16x8, af), __builtin_bit_cast(bf16x8, bfr[1][q]), y1, 0, 0, 0);
    }
    st16_sc1(rws, lane < 16 ? WS_Y + (h * 8 + s) * 1024 + (16 * a + lane) * 16 : OOB, rec(y0[0], y1[0], tagA, tagB));
  }
  if (a == 0) {
    u32x4 g, gf;
    pace_wait((unsigned)__builtin_amdgcn_s_memrealtime(), 20);
#pragma unroll 1
    for (unsigned sp = 0; sp < SPIN_MEM; ++sp) {
      REREAD();
      g = ld16_sc1(rws, (lane >> 1) * (8 * 1024) + (lane & 1) * 16, WS_Y + s * 1024 + h * 32);
      gf = ld16_sc1(rws, ((lane >> 1) & 15) * (D * 8) + (lane & 1) * 16, WS_FFN + col0 * 8);
      if (__all(rok(g, tagA, tagB) && rok(gf, tagA, tagB))) break;
    }
    const float f0 = rbf(parity_sum(r0(gf)) * 0.5f), f1v = rbf(parity_sum(r1(gf)) * 0.5f);
    const float a0 = rbf(parity_sum(r0(g))), a1 = rbf(parity_sum(r1(g)));
    const unsigned o = pack2(rbf(lo2f(xw) + a0) + f0, rbf(hi2f(xw) + a1) + f1v);
    __builtin_amdgcn_raw_buffer_store_b32(o, mkrsrc(out, D * 2), lane < 2 ? (col0 + 2 * lane) * 2 : OOB, 0, SC1);
    __builtin_amdgcn_raw_buffer_store_b64(u32x2{(unsigned)e2, (unsigned)(e2 >> 32)}, rws, (bid == 0 && lane == 0) ? WS_EPOCH : OOB, 0, SC1);
  }
}

extern "C" void kernel_launch(void* const* d_in, const int* in_sizes, int n_in, void* d_out, int out_size, void* d_ws, size_t ws_size,
                              hipStream_t stream) {
  fused_decode_kernel<<<256, NTHR, 0, stream>>>((const u16*)d_in[0], (const u16*)d_in[1], (const u16*)d_in[2], (const u16*)d_in[3],
                                                 (const u16*)d_in[4], (const u16*)d_in[6], (const u16*)d_in[7], (const u16*)d_in[8],
                                                 (u16*)d_out, d_ws);
}
```

```cpp
#include <hip/hip_runtime.h>
#include <stdint.h>

typedef unsigned u32x4 __attribute__((ext_vector_type(4)));
typedef unsigned u32x2 __attribute__((ext_vector_type(2)));
typedef float f32x4 __attribute__((ext_vector_type(4)));
typedef float f32x2 __attribute__((ext_vector_type(2)));
typedef __bf16 bf16x2 __attribute__((ext_vector_type(2)));
typedef __bf16 bf16x8 __attribute__((ext_vector_type(8)));
typedef unsigned short u16;
#define LAS __attribute__((address_space(3)))
typedef __amdgpu_buffer_rsrc_t rsrc_t;

constexpr int T_F1 = 70;
constexpr int T_W2 = 262;
constexpr int T_K0 = 345;
constexpr int T_MAIN = 7055;
constexpr int PACE_G = 2;
constexpr int T_QPOLL = 315;
constexpr int T_HA = 1600, T_HB = 2800;

constexpr int D = 1024, H = 32, SK = 32768, DH = 128, F = 3584;
constexpr int NMAIN = 8, NAUX = 4, NTHR = (NMAIN + NAUX) * 64;
constexpr int NT = 2, SC1 = 16;
constexpr int OOB = 0x40000000;
constexpr unsigned WS_EPOCH = 0, WS_T0 = 1024, WS_ML = 4096, WS_Q = WS_ML + 256 * 128, WS_Y = WS_Q + 256 * 1024, WS_G1 = WS_Y + 256 * 1024,
                   WS_H = WS_G1 + 4 * F * 8, WS_FFN = WS_H + F * 4, WS_END = WS_FFN + 16 * D * 8;
constexpr int L_XN = 0, L_QP = 1024, L_QS = L_QP + 4 * 128, L_MRG = L_QS + 64, L_ML = L_MRG + 8 * 128, L_WG = L_ML + 16, L_FP = L_WG + 8,
              L_H = L_FP + 8 * 64, L_F2 = L_H + 4 * 64, L_OB = L_F2 + 4 * 64, L_CNT = L_OB + 64, L_SC = L_CNT + 16, L_WT = L_SC + 8 * 512,
              WT_STR = 68, L_END = L_WT + 128 * WT_STR;
constexpr int C_Q = 0, C_XN = 1, C_F2 = 2, C_WT = 3, C_ML = 4, C_WG = 5, C_END = 6, C_QR = 7, C_F1 = 8, C_OB = 9, C_NUM = 10, W_T0 = 12;
constexpr unsigned SPIN_MEM = 1u << 20, SPIN_LDS = 1u << 23;

__device__ __forceinline__ float lo2f(unsigned w) { return __builtin_bit_cast(float, w << 16); }
__device__ __forceinline__ float hi2f(unsigned w) { return __builtin_bit_cast(float, w & 0xffff0000u); }
__device__ __forceinline__ float rbf(float x) { return (float)(__bf16)x; }
__device__ __forceinline__ unsigned pack2(float a, float b) { bf16x2 v = {(__bf16)a, (__bf16)b}; return __builtin_bit_cast(unsigned, v); }
__device__ __forceinline__ float dot2(unsigned k, unsigned q, float acc) {
  return __builtin_amdgcn_fdot2_f32_bf16(__builtin_bit_cast(bf16x2, k), __builtin_bit_cast(bf16x2, q), acc, false);
}
template <int CTRL> __device__ __forceinline__ float dpp(float x) {
  return __builtin_bit_cast(float, __builtin_amdgcn_mov_dpp(__builtin_bit_cast(int, x), CTRL, 0xf, 0xf, true));
}
constexpr int XOR1 = 0xB1, XOR2 = 0x4E, XOR7 = 0x141, ROR8 = 0x128, ROR4 = 0x124, BCAST = 0x150;
__device__ __forceinline__ float fold32(float a, float b) {
  auto t = __builtin_amdgcn_permlane32_swap(__float_as_uint(a), __float_as_uint(b), false, false);
  return __uint_as_float(t[0]) + __uint_as_float(t[1]);
}
__device__ __forceinline__ float fold16(float a, float b) {
  auto t = __builtin_amdgcn_permlane16_swap(__float_as_uint(a), __float_as_uint(b), false, false);
  return __uint_as_float(t[0]) + __uint_as_float(t[1]);
}
__device__ __forceinline__ f32x2 fold_rows(const float (&v)[8]) {
  const float u0 = fold32(v[0], v[4]), u1 = fold32(v[1], v[5]), u2 = fold32(v[2], v[6]), u3 = fold32(v[3], v[7]);
  return f32x2{fold16(u0, u2), fold16(u1, u3)};
}
__device__ __forceinline__ float xrow_sum(float x) { x = fold16(x, x); return fold32(x, x); }
__device__ __forceinline__ float xrow_max(float x) {
  auto s = __builtin_amdgcn_permlane16_swap(__float_as_uint(x), __float_as_uint(x), false, false);
  x = fmaxf(__uint_as_float(s[0]), __uint_as_float(s[1]));
  auto t = __builtin_amdgcn_permlane32_swap(__float_as_uint(x), __float_as_uint(x), false, false);
  return fmaxf(__uint_as_float(t[0]), __uint_as_float(t[1]));
}
__device__ __forceinline__ float parity_sum(float x) {
  x += dpp<XOR2>(x); x += dpp<ROR4>(x); x += dpp<ROR8>(x); return xrow_sum(x);
}
__device__ __forceinline__ float wave_sum(float x) { x += dpp<XOR1>(x); return parity_sum(x); }
__device__ __forceinline__ float max8(float x) { x = fmaxf(x, dpp<XOR1>(x)); x = fmaxf(x, dpp<XOR2>(x)); return fmaxf(x, dpp<XOR7>(x)); }
__device__ __forceinline__ float sum8(float x) { x += dpp<XOR1>(x); x += dpp<XOR2>(x); return x + dpp<XOR7>(x); }
__device__ __forceinline__ float wave_max(float x) { x = max8(x); x = fmaxf(x, dpp<ROR8>(x)); return xrow_max(x); }
__device__ __forceinline__ float exp2_(float x) { return __builtin_amdgcn_exp2f(x); }
__device__ __forceinline__ rsrc_t mkrsrc(const void* p, unsigned bytes) { return __builtin_amdgcn_make_buffer_rsrc((void*)p, 0, (int)bytes, 0x00020000); }
__device__ __forceinline__ u32x4 ld16(rsrc_t r, int voff, int soff) { return __builtin_amdgcn_raw_buffer_load_b128(r, voff, soff, 0); }
__device__ __forceinline__ u32x4 ld16_nt(rsrc_t r, int voff, int soff) { return __builtin_amdgcn_raw_buffer_load_b128(r, voff, soff, NT); }
__device__ __forceinline__ u32x4 ld16_sc1(rsrc_t r, int voff, int soff) { return __builtin_amdgcn_raw_buffer_load_b128(r, voff, soff, SC1); }
__device__ __forceinline__ void st16_sc1(rsrc_t r, int off, u32x4 v) { __builtin_amdgcn_raw_buffer_store_b128(v, r, off, 0, SC1); }
#define SB() __builtin_amdgcn_sched_barrier(0)
#define REREAD() asm volatile("" ::: "memory")
__device__ __forceinline__ void pace_wait(unsigned t0, int due) {
  asm volatile("1:\n\ts_memrealtime s[96:97]\n\ts_waitcnt lgkmcnt(0)\n\ts_sub_u32 s96, s96, %0\n\ts_cmp_lt_i32 s96, %1\n\t"
               "s_cbranch_scc0 2f\n\ts_sleep 1\n\ts_branch 1b\n2:"
               :: "s"(t0), "s"(due) : "s96", "s97", "scc", "memory");
}
__device__ __forceinline__ void fma8(float (&acc)[8], float s, u32x4 w) {
#pragma unroll
  for (int t = 0; t < 4; ++t) { acc[2 * t] = fmaf(s, lo2f(w[t]), acc[2 * t]); acc[2 * t + 1] = fmaf(s, hi2f(w[t]), acc[2 * t + 1]); }
}
__device__ __forceinline__ u32x4 rec(float a, float b, unsigned ta, unsigned tb) { return u32x4{__float_as_uint(a), ta, __float_as_uint(b), tb}; }
__device__ __forceinline__ bool rok(u32x4 g, unsigned ta, unsigned tb) { return g[1] == ta && g[3] == tb; }
__device__ __forceinline__ float r0(u32x4 g) { return __uint_as_float(g[0]); }
__device__ __forceinline__ float r1(u32x4 g) { return __uint_as_float(g[2]); }

__device__ __forceinline__ void cnt_arrive(LAS float* lds, int which, int lane) {
  asm volatile("s_waitcnt lgkmcnt(0)" ::: "memory");
  if (lane == 0) __hip_atomic_fetch_add((LAS unsigned*)(lds + L_CNT + which), 1u, __ATOMIC_RELAXED, __HIP_MEMORY_SCOPE_WORKGROUP);
}
template <bool SLEEP = true>
__device__ __forceinline__ void cnt_wait(LAS float* lds, int which, unsigned n) {
#pragma unroll 1
  for (unsigned sp = 0; sp < SPIN_LDS; ++sp) {
    if (__builtin_amdgcn_readfirstlane(*(volatile LAS unsigned*)(lds + L_CNT + which)) >= n) break;
    if (SLEEP) __builtin_amdgcn_s_sleep(1);
  }
  REREAD();
}

__global__ void __launch_bounds__(NTHR, 3)
fused_decode_kernel(const u16* __restrict__ x, const u16* __restrict__ wq, void* ws, const u16* __restrict__ wg,
                    const u16* __restrict__ wu, const u16* __restrict__ Kc, const u16* __restrict__ Vc, const u16* __restrict__ wo,
                    const u16* __restrict__ wd, u16* __restrict__ out) {
  __shared__ __attribute__((aligned(16))) float lds_[L_END];
  LAS float* lds = (LAS float*)lds_;
  const int tid = threadIdx.x, lane = tid & 63, wave = __builtin_amdgcn_readfirstlane(tid >> 6);
  const int r = lane >> 4, c = lane & 15;
  const int bid = blockIdx.x, xl = bid & 7, j = bid >> 3;
  const int h = xl * 4 + (j >> 3), s = j & 7;
  const int f1 = xl * 28 + j, mat = f1 / 112, rb = (f1 / 56) & 1, cb = f1 % 56;
  asm volatile("" :: "s"(x), "s"(wq), "s"(ws), "s"(f1), "s"(wave));

  if (wave < NMAIN) {
    const rsrc_t rk = mkrsrc(Kc, (unsigned)H * SK * DH * 2), rv = mkrsrc(Vc, (unsigned)H * SK * DH * 2);
    u32x4 bA[16], bB[16];
    const unsigned kvbase = (unsigned)h * (8u << 20) + (unsigned)s * (1u << 20) + wave * 1024;
    const unsigned rot = 32u * ((h + (h >> 2) + (h >> 4)) & 3);
    const int kvoff = lane * 16;
    constexpr int STEP = (T_MAIN << 16) / 256 * PACE_G;
    int due = T_K0 * 65536 - STEP + (STEP / 256) * (wave * 32 + j);
#define PACE(T0, I) do { if ((I) % PACE_G == 0) { due += STEP; pace_wait(T0, due >> 16); } } while (0)
    __builtin_amdgcn_s_barrier();
    const unsigned t0 = (unsigned)__builtin_amdgcn_s_memrealtime();
    SB();
    {
      const rsrc_t rg = mkrsrc(mat ? wu : wg, D * F * 2);
      const int voff = j < 28 ? (lane >> 3) * (F * 2) + (lane & 7) * 16 : OOB, sb = (rb * 512 + wave * 64) * (F * 2) + cb * 128;
      constexpr int FSTEP = ((T_W2 - T_F1) << 16) / 8;
      int fdue = T_F1 * 65536 + (FSTEP / 256) * (wave * 32 + j);
#pragma unroll
      for (int i = 0; i < 8; ++i) { pace_wait(t0, fdue >> 16); fdue += FSTEP; bB[i] = ld16_nt(rg, voff, sb + i * 8 * (F * 2)); SB(); }
    }
#pragma unroll
    for (int i = 0; i < 16; ++i) { PACE(t0, i); bA[i] = ld16_nt(rk, kvoff, kvbase + (rot + i) * 8192); SB(); }
    {
      cnt_wait(lds, C_XN, 1);
      float ag[8] = {0.f, 0.f, 0.f, 0.f, 0.f, 0.f, 0.f, 0.f};
#pragma unroll
      for (int i = 0; i < 8; ++i) {
        const int k = rb * 512 + wave * 64 + 8 * i + (lane >> 3);
        fma8(ag, lds[L_XN + (k & 3) * 256 + (k >> 2)], bB[i]);
      }
#pragma unroll
      for (int e = 0; e < 8; ++e) ag[e] += dpp<ROR8>(ag[e]);
      const f32x2 z = fold_rows(ag);
      if (c < 8) *(LAS f32x2*)(lds + L_FP + wave * 64 + 8 * c + 2 * r) = z;
      cnt_arrive(lds, C_F1, lane);
      SB();
    }
#pragma unroll
    for (int i = 0; i < 16; ++i) { PACE(t0, i); bB[i] = ld16_nt(rk, kvoff, kvbase + (rot + 16 + i) * 8192); SB(); }
    cnt_wait(lds, C_QR, 1);
    const u32x4 qp = *(LAS u32x4*)(lds + L_QS + 4 * c);
    const unsigned t0m = __builtin_amdgcn_readfirstlane(*(LAS unsigned*)(lds + L_CNT + W_T0));
    constexpr float SCALE = 0.08838834764831845f, LOG2E = 1.4426950408889634f;
    LAS float* scp = lds + L_SC + wave * 512 + lane * 8;
    float mx = -INFINITY;
    auto kgroup = [&](u32x4 (&kb)[16], const bool more, const rsrc_t rn, const unsigned tb, const int g) __attribute__((always_inline)) {
      float sx[16];
#pragma unroll
      for (int i = 0; i < 16; ++i) {
        float d = 0.f;
#pragma unroll
        for (int t = 0; t < 4; ++t) d = dot2(kb[i][t], qp[t], d);
        asm volatile("" : "+v"(d));
        sx[i] = d;
        SB();
        if (more) { PACE(t0m, i); kb[i] = ld16_nt(rn, kvoff, tb + i * 8192); SB(); }
      }
#define TR_STEP(HALF, CTRL, BIT) _Pragma("unroll") for (int hh = 0; hh < (HALF); ++hh) { \
        const float lo_ = sx[hh], hi_ = sx[hh + (HALF)]; const float t1_ = lo_ + dpp<CTRL>(lo_), t2_ = hi_ + dpp<CTRL>(hi_); \
        sx[hh] = (c & (BIT)) ? t2_ : t1_; }
      TR_STEP(8, ROR8, 8) TR_STEP(4, XOR7, 4) TR_STEP(2, XOR2, 2) TR_STEP(1, XOR1, 1)
#undef TR_STEP
      const float sc = rbf(rbf(sx[0]) * SCALE) * LOG2E;
      mx = fmaxf(mx, sc);
      scp[g] = sc;
      SB();
    };
    kgroup(bA, false, rk, 0, 0);
#pragma unroll 1
    for (int g = 1; g < 8; ++g)
      kgroup(bB, true, mkrsrc(g == 7 ? Vc : Kc, (unsigned)H * SK * DH * 2), kvbase + (((g + 1) * 16 + rot) & 127u) * 8192, g);
    {
      const float m = wave_max(mx);
      f32x4 p0 = *(LAS f32x4*)scp, p1 = *(LAS f32x4*)(scp + 4);
      float l = 0.f;
#pragma unroll
      for (int e = 0; e < 4; ++e) { p0[e] = exp2_(p0[e] - m); p1[e] = exp2_(p1[e] - m); l += p0[e] + p1[e]; }
      *(LAS f32x4*)scp = p0; *(LAS f32x4*)(scp + 4) = p1;
      l = wave_sum(l);
      if (lane == 0) *(LAS f32x2*)(lds + L_ML + 2 * wave) = f32x2{m, l};
      cnt_arrive(lds, C_ML, lane);
    }
    float acc[8] = {0.f, 0.f, 0.f, 0.f, 0.f, 0.f, 0.f, 0.f};
    auto vgroup = [&](const bool more, const unsigned tb, const float p) __attribute__((always_inline)) {
#define PV_STEP(I) { fma8(acc, dpp<BCAST + (I)>(p), bB[I]); \
        asm volatile("" : "+v"(acc[0]), "+v"(acc[1]), "+v"(acc[2]), "+v"(acc[3]), "+v"(acc[4]), "+v"(acc[5]), "+v"(acc[6]), "+v"(acc[7])); SB(); \
        if (more) { PACE(t0m, I); bB[I] = ld16_nt(rv, kvoff, tb + (I) * 8192); SB(); } \
        else if ((I) == 11) {   \
          pace_wait(t0m, T_K0 + T_MAIN); \
          _Pragma("unroll") for (int u = 0; u < 4; ++u) { \
            bB[u] = ld16_nt(mkrsrc(wo, H * DH * D * 2), r * 2048 + c * 16, (h * 128 + wave * 16 + 4 * u) * 2048 + s * 256); SB(); } } }
      PV_STEP(0) PV_STEP(1) PV_STEP(2) PV_STEP(3) PV_STEP(4) PV_STEP(5) PV_STEP(6) PV_STEP(7)
      PV_STEP(8) PV_STEP(9) PV_STEP(10) PV_STEP(11) PV_STEP(12) PV_STEP(13) PV_STEP(14) PV_STEP(15)
#undef PV_STEP
    };
#pragma unroll 1
    for (int g = 0; g < 7; ++g) vgroup(true, kvbase + (((g + 1) * 16 + rot) & 127u) * 8192, scp[g]);
    __builtin_amdgcn_s_setprio(3);
    cnt_wait(lds, C_WG, 1);
    {
      const float wgt = lds[L_WG + wave];
#pragma unroll
      for (int e = 0; e < 8; ++e) acc[e] *= wgt;
      vgroup(false, 0, scp[7] * wgt);
    }
    *(LAS f32x2*)(lds + L_MRG + wave * 128 + 8 * c + 2 * r) = fold_rows(acc);
    cnt_arrive(lds, C_END, lane);
#pragma unroll
    for (int u = 0; u < 4; ++u) *(LAS u32x4*)(lds + L_WT + (wave * 16 + 4 * u + r) * WT_STR + 4 * c) = bB[u];
    cnt_arrive(lds, C_WT, lane);
    return;
#undef PACE
  }

  const int a = wave - NMAIN;
  const rsrc_t rx = mkrsrc(x, D * 2), rws = mkrsrc(ws, WS_END);
  unsigned t0, tagA, tagB, tagC, tagD;
  unsigned long long e2;
  {
    u32x4 xa = ld16(rx, lane * 32, 0), xb = ld16(rx, lane * 32 + 16, 0);
    const u32x2 ep = __builtin_amdgcn_raw_buffer_load_b64(rws, 0, WS_EPOCH, SC1);
    const rsrc_t rwq = mkrsrc(wq, D * H * DH * 2);
    u32x4 w[8];
    const int voff = r * 8192 + c * 16, sb = (s * 128 + a * 32) * 8192 + h * 256;
#pragma unroll
    for (int i = 0; i < 8; ++i) { w[i] = ld16_nt(rwq, voff, sb + i * 32768); SB(); }
    const unsigned ka = __builtin_amdgcn_raw_buffer_load_b32(mkrsrc((const void*)__builtin_amdgcn_kernarg_segment_ptr(), 80), a == 3 ? 64 : OOB, 0, 0);
    if (a == 0 && lane < C_NUM) *(LAS unsigned*)(lds + L_CNT + lane) = 0u;
    __syncthreads();
    t0 = (unsigned)__builtin_amdgcn_s_memrealtime();
    asm volatile("" : "+v"(xa), "+v"(xb));
    float v[16];
#pragma unroll
    for (int i = 0; i < 4; ++i) { v[2 * i] = lo2f(xa[i]); v[2 * i + 1] = hi2f(xa[i]); v[8 + 2 * i] = lo2f(xb[i]); v[9 + 2 * i] = hi2f(xb[i]); }
    float sm = 0.f;
#pragma unroll
    for (int i = 0; i < 16; ++i) sm += v[i];
    const float mean = wave_sum(sm) * (1.f / D);
    float ss = 0.f;
#pragma unroll
    for (int i = 0; i < 16; ++i) { const float d = v[i] - mean; ss = fmaf(d, d, ss); }
    const float var = wave_sum(ss) * (1.f / D);
    const float mb = rbf(mean), den = rbf(sqrtf(rbf(rbf(var) + 1e-5f)));
    const float rinv = 1.0f / den;
#pragma unroll
    for (int i = 0; i < 16; ++i) {
      const float nu = rbf(v[i] - mb), q0 = nu * rinv;
      v[i] = rbf(fmaf(fmaf(-q0, den, nu), rinv, q0));
    }
#pragma unroll
    for (int q = 0; q < 4; ++q) *(LAS f32x4*)(lds + L_XN + q * 256 + 4 * lane) = f32x4{v[q], v[4 + q], v[8 + q], v[12 + q]};
    cnt_arrive(lds, C_XN, lane);
    const unsigned long long e1 = (((unsigned long long)__builtin_amdgcn_readfirstlane(ep[1]) << 32) | (unsigned)__builtin_amdgcn_readfirstlane(ep[0])) + 1ull;
    e2 = e1 + 1ull;
    tagA = (unsigned)e1; tagB = (unsigned)(e1 >> 32) + (unsigned)e1 * 0x9E3779B1u;
    tagC = (unsigned)e2; tagD = (unsigned)(e2 >> 32) + (unsigned)e2 * 0x9E3779B1u;
    float acc[8] = {0.f, 0.f, 0.f, 0.f, 0.f, 0.f, 0.f, 0.f};
#pragma unroll
    for (int q = 0; q < 2; ++q) {
      const f32x4 xv = *(LAS f32x4*)(lds + L_XN + r * 256 + s * 32 + a * 8 + 4 * q);
#pragma unroll
      for (int e = 0; e < 4; ++e) fma8(acc, xv[e], w[4 * q + e]);
    }
    *(LAS f32x2*)(lds + L_QP + a * 128 + 8 * c + 2 * r) = fold_rows(acc);
    cnt_arrive(lds, C_Q, lane);
    asm volatile("" :: "v"(ka));
  }
  if (a == 0) {
    cnt_wait<false>(lds, C_Q, NAUX);
    f32x2 v = *(LAS f32x2*)(lds + L_QP + 2 * lane);
#pragma unroll
    for (int q = 1; q < 4; ++q) v += *(LAS f32x2*)(lds + L_QP + q * 128 + 2 * lane);
    st16_sc1(rws, WS_Q + (h * 8 + s) * 1024 + lane * 16, rec(v[0], v[1], tagA, tagB));
    st16_sc1(rws, (bid == 0 && lane == 0) ? WS_T0 : OOB, u32x4{t0, tagA, t0, tagB});
  }
  if (a == 1) {
    pace_wait(t0, T_QPOLL);
    float q0 = 0.f, q1 = 0.f;
    unsigned tg = t0;
#pragma unroll 1
    for (unsigned sp = 0; sp < SPIN_MEM; ++sp) {
      REREAD();
      u32x4 g[8];
#pragma unroll
      for (int i = 0; i < 8; ++i) g[i] = ld16_sc1(rws, lane * 16, WS_Q + (h * 8 + i) * 1024);
      const u32x4 gt = ld16_sc1(rws, 0, WS_T0);
      bool ok = true;
      tg = rok(gt, tagA, tagB) ? gt[0] : t0;
      q0 = 0.f; q1 = 0.f;
#pragma unroll
      for (int i = 0; i < 8; ++i) { ok = ok && rok(g[i], tagA, tagB); q0 += r0(g[i]); q1 += r1(g[i]); }
      if (__all(ok)) break;
      __builtin_amdgcn_s_sleep(2);
    }
    *(LAS unsigned*)(lds + L_QS + lane) = pack2(q0, q1);
    if (lane == 0) *(LAS unsigned*)(lds + L_CNT + W_T0) = t0 - (unsigned)min(max((int)(t0 - tg), 0), 100);
    cnt_arrive(lds, C_QR, lane);
  }
  const int ks = xl * 2 + (j >> 4), cg = j & 15;
  u32x4 dw[7];
  {
    const rsrc_t rd = mkrsrc(wd, D * F * 2);
    const int voff = (lane >> 3) * 2048 + (lane & 7) * 16, sb = (ks * 224 + a * 56) * 2048 + cg * 128;
    constexpr int WSTEP = ((T_K0 - T_W2) << 16) / 7;
    int wdue = T_W2 * 65536 + (WSTEP / 128) * (a * 32 + j);
#pragma unroll
    for (int i = 0; i < 7; ++i) { pace_wait(t0, wdue >> 16); wdue += WSTEP; dw[i] = ld16_nt(rd, voff, sb + i * 16384); SB(); }
  }
  if (a == 0 && j < 28) {
    cnt_wait(lds, C_F1, NMAIN);
    f32x2 v = *(LAS f32x2*)(lds + L_FP + 2 * (lane & 31));
#pragma unroll
    for (int q = 1; q < 8; ++q) v += *(LAS f32x2*)(lds + L_FP + q * 64 + 2 * (lane & 31));
    if (f1 >= 56) {
      st16_sc1(rws, lane < 32 ? WS_G1 + ((mat * 2 + rb) * F + cb * 64) * 8 + lane * 16 : OOB, rec(v[0], v[1], tagA, tagB));
    } else {
      pace_wait(t0, T_K0 + T_HA);
      u32x4 g[3];
#pragma unroll 1
      for (unsigned sp = 0; sp < SPIN_MEM; ++sp) {
        REREAD();
#pragma unroll
        for (int k = 0; k < 3; ++k) g[k] = ld16_sc1(rws, (lane & 31) * 16, WS_G1 + ((k + 1) * F + cb * 64) * 8);
        if (__all(rok(g[0], tagA, tagB) && rok(g[1], tagA, tagB) && rok(g[2], tagA, tagB))) break;
        __builtin_amdgcn_s_sleep(32);
      }
      float hh[2];
#pragma unroll
      for (int e = 0; e < 2; ++e) {
        const float gs = rbf(v[e] + __uint_as_float(g[0][2 * e]));
        const float us = rbf(__uint_as_float(g[1][2 * e]) + __uint_as_float(g[2][2 * e]));
        const float ge = 0.5f * gs * (1.f + tanhf(0.7978845608028654f * (gs + 0.044715f * gs * gs * gs)));
        hh[e] = rbf(ge) * us;
      }
      const float hp = __uint_as_float(pack2(hh[0], hh[1]));
      st16_sc1(rws, (lane < 32 && !(lane & 1)) ? WS_H + cb * 256 + lane * 8 : OOB, rec(hp, dpp<XOR1>(hp), tagA, tagB));
    }
  }
  {
    pace_wait(t0, T_K0 + T_HB);
    u32x4 g;
#pragma unroll 1
    for (unsigned sp = 0; sp < SPIN_MEM; ++sp) {
      REREAD();
      g = ld16_sc1(rws, (lane < 14 ? lane : 13) * 16, WS_H + (ks * 56 + a * 14) * 16);
      if (__all(rok(g, tagA, tagB))) break;
      __builtin_amdgcn_s_sleep(32);
    }
    if (lane < 14) *(LAS f32x4*)(lds + L_H + a * 64 + 4 * lane) = f32x4{lo2f(g[0]), hi2f(g[0]), lo2f(g[2]), hi2f(g[2])};
    float y[8] = {0.f, 0.f, 0.f, 0.f, 0.f, 0.f, 0.f, 0.f};
#pragma unroll
    for (int i = 0; i < 7; ++i) fma8(y, lds[L_H + a * 64 + 8 * i + (lane >> 3)], dw[i]);
#pragma unroll
    for (int e = 0; e < 8; ++e) y[e] += dpp<ROR8>(y[e]);
    const f32x2 z = fold_rows(y);
    if (c < 8) *(LAS f32x2*)(lds + L_F2 + a * 64 + 8 * c + 2 * r) = z;
    cnt_arrive(lds, C_F2, lane);
  }
  const int col0 = s * 128 + 4 * h;
  unsigned xw = 0;
  if (a == 0) xw = __builtin_amdgcn_raw_buffer_load_b32(rx, (col0 + 2 * (lane & 1)) * 2, 0, 0);
  if (a == 3) {
    const unsigned po = __builtin_amdgcn_raw_buffer_load_b32(mkrsrc(out, D * 2), (col0 + 2 * (lane & 1)) * 2, 0, SC1);
    asm volatile("" :: "v"(po));
  }
  if (a == 1) {
    cnt_wait(lds, C_ML, NMAIN);
    const f32x2 ml = *(LAS f32x2*)(lds + L_ML + 2 * (lane & 7));
    const float Mw = max8(ml[0]), Lw = sum8(exp2_(ml[0] - Mw) * ml[1]);
    st16_sc1(rws, lane == 0 ? WS_ML + (h * 8 + s) * 128 : OOB, rec(Mw, Lw, tagA, tagB));
    pace_wait(t0, T_K0 + T_MAIN / 2 + 1500);
    u32x4 g;
#pragma unroll 1
    for (unsigned sp = 0; sp < SPIN_MEM; ++sp) {
      REREAD();
      g = ld16_sc1(rws, (lane & 7) * 128, WS_ML + h * 8 * 128);
      if (__all(rok(g, tagA, tagB))) break;
      __builtin_amdgcn_s_sleep(32);
    }
    const float M = max8(r0(g)), L = sum8(exp2_(r0(g) - M) * r1(g));
    if (lane < 8) lds[L_WG + lane] = exp2_(ml[0] - M) / L;
    cnt_arrive(lds, C_WG, lane);
  }
  pace_wait(t0, T_K0 + T_MAIN - 100);
  __builtin_amdgcn_s_setprio(3);
  if (a == 2) {
    cnt_wait(lds, C_F2, NAUX);
    f32x2 v = *(LAS f32x2*)(lds + L_F2 + 2 * (lane & 31));
#pragma unroll
    for (int q = 1; q < 4; ++q) v += *(LAS f32x2*)(lds + L_F2 + q * 64 + 2 * (lane & 31));
    pace_wait(t0, T_K0 + T_MAIN + 60);
    st16_sc1(rws, lane < 32 ? WS_FFN + (ks * D + cg * 64) * 8 + lane * 16 : OOB, rec(v[0], v[1], tagA, tagB));
  }
  if (a == 0) {
    cnt_wait<false>(lds, C_END, NMAIN);
    f32x2 v = *(LAS f32x2*)(lds + L_MRG + 2 * lane);
#pragma unroll
    for (int w = 1; w < 8; ++w) v += *(LAS f32x2*)(lds + L_MRG + w * 128 + 2 * lane);
    st16_sc1(rws, WS_Q + (h * 8 + s) * 1024 + lane * 16, rec(v[0], v[1], tagC, tagD));
  } else {
    cnt_wait(lds, C_END, NMAIN);
  }
  u32x4 bfr[2][4];
  cnt_wait<false>(lds, C_WT, NMAIN);
#pragma unroll
  for (int q = 0; q < 4; ++q)
#pragma unroll
    for (int d = 0; d < 4; ++d) {
      const unsigned w0 = *(LAS unsigned*)(lds + L_WT + (32 * q + 8 * r + 2 * d) * WT_STR + 16 * a + c);
      const unsigned w1 = *(LAS unsigned*)(lds + L_WT + (32 * q + 8 * r + 2 * d + 1) * WT_STR + 16 * a + c);
      bfr[0][q][d] = __builtin_amdgcn_perm(w1, w0, 0x05040100u);
      bfr[1][q][d] = __builtin_amdgcn_perm(w1, w0, 0x07060302u);
    }
  {
    u32x4 g0, g1;
    const int vo = r * 1024 + (16 * a + c) * 16;
#pragma unroll 1
    for (unsigned sp = 0; sp < SPIN_MEM; ++sp) {
      REREAD();
      g0 = ld16_sc1(rws, vo, WS_Q + h * 8 * 1024);
      g1 = ld16_sc1(rws, vo, WS_Q + (h * 8 + 4) * 1024);
      if (__all(rok(g0, tagC, tagD) && rok(g1, tagC, tagD))) break;
    }
    float o = fold32(r0(g0) + r0(g1), r1(g0) + r1(g1));
    o = fold16(o, o);
    if ((r & 1) == 0) *((LAS u16*)(lds + L_OB) + 32 * a + 2 * c + (r >> 1)) = __builtin_bit_cast(u16, (__bf16)o);
  }
  cnt_arrive(lds, C_OB, lane);
  cnt_wait<false>(lds, C_OB, NAUX);
  {
    f32x4 y0 = {0.f, 0.f, 0.f, 0.f}, y1 = y0;
#pragma unroll
    for (int q = 0; q < 4; ++q) {
      const u32x4 af = *(LAS u32x4*)(lds + L_OB + 16 * q + 4 * r);
      y0 = __builtin_amdgcn_mfma_f32_16x16x32_bf16(__builtin_bit_cast(bf16x8, af), __builtin_bit_cast(bf16x8, bfr[0][q]), y0, 0, 0, 0);
      y1 = __builtin_amdgcn_mfma_f32_16x16x32_bf16(__builtin_bit_cast(bf16x8, af), __builtin_bit_cast(bf16x8, bfr[1][q]), y1, 0, 0, 0);
    }
    st16_sc1(rws, lane < 16 ? WS_Y + (h * 8 + s) * 1024 + (16 * a + lane) * 16 : OOB, rec(y0[0], y1[0], tagA, tagB));
  }
  if (a == 0) {
    u32x4 g, gf;
    pace_wait((unsigned)__builtin_amdgcn_s_memrealtime(), 20);
#pragma unroll 1
    for (unsigned sp = 0; sp < SPIN_MEM; ++sp) {
      REREAD();
      g = ld16_sc1(rws, (lane >> 1) * (8 * 1024) + (lane & 1) * 16, WS_Y + s * 1024 + h * 32);
      gf = ld16_sc1(rws, ((lane >> 1) & 15) * (D * 8) + (lane & 1) * 16, WS_FFN + col0 * 8);
      if (__all(rok(g, tagA, tagB) && rok(gf, tagA, tagB))) break;
    }
    const float f0 = rbf(parity_sum(r0(gf)) * 0.5f), f1v = rbf(parity_sum(r1(gf)) * 0.5f);
    const float a0 = rbf(parity_sum(r0(g))), a1 = rbf(parity_sum(r1(g)));
    const unsigned o = pack2(rbf(lo2f(xw) + a0) + f0, rbf(hi2f(xw) + a1) + f1v);
    __builtin_amdgcn_raw_buffer_store_b32(o, mkrsrc(out, D * 2), lane < 2 ? (col0 + 2 * lane) * 2 : OOB, 0, SC1);
    __builtin_amdgcn_raw_buffer_store_b64(u32x2{(unsigned)e2, (unsigned)(e2 >> 32)}, rws, (bid == 0 && lane == 0) ? WS_EPOCH : OOB, 0, SC1);
  }
}

extern "C" void kernel_launch(void* const* d_in, const int* in_sizes, int n_in, void* d_out, int out_size, void* d_ws, size_t ws_size,
                              hipStream_t stream) {
  fused_decode_kernel<<<256, NTHR, 0, stream>>>((const u16*)d_in[0], (const u16*)d_in[3], d_ws, (const u16*)d_in[6],
                                                 (const u16*)d_in[7], (const u16*)d_in[1], (const u16*)d_in[2], (const u16*)d_in[4],
                                                 (const u16*)d_in[8], (u16*)d_out);
}
```

```cpp
#include <hip/hip_runtime.h>
#include <stdint.h>

typedef unsigned u32x4 __attribute__((ext_vector_type(4)));
typedef unsigned u32x2 __attribute__((ext_vector_type(2)));
typedef float f32x4 __attribute__((ext_vector_type(4)));
typedef float f32x2 __attribute__((ext_vector_type(2)));
typedef __bf16 bf16x2 __attribute__((ext_vector_type(2)));
typedef __bf16 bf16x8 __attribute__((ext_vector_type(8)));
typedef unsigned short u16;
#define LAS __attribute__((address_space(3)))
typedef __amdgpu_buffer_rsrc_t rsrc_t;

constexpr int T_F1 = 70;
constexpr int T_W2 = 262;
constexpr int T_K0 = 345;
constexpr int T_MAIN = 7055;
constexpr int PACE_G = 2;
constexpr int T_QPOLL = 315;
constexpr int T_HA = 1600, T_HB = 2800;

constexpr int D = 1024, H = 32, SK = 32768, DH = 128, F = 3584;
constexpr int NMAIN = 8, NAUX = 4, NTHR = (NMAIN + NAUX) * 64;
constexpr int NT = 2, SC1 = 16;
constexpr int OOB = 0x40000000;
constexpr unsigned WS_EPOCH = 0, WS_T0 = 1024, WS_ML = 4096, WS_Q = WS_ML + 256 * 128, WS_Y = WS_Q + 256 * 1024, WS_G1 = WS_Y + 256 * 1024,
                   WS_H = WS_G1 + 4 * F * 8, WS_FFN = WS_H + F * 4, WS_END = WS_FFN + 16 * D * 8;
constexpr int L_XN = 0, L_QP = 1024, L_QS = L_QP + 4 * 128, L_MRG = L_QS + 64, L_ML = L_MRG + 8 * 128, L_WG = L_ML + 16, L_FP = L_WG + 8,
              L_H = L_FP + 8 * 64, L_F2 = L_H + 4 * 64, L_OB = L_F2 + 4 * 64, L_CNT = L_OB + 64, L_SC = L_CNT + 16, L_WT = L_SC + 8 * 512,
              WT_STR = 68, L_END = L_WT + 128 * WT_STR;
constexpr int C_Q = 0, C_XN = 1, C_F2 = 2, C_WT = 3, C_ML = 4, C_WG = 5, C_END = 6, C_QR = 7, C_F1 = 8, C_OB = 9, C_NUM = 10, W_T0 = 12;
constexpr unsigned SPIN_MEM = 1u << 20, SPIN_LDS = 1u << 23;

__device__ __forceinline__ float lo2f(unsigned w) { return __builtin_bit_cast(float, w << 16); }
__device__ __forceinline__ float hi2f(unsigned w) { return __builtin_bit_cast(float, w & 0xffff0000u); }
__device__ __forceinline__ float rbf(float x) { return (float)(__bf16)x; }
__device__ __forceinline__ unsigned pack2(float a, float b) { bf16x2 v = {(__bf16)a, (__bf16)b}; return __builtin_bit_cast(unsigned, v); }
__device__ __forceinline__ float dot2(unsigned k, unsigned q, float acc) {
  return __builtin_amdgcn_fdot2_f32_bf16(__builtin_bit_cast(bf16x2, k), __builtin_bit_cast(bf16x2, q), acc, false);
}
template <int CTRL> __device__ __forceinline__ float dpp(float x) {
  return __builtin_bit_cast(float, __builtin_amdgcn_mov_dpp(__builtin_bit_cast(int, x), CTRL, 0xf, 0xf, true));
}
constexpr int XOR1 = 0xB1, XOR2 = 0x4E, XOR7 = 0x141, ROR8 = 0x128, ROR4 = 0x124, BCAST = 0x150;
__device__ __forceinline__ float fold32(float a, float b) {
  auto t = __builtin_amdgcn_permlane32_swap(__float_as_uint(a), __float_as_uint(b), false, false);
  return __uint_as_float(t[0]) + __uint_as_float(t[1]);
}
__device__ __forceinline__ float fold16(float a, float b) {
  auto t = __builtin_amdgcn_permlane16_swap(__float_as_uint(a), __float_as_uint(b), false, false);
  return __uint_as_float(t[0]) + __uint_as_float(t[1]);
}
__device__ __forceinline__ f32x2 fold_rows(const float (&v)[8]) {
  const float u0 = fold32(v[0], v[4]), u1 = fold32(v[1], v[5]), u2 = fold32(v[2], v[6]), u3 = fold32(v[3], v[7]);
  return f32x2{fold16(u0, u2), fold16(u1, u3)};
}
__device__ __forceinline__ float xrow_sum(float x) { x = fold16(x, x); return fold32(x, x); }
__device__ __forceinline__ float xrow_max(float x) {
  auto s = __builtin_amdgcn_permlane16_swap(__float_as_uint(x), __float_as_uint(x), false, false);
  x = fmaxf(__uint_as_float(s[0]), __uint_as_float(s[1]));
  auto t = __builtin_amdgcn_permlane32_swap(__float_as_uint(x), __float_as_uint(x), false, false);
  return fmaxf(__uint_as_float(t[0]), __uint_as_float(t[1]));
}
__device__ __forceinline__ float parity_sum(float x) {
  x += dpp<XOR2>(x); x += dpp<ROR4>(x); x += dpp<ROR8>(x); return xrow_sum(x);
}
__device__ __forceinline__ float wave_sum(float x) { x += dpp<XOR1>(x); return parity_sum(x); }
__device__ __forceinline__ float max8(float x) { x = fmaxf(x, dpp<XOR1>(x)); x = fmaxf(x, dpp<XOR2>(x)); return fmaxf(x, dpp<XOR7>(x)); }
__device__ __forceinline__ float sum8(float x) { x += dpp<XOR1>(x); x += dpp<XOR2>(x); return x + dpp<XOR7>(x); }
__device__ __forceinline__ float wave_max(float x) { x = max8(x); x = fmaxf(x, dpp<ROR8>(x)); return xrow_max(x); }
__device__ __forceinline__ float exp2_(float x) { return __builtin_amdgcn_exp2f(x); }
__device__ __forceinline__ rsrc_t mkrsrc(const void* p, unsigned bytes) { return __builtin_amdgcn_make_buffer_rsrc((void*)p, 0, (int)bytes, 0x00020000); }
__device__ __forceinline__ u32x4 ld16(rsrc_t r, int voff, int soff) { return __builtin_amdgcn_raw_buffer_load_b128(r, voff, soff, 0); }
__device__ __forceinline__ u32x4 ld16_nt(rsrc_t r, int voff, int soff) { return __builtin_amdgcn_raw_buffer_load_b128(r, voff, soff, NT); }
__device__ __forceinline__ u32x4 ld16_sc1(rsrc_t r, int voff, int soff) { return __builtin_amdgcn_raw_buffer_load_b128(r, voff, soff, SC1); }
__device__ __forceinline__ void st16_sc1(rsrc_t r, int off, u32x4 v) { __builtin_amdgcn_raw_buffer_store_b128(v, r, off, 0, SC1); }
#define SB() __builtin_amdgcn_sched_barrier(0)
#define REREAD() asm volatile("" ::: "memory")
__device__ __forceinline__ void pace_wait(unsigned t0, int due) {
  asm volatile("1:\n\ts_memrealtime s[96:97]\n\ts_waitcnt lgkmcnt(0)\n\ts_sub_u32 s96, s96, %0\n\ts_cmp_lt_i32 s96, %1\n\t"
               "s_cbranch_scc0 2f\n\ts_sleep 1\n\ts_branch 1b\n2:"
               :: "s"(t0), "s"(due) : "s96", "s97", "scc", "memory");
}
__device__ __forceinline__ void fma8(float (&acc)[8], float s, u32x4 w) {
#pragma unroll
  for (int t = 0; t < 4; ++t) { acc[2 * t] = fmaf(s, lo2f(w[t]), acc[2 * t]); acc[2 * t + 1] = fmaf(s, hi2f(w[t]), acc[2 * t + 1]); }
}
__device__ __forceinline__ u32x4 rec(float a, float b, unsigned ta, unsigned tb) { return u32x4{__float_as_uint(a), ta, __float_as_uint(b), tb}; }
__device__ __forceinline__ bool rok(u32x4 g, unsigned ta, unsigned tb) { return g[1] == ta && g[3] == tb; }
__device__ __forceinline__ float r0(u32x4 g) { return __uint_as_float(g[0]); }
__device__ __forceinline__ float r1(u32x4 g) { return __uint_as_float(g[2]); }

__device__ __forceinline__ void cnt_arrive(LAS float* lds, int which, int lane) {
  asm volatile("s_waitcnt lgkmcnt(0)" ::: "memory");
  if (lane == 0) __hip_atomic_fetch_add((LAS unsigned*)(lds + L_CNT + which), 1u, __ATOMIC_RELAXED, __HIP_MEMORY_SCOPE_WORKGROUP);
}
template <bool SLEEP = true>
__device__ __forceinline__ void cnt_wait(LAS float* lds, int which, unsigned n) {
#pragma unroll 1
  for (unsigned sp = 0; sp < SPIN_LDS; ++sp) {
    if (__builtin_amdgcn_readfirstlane(*(volatile LAS unsigned*)(lds + L_CNT + which)) >= n) break;
    if (SLEEP) __builtin_amdgcn_s_sleep(1);
  }
  REREAD();
}

__global__ void __launch_bounds__(NTHR, 3)
fused_decode_kernel(const u16* __restrict__ x, const u16* __restrict__ Kc, const u16* __restrict__ Vc, const u16* __restrict__ wq,
                    const u16* __restrict__ wo, const u16* __restrict__ wg, const u16* __restrict__ wu, const u16* __restrict__ wd,
                    u16* __restrict__ out, void* ws) {
  __shared__ __attribute__((aligned(16))) float lds_[L_END];
  LAS float* lds = (LAS float*)lds_;
  const int tid = threadIdx.x, lane = tid & 63, wave = __builtin_amdgcn_readfirstlane(tid >> 6);
  const int r = lane >> 4, c = lane & 15;
  const int bid = blockIdx.x, xl = bid & 7, j = bid >> 3;
  const int h = xl * 4 + (j >> 3), s = j & 7;
  const int f1 = xl * 28 + j, mat = f1 / 112, rb = (f1 / 56) & 1, cb = f1 % 56;
  asm volatile("" :: "s"(x), "s"(wq), "s"(ws), "s"(f1), "s"(wave));

  if (wave < NMAIN) {
    const rsrc_t rk = mkrsrc(Kc, (unsigned)H * SK * DH * 2), rv = mkrsrc(Vc, (unsigned)H * SK * DH * 2);
    u32x4 bA[16], bB[16];
    const unsigned kvbase = (unsigned)h * (8u << 20) + (unsigned)s * (1u << 20) + wave * 1024;
    const unsigned rot = 32u * ((h + (h >> 2) + (h >> 4)) & 3);
    const int kvoff = lane * 16;
    constexpr int STEP = (T_MAIN << 16) / 256 * PACE_G;
    int due = T_K0 * 65536 - STEP + (STEP / 256) * (wave * 32 + j);
#define PACE(T0, I) do { if ((I) % PACE_G == 0) { due += STEP; pace_wait(T0, due >> 16); } } while (0)
    __builtin_amdgcn_s_barrier();
    const unsigned t0 = (unsigned)__builtin_amdgcn_s_memrealtime();
    SB();
    {
      const rsrc_t rg = mkrsrc(mat ? wu : wg, D * F * 2);
      const int voff = j < 28 ? (lane >> 3) * (F * 2) + (lane & 7) * 16 : OOB, sb = (rb * 512 + wave * 64) * (F * 2) + cb * 128;
      constexpr int FSTEP = ((T_W2 - T_F1) << 16) / 8;
      int fdue = T_F1 * 65536 + (FSTEP / 256) * (wave * 32 + j);
#pragma unroll
      for (int i = 0; i < 8; ++i) { pace_wait(t0, fdue >> 16); fdue += FSTEP; bB[i] = ld16_nt(rg, voff, sb + i * 8 * (F * 2)); SB(); }
    }
#pragma unroll
    for (int i = 0; i < 16; ++i) { PACE(t0, i); bA[i] = ld16_nt(rk, kvoff, kvbase + (rot + i) * 8192); SB(); }
    {
      cnt_wait(lds, C_XN, 1);
      float ag[8] = {0.f, 0.f, 0.f, 0.f, 0.f, 0.f, 0.f, 0.f};
#pragma unroll
      for (int i = 0; i < 8; ++i) {
        const int k = rb * 512 + wave * 64 + 8 * i + (lane >> 3);
        fma8(ag, lds[L_XN + (k & 3) * 256 + (k >> 2)], bB[i]);
      }
#pragma unroll
      for (int e = 0; e < 8; ++e) ag[e] += dpp<ROR8>(ag[e]);
      const f32x2 z = fold_rows(ag);
      if (c < 8) *(LAS f32x2*)(lds + L_FP + wave * 64 + 8 * c + 2 * r) = z;
      cnt_arrive(lds, C_F1, lane);
      SB();
    }
#pragma unroll
    for (int i = 0; i < 16; ++i) { PACE(t0, i); bB[i] = ld16_nt(rk, kvoff, kvbase + (rot + 16 + i) * 8192); SB(); }
    cnt_wait(lds, C_QR, 1);
    const u32x4 qp = *(LAS u32x4*)(lds + L_QS + 4 * c);
    const unsigned t0m = __builtin_amdgcn_readfirstlane(*(LAS unsigned*)(lds + L_CNT + W_T0));
    constexpr float SCALE = 0.08838834764831845f, LOG2E = 1.4426950408889634f;
    LAS float* scp = lds + L_SC + wave * 512 + lane * 8;
    float mx = -INFINITY;
    auto kgroup = [&](u32x4 (&kb)[16], const bool more, const rsrc_t rn, const unsigned tb, const int g) __attribute__((always_inline)) {
      float sx[16];
#pragma unroll
      for (int i = 0; i < 16; ++i) {
        float d = 0.f;
#pragma unroll
        for (int t = 0; t < 4; ++t) d = dot2(kb[i][t], qp[t], d);
        asm volatile("" : "+v"(d));
        sx[i] = d;
        SB();
        if (more) { PACE(t0m, i); kb[i] = ld16_nt(rn, kvoff, tb + i * 8192); SB(); }
      }
#define TR_STEP(HALF, CTRL, BIT) _Pragma("unroll") for (int hh = 0; hh < (HALF); ++hh) { \
        const float lo_ = sx[hh], hi_ = sx[hh + (HALF)]; const float t1_ = lo_ + dpp<CTRL>(lo_), t2_ = hi_ + dpp<CTRL>(hi_); \
        sx[hh] = (c & (BIT)) ? t2_ : t1_; }
      TR_STEP(8, ROR8, 8) TR_STEP(4, XOR7, 4) TR_STEP(2, XOR2, 2) TR_STEP(1, XOR1, 1)
#undef TR_STEP
      const float sc = rbf(rbf(sx[0]) * SCALE) * LOG2E;
      mx = fmaxf(mx, sc);
      scp[g] = sc;
      SB();
    };
    kgroup(bA, false, rk, 0, 0);
#pragma unroll 1
    for (int g = 1; g < 8; ++g)
      kgroup(bB, true, mkrsrc(g == 7 ? Vc : Kc, (unsigned)H * SK * DH * 2), kvbase + (((g + 1) * 16 + rot) & 127u) * 8192, g);
    {
      const float m = wave_max(mx);
      f32x4 p0 = *(LAS f32x4*)scp, p1 = *(LAS f32x4*)(scp + 4);
      float l = 0.f;
#pragma unroll
      for (int e = 0; e < 4; ++e) { p0[e] = exp2_(p0[e] - m); p1[e] = exp2_(p1[e] - m); l += p0[e] + p1[e]; }
      *(LAS f32x4*)scp = p0; *(LAS f32x4*)(scp + 4) = p1;
      l = wave_sum(l);
      if (lane == 0) *(LAS f32x2*)(lds + L_ML + 2 * wave) = f32x2{m, l};
      cnt_arrive(lds, C_ML, lane);
    }
    float acc[8] = {0.f, 0.f, 0.f, 0.f, 0.f, 0.f, 0.f, 0.f};
    auto vgroup = [&](const bool more, const unsigned tb, const float p) __attribute__((always_inline)) {
#define PV_STEP(I) { fma8(acc, dpp<BCAST + (I)>(p), bB[I]); \
        asm volatile("" : "+v"(acc[0]), "+v"(acc[1]), "+v"(acc[2]), "+v"(acc[3]), "+v"(acc[4]), "+v"(acc[5]), "+v"(acc[6]), "+v"(acc[7])); SB(); \
        if (more) { PACE(t0m, I); bB[I] = ld16_nt(rv, kvoff, tb + (I) * 8192); SB(); } \
        else if ((I) == 11) {   \
          pace_wait(t0m, T_K0 + T_MAIN); \
          _Pragma("unroll") for (int u = 0; u < 4; ++u) { \
            bB[u] = ld16_nt(mkrsrc(wo, H * DH * D * 2), r * 2048 + c * 16, (h * 128 + wave * 16 + 4 * u) * 2048 + s * 256); SB(); } } }
      PV_STEP(0) PV_STEP(1) PV_STEP(2) PV_STEP(3) PV_STEP(4) PV_STEP(5) PV_STEP(6) PV_STEP(7)
      PV_STEP(8) PV_STEP(9) PV_STEP(10) PV_STEP(11) PV_STEP(12) PV_STEP(13) PV_STEP(14) PV_STEP(15)
#undef PV_STEP
    };
#pragma unroll 1
    for (int g = 0; g < 7; ++g) vgroup(true, kvbase + (((g + 1) * 16 + rot) & 127u) * 8192, scp[g]);
    __builtin_amdgcn_s_setprio(3);
    cnt_wait(lds, C_WG, 1);
    {
      const float wgt = lds[L_WG + wave];
#pragma unroll
      for (int e = 0; e < 8; ++e) acc[e] *= wgt;
      vgroup(false, 0, scp[7] * wgt);
    }
    *(LAS f32x2*)(lds + L_MRG + wave * 128 + 8 * c + 2 * r) = fold_rows(acc);
    cnt_arrive(lds, C_END, lane);
#pragma unroll
    for (int u = 0; u < 4; ++u) *(LAS u32x4*)(lds + L_WT + (wave * 16 + 4 * u + r) * WT_STR + 4 * c) = bB[u];
    cnt_arrive(lds, C_WT, lane);
    return;
#undef PACE
  }

  const int a = wave - NMAIN;
  const rsrc_t rx = mkrsrc(x, D * 2), rws = mkrsrc(ws, WS_END);
  unsigned t0, tagA, tagB, tagC, tagD;
  unsigned long long e2;
  {
    u32x4 xa = ld16(rx, lane * 32, 0), xb = ld16(rx, lane * 32 + 16, 0);
    const u32x2 ep = __builtin_amdgcn_raw_buffer_load_b64(rws, 0, WS_EPOCH, SC1);
    const rsrc_t rwq = mkrsrc(wq, D * H * DH * 2);
    u32x4 w[8];
    const int voff = r * 8192 + c * 16, sb = (s * 128 + a * 32) * 8192 + h * 256;
#pragma unroll
    for (int i = 0; i < 8; ++i) { w[i] = ld16_nt(rwq, voff, sb + i * 32768); SB(); }
    if (a == 0 && lane < C_NUM) *(LAS unsigned*)(lds + L_CNT + lane) = 0u;
    __syncthreads();
    t0 = (unsigned)__builtin_amdgcn_s_memrealtime();
    asm volatile("" : "+v"(xa), "+v"(xb));
    float v[16];
#pragma unroll
    for (int i = 0; i < 4; ++i) { v[2 * i] = lo2f(xa[i]); v[2 * i + 1] = hi2f(xa[i]); v[8 + 2 * i] = lo2f(xb[i]); v[9 + 2 * i] = hi2f(xb[i]); }
    float sm = 0.f;
#pragma unroll
    for (int i = 0; i < 16; ++i) sm += v[i];
    const float mean = wave_sum(sm) * (1.f / D);
    float ss = 0.f;
#pragma unroll
    for (int i = 0; i < 16; ++i) { const float d = v[i] - mean; ss = fmaf(d, d, ss); }
    const float var = wave_sum(ss) * (1.f / D);
    const float mb = rbf(mean), den = rbf(sqrtf(rbf(rbf(var) + 1e-5f)));
    const float rinv = 1.0f / den;
#pragma unroll
    for (int i = 0; i < 16; ++i) {
      const float nu = rbf(v[i] - mb), q0 = nu * rinv;
      v[i] = rbf(fmaf(fmaf(-q0, den, nu), rinv, q0));
    }
#pragma unroll
    for (int q = 0; q < 4; ++q) *(LAS f32x4*)(lds + L_XN + q * 256 + 4 * lane) = f32x4{v[q], v[4 + q], v[8 + q], v[12 + q]};
    cnt_arrive(lds, C_XN, lane);
    const unsigned long long e1 = (((unsigned long long)__builtin_amdgcn_readfirstlane(ep[1]) << 32) | (unsigned)__builtin_amdgcn_readfirstlane(ep[0])) + 1ull;
    e2 = e1 + 1ull;
    tagA = (unsigned)e1; tagB = (unsigned)(e1 >> 32) + (unsigned)e1 * 0x9E3779B1u;
    tagC = (unsigned)e2; tagD = (unsigned)(e2 >> 32) + (unsigned)e2 * 0x9E3779B1u;
    float acc[8] = {0.f, 0.f, 0.f, 0.f, 0.f, 0.f, 0.f, 0.f};
#pragma unroll
    for (int q = 0; q < 2; ++q) {
      const f32x4 xv = *(LAS f32x4*)(lds + L_XN + r * 256 + s * 32 + a * 8 + 4 * q);
#pragma unroll
      for (int e = 0; e < 4; ++e) fma8(acc, xv[e], w[4 * q + e]);
    }
    *(LAS f32x2*)(lds + L_QP + a * 128 + 8 * c + 2 * r) = fold_rows(acc);
  }
  cnt_arrive(lds, C_Q, lane);
  if (a == 0) {
    cnt_wait<false>(lds, C_Q, NAUX);
    f32x2 v = *(LAS f32x2*)(lds + L_QP + 2 * lane);
#pragma unroll
    for (int q = 1; q < 4; ++q) v += *(LAS f32x2*)(lds + L_QP + q * 128 + 2 * lane);
    st16_sc1(rws, WS_Q + (h * 8 + s) * 1024 + lane * 16, rec(v[0], v[1], tagA, tagB));
    st16_sc1(rws, (bid == 0 && lane == 0) ? WS_T0 : OOB, u32x4{t0, tagA, t0, tagB});
  }
  if (a == 1) {
    pace_wait(t0, T_QPOLL);
    float q0 = 0.f, q1 = 0.f;
    unsigned tg = t0;
#pragma unroll 1
    for (unsigned sp = 0; sp < SPIN_MEM; ++sp) {
      REREAD();
      u32x4 g[8];
#pragma unroll
      for (int i = 0; i < 8; ++i) g[i] = ld16_sc1(rws, lane * 16, WS_Q + (h * 8 + i) * 1024);
      const u32x4 gt = ld16_sc1(rws, 0, WS_T0);
      bool ok = true;
      tg = rok(gt, tagA, tagB) ? gt[0] : t0;
      q0 = 0.f; q1 = 0.f;
#pragma unroll
      for (int i = 0; i < 8; ++i) { ok = ok && rok(g[i], tagA, tagB); q0 += r0(g[i]); q1 += r1(g[i]); }
      if (__all(ok)) break;
      __builtin_amdgcn_s_sleep(2);
    }
    *(LAS unsigned*)(lds + L_QS + lane) = pack2(q0, q1);
    if (lane == 0) *(LAS unsigned*)(lds + L_CNT + W_T0) = t0 - (unsigned)min(max((int)(t0 - tg), 0), 100);
    cnt_arrive(lds, C_QR, lane);
  }
  const int ks = xl * 2 + (j >> 4), cg = j & 15;
  u32x4 dw[7];
  {
    const rsrc_t rd = mkrsrc(wd, D * F * 2);
    const int voff = (lane >> 3) * 2048 + (lane & 7) * 16, sb = (ks * 224 + a * 56) * 2048 + cg * 128;
    constexpr int WSTEP = ((T_K0 - T_W2) << 16) / 7;
    int wdue = T_W2 * 65536 + (WSTEP / 128) * (a * 32 + j);
#pragma unroll
    for (int i = 0; i < 7; ++i) { pace_wait(t0, wdue >> 16); wdue += WSTEP; dw[i] = ld16_nt(rd, voff, sb + i * 16384); SB(); }
  }
  if (a == 0 && j < 28) {
    cnt_wait(lds, C_F1, NMAIN);
    f32x2 v = *(LAS f32x2*)(lds + L_FP + 2 * (lane & 31));
#pragma unroll
    for (int q = 1; q < 8; ++q) v += *(LAS f32x2*)(lds + L_FP + q * 64 + 2 * (lane & 31));
    if (f1 >= 56) {
      st16_sc1(rws, lane < 32 ? WS_G1 + ((mat * 2 + rb) * F + cb * 64) * 8 + lane * 16 : OOB, rec(v[0], v[1], tagA, tagB));
    } else {
      pace_wait(t0, T_K0 + T_HA);
      u32x4 g[3];
#pragma unroll 1
      for (unsigned sp = 0; sp < SPIN_MEM; ++sp) {
        REREAD();
#pragma unroll
        for (int k = 0; k < 3; ++k) g[k] = ld16_sc1(rws, (lane & 31) * 16, WS_G1 + ((k + 1) * F + cb * 64) * 8);
        if (__all(rok(g[0], tagA, tagB) && rok(g[1], tagA, tagB) && rok(g[2], tagA, tagB))) break;
        __builtin_amdgcn_s_sleep(32);
      }
      float hh[2];
#pragma unroll
      for (int e = 0; e < 2; ++e) {
        const float gs = rbf(v[e] + __uint_as_float(g[0][2 * e]));
        const float us = rbf(__uint_as_float(g[1][2 * e]) + __uint_as_float(g[2][2 * e]));
        const float ge = 0.5f * gs * (1.f + tanhf(0.7978845608028654f * (gs + 0.044715f * gs * gs * gs)));
        hh[e] = rbf(ge) * us;
      }
      const float hp = __uint_as_float(pack2(hh[0], hh[1]));
      st16_sc1(rws, (lane < 32 && !(lane & 1)) ? WS_H + cb * 256 + lane * 8 : OOB, rec(hp, dpp<XOR1>(hp), tagA, tagB));
    }
  }
  {
    pace_wait(t0, T_K0 + T_HB);
    u32x4 g;
#pragma unroll 1
    for (unsigned sp = 0; sp < SPIN_MEM; ++sp) {
      REREAD();
      g = ld16_sc1(rws, (lane < 14 ? lane : 13) * 16, WS_H + (ks * 56 + a * 14) * 16);
      if (__all(rok(g, tagA, tagB))) break;
      __builtin_amdgcn_s_sleep(32);
    }
    if (lane < 14) *(LAS f32x4*)(lds + L_H + a * 64 + 4 * lane) = f32x4{lo2f(g[0]), hi2f(g[0]), lo2f(g[2]), hi2f(g[2])};
    float y[8] = {0.f, 0.f, 0.f, 0.f, 0.f, 0.f, 0.f, 0.f};
#pragma unroll
    for (int i = 0; i < 7; ++i) fma8(y, lds[L_H + a * 64 + 8 * i + (lane >> 3)], dw[i]);
#pragma unroll
    for (int e = 0; e < 8; ++e) y[e] += dpp<ROR8>(y[e]);
    const f32x2 z = fold_rows(y);
    if (c < 8) *(LAS f32x2*)(lds + L_F2 + a * 64 + 8 * c + 2 * r) = z;
    cnt_arrive(lds, C_F2, lane);
  }
  const int col0 = s * 128 + 4 * h;
  unsigned xw = 0;
  if (a == 0) xw = __builtin_amdgcn_raw_buffer_load_b32(rx, (col0 + 2 * (lane & 1)) * 2, 0, 0);
  if (a == 3) {
    const unsigned po = __builtin_amdgcn_raw_buffer_load_b32(mkrsrc(out, D * 2), (col0 + 2 * (lane & 1)) * 2, 0, SC1);
    asm volatile("" :: "v"(po));
  }
  if (a == 1) {
    cnt_wait(lds, C_ML, NMAIN);
    const f32x2 ml = *(LAS f32x2*)(lds + L_ML + 2 * (lane & 7));
    const float Mw = max8(ml[0]), Lw = sum8(exp2_(ml[0] - Mw) * ml[1]);
    st16_sc1(rws, lane == 0 ? WS_ML + (h * 8 + s) * 128 : OOB, rec(Mw, Lw, tagA, tagB));
    pace_wait(t0, T_K0 + T_MAIN / 2 + 1500);
    u32x4 g;
#pragma unroll 1
    for (unsigned sp = 0; sp < SPIN_MEM; ++sp) {
      REREAD();
      g = ld16_sc1(rws, (lane & 7) * 128, WS_ML + h * 8 * 128);
      if (__all(rok(g, tagA, tagB))) break;
      __builtin_amdgcn_s_sleep(32);
    }
    const float M = max8(r0(g)), L = sum8(exp2_(r0(g) - M) * r1(g));
    if (lane < 8) lds[L_WG + lane] = exp2_(ml[0] - M) / L;
    cnt_arrive(lds, C_WG, lane);
  }
  pace_wait(t0, T_K0 + T_MAIN - 100);
  __builtin_amdgcn_s_setprio(3);
  if (a == 2) {
    cnt_wait(lds, C_F2, NAUX);
    f32x2 v = *(LAS f32x2*)(lds + L_F2 + 2 * (lane & 31));
#pragma unroll
    for (int q = 1; q < 4; ++q) v += *(LAS f32x2*)(lds + L_F2 + q * 64 + 2 * (lane & 31));
    pace_wait(t0, T_K0 + T_MAIN + 60);
    st16_sc1(rws, lane < 32 ? WS_FFN + (ks * D + cg * 64) * 8 + lane * 16 : OOB, rec(v[0], v[1], tagA, tagB));
  }
  if (a == 0) {
    cnt_wait<false>(lds, C_END, NMAIN);
    f32x2 v = *(LAS f32x2*)(lds + L_MRG + 2 * lane);
#pragma unroll
    for (int w = 1; w < 8; ++w) v += *(LAS f32x2*)(lds + L_MRG + w * 128 + 2 * lane);
    st16_sc1(rws, WS_Q + (h * 8 + s) * 1024 + lane * 16, rec(v[0], v[1], tagC, tagD));
  } else {
    cnt_wait(lds, C_END, NMAIN);
  }
  u32x4 bfr[2][4];
  cnt_wait<false>(lds, C_WT, NMAIN);
#pragma unroll
  for (int q = 0; q < 4; ++q)
#pragma unroll
    for (int d = 0; d < 4; ++d) {
      const unsigned w0 = *(LAS unsigned*)(lds + L_WT + (32 * q + 8 * r + 2 * d) * WT_STR + 16 * a + c);
      const unsigned w1 = *(LAS unsigned*)(lds + L_WT + (32 * q + 8 * r + 2 * d + 1) * WT_STR + 16 * a + c);
      bfr[0][q][d] = __builtin_amdgcn_perm(w1, w0, 0x05040100u);
      bfr[1][q][d] = __builtin_amdgcn_perm(w1, w0, 0x07060302u);
    }
  {
    u32x4 g0, g1;
    const int vo = r * 1024 + (16 * a + c) * 16;
#pragma unroll 1
    for (unsigned sp = 0; sp < SPIN_MEM; ++sp) {
      REREAD();
      g0 = ld16_sc1(rws, vo, WS_Q + h * 8 * 1024);
      g1 = ld16_sc1(rws, vo, WS_Q + (h * 8 + 4) * 1024);
      if (__all(rok(g0, tagC, tagD) && rok(g1, tagC, tagD))) break;
    }
    float o = fold32(r0(g0) + r0(g1), r1(g0) + r1(g1));
    o = fold16(o, o);
    if ((r & 1) == 0) *((LAS u16*)(lds + L_OB) + 32 * a + 2 * c + (r >> 1)) = __builtin_bit_cast(u16, (__bf16)o);
  }
  cnt_arrive(lds, C_OB, lane);
  cnt_wait<false>(lds, C_OB, NAUX);
  {
    f32x4 y0 = {0.f, 0.f, 0.f, 0.f}, y1 = y0;
#pragma unroll
    for (int q = 0; q < 4; ++q) {
      const u32x4 af = *(LAS u32x4*)(lds + L_OB + 16 * q + 4 * r);
      y0 = __builtin_amdgcn_mfma_f32_16x16x32_bf16(__builtin_bit_cast(bf16x8, af), __builtin_bit_cast(bf16x8, bfr[0][q]), y0, 0, 0, 0);
      y1 = __builtin_amdgcn_mfma_f32_16x16x32_bf16(__builtin_bit_cast(bf16x8, af), __builtin_bit_cast(bf16x8, bfr[1][q]), y1, 0, 0, 0);
    }
    st16_sc1(rws, lane < 16 ? WS_Y + (h * 8 + s) * 1024 + (16 * a + lane) * 16 : OOB, rec(y0[0], y1[0], tagA, tagB));
  }
  if (a == 0) {
    u32x4 g, gf;
    pace_wait((unsigned)__builtin_amdgcn_s_memrealtime(), 20);
#pragma unroll 1
    for (unsigned sp = 0; sp < SPIN_MEM; ++sp) {
      REREAD();
      g = ld16_sc1(rws, (lane >> 1) * (8 * 1024) + (lane & 1) * 16, WS_Y + s * 1024 + h * 32);
      gf = ld16_sc1(rws, ((lane >> 1) & 15) * (D * 8) + (lane & 1) * 16, WS_FFN + col0 * 8);
      if (__all(rok(g, tagA, tagB) && rok(gf, tagA, tagB))) break;
    }
    const float f0 = rbf(parity_sum(r0(gf)) * 0.5f), f1v = rbf(parity_sum(r1(gf)) * 0.5f);
    const float a0 = rbf(parity_sum(r0(g))), a1 = rbf(parity_sum(r1(g)));
    const unsigned o = pack2(rbf(lo2f(xw) + a0) + f0, rbf(hi2f(xw) + a1) + f1v);
    __builtin_amdgcn_raw_buffer_store_b32(o, mkrsrc(out, D * 2), lane < 2 ? (col0 + 2 * lane) * 2 : OOB, 0, SC1);
    __builtin_amdgcn_raw_buffer_store_b64(u32x2{(unsigned)e2, (unsigned)(e2 >> 32)}, rws, (bid == 0 && lane == 0) ? WS_EPOCH : OOB, 0, SC1);
  }
}

extern "C" void kernel_launch(void* const* d_in, const int* in_sizes, int n_in, void* d_out, int out_size, void* d_ws, size_t ws_size,
                              hipStream_t stream) {
  fused_decode_kernel<<<256, NTHR, 0, stream>>>((const u16*)d_in[0], (const u16*)d_in[1], (const u16*)d_in[2], (const u16*)d_in[3],
                                                 (const u16*)d_in[4], (const u16*)d_in[6], (const u16*)d_in[7], (const u16*)d_in[8],
                                                 (u16*)d_out, d_ws);
}
```

```cpp
#include <hip/hip_runtime.h>
#include <stdint.h>

typedef unsigned u32x4 __attribute__((ext_vector_type(4)));
typedef unsigned u32x2 __attribute__((ext_vector_type(2)));
typedef float f32x4 __attribute__((ext_vector_type(4)));
typedef float f32x2 __attribute__((ext_vector_type(2)));
typedef __bf16 bf16x2 __attribute__((ext_vector_type(2)));
typedef __bf16 bf16x8 __attribute__((ext_vector_type(8)));
typedef unsigned short u16;
#define LAS __attribute__((address_space(3)))
typedef __amdgpu_buffer_rsrc_t rsrc_t;

constexpr int T_F1 = 70;
constexpr int T_W2 = 262;
constexpr int T_K0 = 345;
constexpr int T_MAIN = 7015;
constexpr int PACE_G = 2;
constexpr int T_QPOLL = 315;
constexpr int T_HA = 1600, T_HB = 2800;

constexpr int D = 1024, H = 32, SK = 32768, DH = 128, F = 3584;
constexpr int NMAIN = 8, NAUX = 4, NTHR = (NMAIN + NAUX) * 64;
constexpr int NT = 2, SC1 = 16;
constexpr int OOB = 0x40000000;
constexpr unsigned WS_EPOCH = 0, WS_T0 = 1024, WS_ML = 4096, WS_Q = WS_ML + 256 * 128, WS_Y = WS_Q + 256 * 1024, WS_G1 = WS_Y + 256 * 1024,
                   WS_H = WS_G1 + 4 * F * 8, WS_FFN = WS_H + F * 4, WS_END = WS_FFN + 16 * D * 8;
constexpr int L_XN = 0, L_QP = 1024, L_QS = L_QP + 4 * 128, L_MRG = L_QS + 64, L_ML = L_MRG + 8 * 128, L_WG = L_ML + 16, L_FP = L_WG + 8,
              L_H = L_FP + 8 * 64, L_F2 = L_H + 4 * 64, L_OB = L_F2 + 4 * 64, L_CNT = L_OB + 64, L_SC = L_CNT + 16, L_WT = L_SC + 8 * 512,
              WT_STR = 68, L_END = L_WT + 128 * WT_STR;
constexpr int C_Q = 0, C_XN = 1, C_F2 = 2, C_WT = 3, C_ML = 4, C_WG = 5, C_END = 6, C_QR = 7, C_F1 = 8, C_OB = 9, C_NUM = 10, W_T0 = 12;
constexpr unsigned SPIN_MEM = 1u << 20, SPIN_LDS = 1u << 23;

__device__ __forceinline__ float lo2f(unsigned w) { return __builtin_bit_cast(float, w << 16); }
__device__ __forceinline__ float hi2f(unsigned w) { return __builtin_bit_cast(float, w & 0xffff0000u); }
__device__ __forceinline__ float rbf(float x) { return (float)(__bf16)x; }
__device__ __forceinline__ unsigned pack2(float a, float b) { bf16x2 v = {(__bf16)a, (__bf16)b}; return __builtin_bit_cast(unsigned, v); }
__device__ __forceinline__ float dot2(unsigned k, unsigned q, float acc) {
  return __builtin_amdgcn_fdot2_f32_bf16(__builtin_bit_cast(bf16x2, k), __builtin_bit_cast(bf16x2, q), acc, false);
}
template <int CTRL> __device__ __forceinline__ float dpp(float x) {
  return __builtin_bit_cast(float, __builtin_amdgcn_mov_dpp(__builtin_bit_cast(int, x), CTRL, 0xf, 0xf, true));
}
constexpr int XOR1 = 0xB1, XOR2 = 0x4E, XOR7 = 0x141, ROR8 = 0x128, ROR4 = 0x124, BCAST = 0x150;
__device__ __forceinline__ float fold32(float a, float b) {
  auto t = __builtin_amdgcn_permlane32_swap(__float_as_uint(a), __float_as_uint(b), false, false);
  return __uint_as_float(t[0]) + __uint_as_float(t[1]);
}
__device__ __forceinline__ float fold16(float a, float b) {
  auto t = __builtin_amdgcn_permlane16_swap(__float_as_uint(a), __float_as_uint(b), false, false);
  return __uint_as_float(t[0]) + __uint_as_float(t[1]);
}
__device__ __forceinline__ f32x2 fold_rows(const float (&v)[8]) {
  const float u0 = fold32(v[0], v[4]), u1 = fold32(v[1], v[5]), u2 = fold32(v[2], v[6]), u3 = fold32(v[3], v[7]);
  return f32x2{fold16(u0, u2), fold16(u1, u3)};
}
__device__ __forceinline__ float xrow_sum(float x) { x = fold16(x, x); return fold32(x, x); }
__device__ __forceinline__ float xrow_max(float x) {
  auto s = __builtin_amdgcn_permlane16_swap(__float_as_uint(x), __float_as_uint(x), false, false);
  x = fmaxf(__uint_as_float(s[0]), __uint_as_float(s[1]));
  auto t = __builtin_amdgcn_permlane32_swap(__float_as_uint(x), __float_as_uint(x), false, false);
  return fmaxf(__uint_as_float(t[0]), __uint_as_float(t[1]));
}
__device__ __forceinline__ float parity_sum(float x) {
  x += dpp<XOR2>(x); x += dpp<ROR4>(x); x += dpp<ROR8>(x); return xrow_sum(x);
}
__device__ __forceinline__ float wave_sum(float x) { x += dpp<XOR1>(x); return parity_sum(x); }
__device__ __forceinline__ float max8(float x) { x = fmaxf(x, dpp<XOR1>(x)); x = fmaxf(x, dpp<XOR2>(x)); return fmaxf(x, dpp<XOR7>(x)); }
__device__ __forceinline__ float sum8(float x) { x += dpp<XOR1>(x); x += dpp<XOR2>(x); return x + dpp<XOR7>(x); }
__device__ __forceinline__ float wave_max(float x) { x = max8(x); x = fmaxf(x, dpp<ROR8>(x)); return xrow_max(x); }
__device__ __forceinline__ float exp2_(float x) { return __builtin_amdgcn_exp2f(x); }
__device__ __forceinline__ rsrc_t mkrsrc(const void* p, unsigned bytes) { return __builtin_amdgcn_make_buffer_rsrc((void*)p, 0, (int)bytes, 0x00020000); }
__device__ __forceinline__ u32x4 ld16(rsrc_t r, int voff, int soff) { return __builtin_amdgcn_raw_buffer_load_b128(r, voff, soff, 0); }
__device__ __forceinline__ u32x4 ld16_nt(rsrc_t r, int voff, int soff) { return __builtin_amdgcn_raw_buffer_load_b128(r, voff, soff, NT); }
__device__ __forceinline__ u32x4 ld16_sc1(rsrc_t r, int voff, int soff) { return __builtin_amdgcn_raw_buffer_load_b128(r, voff, soff, SC1); }
__device__ __forceinline__ void st16_sc1(rsrc_t r, int off, u32x4 v) { __builtin_amdgcn_raw_buffer_store_b128(v, r, off, 0, SC1); }
#define SB() __builtin_amdgcn_sched_barrier(0)
#define REREAD() asm volatile("" ::: "memory")
__device__ __forceinline__ void pace_wait(unsigned t0, int due) {
  asm volatile("1:\n\ts_memrealtime s[96:97]\n\ts_waitcnt lgkmcnt(0)\n\ts_sub_u32 s96, s96, %0\n\ts_cmp_lt_i32 s96, %1\n\t"
               "s_cbranch_scc0 2f\n\ts_sleep 1\n\ts_branch 1b\n2:"
               :: "s"(t0), "s"(due) : "s96", "s97", "scc", "memory");
}
__device__ __forceinline__ void fma8(float (&acc)[8], float s, u32x4 w) {
#pragma unroll
  for (int t = 0; t < 4; ++t) { acc[2 * t] = fmaf(s, lo2f(w[t]), acc[2 * t]); acc[2 * t + 1] = fmaf(s, hi2f(w[t]), acc[2 * t + 1]); }
}
__device__ __forceinline__ u32x4 rec(float a, float b, unsigned ta, unsigned tb) { return u32x4{__float_as_uint(a), ta, __float_as_uint(b), tb}; }
__device__ __forceinline__ bool rok(u32x4 g, unsigned ta, unsigned tb) { return g[1] == ta && g[3] == tb; }
__device__ __forceinline__ float r0(u32x4 g) { return __uint_as_float(g[0]); }
__device__ __forceinline__ float r1(u32x4 g) { return __uint_as_float(g[2]); }

__device__ __forceinline__ void cnt_arrive(LAS float* lds, int which, int lane) {
  asm volatile("s_waitcnt lgkmcnt(0)" ::: "memory");
  if (lane == 0) __hip_atomic_fetch_add((LAS unsigned*)(lds + L_CNT + which), 1u, __ATOMIC_RELAXED, __HIP_MEMORY_SCOPE_WORKGROUP);
}
template <bool SLEEP = true>
__device__ __forceinline__ void cnt_wait(LAS float* lds, int which, unsigned n) {
#pragma unroll 1
  for (unsigned sp = 0; sp < SPIN_LDS; ++sp) {
    if (__builtin_amdgcn_readfirstlane(*(volatile LAS unsigned*)(lds + L_CNT + which)) >= n) break;
    if (SLEEP) __builtin_amdgcn_s_sleep(1);
  }
  REREAD();
}

__global__ void __launch_bounds__(NTHR, 3)
fused_decode_kernel(const u16* __restrict__ x, const u16* __restrict__ wq, void* ws, const u16* __restrict__ wg,
                    const u16* __restrict__ wu, const u16* __restrict__ Kc, const u16* __restrict__ Vc, const u16* __restrict__ wo,
                    const u16* __restrict__ wd, u16* __restrict__ out) {
  __shared__ __attribute__((aligned(16))) float lds_[L_END];
  LAS float* lds = (LAS float*)lds_;
  const int tid = threadIdx.x, lane = tid & 63, wave = __builtin_amdgcn_readfirstlane(tid >> 6);
  const int r = lane >> 4, c = lane & 15;
  const int bid = blockIdx.x, xl = bid & 7, j = bid >> 3;
  const int h = xl * 4 + (j >> 3), s = j & 7;
  const int f1 = xl * 28 + j, mat = f1 / 112, rb = (f1 / 56) & 1, cb = f1 % 56;
  asm volatile("" :: "s"(x), "s"(wq), "s"(ws), "s"(f1), "s"(wave));

  if (wave < NMAIN) {
    const rsrc_t rk = mkrsrc(Kc, (unsigned)H * SK * DH * 2), rv = mkrsrc(Vc, (unsigned)H * SK * DH * 2);
    u32x4 bA[16], bB[16];
    const unsigned kvbase = (unsigned)h * (8u << 20) + (unsigned)s * (1u << 20) + wave * 1024;
    const unsigned rot = 32u * ((h + (h >> 2) + (h >> 4)) & 3);
    const int kvoff = lane * 16;
    constexpr int STEP = (T_MAIN << 16) / 256 * PACE_G;
    int due = T_K0 * 65536 - STEP + (STEP / 256) * (wave * 32 + j);
#define PACE(T0, I) do { if ((I) % PACE_G == 0) { due += STEP; pace_wait(T0, due >> 16); } } while (0)
    __builtin_amdgcn_s_barrier();
    const unsigned t0 = (unsigned)__builtin_amdgcn_s_memrealtime();
    SB();
    {
      const rsrc_t rg = mkrsrc(mat ? wu : wg, D * F * 2);
      const int voff = j < 28 ? (lane >> 3) * (F * 2) + (lane & 7) * 16 : OOB, sb = (rb * 512 + wave * 64) * (F * 2) + cb * 128;
      constexpr int FSTEP = ((T_W2 - T_F1) << 16) / 8;
      int fdue = T_F1 * 65536 + (FSTEP / 256) * (wave * 32 + j);
#pragma unroll
      for (int i = 0; i < 8; ++i) { pace_wait(t0, fdue >> 16); fdue += FSTEP; bB[i] = ld16_nt(rg, voff, sb + i * 8 * (F * 2)); SB(); }
    }
#pragma unroll
    for (int i = 0; i < 16; ++i) { PACE(t0, i); bA[i] = ld16_nt(rk, kvoff, kvbase + (rot + i) * 8192); SB(); }
    {
      cnt_wait(lds, C_XN, 1);
      float ag[8] = {0.f, 0.f, 0.f, 0.f, 0.f, 0.f, 0.f, 0.f};
#pragma unroll
      for (int i = 0; i < 8; ++i) {
        const int k = rb * 512 + wave * 64 + 8 * i + (lane >> 3);
        fma8(ag, lds[L_XN + (k & 3) * 256 + (k >> 2)], bB[i]);
      }
#pragma unroll
      for (int e = 0; e < 8; ++e) ag[e] += dpp<ROR8>(ag[e]);
      const f32x2 z = fold_rows(ag);
      if (c < 8) *(LAS f32x2*)(lds + L_FP + wave * 64 + 8 * c + 2 * r) = z;
      cnt_arrive(lds, C_F1, lane);
      SB();
    }
#pragma unroll
    for (int i = 0; i < 16; ++i) { PACE(t0, i); bB[i] = ld16_nt(rk, kvoff, kvbase + (rot + 16 + i) * 8192); SB(); }
    cnt_wait(lds, C_QR, 1);
    const u32x4 qp = *(LAS u32x4*)(lds + L_QS + 4 * c);
    const unsigned t0m = __builtin_amdgcn_readfirstlane(*(LAS unsigned*)(lds + L_CNT + W_T0));
    constexpr float SCALE = 0.08838834764831845f, LOG2E = 1.4426950408889634f;
    LAS float* scp = lds + L_SC + wave * 512 + lane * 8;
    float mx = -INFINITY;
    auto kgroup = [&](u32x4 (&kb)[16], const bool more, const rsrc_t rn, const unsigned tb, const int g) __attribute__((always_inline)) {
      float sx[16];
#pragma unroll
      for (int i = 0; i < 16; ++i) {
        float d = 0.f;
#pragma unroll
        for (int t = 0; t < 4; ++t) d = dot2(kb[i][t], qp[t], d);
        asm volatile("" : "+v"(d));
        sx[i] = d;
        SB();
        if (more) { PACE(t0m, i); kb[i] = ld16_nt(rn, kvoff, tb + i * 8192); SB(); }
      }
#define TR_STEP(HALF, CTRL, BIT) _Pragma("unroll") for (int hh = 0; hh < (HALF); ++hh) { \
        const float lo_ = sx[hh], hi_ = sx[hh + (HALF)]; const float t1_ = lo_ + dpp<CTRL>(lo_), t2_ = hi_ + dpp<CTRL>(hi_); \
        sx[hh] = (c & (BIT)) ? t2_ : t1_; }
      TR_STEP(8, ROR8, 8) TR_STEP(4, XOR7, 4) TR_STEP(2, XOR2, 2) TR_STEP(1, XOR1, 1)
#undef TR_STEP
      const float sc = rbf(rbf(sx[0]) * SCALE) * LOG2E;
      mx = fmaxf(mx, sc);
      scp[g] = sc;
      SB();
    };
    kgroup(bA, false, rk, 0, 0);
#pragma unroll 1
    for (int g = 1; g < 8; ++g)
      kgroup(bB, true, mkrsrc(g == 7 ? Vc : Kc, (unsigned)H * SK * DH * 2), kvbase + (((g + 1) * 16 + rot) & 127u) * 8192, g);
    {
      const float m = wave_max(mx);
      f32x4 p0 = *(LAS f32x4*)scp, p1 = *(LAS f32x4*)(scp + 4);
      float l = 0.f;
#pragma unroll
      for (int e = 0; e < 4; ++e) { p0[e] = exp2_(p0[e] - m); p1[e] = exp2_(p1[e] - m); l += p0[e] + p1[e]; }
      *(LAS f32x4*)scp = p0; *(LAS f32x4*)(scp + 4) = p1;
      l = wave_sum(l);
      if (lane == 0) *(LAS f32x2*)(lds + L_ML + 2 * wave) = f32x2{m, l};
      cnt_arrive(lds, C_ML, lane);
    }
    float acc[8] = {0.f, 0.f, 0.f, 0.f, 0.f, 0.f, 0.f, 0.f};
    auto vgroup = [&](const bool more, const unsigned tb, const float p) __attribute__((always_inline)) {
#define PV_STEP(I) { fma8(acc, dpp<BCAST + (I)>(p), bB[I]); \
        asm volatile("" : "+v"(acc[0]), "+v"(acc[1]), "+v"(acc[2]), "+v"(acc[3]), "+v"(acc[4]), "+v"(acc[5]), "+v"(acc[6]), "+v"(acc[7])); SB(); \
        if (more) { PACE(t0m, I); bB[I] = ld16_nt(rv, kvoff, tb + (I) * 8192); SB(); } \
        else if ((I) == 11) {   \
          pace_wait(t0m, T_K0 + T_MAIN); \
          _Pragma("unroll") for (int u = 0; u < 4; ++u) { \
            bB[u] = ld16_nt(mkrsrc(wo, H * DH * D * 2), r * 2048 + c * 16, (h * 128 + wave * 16 + 4 * u) * 2048 + s * 256); SB(); } } }
      PV_STEP(0) PV_STEP(1) PV_STEP(2) PV_STEP(3) PV_STEP(4) PV_STEP(5) PV_STEP(6) PV_STEP(7)
      PV_STEP(8) PV_STEP(9) PV_STEP(10) PV_STEP(11) PV_STEP(12) PV_STEP(13) PV_STEP(14) PV_STEP(15)
#undef PV_STEP
    };
#pragma unroll 1
    for (int g = 0; g < 7; ++g) vgroup(true, kvbase + (((g + 1) * 16 + rot) & 127u) * 8192, scp[g]);
    __builtin_amdgcn_s_setprio(3);
    cnt_wait(lds, C_WG, 1);
    {
      const float wgt = lds[L_WG + wave];
#pragma unroll
      for (int e = 0; e < 8; ++e) acc[e] *= wgt;
      vgroup(false, 0, scp[7] * wgt);
    }
    *(LAS f32x2*)(lds + L_MRG + wave * 128 + 8 * c + 2 * r) = fold_rows(acc);
    cnt_arrive(lds, C_END, lane);
#pragma unroll
    for (int u = 0; u < 4; ++u) *(LAS u32x4*)(lds + L_WT + (wave * 16 + 4 * u + r) * WT_STR + 4 * c) = bB[u];
    cnt_arrive(lds, C_WT, lane);
    return;
#undef PACE
  }

  const int a = wave - NMAIN;
  const rsrc_t rx = mkrsrc(x, D * 2), rws = mkrsrc(ws, WS_END);
  unsigned t0, tagA, tagB, tagC, tagD;
  unsigned long long e2;
  {
    u32x4 xa = ld16(rx, lane * 32, 0), xb = ld16(rx, lane * 32 + 16, 0);
    const u32x2 ep = __builtin_amdgcn_raw_buffer_load_b64(rws, 0, WS_EPOCH, SC1);
    const rsrc_t rwq = mkrsrc(wq, D * H * DH * 2);
    u32x4 w[8];
    const int voff = r * 8192 + c * 16, sb = (s * 128 + a * 32) * 8192 + h * 256;
#pragma unroll
    for (int i = 0; i < 8; ++i) { w[i] = ld16_nt(rwq, voff, sb + i * 32768); SB(); }
    const unsigned ka = __builtin_amdgcn_raw_buffer_load_b32(mkrsrc((const void*)__builtin_amdgcn_kernarg_segment_ptr(), 80), a == 3 ? 64 : OOB, 0, 0);
    if (a == 0 && lane < C_NUM) *(LAS unsigned*)(lds + L_CNT + lane) = 0u;
    __syncthreads();
    t0 = (unsigned)__builtin_amdgcn_s_memrealtime();
    asm volatile("" : "+v"(xa), "+v"(xb));
    float v[16];
#pragma unroll
    for (int i = 0; i < 4; ++i) { v[2 * i] = lo2f(xa[i]); v[2 * i + 1] = hi2f(xa[i]); v[8 + 2 * i] = lo2f(xb[i]); v[9 + 2 * i] = hi2f(xb[i]); }
    float sm = 0.f;
#pragma unroll
    for (int i = 0; i < 16; ++i) sm += v[i];
    const float mean = wave_sum(sm) * (1.f / D);
    float ss = 0.f;
#pragma unroll
    for (int i = 0; i < 16; ++i) { const float d = v[i] - mean; ss = fmaf(d, d, ss); }
    const float var = wave_sum(ss) * (1.f / D);
    const float mb = rbf(mean), den = rbf(sqrtf(rbf(rbf(var) + 1e-5f)));
    const float rinv = 1.0f / den;
#pragma unroll
    for (int i = 0; i < 16; ++i) {
      const float nu = rbf(v[i] - mb), q0 = nu * rinv;
      v[i] = rbf(fmaf(fmaf(-q0, den, nu), rinv, q0));
    }
#pragma unroll
    for (int q = 0; q < 4; ++q) *(LAS f32x4*)(lds + L_XN + q * 256 + 4 * lane) = f32x4{v[q], v[4 + q], v[8 + q], v[12 + q]};
    cnt_arrive(lds, C_XN, lane);
    const unsigned long long e1 = (((unsigned long long)__builtin_amdgcn_readfirstlane(ep[1]) << 32) | (unsigned)__builtin_amdgcn_readfirstlane(ep[0])) + 1ull;
    e2 = e1 + 1ull;
    tagA = (unsigned)e1; tagB = (unsigned)(e1 >> 32) + (unsigned)e1 * 0x9E3779B1u;
    tagC = (unsigned)e2; tagD = (unsigned)(e2 >> 32) + (unsigned)e2 * 0x9E3779B1u;
    float acc[8] = {0.f, 0.f, 0.f, 0.f, 0.f, 0.f, 0.f, 0.f};
#pragma unroll
    for (int q = 0; q < 2; ++q) {
      const f32x4 xv = *(LAS f32x4*)(lds + L_XN + r * 256 + s * 32 + a * 8 + 4 * q);
#pragma unroll
      for (int e = 0; e < 4; ++e) fma8(acc, xv[e], w[4 * q + e]);
    }
    *(LAS f32x2*)(lds + L_QP + a * 128 + 8 * c + 2 * r) = fold_rows(acc);
    cnt_arrive(lds, C_Q, lane);
    asm volatile("" :: "v"(ka));
  }
  if (a == 0) {
    cnt_wait<false>(lds, C_Q, NAUX);
    f32x2 v = *(LAS f32x2*)(lds + L_QP + 2 * lane);
#pragma unroll
    for (int q = 1; q < 4; ++q) v += *(LAS f32x2*)(lds + L_QP + q * 128 + 2 * lane);
    st16_sc1(rws, WS_Q + (h * 8 + s) * 1024 + lane * 16, rec(v[0], v[1], tagA, tagB));
    st16_sc1(rws, (bid == 0 && lane == 0) ? WS_T0 : OOB, u32x4{t0, tagA, t0, tagB});
  }
  if (a == 1) {
    pace_wait(t0, T_QPOLL);
    float q0 = 0.f, q1 = 0.f;
    unsigned tg = t0;
#pragma unroll 1
    for (unsigned sp = 0; sp < SPIN_MEM; ++sp) {
      REREAD();
      u32x4 g[8];
#pragma unroll
      for (int i = 0; i < 8; ++i) g[i] = ld16_sc1(rws, lane * 16, WS_Q + (h * 8 + i) * 1024);
      const u32x4 gt = ld16_sc1(rws, 0, WS_T0);
      bool ok = true;
      tg = rok(gt, tagA, tagB) ? gt[0] : t0;
      q0 = 0.f; q1 = 0.f;
#pragma unroll
      for (int i = 0; i < 8; ++i) { ok = ok && rok(g[i], tagA, tagB); q0 += r0(g[i]); q1 += r1(g[i]); }
      if (__all(ok)) break;
      __builtin_amdgcn_s_sleep(2);
    }
    *(LAS unsigned*)(lds + L_QS + lane) = pack2(q0, q1);
    if (lane == 0) *(LAS unsigned*)(lds + L_CNT + W_T0) = t0 - (unsigned)min(max((int)(t0 - tg), 0), 100);
    cnt_arrive(lds, C_QR, lane);
  }
  const int ks = xl * 2 + (j >> 4), cg = j & 15;
  u32x4 dw[7];
  {
    const rsrc_t rd = mkrsrc(wd, D * F * 2);
    const int voff = (lane >> 3) * 2048 + (lane & 7) * 16, sb = (ks * 224 + a * 56) * 2048 + cg * 128;
    constexpr int WSTEP = ((T_K0 - T_W2) << 16) / 7;
    int wdue = T_W2 * 65536 + (WSTEP / 128) * (a * 32 + j);
#pragma unroll
    for (int i = 0; i < 7; ++i) { pace_wait(t0, wdue >> 16); wdue += WSTEP; dw[i] = ld16_nt(rd, voff, sb + i * 16384); SB(); }
  }
  if (a == 0 && j < 28) {
    cnt_wait(lds, C_F1, NMAIN);
    f32x2 v = *(LAS f32x2*)(lds + L_FP + 2 * (lane & 31));
#pragma unroll
    for (int q = 1; q < 8; ++q) v += *(LAS f32x2*)(lds + L_FP + q * 64 + 2 * (lane & 31));
    if (f1 >= 56) {
      st16_sc1(rws, lane < 32 ? WS_G1 + ((mat * 2 + rb) * F + cb * 64) * 8 + lane * 16 : OOB, rec(v[0], v[1], tagA, tagB));
    } else {
      pace_wait(t0, T_K0 + T_HA);
      u32x4 g[3];
#pragma unroll 1
      for (unsigned sp = 0; sp < SPIN_MEM; ++sp) {
        REREAD();
#pragma unroll
        for (int k = 0; k < 3; ++k) g[k] = ld16_sc1(rws, (lane & 31) * 16, WS_G1 + ((k + 1) * F + cb * 64) * 8);
        if (__all(rok(g[0], tagA, tagB) && rok(g[1], tagA, tagB) && rok(g[2], tagA, tagB))) break;
        __builtin_amdgcn_s_sleep(32);
      }
      float hh[2];
#pragma unroll
      for (int e = 0; e < 2; ++e) {
        const float gs = rbf(v[e] + __uint_as_float(g[0][2 * e]));
        const float us = rbf(__uint_as_float(g[1][2 * e]) + __uint_as_float(g[2][2 * e]));
        const float ge = 0.5f * gs * (1.f + tanhf(0.7978845608028654f * (gs + 0.044715f * gs * gs * gs)));
        hh[e] = rbf(ge) * us;
      }
      const float hp = __uint_as_float(pack2(hh[0], hh[1]));
      st16_sc1(rws, (lane < 32 && !(lane & 1)) ? WS_H + cb * 256 + lane * 8 : OOB, rec(hp, dpp<XOR1>(hp), tagA, tagB));
    }
  }
  {
    pace_wait(t0, T_K0 + T_HB);
    u32x4 g;
#pragma unroll 1
    for (unsigned sp = 0; sp < SPIN_MEM; ++sp) {
      REREAD();
      g = ld16_sc1(rws, (lane < 14 ? lane : 13) * 16, WS_H + (ks * 56 + a * 14) * 16);
      if (__all(rok(g, tagA, tagB))) break;
      __builtin_amdgcn_s_sleep(32);
    }
    if (lane < 14) *(LAS f32x4*)(lds + L_H + a * 64 + 4 * lane) = f32x4{lo2f(g[0]), hi2f(g[0]), lo2f(g[2]), hi2f(g[2])};
    float y[8] = {0.f, 0.f, 0.f, 0.f, 0.f, 0.f, 0.f, 0.f};
#pragma unroll
    for (int i = 0; i < 7; ++i) fma8(y, lds[L_H + a * 64 + 8 * i + (lane >> 3)], dw[i]);
#pragma unroll
    for (int e = 0; e < 8; ++e) y[e] += dpp<ROR8>(y[e]);
    const f32x2 z = fold_rows(y);
    if (c < 8) *(LAS f32x2*)(lds + L_F2 + a * 64 + 8 * c + 2 * r) = z;
    cnt_arrive(lds, C_F2, lane);
  }
  const int col0 = s * 128 + 4 * h;
  unsigned xw = 0;
  if (a == 0) xw = __builtin_amdgcn_raw_buffer_load_b32(rx, (col0 + 2 * (lane & 1)) * 2, 0, 0);
  if (a == 3) {
    const unsigned po = __builtin_amdgcn_raw_buffer_load_b32(mkrsrc(out, D * 2), (col0 + 2 * (lane & 1)) * 2, 0, SC1);
    asm volatile("" :: "v"(po));
  }
  if (a == 1) {
    cnt_wait(lds, C_ML, NMAIN);
    const f32x2 ml = *(LAS f32x2*)(lds + L_ML + 2 * (lane & 7));
    const float Mw = max8(ml[0]), Lw = sum8(exp2_(ml[0] - Mw) * ml[1]);
    st16_sc1(rws, lane == 0 ? WS_ML + (h * 8 + s) * 128 : OOB, rec(Mw, Lw, tagA, tagB));
    pace_wait(t0, T_K0 + T_MAIN / 2 + 1500);
    u32x4 g;
#pragma unroll 1
    for (unsigned sp = 0; sp < SPIN_MEM; ++sp) {
      REREAD();
      g = ld16_sc1(rws, (lane & 7) * 128, WS_ML + h * 8 * 128);
      if (__all(rok(g, tagA, tagB))) break;
      __builtin_amdgcn_s_sleep(32);
    }
    const float M = max8(r0(g)), L = sum8(exp2_(r0(g) - M) * r1(g));
    if (lane < 8) lds[L_WG + lane] = exp2_(ml[0] - M) / L;
    cnt_arrive(lds, C_WG, lane);
  }
  pace_wait(t0, T_K0 + T_MAIN - 100);
  __builtin_amdgcn_s_setprio(3);
  if (a == 2) {
    cnt_wait(lds, C_F2, NAUX);
    f32x2 v = *(LAS f32x2*)(lds + L_F2 + 2 * (lane & 31));
#pragma unroll
    for (int q = 1; q < 4; ++q) v += *(LAS f32x2*)(lds + L_F2 + q * 64 + 2 * (lane & 31));
    pace_wait(t0, T_K0 + T_MAIN + 60);
    st16_sc1(rws, lane < 32 ? WS_FFN + (ks * D + cg * 64) * 8 + lane * 16 : OOB, rec(v[0], v[1], tagA, tagB));
  }
  if (a == 0) {
    cnt_wait<false>(lds, C_END, NMAIN);
    f32x2 v = *(LAS f32x2*)(lds + L_MRG + 2 * lane);
#pragma unroll
    for (int w = 1; w < 8; ++w) v += *(LAS f32x2*)(lds + L_MRG + w * 128 + 2 * lane);
    st16_sc1(rws, WS_Q + (h * 8 + s) * 1024 + lane * 16, rec(v[0], v[1], tagC, tagD));
  } else {
    cnt_wait(lds, C_END, NMAIN);
  }
  u32x4 bfr[2][4];
  cnt_wait<false>(lds, C_WT, NMAIN);
#pragma unroll
  for (int q = 0; q < 4; ++q)
#pragma unroll
    for (int d = 0; d < 4; ++d) {
      const unsigned w0 = *(LAS unsigned*)(lds + L_WT + (32 * q + 8 * r + 2 * d) * WT_STR + 16 * a + c);
      const unsigned w1 = *(LAS unsigned*)(lds + L_WT + (32 * q + 8 * r + 2 * d + 1) * WT_STR + 16 * a + c);
      bfr[0][q][d] = __builtin_amdgcn_perm(w1, w0, 0x05040100u);
      bfr[1][q][d] = __builtin_amdgcn_perm(w1, w0, 0x07060302u);
    }
  {
    u32x4 g0, g1;
    const int vo = r * 1024 + (16 * a + c) * 16;
#pragma unroll 1
    for (unsigned sp = 0; sp < SPIN_MEM; ++sp) {
      REREAD();
      g0 = ld16_sc1(rws, vo, WS_Q + h * 8 * 1024);
      g1 = ld16_sc1(rws, vo, WS_Q + (h * 8 + 4) * 1024);
      if (__all(rok(g0, tagC, tagD) && rok(g1, tagC, tagD))) break;
    }
    float o = fold32(r0(g0) + r0(g1), r1(g0) + r1(g1));
    o = fold16(o, o);
    if ((r & 1) == 0) *((LAS u16*)(lds + L_OB) + 32 * a + 2 * c + (r >> 1)) = __builtin_bit_cast(u16, (__bf16)o);
  }
  cnt_arrive(lds, C_OB, lane);
  cnt_wait<false>(lds, C_OB, NAUX);
  {
    f32x4 y0 = {0.f, 0.f, 0.f, 0.f}, y1 = y0;
#pragma unroll
    for (int q = 0; q < 4; ++q) {
      const u32x4 af = *(LAS u32x4*)(lds + L_OB + 16 * q + 4 * r);
      y0 = __builtin_amdgcn_mfma_f32_16x16x32_bf16(__builtin_bit_cast(bf16x8, af), __builtin_bit_cast(bf16x8, bfr[0][q]), y0, 0, 0, 0);
      y1 = __builtin_amdgcn_mfma_f32_16x16x32_bf16(__builtin_bit_cast(bf16x8, af), __builtin_bit_cast(bf16x8, bfr[1][q]), y1, 0, 0, 0);
    }
    st16_sc1(rws, lane < 16 ? WS_Y + (h * 8 + s) * 1024 + (16 * a + lane) * 16 : OOB, rec(y0[0], y1[0], tagA, tagB));
  }
  if (a == 0) {
    u32x4 g, gf;
    pace_wait((unsigned)__builtin_amdgcn_s_memrealtime(), 20);
#pragma unroll 1
    for (unsigned sp = 0; sp < SPIN_MEM; ++sp) {
      REREAD();
      g = ld16_sc1(rws, (lane >> 1) * (8 * 1024) + (lane & 1) * 16, WS_Y + s * 1024 + h * 32);
      gf = ld16_sc1(rws, ((lane >> 1) & 15) * (D * 8) + (lane & 1) * 16, WS_FFN + col0 * 8);
      if (__all(rok(g, tagA, tagB) && rok(gf, tagA, tagB))) break;
    }
    const float f0 = rbf(parity_sum(r0(gf)) * 0.5f), f1v = rbf(parity_sum(r1(gf)) * 0.5f);
    const float a0 = rbf(parity_sum(r0(g))), a1 = rbf(parity_sum(r1(g)));
    const unsigned o = pack2(rbf(lo2f(xw) + a0) + f0, rbf(hi2f(xw) + a1) + f1v);
    __builtin_amdgcn_raw_buffer_store_b32(o, mkrsrc(out, D * 2), lane < 2 ? (col0 + 2 * lane) * 2 : OOB, 0, SC1);
    __builtin_amdgcn_raw_buffer_store_b64(u32x2{(unsigned)e2, (unsigned)(e2 >> 32)}, rws, (bid == 0 && lane == 0) ? WS_EPOCH : OOB, 0, SC1);
  }
}

extern "C" void kernel_launch(void* const* d_in, const int* in_sizes, int n_in, void* d_out, int out_size, void* d_ws, size_t ws_size,
                              hipStream_t stream) {
  fused_decode_kernel<<<256, NTHR, 0, stream>>>((const u16*)d_in[0], (const u16*)d_in[3], d_ws, (const u16*)d_in[6],
                                                 (const u16*)d_in[7], (const u16*)d_in[1], (const u16*)d_in[2], (const u16*)d_in[4],
                                                 (const u16*)d_in[8], (u16*)d_out);
}
```

```cpp
#include <hip/hip_runtime.h>
#include <stdint.h>

typedef unsigned u32x4 __attribute__((ext_vector_type(4)));
typedef unsigned u32x2 __attribute__((ext_vector_type(2)));
typedef float f32x4 __attribute__((ext_vector_type(4)));
typedef float f32x2 __attribute__((ext_vector_type(2)));
typedef __bf16 bf16x2 __attribute__((ext_vector_type(2)));
typedef __bf16 bf16x8 __attribute__((ext_vector_type(8)));
typedef unsigned short u16;
#define LAS __attribute__((address_space(3)))
typedef __amdgpu_buffer_rsrc_t rsrc_t;

constexpr int T_F1 = 70;
constexpr int T_W2 = 262;
constexpr int T_K0 = 345;
constexpr int T_MAIN = 7060;
constexpr int PACE_G = 2;
constexpr int T_QPOLL = 315;
constexpr int T_HA = 1600, T_HB = 2800;

constexpr int D = 1024, H = 32, SK = 32768, DH = 128, F = 3584;
constexpr int NMAIN = 8, NAUX = 4, NTHR = (NMAIN + NAUX) * 64;
constexpr int NT = 2, SC1 = 16;
constexpr int OOB = 0x40000000;
constexpr unsigned WS_EPOCH = 0, WS_T0 = 1024, WS_ML = 4096, WS_Q = WS_ML + 256 * 128, WS_Y = WS_Q + 256 * 1024, WS_G1 = WS_Y + 256 * 1024,
                   WS_H = WS_G1 + 4 * F * 8, WS_FFN = WS_H + F * 4, WS_END = WS_FFN + 16 * D * 8;
constexpr int L_XN = 0, L_QP = 1024, L_QS = L_QP + 4 * 128, L_MRG = L_QS + 64, L_ML = L_MRG + 8 * 128, L_WG = L_ML + 16, L_FP = L_WG + 8,
              L_H = L_FP + 8 * 64, L_F2 = L_H + 4 * 64, L_OB = L_F2 + 4 * 64, L_CNT = L_OB + 64, L_SC = L_CNT + 16, L_WT = L_SC + 8 * 512,
              WT_STR = 68, L_END = L_WT + 128 * WT_STR;
constexpr int C_Q = 0, C_XN = 1, C_F2 = 2, C_WT = 3, C_ML = 4, C_WG = 5, C_END = 6, C_QR = 7, C_F1 = 8, C_OB = 9, C_NUM = 10, W_T0 = 12;
constexpr unsigned SPIN_MEM = 1u << 20, SPIN_LDS = 1u << 23;

__device__ __forceinline__ float lo2f(unsigned w) { return __builtin_bit_cast(float, w << 16); }
__device__ __forceinline__ float hi2f(unsigned w) { return __builtin_bit_cast(float, w & 0xffff0000u); }
__device__ __forceinline__ float rbf(float x) { return (float)(__bf16)x; }
__device__ __forceinline__ unsigned pack2(float a, float b) { bf16x2 v = {(__bf16)a, (__bf16)b}; return __builtin_bit_cast(unsigned, v); }
__device__ __forceinline__ float dot2(unsigned k, unsigned q, float acc) {
  return __builtin_amdgcn_fdot2_f32_bf16(__builtin_bit_cast(bf16x2, k), __builtin_bit_cast(bf16x2, q), acc, false);
}
template <int CTRL> __device__ __forceinline__ float dpp(float x) {
  return __builtin_bit_cast(float, __builtin_amdgcn_mov_dpp(__builtin_bit_cast(int, x), CTRL, 0xf, 0xf, true));
}
constexpr int XOR1 = 0xB1, XOR2 = 0x4E, XOR7 = 0x141, ROR8 = 0x128, ROR4 = 0x124, BCAST = 0x150;
__device__ __forceinline__ float fold32(float a, float b) {
  auto t = __builtin_amdgcn_permlane32_swap(__float_as_uint(a), __float_as_uint(b), false, false);
  return __uint_as_float(t[0]) + __uint_as_float(t[1]);
}
__device__ __forceinline__ float fold16(float a, float b) {
  auto t = __builtin_amdgcn_permlane16_swap(__float_as_uint(a), __float_as_uint(b), false, false);
  return __uint_as_float(t[0]) + __uint_as_float(t[1]);
}
__device__ __forceinline__ f32x2 fold_rows(const float (&v)[8]) {
  const float u0 = fold32(v[0], v[4]), u1 = fold32(v[1], v[5]), u2 = fold32(v[2], v[6]), u3 = fold32(v[3], v[7]);
  return f32x2{fold16(u0, u2), fold16(u1, u3)};
}
__device__ __forceinline__ float xrow_sum(float x) { x = fold16(x, x); return fold32(x, x); }
__device__ __forceinline__ float xrow_max(float x) {
  auto s = __builtin_amdgcn_permlane16_swap(__float_as_uint(x), __float_as_uint(x), false, false);
  x = fmaxf(__uint_as_float(s[0]), __uint_as_float(s[1]));
  auto t = __builtin_amdgcn_permlane32_swap(__float_as_uint(x), __float_as_uint(x), false, false);
  return fmaxf(__uint_as_float(t[0]), __uint_as_float(t[1]));
}
__device__ __forceinline__ float parity_sum(float x) {
  x += dpp<XOR2>(x); x += dpp<ROR4>(x); x += dpp<ROR8>(x); return xrow_sum(x);
}
__device__ __forceinline__ float wave_sum(float x) { x += dpp<XOR1>(x); return parity_sum(x); }
__device__ __forceinline__ float max8(float x) { x = fmaxf(x, dpp<XOR1>(x)); x = fmaxf(x, dpp<XOR2>(x)); return fmaxf(x, dpp<XOR7>(x)); }
__device__ __forceinline__ float sum8(float x) { x += dpp<XOR1>(x); x += dpp<XOR2>(x); return x + dpp<XOR7>(x); }
__device__ __forceinline__ float wave_max(float x) { x = max8(x); x = fmaxf(x, dpp<ROR8>(x)); return xrow_max(x); }
__device__ __forceinline__ float exp2_(float x) { return __builtin_amdgcn_exp2f(x); }
__device__ __forceinline__ rsrc_t mkrsrc(const void* p, unsigned bytes) { return __builtin_amdgcn_make_buffer_rsrc((void*)p, 0, (int)bytes, 0x00020000); }
__device__ __forceinline__ u32x4 ld16(rsrc_t r, int voff, int soff) { return __builtin_amdgcn_raw_buffer_load_b128(r, voff, soff, 0); }
__device__ __forceinline__ u32x4 ld16_nt(rsrc_t r, int voff, int soff) { return __builtin_amdgcn_raw_buffer_load_b128(r, voff, soff, NT); }
__device__ __forceinline__ u32x4 ld16_sc1(rsrc_t r, int voff, int soff) { return __builtin_amdgcn_raw_buffer_load_b128(r, voff, soff, SC1); }
__device__ __forceinline__ void st16_sc1(rsrc_t r, int off, u32x4 v) { __builtin_amdgcn_raw_buffer_store_b128(v, r, off, 0, SC1); }
#define SB() __builtin_amdgcn_sched_barrier(0)
#define REREAD() asm volatile("" ::: "memory")
__device__ __forceinline__ void pace_wait(unsigned t0, int due) {
  asm volatile("1:\n\ts_memrealtime s[96:97]\n\ts_waitcnt lgkmcnt(0)\n\ts_sub_u32 s96, s96, %0\n\ts_cmp_lt_i32 s96, %1\n\t"
               "s_cbranch_scc0 2f\n\ts_sleep 1\n\ts_branch 1b\n2:"
               :: "s"(t0), "s"(due) : "s96", "s97", "scc", "memory");
}
__device__ __forceinline__ void fma8(float (&acc)[8], float s, u32x4 w) {
#pragma unroll
  for (int t = 0; t < 4; ++t) { acc[2 * t] = fmaf(s, lo2f(w[t]), acc[2 * t]); acc[2 * t + 1] = fmaf(s, hi2f(w[t]), acc[2 * t + 1]); }
}
__device__ __forceinline__ u32x4 rec(float a, float b, unsigned ta, unsigned tb) { return u32x4{__float_as_uint(a), ta, __float_as_uint(b), tb}; }
__device__ __forceinline__ bool rok(u32x4 g, unsigned ta, unsigned tb) { return g[1] == ta && g[3] == tb; }
__device__ __forceinline__ float r0(u32x4 g) { return __uint_as_float(g[0]); }
__device__ __forceinline__ float r1(u32x4 g) { return __uint_as_float(g[2]); }

__device__ __forceinline__ void cnt_arrive(LAS float* lds, int which, int lane) {
  asm volatile("s_waitcnt lgkmcnt(0)" ::: "memory");
  if (lane == 0) __hip_atomic_fetch_add((LAS unsigned*)(lds + L_CNT + which), 1u, __ATOMIC_RELAXED, __HIP_MEMORY_SCOPE_WORKGROUP);
}
template <bool SLEEP = true>
__device__ __forceinline__ void cnt_wait(LAS float* lds, int which, unsigned n) {
#pragma unroll 1
  for (unsigned sp = 0; sp < SPIN_LDS; ++sp) {
    if (__builtin_amdgcn_readfirstlane(*(volatile LAS unsigned*)(lds + L_CNT + which)) >= n) break;
    if (SLEEP) __builtin_amdgcn_s_sleep(1);
  }
  REREAD();
}

__global__ void __launch_bounds__(NTHR, 3)
fused_decode_kernel(const u16* __restrict__ x, const u16* __restrict__ Kc, const u16* __restrict__ Vc, const u16* __restrict__ wq,
                    const u16* __restrict__ wo, const u16* __restrict__ wg, const u16* __restrict__ wu, const u16* __restrict__ wd,
                    u16* __restrict__ out, void* ws) {
  __shared__ __attribute__((aligned(16))) float lds_[L_END];
  LAS float* lds = (LAS float*)lds_;
  unsigned long long ta_, tb_, tm_;
  asm volatile("s_memrealtime %0\n\ts_waitcnt lgkmcnt(0)\n\ts_memrealtime %2\n\ts_waitcnt lgkmcnt(0)\n\t.rept 64\n\tv_nop\n\t.endr\n\ts_memrealtime %1\n\ts_waitcnt lgkmcnt(0)" : "=&s"(ta_), "=&s"(tb_), "=&s"(tm_) :: "memory");
  SB();
  const int tid = threadIdx.x, lane = tid & 63, wave = __builtin_amdgcn_readfirstlane(tid >> 6);
  const int r = lane >> 4, c = lane & 15;
  const int bid = blockIdx.x, xl = bid & 7, j = bid >> 3;
  const int h = xl * 4 + (j >> 3), s = j & 7;
  const int f1 = xl * 28 + j, mat = f1 / 112, rb = (f1 / 56) & 1, cb = f1 % 56;
  asm volatile("" :: "s"(x), "s"(wq), "s"(ws), "s"(f1), "s"(wave));

  const unsigned tc_ = (unsigned)__builtin_amdgcn_s_memrealtime();
  SB();
  if (wave < NMAIN) {
    const rsrc_t rk = mkrsrc(Kc, (unsigned)H * SK * DH * 2), rv = mkrsrc(Vc, (unsigned)H * SK * DH * 2);
    u32x4 bA[16], bB[16];
    const unsigned kvbase = (unsigned)h * (8u << 20) + (unsigned)s * (1u << 20) + wave * 1024;
    const unsigned rot = 32u * ((h + (h >> 2) + (h >> 4)) & 3);
    const int kvoff = lane * 16;
    constexpr int STEP = (T_MAIN << 16) / 256 * PACE_G;
    int due = T_K0 * 65536 - STEP + (STEP / 256) * (wave * 32 + j);
#define PACE(T0, I) do { if ((I) % PACE_G == 0) { due += STEP; pace_wait(T0, due >> 16); } } while (0)
    __builtin_amdgcn_s_barrier();
    const unsigned t0 = (unsigned)__builtin_amdgcn_s_memrealtime();
    SB();
    {
      const rsrc_t rg = mkrsrc(mat ? wu : wg, D * F * 2);
      const int voff = j < 28 ? (lane >> 3) * (F * 2) + (lane & 7) * 16 : OOB, sb = (rb * 512 + wave * 64) * (F * 2) + cb * 128;
      constexpr int FSTEP = ((T_W2 - T_F1) << 16) / 8;
      int fdue = T_F1 * 65536 + (FSTEP / 256) * (wave * 32 + j);
#pragma unroll
      for (int i = 0; i < 8; ++i) { pace_wait(t0, fdue >> 16); fdue += FSTEP; bB[i] = ld16_nt(rg, voff, sb + i * 8 * (F * 2)); SB(); }
    }
#pragma unroll
    for (int i = 0; i < 16; ++i) { PACE(t0, i); bA[i] = ld16_nt(rk, kvoff, kvbase + (rot + i) * 8192); SB(); }
    {
      cnt_wait(lds, C_XN, 1);
      float ag[8] = {0.f, 0.f, 0.f, 0.f, 0.f, 0.f, 0.f, 0.f};
#pragma unroll
      for (int i = 0; i < 8; ++i) {
        const int k = rb * 512 + wave * 64 + 8 * i + (lane >> 3);
        fma8(ag, lds[L_XN + (k & 3) * 256 + (k >> 2)], bB[i]);
      }
#pragma unroll
      for (int e = 0; e < 8; ++e) ag[e] += dpp<ROR8>(ag[e]);
      const f32x2 z = fold_rows(ag);
      if (c < 8) *(LAS f32x2*)(lds + L_FP + wave * 64 + 8 * c + 2 * r) = z;
      cnt_arrive(lds, C_F1, lane);
      SB();
    }
#pragma unroll
    for (int i = 0; i < 16; ++i) { PACE(t0, i); bB[i] = ld16_nt(rk, kvoff, kvbase + (rot + 16 + i) * 8192); SB(); }
    cnt_wait(lds, C_QR, 1);
    const u32x4 qp = *(LAS u32x4*)(lds + L_QS + 4 * c);
    const unsigned t0m = __builtin_amdgcn_readfirstlane(*(LAS unsigned*)(lds + L_CNT + W_T0));
    constexpr float SCALE = 0.08838834764831845f, LOG2E = 1.4426950408889634f;
    LAS float* scp = lds + L_SC + wave * 512 + lane * 8;
    float mx = -INFINITY;
    auto kgroup = [&](u32x4 (&kb)[16], const bool more, const rsrc_t rn, const unsigned tb, const int g) __attribute__((always_inline)) {
      float sx[16];
#pragma unroll
      for (int i = 0; i < 16; ++i) {
        float d = 0.f;
#pragma unroll
        for (int t = 0; t < 4; ++t) d = dot2(kb[i][t], qp[t], d);
        asm volatile("" : "+v"(d));
        sx[i] = d;
        SB();
        if (more) { PACE(t0m, i); kb[i] = ld16_nt(rn, kvoff, tb + i * 8192); SB(); }
      }
#define TR_STEP(HALF, CTRL, BIT) _Pragma("unroll") for (int hh = 0; hh < (HALF); ++hh) { \
        const float lo_ = sx[hh], hi_ = sx[hh + (HALF)]; const float t1_ = lo_ + dpp<CTRL>(lo_), t2_ = hi_ + dpp<CTRL>(hi_); \
        sx[hh] = (c & (BIT)) ? t2_ : t1_; }
      TR_STEP(8, ROR8, 8) TR_STEP(4, XOR7, 4) TR_STEP(2, XOR2, 2) TR_STEP(1, XOR1, 1)
#undef TR_STEP
      const float sc = rbf(rbf(sx[0]) * SCALE) * LOG2E;
      mx = fmaxf(mx, sc);
      scp[g] = sc;
      SB();
    };
    kgroup(bA, false, rk, 0, 0);
#pragma unroll 1
    for (int g = 1; g < 8; ++g)
      kgroup(bB, true, mkrsrc(g == 7 ? Vc : Kc, (unsigned)H * SK * DH * 2), kvbase + (((g + 1) * 16 + rot) & 127u) * 8192, g);
    {
      const float m = wave_max(mx);
      f32x4 p0 = *(LAS f32x4*)scp, p1 = *(LAS f32x4*)(scp + 4);
      float l = 0.f;
#pragma unroll
      for (int e = 0; e < 4; ++e) { p0[e] = exp2_(p0[e] - m); p1[e] = exp2_(p1[e] - m); l += p0[e] + p1[e]; }
      *(LAS f32x4*)scp = p0; *(LAS f32x4*)(scp + 4) = p1;
      l = wave_sum(l);
      if (lane == 0) *(LAS f32x2*)(lds + L_ML + 2 * wave) = f32x2{m, l};
      cnt_arrive(lds, C_ML, lane);
    }
    float acc[8] = {0.f, 0.f, 0.f, 0.f, 0.f, 0.f, 0.f, 0.f};
    auto vgroup = [&](const bool more, const unsigned tb, const float p) __attribute__((always_inline)) {
#define PV_STEP(I) { fma8(acc, dpp<BCAST + (I)>(p), bB[I]); \
        asm volatile("" : "+v"(acc[0]), "+v"(acc[1]), "+v"(acc[2]), "+v"(acc[3]), "+v"(acc[4]), "+v"(acc[5]), "+v"(acc[6]), "+v"(acc[7])); SB(); \
        if (more) { PACE(t0m, I); bB[I] = ld16_nt(rv, kvoff, tb + (I) * 8192); SB(); } \
        else if ((I) == 11) {   \
          pace_wait(t0m, T_K0 + T_MAIN); \
          _Pragma("unroll") for (int u = 0; u < 4; ++u) { \
            bB[u] = ld16_nt(mkrsrc(wo, H * DH * D * 2), r * 2048 + c * 16, (h * 128 + wave * 16 + 4 * u) * 2048 + s * 256); SB(); } } }
      PV_STEP(0) PV_STEP(1) PV_STEP(2) PV_STEP(3) PV_STEP(4) PV_STEP(5) PV_STEP(6) PV_STEP(7)
      PV_STEP(8) PV_STEP(9) PV_STEP(10) PV_STEP(11) PV_STEP(12) PV_STEP(13) PV_STEP(14) PV_STEP(15)
#undef PV_STEP
    };
#pragma unroll 1
    for (int g = 0; g < 7; ++g) vgroup(true, kvbase + (((g + 1) * 16 + rot) & 127u) * 8192, scp[g]);
    __builtin_amdgcn_s_setprio(3);
    cnt_wait(lds, C_WG, 1);
    {
      const float wgt = lds[L_WG + wave];
#pragma unroll
      for (int e = 0; e < 8; ++e) acc[e] *= wgt;
      vgroup(false, 0, scp[7] * wgt);
    }
    *(LAS f32x2*)(lds + L_MRG + wave * 128 + 8 * c + 2 * r) = fold_rows(acc);
    cnt_arrive(lds, C_END, lane);
#pragma unroll
    for (int u = 0; u < 4; ++u) *(LAS u32x4*)(lds + L_WT + (wave * 16 + 4 * u + r) * WT_STR + 4 * c) = bB[u];
    cnt_arrive(lds, C_WT, lane);
    return;
#undef PACE
  }

  const int a = wave - NMAIN;
  const rsrc_t rx = mkrsrc(x, D * 2), rws = mkrsrc(ws, WS_END);
  unsigned t0, tagA, tagB, tagC, tagD;
  unsigned long long e2;
  {
    u32x4 xa = ld16(rx, lane * 32, 0), xb = ld16(rx, lane * 32 + 16, 0);
    const u32x2 ep = __builtin_amdgcn_raw_buffer_load_b64(rws, 0, WS_EPOCH, SC1);
    const rsrc_t rwq = mkrsrc(wq, D * H * DH * 2);
    u32x4 w[8];
    const int voff = r * 8192 + c * 16, sb = (s * 128 + a * 32) * 8192 + h * 256;
#pragma unroll
    for (int i = 0; i < 8; ++i) { w[i] = ld16_nt(rwq, voff, sb + i * 32768); SB(); }
    if (a == 0 && lane < C_NUM) *(LAS unsigned*)(lds + L_CNT + lane) = 0u;
    __syncthreads();
    t0 = (unsigned)__builtin_amdgcn_s_memrealtime();
    asm volatile("" : "+v"(xa), "+v"(xb));
    float v[16];
#pragma unroll
    for (int i = 0; i < 4; ++i) { v[2 * i] = lo2f(xa[i]); v[2 * i + 1] = hi2f(xa[i]); v[8 + 2 * i] = lo2f(xb[i]); v[9 + 2 * i] = hi2f(xb[i]); }
    float sm = 0.f;
#pragma unroll
    for (int i = 0; i < 16; ++i) sm += v[i];
    const float mean = wave_sum(sm) * (1.f / D);
    float ss = 0.f;
#pragma unroll
    for (int i = 0; i < 16; ++i) { const float d = v[i] - mean; ss = fmaf(d, d, ss); }
    const float var = wave_sum(ss) * (1.f / D);
    const float mb = rbf(mean), den = rbf(sqrtf(rbf(rbf(var) + 1e-5f)));
    const float rinv = 1.0f / den;
#pragma unroll
    for (int i = 0; i < 16; ++i) {
      const float nu = rbf(v[i] - mb), q0 = nu * rinv;
      v[i] = rbf(fmaf(fmaf(-q0, den, nu), rinv, q0));
    }
#pragma unroll
    for (int q = 0; q < 4; ++q) *(LAS f32x4*)(lds + L_XN + q * 256 + 4 * lane) = f32x4{v[q], v[4 + q], v[8 + q], v[12 + q]};
    cnt_arrive(lds, C_XN, lane);
    const unsigned long long e1 = (((unsigned long long)__builtin_amdgcn_readfirstlane(ep[1]) << 32) | (unsigned)__builtin_amdgcn_readfirstlane(ep[0])) + 1ull;
    e2 = e1 + 1ull;
    tagA = (unsigned)e1; tagB = (unsigned)(e1 >> 32) + (unsigned)e1 * 0x9E3779B1u;
    tagC = (unsigned)e2; tagD = (unsigned)(e2 >> 32) + (unsigned)e2 * 0x9E3779B1u;
    float acc[8] = {0.f, 0.f, 0.f, 0.f, 0.f, 0.f, 0.f, 0.f};
#pragma unroll
    for (int q = 0; q < 2; ++q) {
      const f32x4 xv = *(LAS f32x4*)(lds + L_XN + r * 256 + s * 32 + a * 8 + 4 * q);
#pragma unroll
      for (int e = 0; e < 4; ++e) fma8(acc, xv[e], w[4 * q + e]);
    }
    *(LAS f32x2*)(lds + L_QP + a * 128 + 8 * c + 2 * r) = fold_rows(acc);
  }
  cnt_arrive(lds, C_Q, lane);
  if (a == 0) {
    cnt_wait<false>(lds, C_Q, NAUX);
    f32x2 v = *(LAS f32x2*)(lds + L_QP + 2 * lane);
#pragma unroll
    for (int q = 1; q < 4; ++q) v += *(LAS f32x2*)(lds + L_QP + q * 128 + 2 * lane);
    st16_sc1(rws, WS_Q + (h * 8 + s) * 1024 + lane * 16, rec(v[0], v[1], tagA, tagB));
    st16_sc1(rws, (bid == 0 && lane == 0) ? WS_T0 : OOB, u32x4{t0, tagA, t0, tagB});
  }
  if (a == 1) {
    pace_wait(t0, T_QPOLL);
    float q0 = 0.f, q1 = 0.f;
    unsigned tg = t0;
#pragma unroll 1
    for (unsigned sp = 0; sp < SPIN_MEM; ++sp) {
      REREAD();
      u32x4 g[8];
#pragma unroll
      for (int i = 0; i < 8; ++i) g[i] = ld16_sc1(rws, lane * 16, WS_Q + (h * 8 + i) * 1024);
      const u32x4 gt = ld16_sc1(rws, 0, WS_T0);
      bool ok = true;
      tg = rok(gt, tagA, tagB) ? gt[0] : t0;
      q0 = 0.f; q1 = 0.f;
#pragma unroll
      for (int i = 0; i < 8; ++i) { ok = ok && rok(g[i], tagA, tagB); q0 += r0(g[i]); q1 += r1(g[i]); }
      if (__all(ok)) break;
      __builtin_amdgcn_s_sleep(2);
    }
    *(LAS unsigned*)(lds + L_QS + lane) = pack2(q0, q1);
    if (lane == 0) *(LAS unsigned*)(lds + L_CNT + W_T0) = t0 - (unsigned)min(max((int)(t0 - tg), 0), 100);
    cnt_arrive(lds, C_QR, lane);
  }
  const int ks = xl * 2 + (j >> 4), cg = j & 15;
  u32x4 dw[7];
  {
    const rsrc_t rd = mkrsrc(wd, D * F * 2);
    const int voff = (lane >> 3) * 2048 + (lane & 7) * 16, sb = (ks * 224 + a * 56) * 2048 + cg * 128;
    constexpr int WSTEP = ((T_K0 - T_W2) << 16) / 7;
    int wdue = T_W2 * 65536 + (WSTEP / 128) * (a * 32 + j);
#pragma unroll
    for (int i = 0; i < 7; ++i) { pace_wait(t0, wdue >> 16); wdue += WSTEP; dw[i] = ld16_nt(rd, voff, sb + i * 16384); SB(); }
  }
  if (a == 0 && j < 28) {
    cnt_wait(lds, C_F1, NMAIN);
    f32x2 v = *(LAS f32x2*)(lds + L_FP + 2 * (lane & 31));
#pragma unroll
    for (int q = 1; q < 8; ++q) v += *(LAS f32x2*)(lds + L_FP + q * 64 + 2 * (lane & 31));
    if (f1 >= 56) {
      st16_sc1(rws, lane < 32 ? WS_G1 + ((mat * 2 + rb) * F + cb * 64) * 8 + lane * 16 : OOB, rec(v[0], v[1], tagA, tagB));
    } else {
      pace_wait(t0, T_K0 + T_HA);
      u32x4 g[3];
#pragma unroll 1
      for (unsigned sp = 0; sp < SPIN_MEM; ++sp) {
        REREAD();
#pragma unroll
        for (int k = 0; k < 3; ++k) g[k] = ld16_sc1(rws, (lane & 31) * 16, WS_G1 + ((k + 1) * F + cb * 64) * 8);
        if (__all(rok(g[0], tagA, tagB) && rok(g[1], tagA, tagB) && rok(g[2], tagA, tagB))) break;
        __builtin_amdgcn_s_sleep(32);
      }
      float hh[2];
#pragma unroll
      for (int e = 0; e < 2; ++e) {
        const float gs = rbf(v[e] + __uint_as_float(g[0][2 * e]));
        const float us = rbf(__uint_as_float(g[1][2 * e]) + __uint_as_float(g[2][2 * e]));
        const float ge = 0.5f * gs * (1.f + tanhf(0.7978845608028654f * (gs + 0.044715f * gs * gs * gs)));
        hh[e] = rbf(ge) * us;
      }
      const float hp = __uint_as_float(pack2(hh[0], hh[1]));
      st16_sc1(rws, (lane < 32 && !(lane & 1)) ? WS_H + cb * 256 + lane * 8 : OOB, rec(hp, dpp<XOR1>(hp), tagA, tagB));
    }
  }
  {
    pace_wait(t0, T_K0 + T_HB);
    u32x4 g;
#pragma unroll 1
    for (unsigned sp = 0; sp < SPIN_MEM; ++sp) {
      REREAD();
      g = ld16_sc1(rws, (lane < 14 ? lane : 13) * 16, WS_H + (ks * 56 + a * 14) * 16);
      if (__all(rok(g, tagA, tagB))) break;
      __builtin_amdgcn_s_sleep(32);
    }
    if (lane < 14) *(LAS f32x4*)(lds + L_H + a * 64 + 4 * lane) = f32x4{lo2f(g[0]), hi2f(g[0]), lo2f(g[2]), hi2f(g[2])};
    float y[8] = {0.f, 0.f, 0.f, 0.f, 0.f, 0.f, 0.f, 0.f};
#pragma unroll
    for (int i = 0; i < 7; ++i) fma8(y, lds[L_H + a * 64 + 8 * i + (lane >> 3)], dw[i]);
#pragma unroll
    for (int e = 0; e < 8; ++e) y[e] += dpp<ROR8>(y[e]);
    const f32x2 z = fold_rows(y);
    if (c < 8) *(LAS f32x2*)(lds + L_F2 + a * 64 + 8 * c + 2 * r) = z;
    cnt_arrive(lds, C_F2, lane);
  }
  const int col0 = s * 128 + 4 * h;
  unsigned xw = 0;
  if (a == 0) xw = __builtin_amdgcn_raw_buffer_load_b32(rx, (col0 + 2 * (lane & 1)) * 2, 0, 0);
  if (a == 3) {
    const unsigned po = __builtin_amdgcn_raw_buffer_load_b32(mkrsrc(out, D * 2), (col0 + 2 * (lane & 1)) * 2, 0, SC1);
    asm volatile("" :: "v"(po));
  }
  if (a == 1) {
    cnt_wait(lds, C_ML, NMAIN);
    const f32x2 ml = *(LAS f32x2*)(lds + L_ML + 2 * (lane & 7));
    const float Mw = max8(ml[0]), Lw = sum8(exp2_(ml[0] - Mw) * ml[1]);
    st16_sc1(rws, lane == 0 ? WS_ML + (h * 8 + s) * 128 : OOB, rec(Mw, Lw, tagA, tagB));
    pace_wait(t0, T_K0 + T_MAIN / 2 + 1500);
    u32x4 g;
#pragma unroll 1
    for (unsigned sp = 0; sp < SPIN_MEM; ++sp) {
      REREAD();
      g = ld16_sc1(rws, (lane & 7) * 128, WS_ML + h * 8 * 128);
      if (__all(rok(g, tagA, tagB))) break;
      __builtin_amdgcn_s_sleep(32);
    }
    const float M = max8(r0(g)), L = sum8(exp2_(r0(g) - M) * r1(g));
    if (lane < 8) lds[L_WG + lane] = exp2_(ml[0] - M) / L;
    cnt_arrive(lds, C_WG, lane);
  }
  pace_wait(t0, T_K0 + T_MAIN - 100);
  __builtin_amdgcn_s_setprio(3);
  if (a == 2) {
    cnt_wait(lds, C_F2, NAUX);
    f32x2 v = *(LAS f32x2*)(lds + L_F2 + 2 * (lane & 31));
#pragma unroll
    for (int q = 1; q < 4; ++q) v += *(LAS f32x2*)(lds + L_F2 + q * 64 + 2 * (lane & 31));
    pace_wait(t0, T_K0 + T_MAIN + 60);
    st16_sc1(rws, lane < 32 ? WS_FFN + (ks * D + cg * 64) * 8 + lane * 16 : OOB, rec(v[0], v[1], tagA, tagB));
  }
  if (a == 0) {
    cnt_wait<false>(lds, C_END, NMAIN);
    f32x2 v = *(LAS f32x2*)(lds + L_MRG + 2 * lane);
#pragma unroll
    for (int w = 1; w < 8; ++w) v += *(LAS f32x2*)(lds + L_MRG + w * 128 + 2 * lane);
    st16_sc1(rws, WS_Q + (h * 8 + s) * 1024 + lane * 16, rec(v[0], v[1], tagC, tagD));
  } else {
    cnt_wait(lds, C_END, NMAIN);
  }
  u32x4 bfr[2][4];
  cnt_wait<false>(lds, C_WT, NMAIN);
#pragma unroll
  for (int q = 0; q < 4; ++q)
#pragma unroll
    for (int d = 0; d < 4; ++d) {
      const unsigned w0 = *(LAS unsigned*)(lds + L_WT + (32 * q + 8 * r + 2 * d) * WT_STR + 16 * a + c);
      const unsigned w1 = *(LAS unsigned*)(lds + L_WT + (32 * q + 8 * r + 2 * d + 1) * WT_STR + 16 * a + c);
      bfr[0][q][d] = __builtin_amdgcn_perm(w1, w0, 0x05040100u);
      bfr[1][q][d] = __builtin_amdgcn_perm(w1, w0, 0x07060302u);
    }
  {
    u32x4 g0, g1;
    const int vo = r * 1024 + (16 * a + c) * 16;
#pragma unroll 1
    for (unsigned sp = 0; sp < SPIN_MEM; ++sp) {
      REREAD();
      g0 = ld16_sc1(rws, vo, WS_Q + h * 8 * 1024);
      g1 = ld16_sc1(rws, vo, WS_Q + (h * 8 + 4) * 1024);
      if (__all(rok(g0, tagC, tagD) && rok(g1, tagC, tagD))) break;
    }
    float o = fold32(r0(g0) + r0(g1), r1(g0) + r1(g1));
    o = fold16(o, o);
    if ((r & 1) == 0) *((LAS u16*)(lds + L_OB) + 32 * a + 2 * c + (r >> 1)) = __builtin_bit_cast(u16, (__bf16)o);
  }
  cnt_arrive(lds, C_OB, lane);
  cnt_wait<false>(lds, C_OB, NAUX);
  {
    f32x4 y0 = {0.f, 0.f, 0.f, 0.f}, y1 = y0;
#pragma unroll
    for (int q = 0; q < 4; ++q) {
      const u32x4 af = *(LAS u32x4*)(lds + L_OB + 16 * q + 4 * r);
      y0 = __builtin_amdgcn_mfma_f32_16x16x32_bf16(__builtin_bit_cast(bf16x8, af), __builtin_bit_cast(bf16x8, bfr[0][q]), y0, 0, 0, 0);
      y1 = __builtin_amdgcn_mfma_f32_16x16x32_bf16(__builtin_bit_cast(bf16x8, af), __builtin_bit_cast(bf16x8, bfr[1][q]), y1, 0, 0, 0);
    }
    st16_sc1(rws, lane < 16 ? WS_Y + (h * 8 + s) * 1024 + (16 * a + lane) * 16 : OOB, rec(y0[0], y1[0], tagA, tagB));
  }
  const rsrc_t rdg = mkrsrc((char*)ws + (6u << 20), 8192);
  if (a == 2) st16_sc1(rdg, lane == 0 ? bid * 16 : OOB, u32x4{t0, tagA, (unsigned)(tm_ - ta_), tagB});
  if (a == 0) {
    u32x4 g, gf;
    pace_wait((unsigned)__builtin_amdgcn_s_memrealtime(), 20);
#pragma unroll 1
    for (unsigned sp = 0; sp < SPIN_MEM; ++sp) {
      REREAD();
      g = ld16_sc1(rws, (lane >> 1) * (8 * 1024) + (lane & 1) * 16, WS_Y + s * 1024 + h * 32);
      gf = ld16_sc1(rws, ((lane >> 1) & 15) * (D * 8) + (lane & 1) * 16, WS_FFN + col0 * 8);
      if (__all(rok(g, tagA, tagB) && rok(gf, tagA, tagB))) break;
    }
    const float f0 = rbf(parity_sum(r0(gf)) * 0.5f), f1v = rbf(parity_sum(r1(gf)) * 0.5f);
    const float a0 = rbf(parity_sum(r0(g))), a1 = rbf(parity_sum(r1(g)));
    const unsigned o = pack2(rbf(lo2f(xw) + a0) + f0, rbf(hi2f(xw) + a1) + f1v);
    __builtin_amdgcn_raw_buffer_store_b32(o, mkrsrc(out, D * 2), lane < 2 ? (col0 + 2 * lane) * 2 : OOB, 0, SC1);
    __builtin_amdgcn_raw_buffer_store_b64(u32x2{(unsigned)e2, (unsigned)(e2 >> 32)}, rws, (bid == 0 && lane == 0) ? WS_EPOCH : OOB, 0, SC1);
    if (bid == 0) {
      u32x4 gg[4];
      for (unsigned sp = 0; sp < SPIN_MEM; ++sp) { REREAD();
        for (int k = 0; k < 4; ++k) gg[k] = ld16_sc1(rdg, lane * 16 + k * 1024, 0);
        if (__all(rok(gg[0], tagA, tagB) && rok(gg[1], tagA, tagB) && rok(gg[2], tagA, tagB) && rok(gg[3], tagA, tagB))) break; }
      int m2 = 0;
      for (int k = 0; k < 4; ++k) m2 += (int)gg[k][2];
      for (int o = 1; o < 64; o <<= 1) m2 += __shfl_xor(m2, o);
      int v = m2 / 256;
      v = v < 0 ? 0 : v > 4000 ? 4000 : v;
      pace_wait((unsigned)ta_, __builtin_amdgcn_readfirstlane(9500 + v * 20));
    }
  }
}

extern "C" void kernel_launch(void* const* d_in, const int* in_sizes, int n_in, void* d_out, int out_size, void* d_ws, size_t ws_size,
                              hipStream_t stream) {
  fused_decode_kernel<<<256, NTHR, 0, stream>>>((const u16*)d_in[0], (const u16*)d_in[1], (const u16*)d_in[2], (const u16*)d_in[3],
                                                 (const u16*)d_in[4], (const u16*)d_in[6], (const u16*)d_in[7], (const u16*)d_in[8],
                                                 (u16*)d_out, d_ws);
}
```

```cpp
#include <hip/hip_runtime.h>
#include <stdint.h>

typedef unsigned u32x4 __attribute__((ext_vector_type(4)));
typedef unsigned u32x2 __attribute__((ext_vector_type(2)));
typedef float f32x4 __attribute__((ext_vector_type(4)));
typedef float f32x2 __attribute__((ext_vector_type(2)));
typedef __bf16 bf16x2 __attribute__((ext_vector_type(2)));
typedef __bf16 bf16x8 __attribute__((ext_vector_type(8)));
typedef unsigned short u16;
#define LAS __attribute__((address_space(3)))
typedef __amdgpu_buffer_rsrc_t rsrc_t;

constexpr int T_F1 = 70;
constexpr int T_W2 = 262;
constexpr int T_K0 = 345;
constexpr int T_MAIN = 7060;
constexpr int PACE_G = 2;
constexpr int T_QPOLL = 315;
constexpr int T_HA = 1600, T_HB = 2800;

constexpr int D = 1024, H = 32, SK = 32768, DH = 128, F = 3584;
constexpr int NMAIN = 8, NAUX = 4, NTHR = (NMAIN + NAUX) * 64;
constexpr int NT = 2, SC1 = 16;
constexpr int OOB = 0x40000000;
constexpr unsigned WS_EPOCH = 0, WS_T0 = 1024, WS_ML = 4096, WS_Q = WS_ML + 256 * 128, WS_Y = WS_Q + 256 * 1024, WS_G1 = WS_Y + 256 * 1024,
                   WS_H = WS_G1 + 4 * F * 8, WS_FFN = WS_H + F * 4, WS_END = WS_FFN + 16 * D * 8;
constexpr int L_XN = 0, L_QP = 1024, L_QS = L_QP + 4 * 128, L_MRG = L_QS + 64, L_ML = L_MRG + 8 * 128, L_WG = L_ML + 16, L_FP = L_WG + 8,
              L_H = L_FP + 8 * 64, L_F2 = L_H + 4 * 64, L_OB = L_F2 + 4 * 64, L_CNT = L_OB + 64, L_SC = L_CNT + 16, L_WT = L_SC + 8 * 512,
              WT_STR = 68, L_END = L_WT + 128 * WT_STR;
constexpr int C_Q = 0, C_XN = 1, C_F2 = 2, C_WT = 3, C_ML = 4, C_WG = 5, C_END = 6, C_QR = 7, C_F1 = 8, C_OB = 9, C_NUM = 10, W_T0 = 12;
constexpr unsigned SPIN_MEM = 1u << 20, SPIN_LDS = 1u << 23;

__device__ __forceinline__ float lo2f(unsigned w) { return __builtin_bit_cast(float, w << 16); }
__device__ __forceinline__ float hi2f(unsigned w) { return __builtin_bit_cast(float, w & 0xffff0000u); }
__device__ __forceinline__ float rbf(float x) { return (float)(__bf16)x; }
__device__ __forceinline__ unsigned pack2(float a, float b) { bf16x2 v = {(__bf16)a, (__bf16)b}; return __builtin_bit_cast(unsigned, v); }
__device__ __forceinline__ float dot2(unsigned k, unsigned q, float acc) {
  return __builtin_amdgcn_fdot2_f32_bf16(__builtin_bit_cast(bf16x2, k), __builtin_bit_cast(bf16x2, q), acc, false);
}
template <int CTRL> __device__ __forceinline__ float dpp(float x) {
  return __builtin_bit_cast(float, __builtin_amdgcn_mov_dpp(__builtin_bit_cast(int, x), CTRL, 0xf, 0xf, true));
}
constexpr int XOR1 = 0xB1, XOR2 = 0x4E, XOR7 = 0x141, ROR8 = 0x128, ROR4 = 0x124, BCAST = 0x150;
__device__ __forceinline__ float fold32(float a, float b) {
  auto t = __builtin_amdgcn_permlane32_swap(__float_as_uint(a), __float_as_uint(b), false, false);
  return __uint_as_float(t[0]) + __uint_as_float(t[1]);
}
__device__ __forceinline__ float fold16(float a, float b) {
  auto t = __builtin_amdgcn_permlane16_swap(__float_as_uint(a), __float_as_uint(b), false, false);
  return __uint_as_float(t[0]) + __uint_as_float(t[1]);
}
__device__ __forceinline__ f32x2 fold_rows(const float (&v)[8]) {
  const float u0 = fold32(v[0], v[4]), u1 = fold32(v[1], v[5]), u2 = fold32(v[2], v[6]), u3 = fold32(v[3], v[7]);
  return f32x2{fold16(u0, u2), fold16(u1, u3)};
}
__device__ __forceinline__ float xrow_sum(float x) { x = fold16(x, x); return fold32(x, x); }
__device__ __forceinline__ float xrow_max(float x) {
  auto s = __builtin_amdgcn_permlane16_swap(__float_as_uint(x), __float_as_uint(x), false, false);
  x = fmaxf(__uint_as_float(s[0]), __uint_as_float(s[1]));
  auto t = __builtin_amdgcn_permlane32_swap(__float_as_uint(x), __float_as_uint(x), false, false);
  return fmaxf(__uint_as_float(t[0]), __uint_as_float(t[1]));
}
__device__ __forceinline__ float parity_sum(float x) {
  x += dpp<XOR2>(x); x += dpp<ROR4>(x); x += dpp<ROR8>(x); return xrow_sum(x);
}
__device__ __forceinline__ float wave_sum(float x) { x += dpp<XOR1>(x); return parity_sum(x); }
__device__ __forceinline__ float max8(float x) { x = fmaxf(x, dpp<XOR1>(x)); x = fmaxf(x, dpp<XOR2>(x)); return fmaxf(x, dpp<XOR7>(x)); }
__device__ __forceinline__ float sum8(float x) { x += dpp<XOR1>(x); x += dpp<XOR2>(x); return x + dpp<XOR7>(x); }
__device__ __forceinline__ float wave_max(float x) { x = max8(x); x = fmaxf(x, dpp<ROR8>(x)); return xrow_max(x); }
__device__ __forceinline__ float exp2_(float x) { return __builtin_amdgcn_exp2f(x); }
__device__ __forceinline__ rsrc_t mkrsrc(const void* p, unsigned bytes) { return __builtin_amdgcn_make_buffer_rsrc((void*)p, 0, (int)bytes, 0x00020000); }
__device__ __forceinline__ u32x4 ld16(rsrc_t r, int voff, int soff) { return __builtin_amdgcn_raw_buffer_load_b128(r, voff, soff, 0); }
__device__ __forceinline__ u32x4 ld16_nt(rsrc_t r, int voff, int soff) { return __builtin_amdgcn_raw_buffer_load_b128(r, voff, soff, NT); }
__device__ __forceinline__ u32x4 ld16_sc1(rsrc_t r, int voff, int soff) { return __builtin_amdgcn_raw_buffer_load_b128(r, voff, soff, SC1); }
__device__ __forceinline__ void st16_sc1(rsrc_t r, int off, u32x4 v) { __builtin_amdgcn_raw_buffer_store_b128(v, r, off, 0, SC1); }
#define SB() __builtin_amdgcn_sched_barrier(0)
#define REREAD() asm volatile("" ::: "memory")
__device__ __forceinline__ void pace_wait(unsigned t0, int due) {
  asm volatile("1:\n\ts_memrealtime s[96:97]\n\ts_waitcnt lgkmcnt(0)\n\ts_sub_u32 s96, s96, %0\n\ts_cmp_lt_i32 s96, %1\n\t"
               "s_cbranch_scc0 2f\n\ts_sleep 1\n\ts_branch 1b\n2:"
               :: "s"(t0), "s"(due) : "s96", "s97", "scc", "memory");
}
__device__ __forceinline__ void fma8(float (&acc)[8], float s, u32x4 w) {
#pragma unroll
  for (int t = 0; t < 4; ++t) { acc[2 * t] = fmaf(s, lo2f(w[t]), acc[2 * t]); acc[2 * t + 1] = fmaf(s, hi2f(w[t]), acc[2 * t + 1]); }
}
__device__ __forceinline__ u32x4 rec(float a, float b, unsigned ta, unsigned tb) { return u32x4{__float_as_uint(a), ta, __float_as_uint(b), tb}; }
__device__ __forceinline__ bool rok(u32x4 g, unsigned ta, unsigned tb) { return g[1] == ta && g[3] == tb; }
__device__ __forceinline__ float r0(u32x4 g) { return __uint_as_float(g[0]); }
__device__ __forceinline__ float r1(u32x4 g) { return __uint_as_float(g[2]); }

__device__ __forceinline__ void cnt_arrive(LAS float* lds, int which, int lane) {
  asm volatile("s_waitcnt lgkmcnt(0)" ::: "memory");
  if (lane == 0) __hip_atomic_fetch_add((LAS unsigned*)(lds + L_CNT + which), 1u, __ATOMIC_RELAXED, __HIP_MEMORY_SCOPE_WORKGROUP);
}
template <bool SLEEP = true>
__device__ __forceinline__ void cnt_wait(LAS float* lds, int which, unsigned n) {
#pragma unroll 1
  for (unsigned sp = 0; sp < SPIN_LDS; ++sp) {
    if (__builtin_amdgcn_readfirstlane(*(volatile LAS unsigned*)(lds + L_CNT + which)) >= n) break;
    if (SLEEP) __builtin_amdgcn_s_sleep(1);
  }
  REREAD();
}

__global__ void __launch_bounds__(NTHR, 3)
fused_decode_kernel(const u16* __restrict__ x, const u16* __restrict__ Kc, const u16* __restrict__ Vc, const u16* __restrict__ wq,
                    const u16* __restrict__ wo, const u16* __restrict__ wg, const u16* __restrict__ wu, const u16* __restrict__ wd,
                    u16* __restrict__ out, void* ws) {
  __shared__ __attribute__((aligned(16))) float lds_[L_END];
  LAS float* lds = (LAS float*)lds_;
  const int tid = threadIdx.x, lane = tid & 63, wave = __builtin_amdgcn_readfirstlane(tid >> 6);
  const int r = lane >> 4, c = lane & 15;
  const int bid = blockIdx.x, xl = bid & 7, j = bid >> 3;
  const int h = xl * 4 + (j >> 3), s = j & 7;
  const int f1 = xl * 28 + j, mat = f1 / 112, rb = (f1 / 56) & 1, cb = f1 % 56;
  asm volatile("" :: "s"(x), "s"(wq), "s"(ws), "s"(f1), "s"(wave));

  if (wave < NMAIN) {
    const rsrc_t rk = mkrsrc(Kc, (unsigned)H * SK * DH * 2), rv = mkrsrc(Vc, (unsigned)H * SK * DH * 2);
    u32x4 bA[16], bB[16];
    const unsigned kvbase = (unsigned)h * (8u << 20) + (unsigned)s * (1u << 20) + wave * 1024;
    const unsigned rot = 32u * ((h + (h >> 2) + (h >> 4)) & 3);
    const int kvoff = lane * 16;
    constexpr int STEP = (T_MAIN << 16) / 256 * PACE_G;
    int due = T_K0 * 65536 - STEP + (STEP / 256) * (wave * 32 + j);
#define PACE(T0, I) do { if ((I) % PACE_G == 0) { due += STEP; pace_wait(T0, due >> 16); } } while (0)
    __builtin_amdgcn_s_barrier();
    const unsigned t0 = (unsigned)__builtin_amdgcn_s_memrealtime();
    SB();
    {
      const rsrc_t rg = mkrsrc(mat ? wu : wg, D * F * 2);
      const int voff = j < 28 ? (lane >> 3) * (F * 2) + (lane & 7) * 16 : OOB, sb = (rb * 512 + wave * 64) * (F * 2) + cb * 128;
      constexpr int FSTEP = ((T_W2 - T_F1) << 16) / 8;
      int fdue = T_F1 * 65536 + (FSTEP / 256) * (wave * 32 + j);
#pragma unroll
      for (int i = 0; i < 8; ++i) { pace_wait(t0, fdue >> 16); fdue += FSTEP; bB[i] = ld16_nt(rg, voff, sb + i * 8 * (F * 2)); SB(); }
    }
#pragma unroll
    for (int i = 0; i < 16; ++i) { PACE(t0, i); bA[i] = ld16_nt(rk, kvoff, kvbase + (rot + i) * 8192); SB(); }
    {
      cnt_wait(lds, C_XN, 1);
      float ag[8] = {0.f, 0.f, 0.f, 0.f, 0.f, 0.f, 0.f, 0.f};
#pragma unroll
      for (int i = 0; i < 8; ++i) {
        const int k = rb * 512 + wave * 64 + 8 * i + (lane >> 3);
        fma8(ag, lds[L_XN + (k & 3) * 256 + (k >> 2)], bB[i]);
      }
#pragma unroll
      for (int e = 0; e < 8; ++e) ag[e] += dpp<ROR8>(ag[e]);
      const f32x2 z = fold_rows(ag);
      if (c < 8) *(LAS f32x2*)(lds + L_FP + wave * 64 + 8 * c + 2 * r) = z;
      cnt_arrive(lds, C_F1, lane);
      SB();
    }
#pragma unroll
    for (int i = 0; i < 16; ++i) { PACE(t0, i); bB[i] = ld16_nt(rk, kvoff, kvbase + (rot + 16 + i) * 8192); SB(); }
    cnt_wait(lds, C_QR, 1);
    const u32x4 qp = *(LAS u32x4*)(lds + L_QS + 4 * c);
    const unsigned t0m = __builtin_amdgcn_readfirstlane(*(LAS unsigned*)(lds + L_CNT + W_T0));
    constexpr float SCALE = 0.08838834764831845f, LOG2E = 1.4426950408889634f;
    LAS float* scp = lds + L_SC + wave * 512 + lane * 8;
    float mx = -INFINITY;
    auto kgroup = [&](u32x4 (&kb)[16], const bool more, const rsrc_t rn, const unsigned tb, const int g) __attribute__((always_inline)) {
      float sx[16];
#pragma unroll
      for (int i = 0; i < 16; ++i) {
        float d = 0.f;
#pragma unroll
        for (int t = 0; t < 4; ++t) d = dot2(kb[i][t], qp[t], d);
        asm volatile("" : "+v"(d));
        sx[i] = d;
        SB();
        if (more) { PACE(t0m, i); kb[i] = ld16_nt(rn, kvoff, tb + i * 8192); SB(); }
      }
#define TR_STEP(HALF, CTRL, BIT) _Pragma("unroll") for (int hh = 0; hh < (HALF); ++hh) { \
        const float lo_ = sx[hh], hi_ = sx[hh + (HALF)]; const float t1_ = lo_ + dpp<CTRL>(lo_), t2_ = hi_ + dpp<CTRL>(hi_); \
        sx[hh] = (c & (BIT)) ? t2_ : t1_; }
      TR_STEP(8, ROR8, 8) TR_STEP(4, XOR7, 4) TR_STEP(2, XOR2, 2) TR_STEP(1, XOR1, 1)
#undef TR_STEP
      const float sc = rbf(rbf(sx[0]) * SCALE) * LOG2E;
      mx = fmaxf(mx, sc);
      scp[g] = sc;
      SB();
    };
    kgroup(bA, false, rk, 0, 0);
#pragma unroll 1
    for (int g = 1; g < 8; ++g)
      kgroup(bB, true, mkrsrc(g == 7 ? Vc : Kc, (unsigned)H * SK * DH * 2), kvbase + (((g + 1) * 16 + rot) & 127u) * 8192, g);
    {
      const float m = wave_max(mx);
      f32x4 p0 = *(LAS f32x4*)scp, p1 = *(LAS f32x4*)(scp + 4);
      float l = 0.f;
#pragma unroll
      for (int e = 0; e < 4; ++e) { p0[e] = exp2_(p0[e] - m); p1[e] = exp2_(p1[e] - m); l += p0[e] + p1[e]; }
      *(LAS f32x4*)scp = p0; *(LAS f32x4*)(scp + 4) = p1;
      l = wave_sum(l);
      if (lane == 0) *(LAS f32x2*)(lds + L_ML + 2 * wave) = f32x2{m, l};
      cnt_arrive(lds, C_ML, lane);
    }
    float acc[8] = {0.f, 0.f, 0.f, 0.f, 0.f, 0.f, 0.f, 0.f};
    auto vgroup = [&](const bool more, const unsigned tb, const float p) __attribute__((always_inline)) {
#define PV_STEP(I) { fma8(acc, dpp<BCAST + (I)>(p), bB[I]); \
        asm volatile("" : "+v"(acc[0]), "+v"(acc[1]), "+v"(acc[2]), "+v"(acc[3]), "+v"(acc[4]), "+v"(acc[5]), "+v"(acc[6]), "+v"(acc[7])); SB(); \
        if (more) { PACE(t0m, I); bB[I] = ld16_nt(rv, kvoff, tb + (I) * 8192); SB(); } \
        else if ((I) == 11) {   \
          pace_wait(t0m, T_K0 + T_MAIN); \
          _Pragma("unroll") for (int u = 0; u < 4; ++u) { \
            bB[u] = ld16_nt(mkrsrc(wo, H * DH * D * 2), r * 2048 + c * 16, (h * 128 + wave * 16 + 4 * u) * 2048 + s * 256); SB(); } } }
      PV_STEP(0) PV_STEP(1) PV_STEP(2) PV_STEP(3) PV_STEP(4) PV_STEP(5) PV_STEP(6) PV_STEP(7)
      PV_STEP(8) PV_STEP(9) PV_STEP(10) PV_STEP(11) PV_STEP(12) PV_STEP(13) PV_STEP(14) PV_STEP(15)
#undef PV_STEP
    };
#pragma unroll 1
    for (int g = 0; g < 7; ++g) vgroup(true, kvbase + (((g + 1) * 16 + rot) & 127u) * 8192, scp[g]);
    __builtin_amdgcn_s_setprio(3);
    cnt_wait(lds, C_WG, 1);
    {
      const float wgt = lds[L_WG + wave];
#pragma unroll
      for (int e = 0; e < 8; ++e) acc[e] *= wgt;
      vgroup(false, 0, scp[7] * wgt);
    }
    *(LAS f32x2*)(lds + L_MRG + wave * 128 + 8 * c + 2 * r) = fold_rows(acc);
    cnt_arrive(lds, C_END, lane);
#pragma unroll
    for (int u = 0; u < 4; ++u) *(LAS u32x4*)(lds + L_WT + (wave * 16 + 4 * u + r) * WT_STR + 4 * c) = bB[u];
    cnt_arrive(lds, C_WT, lane);
    return;
#undef PACE
  }

  const int a = wave - NMAIN;
  const rsrc_t rx = mkrsrc(x, D * 2), rws = mkrsrc(ws, WS_END);
  unsigned t0, tagA, tagB, tagC, tagD;
  unsigned long long e2;
  {
    u32x4 xa = ld16(rx, lane * 32, 0), xb = ld16(rx, lane * 32 + 16, 0);
    const u32x2 ep = __builtin_amdgcn_raw_buffer_load_b64(rws, 0, WS_EPOCH, SC1);
    const rsrc_t rwq = mkrsrc(wq, D * H * DH * 2);
    u32x4 w[8];
    const int voff = r * 8192 + c * 16, sb = (s * 128 + a * 32) * 8192 + h * 256;
#pragma unroll
    for (int i = 0; i < 8; ++i) { w[i] = ld16_nt(rwq, voff, sb + i * 32768); SB(); }
    if (a == 0 && lane < C_NUM) *(LAS unsigned*)(lds + L_CNT + lane) = 0u;
    __syncthreads();
    t0 = (unsigned)__builtin_amdgcn_s_memrealtime();
    asm volatile("" : "+v"(xa), "+v"(xb));
    float v[16];
#pragma unroll
    for (int i = 0; i < 4; ++i) { v[2 * i] = lo2f(xa[i]); v[2 * i + 1] = hi2f(xa[i]); v[8 + 2 * i] = lo2f(xb[i]); v[9 + 2 * i] = hi2f(xb[i]); }
    float sm = 0.f;
#pragma unroll
    for (int i = 0; i < 16; ++i) sm += v[i];
    const float mean = wave_sum(sm) * (1.f / D);
    float ss = 0.f;
#pragma unroll
    for (int i = 0; i < 16; ++i) { const float d = v[i] - mean; ss = fmaf(d, d, ss); }
    const float var = wave_sum(ss) * (1.f / D);
    const float mb = rbf(mean), den = rbf(sqrtf(rbf(rbf(var) + 1e-5f)));
    const float rinv = 1.0f / den;
#pragma unroll
    for (int i = 0; i < 16; ++i) {
      const float nu = rbf(v[i] - mb), q0 = nu * rinv;
      v[i] = rbf(fmaf(fmaf(-q0, den, nu), rinv, q0));
    }
#pragma unroll
    for (int q = 0; q < 4; ++q) *(LAS f32x4*)(lds + L_XN + q * 256 + 4 * lane) = f32x4{v[q], v[4 + q], v[8 + q], v[12 + q]};
    cnt_arrive(lds, C_XN, lane);
    const unsigned long long e1 = (((unsigned long long)__builtin_amdgcn_readfirstlane(ep[1]) << 32) | (unsigned)__builtin_amdgcn_readfirstlane(ep[0])) + 1ull;
    e2 = e1 + 1ull;
    tagA = (unsigned)e1; tagB = (unsigned)(e1 >> 32) + (unsigned)e1 * 0x9E3779B1u;
    tagC = (unsigned)e2; tagD = (unsigned)(e2 >> 32) + (unsigned)e2 * 0x9E3779B1u;
    float acc[8] = {0.f, 0.f, 0.f, 0.f, 0.f, 0.f, 0.f, 0.f};
#pragma unroll
    for (int q = 0; q < 2; ++q) {
      const f32x4 xv = *(LAS f32x4*)(lds + L_XN + r * 256 + s * 32 + a * 8 + 4 * q);
#pragma unroll
      for (int e = 0; e < 4; ++e) fma8(acc, xv[e], w[4 * q + e]);
    }
    *(LAS f32x2*)(lds + L_QP + a * 128 + 8 * c + 2 * r) = fold_rows(acc);
  }
  cnt_arrive(lds, C_Q, lane);
  if (a == 0) {
    cnt_wait<false>(lds, C_Q, NAUX);
    f32x2 v = *(LAS f32x2*)(lds + L_QP + 2 * lane);
#pragma unroll
    for (int q = 1; q < 4; ++q) v += *(LAS f32x2*)(lds + L_QP + q * 128 + 2 * lane);
    st16_sc1(rws, WS_Q + (h * 8 + s) * 1024 + lane * 16, rec(v[0], v[1], tagA, tagB));
    st16_sc1(rws, (bid == 0 && lane == 0) ? WS_T0 : OOB, u32x4{t0, tagA, t0, tagB});
  }
  if (a == 1) {
    pace_wait(t0, T_QPOLL);
    float q0 = 0.f, q1 = 0.f;
    unsigned tg = t0;
#pragma unroll 1
    for (unsigned sp = 0; sp < SPIN_MEM; ++sp) {
      REREAD();
      u32x4 g[8];
#pragma unroll
      for (int i = 0; i < 8; ++i) g[i] = ld16_sc1(rws, lane * 16, WS_Q + (h * 8 + i) * 1024);
      const u32x4 gt = ld16_sc1(rws, 0, WS_T0);
      bool ok = true;
      tg = rok(gt, tagA, tagB) ? gt[0] : t0;
      q0 = 0.f; q1 = 0.f;
#pragma unroll
      for (int i = 0; i < 8; ++i) { ok = ok && rok(g[i], tagA, tagB); q0 += r0(g[i]); q1 += r1(g[i]); }
      if (__all(ok)) break;
      __builtin_amdgcn_s_sleep(2);
    }
    *(LAS unsigned*)(lds + L_QS + lane) = pack2(q0, q1);
    if (lane == 0) *(LAS unsigned*)(lds + L_CNT + W_T0) = t0 - (unsigned)min(max((int)(t0 - tg), 0), 100);
    cnt_arrive(lds, C_QR, lane);
  }
  const int ks = xl * 2 + (j >> 4), cg = j & 15;
  u32x4 dw[7];
  {
    const rsrc_t rd = mkrsrc(wd, D * F * 2);
    const int voff = (lane >> 3) * 2048 + (lane & 7) * 16, sb = (ks * 224 + a * 56) * 2048 + cg * 128;
    constexpr int WSTEP = ((T_K0 - T_W2) << 16) / 7;
    int wdue = T_W2 * 65536 + (WSTEP / 128) * (a * 32 + j);
#pragma unroll
    for (int i = 0; i < 7; ++i) { pace_wait(t0, wdue >> 16); wdue += WSTEP; dw[i] = ld16_nt(rd, voff, sb + i * 16384); SB(); }
  }
  if (a == 0 && j < 28) {
    cnt_wait(lds, C_F1, NMAIN);
    f32x2 v = *(LAS f32x2*)(lds + L_FP + 2 * (lane & 31));
#pragma unroll
    for (int q = 1; q < 8; ++q) v += *(LAS f32x2*)(lds + L_FP + q * 64 + 2 * (lane & 31));
    if (f1 >= 56) {
      st16_sc1(rws, lane < 32 ? WS_G1 + ((mat * 2 + rb) * F + cb * 64) * 8 + lane * 16 : OOB, rec(v[0], v[1], tagA, tagB));
    } else {
      pace_wait(t0, T_K0 + T_HA);
      u32x4 g[3];
#pragma unroll 1
      for (unsigned sp = 0; sp < SPIN_MEM; ++sp) {
        REREAD();
#pragma unroll
        for (int k = 0; k < 3; ++k) g[k] = ld16_sc1(rws, (lane & 31) * 16, WS_G1 + ((k + 1) * F + cb * 64) * 8);
        if (__all(rok(g[0], tagA, tagB) && rok(g[1], tagA, tagB) && rok(g[2], tagA, tagB))) break;
        __builtin_amdgcn_s_sleep(32);
      }
      float hh[2];
#pragma unroll
      for (int e = 0; e < 2; ++e) {
        const float gs = rbf(v[e] + __uint_as_float(g[0][2 * e]));
        const float us = rbf(__uint_as_float(g[1][2 * e]) + __uint_as_float(g[2][2 * e]));
        const float ge = 0.5f * gs * (1.f + tanhf(0.7978845608028654f * (gs + 0.044715f * gs * gs * gs)));
        hh[e] = rbf(ge) * us;
      }
      const float hp = __uint_as_float(pack2(hh[0], hh[1]));
      st16_sc1(rws, (lane < 32 && !(lane & 1)) ? WS_H + cb * 256 + lane * 8 : OOB, rec(hp, dpp<XOR1>(hp), tagA, tagB));
    }
  }
  {
    pace_wait(t0, T_K0 + T_HB);
    u32x4 g;
#pragma unroll 1
    for (unsigned sp = 0; sp < SPIN_MEM; ++sp) {
      REREAD();
      g = ld16_sc1(rws, (lane < 14 ? lane : 13) * 16, WS_H + (ks * 56 + a * 14) * 16);
      if (__all(rok(g, tagA, tagB))) break;
      __builtin_amdgcn_s_sleep(32);
    }
    if (lane < 14) *(LAS f32x4*)(lds + L_H + a * 64 + 4 * lane) = f32x4{lo2f(g[0]), hi2f(g[0]), lo2f(g[2]), hi2f(g[2])};
    float y[8] = {0.f, 0.f, 0.f, 0.f, 0.f, 0.f, 0.f, 0.f};
#pragma unroll
    for (int i = 0; i < 7; ++i) fma8(y, lds[L_H + a * 64 + 8 * i + (lane >> 3)], dw[i]);
#pragma unroll
    for (int e = 0; e < 8; ++e) y[e] += dpp<ROR8>(y[e]);
    const f32x2 z = fold_rows(y);
    if (c < 8) *(LAS f32x2*)(lds + L_F2 + a * 64 + 8 * c + 2 * r) = z;
    cnt_arrive(lds, C_F2, lane);
  }
  const int col0 = s * 128 + 4 * h;
  unsigned xw = 0;
  if (a == 0) xw = __builtin_amdgcn_raw_buffer_load_b32(rx, (col0 + 2 * (lane & 1)) * 2, 0, 0);
  if (a == 3) {
    const unsigned po = __builtin_amdgcn_raw_buffer_load_b32(mkrsrc(out, D * 2), (col0 + 2 * (lane & 1)) * 2, 0, SC1);
    asm volatile("" :: "v"(po));
  }
  if (a == 1) {
    cnt_wait(lds, C_ML, NMAIN);
    const f32x2 ml = *(LAS f32x2*)(lds + L_ML + 2 * (lane & 7));
    const float Mw = max8(ml[0]), Lw = sum8(exp2_(ml[0] - Mw) * ml[1]);
    st16_sc1(rws, lane == 0 ? WS_ML + (h * 8 + s) * 128 : OOB, rec(Mw, Lw, tagA, tagB));
    pace_wait(t0, T_K0 + T_MAIN / 2 + 1500);
    u32x4 g;
#pragma unroll 1
    for (unsigned sp = 0; sp < SPIN_MEM; ++sp) {
      REREAD();
      g = ld16_sc1(rws, (lane & 7) * 128, WS_ML + h * 8 * 128);
      if (__all(rok(g, tagA, tagB))) break;
      __builtin_amdgcn_s_sleep(32);
    }
    const float M = max8(r0(g)), L = sum8(exp2_(r0(g) - M) * r1(g));
    if (lane < 8) lds[L_WG + lane] = exp2_(ml[0] - M) / L;
    cnt_arrive(lds, C_WG, lane);
  }
  pace_wait(t0, T_K0 + T_MAIN - 100);
  __builtin_amdgcn_s_setprio(3);
  if (a == 2) {
    cnt_wait(lds, C_F2, NAUX);
    f32x2 v = *(LAS f32x2*)(lds + L_F2 + 2 * (lane & 31));
#pragma unroll
    for (int q = 1; q < 4; ++q) v += *(LAS f32x2*)(lds + L_F2 + q * 64 + 2 * (lane & 31));
    pace_wait(t0, T_K0 + T_MAIN + 60);
    st16_sc1(rws, lane < 32 ? WS_FFN + (ks * D + cg * 64) * 8 + lane * 16 : OOB, rec(v[0], v[1], tagA, tagB));
  }
  if (a == 0) {
    cnt_wait<false>(lds, C_END, NMAIN);
    f32x2 v = *(LAS f32x2*)(lds + L_MRG + 2 * lane);
#pragma unroll
    for (int w = 1; w < 8; ++w) v += *(LAS f32x2*)(lds + L_MRG + w * 128 + 2 * lane);
    st16_sc1(rws, WS_Q + (h * 8 + s) * 1024 + lane * 16, rec(v[0], v[1], tagC, tagD));
  } else {
    cnt_wait(lds, C_END, NMAIN);
  }
  u32x4 bfr[2][4];
  cnt_wait<false>(lds, C_WT, NMAIN);
#pragma unroll
  for (int q = 0; q < 4; ++q)
#pragma unroll
    for (int d = 0; d < 4; ++d) {
      const unsigned w0 = *(LAS unsigned*)(lds + L_WT + (32 * q + 8 * r + 2 * d) * WT_STR + 16 * a + c);
      const unsigned w1 = *(LAS unsigned*)(lds + L_WT + (32 * q + 8 * r + 2 * d + 1) * WT_STR + 16 * a + c);
      bfr[0][q][d] = __builtin_amdgcn_perm(w1, w0, 0x05040100u);
      bfr[1][q][d] = __builtin_amdgcn_perm(w1, w0, 0x07060302u);
    }
  {
    u32x4 g0, g1;
    const int vo = r * 1024 + (16 * a + c) * 16;
#pragma unroll 1
    for (unsigned sp = 0; sp < SPIN_MEM; ++sp) {
      REREAD();
      g0 = ld16_sc1(rws, vo, WS_Q + h * 8 * 1024);
      g1 = ld16_sc1(rws, vo, WS_Q + (h * 8 + 4) * 1024);
      if (__all(rok(g0, tagC, tagD) && rok(g1, tagC, tagD))) break;
    }
    float o = fold32(r0(g0) + r0(g1), r1(g0) + r1(g1));
    o = fold16(o, o);
    if ((r & 1) == 0) *((LAS u16*)(lds + L_OB) + 32 * a + 2 * c + (r >> 1)) = __builtin_bit_cast(u16, (__bf16)o);
  }
  cnt_arrive(lds, C_OB, lane);
  cnt_wait<false>(lds, C_OB, NAUX);
  {
    f32x4 y0 = {0.f, 0.f, 0.f, 0.f}, y1 = y0;
#pragma unroll
    for (int q = 0; q < 4; ++q) {
      const u32x4 af = *(LAS u32x4*)(lds + L_OB + 16 * q + 4 * r);
      y0 = __builtin_amdgcn_mfma_f32_16x16x32_bf16(__builtin_bit_cast(bf16x8, af), __builtin_bit_cast(bf16x8, bfr[0][q]), y0, 0, 0, 0);
      y1 = __builtin_amdgcn_mfma_f32_16x16x32_bf16(__builtin_bit_cast(bf16x8, af), __builtin_bit_cast(bf16x8, bfr[1][q]), y1, 0, 0, 0);
    }
    st16_sc1(rws, lane < 16 ? WS_Y + (h * 8 + s) * 1024 + (16 * a + lane) * 16 : OOB, rec(y0[0], y1[0], tagA, tagB));
  }
  if (a == 0) {
    u32x4 g, gf;
    pace_wait((unsigned)__builtin_amdgcn_s_memrealtime(), 20);
#pragma unroll 1
    for (unsigned sp = 0; sp < SPIN_MEM; ++sp) {
      REREAD();
      g = ld16_sc1(rws, (lane >> 1) * (8 * 1024) + (lane & 1) * 16, WS_Y + s * 1024 + h * 32);
      gf = ld16_sc1(rws, ((lane >> 1) & 15) * (D * 8) + (lane & 1) * 16, WS_FFN + col0 * 8);
      if (__all(rok(g, tagA, tagB) && rok(gf, tagA, tagB))) break;
    }
    const float f0 = rbf(parity_sum(r0(gf)) * 0.5f), f1v = rbf(parity_sum(r1(gf)) * 0.5f);
    const float a0 = rbf(parity_sum(r0(g))), a1 = rbf(parity_sum(r1(g)));
    const unsigned o = pack2(rbf(lo2f(xw) + a0) + f0, rbf(hi2f(xw) + a1) + f1v);
    __builtin_amdgcn_raw_buffer_store_b32(o, mkrsrc(out, D * 2), lane < 2 ? (col0 + 2 * lane) * 2 : OOB, 0, SC1);
    __builtin_amdgcn_raw_buffer_store_b64(u32x2{(unsigned)e2, (unsigned)(e2 >> 32)}, rws, (bid == 0 && lane == 0) ? WS_EPOCH : OOB, 0, SC1);
  }
}

extern "C" void kernel_launch(void* const* d_in, const int* in_sizes, int n_in, void* d_out, int out_size, void* d_ws, size_t ws_size,
                              hipStream_t stream) {
  fused_decode_kernel<<<256, NTHR, 0, stream>>>((const u16*)d_in[0], (const u16*)d_in[1], (const u16*)d_in[2], (const u16*)d_in[3],
                                                 (const u16*)d_in[4], (const u16*)d_in[6], (const u16*)d_in[7], (const u16*)d_in[8],
                                                 (u16*)d_out, d_ws);
}
```
